```python
import torch

def _softmax(x, axis=-1):
    return torch.softmax(x, dim=axis)
def _gelu(x):
    return torch.nn.functional.gelu(x, approximate="tanh")
def _silu(x):
    return torch.nn.functional.silu(x)
def _sigmoid(x):
    return torch.sigmoid(x)
def _layernorm(x, axis=-1, eps=1e-5):
    m = x.mean(dim=axis, keepdim=True)
    v = x.var(dim=axis, keepdim=True, unbiased=False)
    return (x - m) / torch.sqrt(v + eps)
def _scatter_add(tgt, idx, src, axis=0):
    return tgt.clone().index_add_(axis, idx, src)
def _segment_sum(src, seg, n):
    out = torch.zeros((n,) + src.shape[1:], dtype=src.dtype)
    return out.index_add_(0, seg, src)
def _pad(x, pw):
    flat = tuple(v for pair in reversed(pw) for v in pair)
    return torch.nn.functional.pad(x, flat)

kernel_name = 'concat_heads__gqa_block_cached__mha_block_cached__near_ridge'

def setup_inputs():

    _w = (lambda a: (lambda t: t.to(torch.bfloat16) if t.is_floating_point() else t)(torch.as_tensor(a)))
    def _A_setup():
        B, Sq, D = 8, 176, 448
        Hq, Hkv, Sk, Dh = 16, 8, 2048, 256
        assert Hq % Hkv == 0, (Hq, Hkv)
        return {
            "x": _w((lambda *s: torch.randn(*s, dtype=torch.bfloat16) * 0.8)(B, Sq, D)),
            "k_cache": _w((lambda *s: torch.randn(*s, dtype=torch.bfloat16) * 0.8)(B, Hkv, Sk, Dh)),
            "v_cache": _w((lambda *s: torch.randn(*s, dtype=torch.bfloat16) * 0.05)(B, Hkv, Sk, Dh)),
            "wq": _w((lambda *s: torch.randn(*s, dtype=torch.bfloat16) * 0.8)(D, Hq * Dh) * (D ** -0.5)),
            "wo": _w((lambda *s: torch.randn(*s, dtype=torch.bfloat16) * 0.05)(Hq * Dh, D) * ((Hq * Dh) ** -0.5)),
        }
    _A_in = _A_setup()

    def _B_setup():
        B, Sq, D = 8, 176, 448
        H, Sk, Dh = 16, 2048, 256
        return {
            "x": _w((lambda *s: torch.randn(*s, dtype=torch.bfloat16) * 0.8)(B, Sq, D)),
            "k_cache": _w((lambda *s: torch.randn(*s, dtype=torch.bfloat16) * 0.8)(B, H, Sk, Dh)),
            "v_cache": _w((lambda *s: torch.randn(*s, dtype=torch.bfloat16) * 0.05)(B, H, Sk, Dh)),
            "wq": _w((lambda *s: torch.randn(*s, dtype=torch.bfloat16) * 0.8)(D, H * Dh) * (D ** -0.5)),
            "wo": _w((lambda *s: torch.randn(*s, dtype=torch.bfloat16) * 0.05)(H * Dh, D) * ((H * Dh) ** -0.5)),
        }
    _B_in = _B_setup()

    return {
        "A_x": _A_in["x"],
        "A_k_cache": _A_in["k_cache"],
        "A_v_cache": _A_in["v_cache"],
        "A_wq": _A_in["wq"],
        "A_wo": _A_in["wo"],
        "B_x": _B_in["x"],
        "B_k_cache": _B_in["k_cache"],
        "B_v_cache": _B_in["v_cache"],
        "B_wq": _B_in["wq"],
        "B_wo": _B_in["wo"],
    }

def reference(A_x, A_k_cache, A_v_cache, A_wq, A_wo, B_x, B_k_cache, B_v_cache, B_wq, B_wo):
    def _A_ref(x, k_cache, v_cache, wq, wo):
        B, Sq, D = x.shape
        Hq, Dh = 16, 256
        Hkv = k_cache.shape[1]
        rep = Hq // Hkv
        q = (lambda x, *d: x.permute(*d))((x @ wq).reshape(B, Sq, Hq, Dh), 0, 2, 1, 3)
        k = (lambda x, n, axis: torch.repeat_interleave(x, n, dim=axis))(k_cache, rep, axis=1)
        v = (lambda x, n, axis: torch.repeat_interleave(x, n, dim=axis))(v_cache, rep, axis=1)
        scores = torch.einsum("bhqd,bhkd->bhqk", q, k) * (Dh ** -0.5)
        p = _softmax(scores, axis=-1)
        o = torch.einsum("bhqk,bhkd->bhqd", p, v)
        return (lambda x, *d: x.permute(*d))(o, 0, 2, 1, 3).reshape(B, Sq, Hq * Dh) @ wo

    def _B_ref(x, k_cache, v_cache, wq, wo):
        B, Sq, D = x.shape
        H, Dh = 16, 256
        q = (lambda x, *d: x.permute(*d))((x @ wq).reshape(B, Sq, H, Dh), 0, 2, 1, 3)
        scores = torch.einsum("bhqd,bhkd->bhqk", q, k_cache) * (Dh ** -0.5)
        p = _softmax(scores, axis=-1)
        o = torch.einsum("bhqk,bhkd->bhqd", p, v_cache)
        return (lambda x, *d: x.permute(*d))(o, 0, 2, 1, 3).reshape(B, Sq, H * Dh) @ wo

    a = _A_ref(A_x, A_k_cache, A_v_cache, A_wq, A_wo)
    b = _B_ref(B_x, B_k_cache, B_v_cache, B_wq, B_wo)
    return torch.concatenate([a, b], axis=-1)
```

```cpp
#define TOPPF 1
#include <hip/hip_runtime.h>
#include <hip/hip_bf16.h>
#include <stdint.h>
#define FUSE 0

typedef float    f32x4  __attribute__((ext_vector_type(4)));
typedef __bf16   bf16x8 __attribute__((ext_vector_type(8)));
typedef __bf16   bf16x2 __attribute__((ext_vector_type(2)));
typedef float    f32x2  __attribute__((ext_vector_type(2)));
typedef short    s16x4  __attribute__((ext_vector_type(4)));
typedef short    s16x8  __attribute__((ext_vector_type(8)));
typedef unsigned u32x4  __attribute__((ext_vector_type(4)));
typedef unsigned u32x2  __attribute__((ext_vector_type(2)));

constexpr int NB = 8, SQ = 176, DM = 448, NH = 16, SK = 2048, DH = 256;
constexpr int LDS_BYTES = 163840;

extern "C" __global__ __launch_bounds__(256, 1)
void attn_fused(const char* __restrict__ xA, const char* __restrict__ wqA, const char* __restrict__ kA, const char* __restrict__ vA,
                const char* __restrict__ xB, const char* __restrict__ wqB, const char* __restrict__ kB, const char* __restrict__ vB,
                const char* __restrict__ woA, const char* __restrict__ woB, char* __restrict__ part0, unsigned short* __restrict__ out) {
  extern __shared__ __attribute__((aligned(16))) char lds[];
  {
    unsigned long long pc; unsigned t0, u0, u1, u2, u3;
    asm volatile("s_getpc_b64 %[pc]\nL_pf0:\n"
                 "v_lshlrev_b32 %[t], 7, %[tid]\n"
                 "v_min_u32 %[u0], L_end-L_pf0-4, %[t]\n v_add_u32 %[t], 0x8000, %[t]\n"
                 "v_min_u32 %[u1], L_end-L_pf0-4, %[t]\n v_add_u32 %[t], 0x8000, %[t]\n"
                 "v_min_u32 %[u2], L_end-L_pf0-4, %[t]\n v_add_u32 %[t], 0x8000, %[t]\n"
                 "v_min_u32 %[u3], L_end-L_pf0-4, %[t]\n"
                 "global_load_dword a252, %[u0], %[pc]\n global_load_dword a253, %[u1], %[pc]\n"
                 "global_load_dword a254, %[u2], %[pc]\n global_load_dword a255, %[u3], %[pc]\n"
                 : [pc] "=&s"(pc), [t] "=&v"(t0), [u0] "=&v"(u0), [u1] "=&v"(u1), [u2] "=&v"(u2), [u3] "=&v"(u3)
                 : [tid] "v"(threadIdx.x) : "a252", "a253", "a254", "a255", "memory");
  }
  const unsigned bid = blockIdx.x;
  const unsigned xcd = bid & 7u, idx = bid >> 3, p = idx >> 4, jj = idx & 15u;
  const unsigned h = 4u * (xcd & 3u) + (jj & 3u), b = 4u * (xcd >> 2) + (jj >> 2);
  const unsigned tid = threadIdx.x, l = tid & 63u;
  const unsigned wave = __builtin_amdgcn_readfirstlane(tid >> 6);
  const char* x  = (p ? xB : xA) + (size_t)b * (SQ * DM * 2);
  const char* wq = (p ? wqB : wqA) + h * 512u;
  const size_t kvoff = (size_t)(p ? (b * 16u + h) : (b * 8u + (h >> 1))) * (SK * DH * 2);
  const char* k = (p ? kB : kA) + kvoff;
  const char* v = (p ? vB : vA) + kvoff;
  const char* wo = (p ? woB : woA) + h * (256u * 896u);
  char* part = part0 + (size_t)((p * 8u + b) * 16u + h) * (11u * 14336u);
  const unsigned wnr = DM * 8192u - h * 512u;
  char* linv = part0 + (48u << 20) + (size_t)((p * 8u + b) * 16u + h) * (176u * 4u);
  const unsigned g = l >> 4, q = (l >> 2) & 3u, pp = l & 3u;
  const unsigned drow = (l >> 2) & 7u, dst = l >> 5, dch = pp ^ ((l >> 3) & 2u);
  const unsigned dmakv = drow * 512u + dst * 64u + dch * 16u;
  const unsigned dmaw = drow * 8192u + dst * 64u + dch * 16u;
  const unsigned klane = ((l & 15u) >> 3) * 4096u + (l & 7u) * 64u + 16u * (g ^ (((l >> 2) & 1u) << 1));
  const unsigned vl0 = (g >> 1) * 4096u + (4u * (g & 1u) + q) * 64u + 16u * ((0u + (pp >> 1)) ^ (2u * (g & 1u))) + 8u * (pp & 1u);
  const unsigned vl1 = (g >> 1) * 4096u + (4u * (g & 1u) + q) * 64u + 16u * ((2u + (pp >> 1)) ^ (2u * (g & 1u))) + 8u * (pp & 1u);
  const unsigned wl0 = g * 4096u + q * 64u + 16u * pp;
  const unsigned wl1 = g * 4096u + (4u + q) * 64u + 16u * (pp ^ 2u);
  const unsigned dmax = (l >> 3) * 896u + 16u * ((l & 7u) ^ (l >> 3));
  const unsigned xrdb = (unsigned)(size_t)lds + wave * 16384u + ((l & 15u) >> 3) * 1024u + (l & 7u) * 128u;
  const unsigned xrd0 = xrdb + 16u * (g ^ (l & 7u)), xrd1 = xrdb + 16u * ((4u + g) ^ (l & 7u));
  const unsigned ldsb = (unsigned)(size_t)lds;
  asm volatile(
"s_mov_b64 s[40:41], %[k]\n"
"s_mov_b32 s43, 0x00020000\n"
"s_mov_b64 s[44:45], %[v]\n"
"s_mov_b32 s47, 0x00020000\n"
"s_mov_b64 s[48:49], %[wq]\n"
"s_mov_b32 s51, 0x00020000\n"
"s_mov_b64 s[52:53], %[x]\n"
"s_mov_b32 s55, 0x00020000\n"
"s_mov_b32 s42, 0x100000\n"
"s_mov_b32 s46, 0x100000\n"
"s_mov_b32 s50, %[wnr]\n"
"s_mov_b32 s54, 157696\n"
"s_mov_b32 s72, 0\n"
"s_cmp_eq_u32 %[wave], 1\n"
"s_cselect_b32 s72, 2, s72\n"
"s_cmp_eq_u32 %[wave], 2\n"
"s_cselect_b32 s72, 4, s72\n"
"s_cmp_eq_u32 %[wave], 3\n"
"s_cselect_b32 s72, 6, s72\n"
"s_lshl_b32 s81, s72, 12\n"
"s_lshl_b32 s82, s72, 16\n"
"s_lshl_b32 s84, s72, 2\n"
"s_add_u32 s40, s40, s81\n"
"s_addc_u32 s41, s41, 0\n"
"s_sub_u32 s42, s42, s81\n"
"s_add_u32 s44, s44, s81\n"
"s_addc_u32 s45, s45, 0\n"
"s_sub_u32 s46, s46, s81\n"
"s_add_u32 s48, s48, s82\n"
"s_addc_u32 s49, s49, 0\n"
"s_sub_u32 s50, s50, s82\n"
"s_mov_b32 s60, %[lds]\n"
"s_add_u32 s61, %[lds], 163840\n"
"s_mov_b32 s57, %[lds]\n"
"s_add_u32 s58, %[lds], s81\n"
"s_add_u32 s59, s58, 163840\n"
"s_mov_b32 s56, s58\n"
"s_mov_b32 s62, 4096\n"
"s_mov_b32 s63, 65536\n"
"s_mov_b64 s[64:65], %[part]\n"
"s_mov_b32 s66, 157696\n"
"s_mov_b32 s67, 0x00020000\n"
"s_mul_i32 s68, %[wave], 40960\n"
"s_mov_b32 s71, 0\n"
"s_mov_b32 s73, 0x41000000\n"
"s_mov_b32 s86, 0x3db8aa3b\n"
"s_mov_b32 s87, 0x3db8aa3b\n"
"s_mul_i32 s74, %[wave], 43008\n"
"s_add_u32 s75, s74, 14336\n"
"s_add_u32 s76, s74, 28672\n"
"s_add_u32 s77, s74, 7168\n"
"s_add_u32 s78, s75, 7168\n"
"s_add_u32 s79, s76, 7168\n"
"s_lshl_b32 s85, %[wave], 14\n"
"s_add_u32 s85, s85, %[lds]\n"
"s_cmp_eq_u32 %[wave], 3\n"
"s_cbranch_scc1 L_wave3\n"
"v_mbcnt_lo_u32_b32 v10, -1, 0\n"
"v_mbcnt_hi_u32_b32 v10, -1, v10\n"
"v_lshlrev_b32 v10, 7, v10\n"
"s_add_u32 m0, s85, 1024\n"
"s_nop 0\n"
"buffer_load_dwordx4 %[dmax], s[52:55], s74 offen offset:0 lds\n"
"s_add_u32 m0, s85, 2048\n"
"s_nop 0\n"
"buffer_load_dwordx4 %[dmax], s[52:55], s77 offen offset:0 lds\n"
"s_add_u32 m0, s85, 3072\n"
"s_nop 0\n"
"buffer_load_dwordx4 %[dmax], s[52:55], s75 offen offset:0 lds\n"
"s_add_u32 m0, s85, 4096\n"
"s_nop 0\n"
"buffer_load_dwordx4 %[dmax], s[52:55], s78 offen offset:0 lds\n"
"s_add_u32 m0, s85, 5120\n"
"s_nop 0\n"
"buffer_load_dwordx4 %[dmax], s[52:55], s76 offen offset:0 lds\n"
"s_add_u32 m0, s85, 6144\n"
"s_nop 0\n"
"buffer_load_dwordx4 %[dmax], s[52:55], s79 offen offset:0 lds\n"
"s_add_u32 m0, s85, 9088\n"
"s_nop 0\n"
"buffer_load_dwordx4 %[dmax], s[52:55], s74 offen offset:128 lds\n"
"s_add_u32 m0, s85, 10112\n"
"s_nop 0\n"
"buffer_load_dwordx4 %[dmax], s[52:55], s77 offen offset:128 lds\n"
"s_add_u32 m0, s85, 11136\n"
"s_nop 0\n"
"buffer_load_dwordx4 %[dmax], s[52:55], s75 offen offset:128 lds\n"
"s_add_u32 m0, s85, 12160\n"
"s_nop 0\n"
"buffer_load_dwordx4 %[dmax], s[52:55], s78 offen offset:128 lds\n"
"s_add_u32 m0, s85, 13184\n"
"s_nop 0\n"
"buffer_load_dwordx4 %[dmax], s[52:55], s76 offen offset:128 lds\n"
"s_add_u32 m0, s85, 14208\n"
"s_nop 0\n"
"buffer_load_dwordx4 %[dmax], s[52:55], s79 offen offset:128 lds\n"
"s_add_u32 s56, s58, 131072\n"
"s_add_u32 m0, s56, 0\n"
"s_nop 0\n"
"buffer_load_dwordx4 %[dmaw], s[48:51], 0 offen offset:0 lds\n"
"s_add_u32 m0, s56, 896\n"
"s_nop 0\n"
"buffer_load_dwordx4 %[dmaw], s[48:51], 0 offen offset:128 lds\n"
"s_add_u32 m0, s56, 1792\n"
"s_nop 0\n"
"buffer_load_dwordx4 %[dmaw], s[48:51], 0 offen offset:256 lds\n"
"s_add_u32 m0, s56, 2688\n"
"s_nop 0\n"
"buffer_load_dwordx4 %[dmaw], s[48:51], 0 offen offset:384 lds\n"
"s_add_u32 m0, s56, 4096\n"
"s_nop 0\n"
"buffer_load_dwordx4 %[dmaw], s[48:51], s63 offen offset:0 lds\n"
"s_add_u32 m0, s56, 4992\n"
"s_nop 0\n"
"buffer_load_dwordx4 %[dmaw], s[48:51], s63 offen offset:128 lds\n"
"s_add_u32 m0, s56, 5888\n"
"s_nop 0\n"
"buffer_load_dwordx4 %[dmaw], s[48:51], s63 offen offset:256 lds\n"
"s_add_u32 m0, s56, 6784\n"
"s_nop 0\n"
"buffer_load_dwordx4 %[dmaw], s[48:51], s63 offen offset:384 lds\n"
"s_add_u32 s48, s48, 0x80000\n"
"s_addc_u32 s49, s49, 0\n"
"s_sub_u32 s50, s50, 0x80000\n"
"s_cselect_b32 s50, 0, s50\n"
"s_add_u32 s56, s58, 65536\n"
"s_add_u32 m0, s56, 0\n"
"s_nop 0\n"
"buffer_load_dwordx4 %[dmaw], s[48:51], 0 offen offset:0 lds\n"
"s_add_u32 m0, s56, 896\n"
"s_nop 0\n"
"buffer_load_dwordx4 %[dmaw], s[48:51], 0 offen offset:128 lds\n"
"s_add_u32 m0, s56, 1792\n"
"s_nop 0\n"
"buffer_load_dwordx4 %[dmaw], s[48:51], 0 offen offset:256 lds\n"
"s_add_u32 m0, s56, 2688\n"
"s_nop 0\n"
"buffer_load_dwordx4 %[dmaw], s[48:51], 0 offen offset:384 lds\n"
"s_add_u32 m0, s56, 4096\n"
"s_nop 0\n"
"buffer_load_dwordx4 %[dmaw], s[48:51], s63 offen offset:0 lds\n"
"s_add_u32 m0, s56, 4992\n"
"s_nop 0\n"
"buffer_load_dwordx4 %[dmaw], s[48:51], s63 offen offset:128 lds\n"
"s_add_u32 m0, s56, 5888\n"
"s_nop 0\n"
"buffer_load_dwordx4 %[dmaw], s[48:51], s63 offen offset:256 lds\n"
"s_add_u32 m0, s56, 6784\n"
"s_nop 0\n"
"buffer_load_dwordx4 %[dmaw], s[48:51], s63 offen offset:384 lds\n"
"s_add_u32 s48, s48, 0x80000\n"
"s_addc_u32 s49, s49, 0\n"
"s_sub_u32 s50, s50, 0x80000\n"
"s_cselect_b32 s50, 0, s50\n"
"s_add_u32 s56, s58, 98304\n"
"s_add_u32 m0, s56, 0\n"
"s_nop 0\n"
"buffer_load_dwordx4 %[dmaw], s[48:51], 0 offen offset:0 lds\n"
"s_add_u32 m0, s56, 896\n"
"s_nop 0\n"
"buffer_load_dwordx4 %[dmaw], s[48:51], 0 offen offset:128 lds\n"
"s_add_u32 m0, s56, 1792\n"
"s_nop 0\n"
"buffer_load_dwordx4 %[dmaw], s[48:51], 0 offen offset:256 lds\n"
"s_add_u32 m0, s56, 2688\n"
"s_nop 0\n"
"buffer_load_dwordx4 %[dmaw], s[48:51], 0 offen offset:384 lds\n"
"s_add_u32 m0, s56, 4096\n"
"s_nop 0\n"
"buffer_load_dwordx4 %[dmaw], s[48:51], s63 offen offset:0 lds\n"
"s_add_u32 m0, s56, 4992\n"
"s_nop 0\n"
"buffer_load_dwordx4 %[dmaw], s[48:51], s63 offen offset:128 lds\n"
"s_add_u32 m0, s56, 5888\n"
"s_nop 0\n"
"buffer_load_dwordx4 %[dmaw], s[48:51], s63 offen offset:256 lds\n"
"s_add_u32 m0, s56, 6784\n"
"s_nop 0\n"
"buffer_load_dwordx4 %[dmaw], s[48:51], s63 offen offset:384 lds\n"
"s_add_u32 s48, s48, 0x80000\n"
"s_addc_u32 s49, s49, 0\n"
"s_sub_u32 s50, s50, 0x80000\n"
"s_cselect_b32 s50, 0, s50\n"
"s_waitcnt vmcnt(30)\n"
"ds_read_b128 a[0:3], %[xrd0] offset:1024\n"
"ds_read_b128 a[4:7], %[xrd1] offset:1024\n"
"ds_read_b128 a[56:59], %[xrd0] offset:3072\n"
"ds_read_b128 a[60:63], %[xrd1] offset:3072\n"
"ds_read_b128 a[112:115], %[xrd0] offset:5120\n"
"ds_read_b128 a[116:119], %[xrd1] offset:5120\n"
"s_waitcnt vmcnt(24)\n"
"ds_read_b128 a[8:11], %[xrd0] offset:9216\n"
"ds_read_b128 a[12:15], %[xrd1] offset:9216\n"
"ds_read_b128 a[64:67], %[xrd0] offset:11264\n"
"ds_read_b128 a[68:71], %[xrd1] offset:11264\n"
"ds_read_b128 a[120:123], %[xrd0] offset:13312\n"
"ds_read_b128 a[124:127], %[xrd1] offset:13312\n"
"s_waitcnt lgkmcnt(0)\n"
"s_add_u32 m0, s85, 768\n"
"s_nop 0\n"
"buffer_load_dwordx4 %[dmax], s[52:55], s74 offen offset:256 lds\n"
"s_add_u32 m0, s85, 1792\n"
"s_nop 0\n"
"buffer_load_dwordx4 %[dmax], s[52:55], s77 offen offset:256 lds\n"
"s_add_u32 m0, s85, 2816\n"
"s_nop 0\n"
"buffer_load_dwordx4 %[dmax], s[52:55], s75 offen offset:256 lds\n"
"s_add_u32 m0, s85, 3840\n"
"s_nop 0\n"
"buffer_load_dwordx4 %[dmax], s[52:55], s78 offen offset:256 lds\n"
"s_add_u32 m0, s85, 4864\n"
"s_nop 0\n"
"buffer_load_dwordx4 %[dmax], s[52:55], s76 offen offset:256 lds\n"
"s_add_u32 m0, s85, 5888\n"
"s_nop 0\n"
"buffer_load_dwordx4 %[dmax], s[52:55], s79 offen offset:256 lds\n"
"s_add_u32 m0, s85, 8832\n"
"s_nop 0\n"
"buffer_load_dwordx4 %[dmax], s[52:55], s74 offen offset:384 lds\n"
"s_add_u32 m0, s85, 9856\n"
"s_nop 0\n"
"buffer_load_dwordx4 %[dmax], s[52:55], s77 offen offset:384 lds\n"
"s_add_u32 m0, s85, 10880\n"
"s_nop 0\n"
"buffer_load_dwordx4 %[dmax], s[52:55], s75 offen offset:384 lds\n"
"s_add_u32 m0, s85, 11904\n"
"s_nop 0\n"
"buffer_load_dwordx4 %[dmax], s[52:55], s78 offen offset:384 lds\n"
"s_add_u32 m0, s85, 12928\n"
"s_nop 0\n"
"buffer_load_dwordx4 %[dmax], s[52:55], s76 offen offset:384 lds\n"
"s_add_u32 m0, s85, 13952\n"
"s_nop 0\n"
"buffer_load_dwordx4 %[dmax], s[52:55], s79 offen offset:384 lds\n"
"s_waitcnt vmcnt(28)\n"
"s_barrier\n"
"s_add_u32 s57, s60, 131072\n"
"v_add_u32 v11, s57, %[wl0]\n"
"v_add_u32 v12, s57, %[wl1]\n"
"ds_read_b64_tr_b16 v[20:21], v11 offset:0\n"
"ds_read_b64_tr_b16 v[22:23], v12 offset:0\n"
"ds_read_b64_tr_b16 v[24:25], v11 offset:8\n"
"ds_read_b64_tr_b16 v[26:27], v12 offset:8\n"
"ds_read_b64_tr_b16 v[28:29], v11 offset:512\n"
"ds_read_b64_tr_b16 v[30:31], v12 offset:512\n"
"ds_read_b64_tr_b16 v[32:33], v11 offset:520\n"
"ds_read_b64_tr_b16 v[34:35], v12 offset:520\n"
"ds_read_b64_tr_b16 v[36:37], v11 offset:1024\n"
"ds_read_b64_tr_b16 v[38:39], v12 offset:1024\n"
"ds_read_b64_tr_b16 v[40:41], v11 offset:1032\n"
"ds_read_b64_tr_b16 v[42:43], v12 offset:1032\n"
"ds_read_b64_tr_b16 v[44:45], v11 offset:1536\n"
"ds_read_b64_tr_b16 v[46:47], v12 offset:1536\n"
"s_waitcnt lgkmcnt(10)\n"
"v_mfma_f32_16x16x32_bf16 v[64:67], v[20:23], a[0:3], 0\n"
"ds_read_b64_tr_b16 v[48:49], v11 offset:1544\n"
"ds_read_b64_tr_b16 v[50:51], v12 offset:1544\n"
"v_mfma_f32_16x16x32_bf16 v[128:131], v[20:23], a[56:59], 0\n"
"v_mfma_f32_16x16x32_bf16 v[192:195], v[20:23], a[112:115], 0\n"
"v_mfma_f32_16x16x32_bf16 v[68:71], v[24:27], a[0:3], 0\n"
"ds_read_b64_tr_b16 v[20:21], v11 offset:2048\n"
"ds_read_b64_tr_b16 v[22:23], v12 offset:2048\n"
"v_mfma_f32_16x16x32_bf16 v[132:135], v[24:27], a[56:59], 0\n"
"s_getpc_b64 s[64:65]\n"
"L_pc_L_q1_w0:\n"
"s_add_u32 s64, s64, L_q1_w0-L_pc_L_q1_w0\n"
"s_addc_u32 s65, s65, 0\n"
"v_mfma_f32_16x16x32_bf16 v[196:199], v[24:27], a[112:115], 0\n"
"s_waitcnt lgkmcnt(10)\n"
"v_mfma_f32_16x16x32_bf16 v[72:75], v[28:31], a[0:3], 0\n"
"ds_read_b64_tr_b16 v[24:25], v11 offset:2056\n"
"ds_read_b64_tr_b16 v[26:27], v12 offset:2056\n"
"v_mfma_f32_16x16x32_bf16 v[136:139], v[28:31], a[56:59], 0\n"
"v_mfma_f32_16x16x32_bf16 v[200:203], v[28:31], a[112:115], 0\n"
"v_mfma_f32_16x16x32_bf16 v[76:79], v[32:35], a[0:3], 0\n"
"ds_read_b64_tr_b16 v[28:29], v11 offset:2560\n"
"ds_read_b64_tr_b16 v[30:31], v12 offset:2560\n"
"v_mfma_f32_16x16x32_bf16 v[140:143], v[32:35], a[56:59], 0\n"
"v_mbcnt_lo_u32_b32 v13, -1, 0\n"
"v_mbcnt_hi_u32_b32 v13, -1, v13\n"
"v_lshlrev_b32 v13, 7, v13\n"
"v_add_u32 v13, 0, v13\n"
"s_lshl_b32 s72, %[wave], 13\n"
"v_add_u32 v13, s72, v13\n"
"v_min_u32 v13, L_tail_w0-L_q1_w0-4, v13\n"
"global_load_dword a201, v13, s[64:65]\n"
"v_mfma_f32_16x16x32_bf16 v[204:207], v[32:35], a[112:115], 0\n"
"s_waitcnt lgkmcnt(10)\n"
"v_mfma_f32_16x16x32_bf16 v[80:83], v[36:39], a[0:3], 0\n"
"ds_read_b64_tr_b16 v[32:33], v11 offset:2568\n"
"ds_read_b64_tr_b16 v[34:35], v12 offset:2568\n"
"v_mfma_f32_16x16x32_bf16 v[144:147], v[36:39], a[56:59], 0\n"
"s_waitcnt vmcnt(7)\n"
"ds_read_b128 a[16:19], %[xrd0] offset:1024\n"
"v_mfma_f32_16x16x32_bf16 v[208:211], v[36:39], a[112:115], 0\n"
"ds_read_b128 a[20:23], %[xrd1] offset:1024\n"
"ds_read_b128 a[72:75], %[xrd0] offset:3072\n"
"v_mfma_f32_16x16x32_bf16 v[84:87], v[40:43], a[0:3], 0\n"
"ds_read_b64_tr_b16 v[36:37], v11 offset:3072\n"
"ds_read_b64_tr_b16 v[38:39], v12 offset:3072\n"
"v_mfma_f32_16x16x32_bf16 v[148:151], v[40:43], a[56:59], 0\n"
"ds_read_b128 a[76:79], %[xrd1] offset:3072\n"
"ds_read_b128 a[128:131], %[xrd0] offset:5120\n"
"v_mfma_f32_16x16x32_bf16 v[212:215], v[40:43], a[112:115], 0\n"
"ds_read_b128 a[132:135], %[xrd1] offset:5120\n"
"s_waitcnt lgkmcnt(15)\n"
"v_mfma_f32_16x16x32_bf16 v[88:91], v[44:47], a[0:3], 0\n"
"ds_read_b64_tr_b16 v[40:41], v11 offset:3080\n"
"ds_read_b64_tr_b16 v[42:43], v12 offset:3080\n"
"v_mfma_f32_16x16x32_bf16 v[152:155], v[44:47], a[56:59], 0\n"
"v_mbcnt_lo_u32_b32 v13, -1, 0\n"
"v_mbcnt_hi_u32_b32 v13, -1, v13\n"
"v_lshlrev_b32 v13, 7, v13\n"
"v_add_u32 v13, 24576, v13\n"
"s_lshl_b32 s72, %[wave], 13\n"
"v_add_u32 v13, s72, v13\n"
"v_min_u32 v13, L_tail_w0-L_q1_w0-4, v13\n"
"global_load_dword a201, v13, s[64:65]\n"
"v_mfma_f32_16x16x32_bf16 v[216:219], v[44:47], a[112:115], 0\n"
"v_mfma_f32_16x16x32_bf16 v[92:95], v[48:51], a[0:3], 0\n"
"ds_read_b64_tr_b16 v[44:45], v11 offset:3584\n"
"ds_read_b64_tr_b16 v[46:47], v12 offset:3584\n"
"v_mfma_f32_16x16x32_bf16 v[156:159], v[48:51], a[56:59], 0\n"
"v_mfma_f32_16x16x32_bf16 v[220:223], v[48:51], a[112:115], 0\n"
"s_waitcnt lgkmcnt(15)\n"
"v_mfma_f32_16x16x32_bf16 v[96:99], v[20:23], a[0:3], 0\n"
"ds_read_b64_tr_b16 v[48:49], v11 offset:3592\n"
"ds_read_b64_tr_b16 v[50:51], v12 offset:3592\n"
"v_mfma_f32_16x16x32_bf16 v[160:163], v[20:23], a[56:59], 0\n"
"s_waitcnt vmcnt(2)\n"
"ds_read_b128 a[24:27], %[xrd0] offset:9216\n"
"v_mfma_f32_16x16x32_bf16 v[224:227], v[20:23], a[112:115], 0\n"
"ds_read_b128 a[28:31], %[xrd1] offset:9216\n"
"ds_read_b128 a[80:83], %[xrd0] offset:11264\n"
"v_mfma_f32_16x16x32_bf16 v[100:103], v[24:27], a[0:3], 0\n"
"ds_read_b64_tr_b16 v[20:21], v11 offset:16384\n"
"ds_read_b64_tr_b16 v[22:23], v12 offset:16384\n"
"v_mfma_f32_16x16x32_bf16 v[164:167], v[24:27], a[56:59], 0\n"
"ds_read_b128 a[84:87], %[xrd1] offset:11264\n"
"ds_read_b128 a[136:139], %[xrd0] offset:13312\n"
"v_mfma_f32_16x16x32_bf16 v[228:231], v[24:27], a[112:115], 0\n"
"ds_read_b128 a[140:143], %[xrd1] offset:13312\n"
"s_waitcnt lgkmcnt(15)\n"
"v_mfma_f32_16x16x32_bf16 v[104:107], v[28:31], a[0:3], 0\n"
"ds_read_b64_tr_b16 v[24:25], v11 offset:16392\n"
"ds_read_b64_tr_b16 v[26:27], v12 offset:16392\n"
"v_mfma_f32_16x16x32_bf16 v[168:171], v[28:31], a[56:59], 0\n"
"v_mfma_f32_16x16x32_bf16 v[232:235], v[28:31], a[112:115], 0\n"
"v_mfma_f32_16x16x32_bf16 v[108:111], v[32:35], a[0:3], 0\n"
"ds_read_b64_tr_b16 v[28:29], v11 offset:16896\n"
"ds_read_b64_tr_b16 v[30:31], v12 offset:16896\n"
"v_mfma_f32_16x16x32_bf16 v[172:175], v[32:35], a[56:59], 0\n"
"v_mfma_f32_16x16x32_bf16 v[236:239], v[32:35], a[112:115], 0\n"
"s_waitcnt lgkmcnt(15)\n"
"v_mfma_f32_16x16x32_bf16 v[112:115], v[36:39], a[0:3], 0\n"
"ds_read_b64_tr_b16 v[32:33], v11 offset:16904\n"
"ds_read_b64_tr_b16 v[34:35], v12 offset:16904\n"
"v_mfma_f32_16x16x32_bf16 v[176:179], v[36:39], a[56:59], 0\n"
"v_mfma_f32_16x16x32_bf16 v[240:243], v[36:39], a[112:115], 0\n"
"v_mfma_f32_16x16x32_bf16 v[116:119], v[40:43], a[0:3], 0\n"
"ds_read_b64_tr_b16 v[36:37], v11 offset:17408\n"
"ds_read_b64_tr_b16 v[38:39], v12 offset:17408\n"
"v_mfma_f32_16x16x32_bf16 v[180:183], v[40:43], a[56:59], 0\n"
"v_mfma_f32_16x16x32_bf16 v[244:247], v[40:43], a[112:115], 0\n"
"s_waitcnt lgkmcnt(15)\n"
"v_mfma_f32_16x16x32_bf16 v[120:123], v[44:47], a[0:3], 0\n"
"ds_read_b64_tr_b16 v[40:41], v11 offset:17416\n"
"ds_read_b64_tr_b16 v[42:43], v12 offset:17416\n"
"v_mfma_f32_16x16x32_bf16 v[184:187], v[44:47], a[56:59], 0\n"
"s_waitcnt lgkmcnt(15)\n"
"s_add_u32 m0, s85, 512\n"
"v_mfma_f32_16x16x32_bf16 v[248:251], v[44:47], a[112:115], 0\n"
"buffer_load_dwordx4 %[dmax], s[52:55], s74 offen offset:512 lds\n"
"s_add_u32 m0, s85, 1536\n"
"v_mfma_f32_16x16x32_bf16 v[124:127], v[48:51], a[0:3], 0\n"
"ds_read_b64_tr_b16 v[44:45], v11 offset:17920\n"
"ds_read_b64_tr_b16 v[46:47], v12 offset:17920\n"
"v_mfma_f32_16x16x32_bf16 v[188:191], v[48:51], a[56:59], 0\n"
"buffer_load_dwordx4 %[dmax], s[52:55], s77 offen offset:512 lds\n"
"s_add_u32 m0, s85, 2560\n"
"v_mfma_f32_16x16x32_bf16 v[252:255], v[48:51], a[112:115], 0\n"
"buffer_load_dwordx4 %[dmax], s[52:55], s75 offen offset:512 lds\n"
"s_waitcnt lgkmcnt(10)\n"
"v_mfma_f32_16x16x32_bf16 v[64:67], v[20:23], a[4:7], v[64:67]\n"
"ds_read_b64_tr_b16 v[48:49], v11 offset:17928\n"
"ds_read_b64_tr_b16 v[50:51], v12 offset:17928\n"
"v_mfma_f32_16x16x32_bf16 v[128:131], v[20:23], a[60:63], v[128:131]\n"
"s_add_u32 m0, s85, 3584\n"
"v_mfma_f32_16x16x32_bf16 v[192:195], v[20:23], a[116:119], v[192:195]\n"
"buffer_load_dwordx4 %[dmax], s[52:55], s78 offen offset:512 lds\n"
"s_add_u32 m0, s85, 4608\n"
"v_mfma_f32_16x16x32_bf16 v[68:71], v[24:27], a[4:7], v[68:71]\n"
"ds_read_b64_tr_b16 v[20:21], v11 offset:18432\n"
"ds_read_b64_tr_b16 v[22:23], v12 offset:18432\n"
"v_mfma_f32_16x16x32_bf16 v[132:135], v[24:27], a[60:63], v[132:135]\n"
"buffer_load_dwordx4 %[dmax], s[52:55], s76 offen offset:512 lds\n"
"s_add_u32 m0, s85, 5632\n"
"v_mfma_f32_16x16x32_bf16 v[196:199], v[24:27], a[116:119], v[196:199]\n"
"buffer_load_dwordx4 %[dmax], s[52:55], s79 offen offset:512 lds\n"
"s_waitcnt lgkmcnt(10)\n"
"v_mfma_f32_16x16x32_bf16 v[72:75], v[28:31], a[4:7], v[72:75]\n"
"ds_read_b64_tr_b16 v[24:25], v11 offset:18440\n"
"ds_read_b64_tr_b16 v[26:27], v12 offset:18440\n"
"v_mfma_f32_16x16x32_bf16 v[136:139], v[28:31], a[60:63], v[136:139]\n"
"s_waitcnt lgkmcnt(15)\n"
"s_add_u32 m0, s85, 8576\n"
"v_mfma_f32_16x16x32_bf16 v[200:203], v[28:31], a[116:119], v[200:203]\n"
"buffer_load_dwordx4 %[dmax], s[52:55], s74 offen offset:640 lds\n"
"s_add_u32 m0, s85, 9600\n"
"v_mfma_f32_16x16x32_bf16 v[76:79], v[32:35], a[4:7], v[76:79]\n"
"ds_read_b64_tr_b16 v[28:29], v11 offset:18944\n"
"ds_read_b64_tr_b16 v[30:31], v12 offset:18944\n"
"v_mfma_f32_16x16x32_bf16 v[140:143], v[32:35], a[60:63], v[140:143]\n"
"buffer_load_dwordx4 %[dmax], s[52:55], s77 offen offset:640 lds\n"
"s_add_u32 m0, s85, 10624\n"
"v_mfma_f32_16x16x32_bf16 v[204:207], v[32:35], a[116:119], v[204:207]\n"
"buffer_load_dwordx4 %[dmax], s[52:55], s75 offen offset:640 lds\n"
"s_waitcnt lgkmcnt(10)\n"
"v_mfma_f32_16x16x32_bf16 v[80:83], v[36:39], a[4:7], v[80:83]\n"
"ds_read_b64_tr_b16 v[32:33], v11 offset:18952\n"
"ds_read_b64_tr_b16 v[34:35], v12 offset:18952\n"
"v_mfma_f32_16x16x32_bf16 v[144:147], v[36:39], a[60:63], v[144:147]\n"
"s_add_u32 m0, s85, 11648\n"
"v_mfma_f32_16x16x32_bf16 v[208:211], v[36:39], a[116:119], v[208:211]\n"
"buffer_load_dwordx4 %[dmax], s[52:55], s78 offen offset:640 lds\n"
"s_add_u32 m0, s85, 12672\n"
"v_mfma_f32_16x16x32_bf16 v[84:87], v[40:43], a[4:7], v[84:87]\n"
"ds_read_b64_tr_b16 v[36:37], v11 offset:19456\n"
"ds_read_b64_tr_b16 v[38:39], v12 offset:19456\n"
"v_mfma_f32_16x16x32_bf16 v[148:151], v[40:43], a[60:63], v[148:151]\n"
"buffer_load_dwordx4 %[dmax], s[52:55], s76 offen offset:640 lds\n"
"s_add_u32 m0, s85, 13696\n"
"v_mfma_f32_16x16x32_bf16 v[212:215], v[40:43], a[116:119], v[212:215]\n"
"buffer_load_dwordx4 %[dmax], s[52:55], s79 offen offset:640 lds\n"
"s_waitcnt lgkmcnt(10)\n"
"v_mfma_f32_16x16x32_bf16 v[88:91], v[44:47], a[4:7], v[88:91]\n"
"ds_read_b64_tr_b16 v[40:41], v11 offset:19464\n"
"ds_read_b64_tr_b16 v[42:43], v12 offset:19464\n"
"v_mfma_f32_16x16x32_bf16 v[152:155], v[44:47], a[60:63], v[152:155]\n"
"v_mfma_f32_16x16x32_bf16 v[216:219], v[44:47], a[116:119], v[216:219]\n"
"v_mfma_f32_16x16x32_bf16 v[92:95], v[48:51], a[4:7], v[92:95]\n"
"ds_read_b64_tr_b16 v[44:45], v11 offset:19968\n"
"ds_read_b64_tr_b16 v[46:47], v12 offset:19968\n"
"v_mfma_f32_16x16x32_bf16 v[156:159], v[48:51], a[60:63], v[156:159]\n"
"v_mfma_f32_16x16x32_bf16 v[220:223], v[48:51], a[116:119], v[220:223]\n"
"s_waitcnt lgkmcnt(10)\n"
"v_mfma_f32_16x16x32_bf16 v[96:99], v[20:23], a[4:7], v[96:99]\n"
"ds_read_b64_tr_b16 v[48:49], v11 offset:19976\n"
"ds_read_b64_tr_b16 v[50:51], v12 offset:19976\n"
"v_mfma_f32_16x16x32_bf16 v[160:163], v[20:23], a[60:63], v[160:163]\n"
"v_mfma_f32_16x16x32_bf16 v[224:227], v[20:23], a[116:119], v[224:227]\n"
"v_mfma_f32_16x16x32_bf16 v[100:103], v[24:27], a[4:7], v[100:103]\n"
"s_add_u32 s57, s60, 65536\n"
"v_mfma_f32_16x16x32_bf16 v[164:167], v[24:27], a[60:63], v[164:167]\n"
"v_add_u32 v11, s57, %[wl0]\n"
"v_add_u32 v12, s57, %[wl1]\n"
"v_mfma_f32_16x16x32_bf16 v[228:231], v[24:27], a[116:119], v[228:231]\n"
"s_waitcnt lgkmcnt(8)\n"
"v_mfma_f32_16x16x32_bf16 v[104:107], v[28:31], a[4:7], v[104:107]\n"
"v_mfma_f32_16x16x32_bf16 v[168:171], v[28:31], a[60:63], v[168:171]\n"
"v_mfma_f32_16x16x32_bf16 v[232:235], v[28:31], a[116:119], v[232:235]\n"
"s_waitcnt vmcnt(34) lgkmcnt(0)\n"
"s_barrier\n"
"v_mfma_f32_16x16x32_bf16 v[108:111], v[32:35], a[4:7], v[108:111]\n"
"ds_read_b64_tr_b16 v[20:21], v11 offset:0\n"
"ds_read_b64_tr_b16 v[22:23], v12 offset:0\n"
"v_mfma_f32_16x16x32_bf16 v[172:175], v[32:35], a[60:63], v[172:175]\n"
"ds_read_b64_tr_b16 v[24:25], v11 offset:8\n"
"ds_read_b64_tr_b16 v[26:27], v12 offset:8\n"
"v_mfma_f32_16x16x32_bf16 v[236:239], v[32:35], a[116:119], v[236:239]\n"
"ds_read_b64_tr_b16 v[28:29], v11 offset:512\n"
"ds_read_b64_tr_b16 v[30:31], v12 offset:512\n"
"v_mfma_f32_16x16x32_bf16 v[112:115], v[36:39], a[4:7], v[112:115]\n"
"ds_read_b64_tr_b16 v[32:33], v11 offset:520\n"
"ds_read_b64_tr_b16 v[34:35], v12 offset:520\n"
"v_mfma_f32_16x16x32_bf16 v[176:179], v[36:39], a[60:63], v[176:179]\n"
"v_mfma_f32_16x16x32_bf16 v[240:243], v[36:39], a[116:119], v[240:243]\n"
"v_mfma_f32_16x16x32_bf16 v[116:119], v[40:43], a[4:7], v[116:119]\n"
"ds_read_b64_tr_b16 v[36:37], v11 offset:1024\n"
"ds_read_b64_tr_b16 v[38:39], v12 offset:1024\n"
"v_mfma_f32_16x16x32_bf16 v[180:183], v[40:43], a[60:63], v[180:183]\n"
"v_mfma_f32_16x16x32_bf16 v[244:247], v[40:43], a[116:119], v[244:247]\n"
"v_mfma_f32_16x16x32_bf16 v[120:123], v[44:47], a[4:7], v[120:123]\n"
"ds_read_b64_tr_b16 v[40:41], v11 offset:1032\n"
"ds_read_b64_tr_b16 v[42:43], v12 offset:1032\n"
"v_mfma_f32_16x16x32_bf16 v[184:187], v[44:47], a[60:63], v[184:187]\n"
"v_mfma_f32_16x16x32_bf16 v[248:251], v[44:47], a[116:119], v[248:251]\n"
"v_mfma_f32_16x16x32_bf16 v[124:127], v[48:51], a[4:7], v[124:127]\n"
"ds_read_b64_tr_b16 v[44:45], v11 offset:1536\n"
"ds_read_b64_tr_b16 v[46:47], v12 offset:1536\n"
"v_mfma_f32_16x16x32_bf16 v[188:191], v[48:51], a[60:63], v[188:191]\n"
"v_mfma_f32_16x16x32_bf16 v[252:255], v[48:51], a[116:119], v[252:255]\n"
"L_q1_w0:\n"
"s_waitcnt lgkmcnt(10)\n"
"v_mfma_f32_16x16x32_bf16 v[64:67], v[20:23], a[8:11], v[64:67]\n"
"ds_read_b64_tr_b16 v[48:49], v11 offset:1544\n"
"ds_read_b64_tr_b16 v[50:51], v12 offset:1544\n"
"v_mfma_f32_16x16x32_bf16 v[128:131], v[20:23], a[64:67], v[128:131]\n"
"s_add_u32 s56, s58, 131072\n"
"v_mfma_f32_16x16x32_bf16 v[192:195], v[20:23], a[120:123], v[192:195]\n"
"v_mfma_f32_16x16x32_bf16 v[68:71], v[24:27], a[8:11], v[68:71]\n"
"ds_read_b64_tr_b16 v[20:21], v11 offset:2048\n"
"ds_read_b64_tr_b16 v[22:23], v12 offset:2048\n"
"v_mfma_f32_16x16x32_bf16 v[132:135], v[24:27], a[64:67], v[132:135]\n"
"s_add_u32 m0, s56, 0\n"
"v_mfma_f32_16x16x32_bf16 v[196:199], v[24:27], a[120:123], v[196:199]\n"
"s_waitcnt lgkmcnt(10)\n"
"v_mfma_f32_16x16x32_bf16 v[72:75], v[28:31], a[8:11], v[72:75]\n"
"ds_read_b64_tr_b16 v[24:25], v11 offset:2056\n"
"ds_read_b64_tr_b16 v[26:27], v12 offset:2056\n"
"v_mfma_f32_16x16x32_bf16 v[136:139], v[28:31], a[64:67], v[136:139]\n"
"buffer_load_dwordx4 %[dmaw], s[48:51], 0 offen offset:0 lds\n"
"v_mfma_f32_16x16x32_bf16 v[200:203], v[28:31], a[120:123], v[200:203]\n"
"v_mfma_f32_16x16x32_bf16 v[76:79], v[32:35], a[8:11], v[76:79]\n"
"ds_read_b64_tr_b16 v[28:29], v11 offset:2560\n"
"ds_read_b64_tr_b16 v[30:31], v12 offset:2560\n"
"v_mfma_f32_16x16x32_bf16 v[140:143], v[32:35], a[64:67], v[140:143]\n"
"v_mfma_f32_16x16x32_bf16 v[204:207], v[32:35], a[120:123], v[204:207]\n"
"s_add_u32 m0, s56, 896\n"
"s_waitcnt lgkmcnt(10)\n"
"v_mfma_f32_16x16x32_bf16 v[80:83], v[36:39], a[8:11], v[80:83]\n"
"ds_read_b64_tr_b16 v[32:33], v11 offset:2568\n"
"ds_read_b64_tr_b16 v[34:35], v12 offset:2568\n"
"v_mfma_f32_16x16x32_bf16 v[144:147], v[36:39], a[64:67], v[144:147]\n"
"s_waitcnt vmcnt(7)\n"
"ds_read_b128 a[32:35], %[xrd0] offset:1024\n"
"v_mfma_f32_16x16x32_bf16 v[208:211], v[36:39], a[120:123], v[208:211]\n"
"ds_read_b128 a[36:39], %[xrd1] offset:1024\n"
"ds_read_b128 a[88:91], %[xrd0] offset:3072\n"
"v_mfma_f32_16x16x32_bf16 v[84:87], v[40:43], a[8:11], v[84:87]\n"
"ds_read_b64_tr_b16 v[36:37], v11 offset:3072\n"
"ds_read_b64_tr_b16 v[38:39], v12 offset:3072\n"
"v_mfma_f32_16x16x32_bf16 v[148:151], v[40:43], a[64:67], v[148:151]\n"
"buffer_load_dwordx4 %[dmaw], s[48:51], 0 offen offset:128 lds\n"
"ds_read_b128 a[92:95], %[xrd1] offset:3072\n"
"v_mfma_f32_16x16x32_bf16 v[212:215], v[40:43], a[120:123], v[212:215]\n"
"ds_read_b128 a[144:147], %[xrd0] offset:5120\n"
"s_waitcnt lgkmcnt(15)\n"
"v_mfma_f32_16x16x32_bf16 v[88:91], v[44:47], a[8:11], v[88:91]\n"
"ds_read_b64_tr_b16 v[40:41], v11 offset:3080\n"
"ds_read_b64_tr_b16 v[42:43], v12 offset:3080\n"
"v_mfma_f32_16x16x32_bf16 v[152:155], v[44:47], a[64:67], v[152:155]\n"
"s_add_u32 m0, s56, 1792\n"
"ds_read_b128 a[148:151], %[xrd1] offset:5120\n"
"v_mfma_f32_16x16x32_bf16 v[216:219], v[44:47], a[120:123], v[216:219]\n"
"v_mfma_f32_16x16x32_bf16 v[92:95], v[48:51], a[8:11], v[92:95]\n"
"ds_read_b64_tr_b16 v[44:45], v11 offset:3584\n"
"ds_read_b64_tr_b16 v[46:47], v12 offset:3584\n"
"v_mfma_f32_16x16x32_bf16 v[156:159], v[48:51], a[64:67], v[156:159]\n"
"v_mfma_f32_16x16x32_bf16 v[220:223], v[48:51], a[120:123], v[220:223]\n"
"buffer_load_dwordx4 %[dmaw], s[48:51], 0 offen offset:256 lds\n"
"s_waitcnt lgkmcnt(15)\n"
"v_mfma_f32_16x16x32_bf16 v[96:99], v[20:23], a[8:11], v[96:99]\n"
"ds_read_b64_tr_b16 v[48:49], v11 offset:3592\n"
"ds_read_b64_tr_b16 v[50:51], v12 offset:3592\n"
"v_mfma_f32_16x16x32_bf16 v[160:163], v[20:23], a[64:67], v[160:163]\n"
"s_waitcnt vmcnt(3)\n"
"ds_read_b128 a[40:43], %[xrd0] offset:9216\n"
"v_mfma_f32_16x16x32_bf16 v[224:227], v[20:23], a[120:123], v[224:227]\n"
"ds_read_b128 a[44:47], %[xrd1] offset:9216\n"
"ds_read_b128 a[96:99], %[xrd0] offset:11264\n"
"v_mfma_f32_16x16x32_bf16 v[100:103], v[24:27], a[8:11], v[100:103]\n"
"ds_read_b64_tr_b16 v[20:21], v11 offset:16384\n"
"ds_read_b64_tr_b16 v[22:23], v12 offset:16384\n"
"v_mfma_f32_16x16x32_bf16 v[164:167], v[24:27], a[64:67], v[164:167]\n"
"s_add_u32 m0, s56, 2688\n"
"ds_read_b128 a[100:103], %[xrd1] offset:11264\n"
"v_mfma_f32_16x16x32_bf16 v[228:231], v[24:27], a[120:123], v[228:231]\n"
"ds_read_b128 a[152:155], %[xrd0] offset:13312\n"
"s_waitcnt lgkmcnt(15)\n"
"v_mfma_f32_16x16x32_bf16 v[104:107], v[28:31], a[8:11], v[104:107]\n"
"ds_read_b64_tr_b16 v[24:25], v11 offset:16392\n"
"ds_read_b64_tr_b16 v[26:27], v12 offset:16392\n"
"v_mfma_f32_16x16x32_bf16 v[168:171], v[28:31], a[64:67], v[168:171]\n"
"buffer_load_dwordx4 %[dmaw], s[48:51], 0 offen offset:384 lds\n"
"ds_read_b128 a[156:159], %[xrd1] offset:13312\n"
"v_mfma_f32_16x16x32_bf16 v[232:235], v[28:31], a[120:123], v[232:235]\n"
"v_mfma_f32_16x16x32_bf16 v[108:111], v[32:35], a[8:11], v[108:111]\n"
"ds_read_b64_tr_b16 v[28:29], v11 offset:16896\n"
"ds_read_b64_tr_b16 v[30:31], v12 offset:16896\n"
"v_mfma_f32_16x16x32_bf16 v[172:175], v[32:35], a[64:67], v[172:175]\n"
"v_mfma_f32_16x16x32_bf16 v[236:239], v[32:35], a[120:123], v[236:239]\n"
"s_add_u32 m0, s56, 4096\n"
"s_waitcnt lgkmcnt(15)\n"
"v_mfma_f32_16x16x32_bf16 v[112:115], v[36:39], a[8:11], v[112:115]\n"
"ds_read_b64_tr_b16 v[32:33], v11 offset:16904\n"
"ds_read_b64_tr_b16 v[34:35], v12 offset:16904\n"
"v_mfma_f32_16x16x32_bf16 v[176:179], v[36:39], a[64:67], v[176:179]\n"
"v_mfma_f32_16x16x32_bf16 v[240:243], v[36:39], a[120:123], v[240:243]\n"
"v_mfma_f32_16x16x32_bf16 v[116:119], v[40:43], a[8:11], v[116:119]\n"
"ds_read_b64_tr_b16 v[36:37], v11 offset:17408\n"
"ds_read_b64_tr_b16 v[38:39], v12 offset:17408\n"
"v_mfma_f32_16x16x32_bf16 v[180:183], v[40:43], a[64:67], v[180:183]\n"
"buffer_load_dwordx4 %[dmaw], s[48:51], s63 offen offset:0 lds\n"
"v_mfma_f32_16x16x32_bf16 v[244:247], v[40:43], a[120:123], v[244:247]\n"
"s_waitcnt lgkmcnt(15)\n"
"v_mfma_f32_16x16x32_bf16 v[120:123], v[44:47], a[8:11], v[120:123]\n"
"ds_read_b64_tr_b16 v[40:41], v11 offset:17416\n"
"ds_read_b64_tr_b16 v[42:43], v12 offset:17416\n"
"v_mfma_f32_16x16x32_bf16 v[184:187], v[44:47], a[64:67], v[184:187]\n"
"s_waitcnt lgkmcnt(15)\n"
"s_add_u32 m0, s85, 256\n"
"v_mfma_f32_16x16x32_bf16 v[248:251], v[44:47], a[120:123], v[248:251]\n"
"buffer_load_dwordx4 %[dmax], s[52:55], s74 offen offset:768 lds\n"
"s_add_u32 m0, s85, 1280\n"
"v_mfma_f32_16x16x32_bf16 v[124:127], v[48:51], a[8:11], v[124:127]\n"
"ds_read_b64_tr_b16 v[44:45], v11 offset:17920\n"
"ds_read_b64_tr_b16 v[46:47], v12 offset:17920\n"
"v_mfma_f32_16x16x32_bf16 v[188:191], v[48:51], a[64:67], v[188:191]\n"
"buffer_load_dwordx4 %[dmax], s[52:55], s77 offen offset:768 lds\n"
"s_add_u32 m0, s85, 2304\n"
"v_mfma_f32_16x16x32_bf16 v[252:255], v[48:51], a[120:123], v[252:255]\n"
"buffer_load_dwordx4 %[dmax], s[52:55], s75 offen offset:768 lds\n"
"s_waitcnt lgkmcnt(11)\n"
"v_mfma_f32_16x16x32_bf16 v[64:67], v[20:23], a[12:15], v[64:67]\n"
"ds_read_b64_tr_b16 v[48:49], v11 offset:17928\n"
"ds_read_b64_tr_b16 v[50:51], v12 offset:17928\n"
"v_mfma_f32_16x16x32_bf16 v[128:131], v[20:23], a[68:71], v[128:131]\n"
"s_add_u32 m0, s85, 3328\n"
"v_mfma_f32_16x16x32_bf16 v[192:195], v[20:23], a[124:127], v[192:195]\n"
"buffer_load_dwordx4 %[dmax], s[52:55], s78 offen offset:768 lds\n"
"s_add_u32 m0, s85, 4352\n"
"v_mfma_f32_16x16x32_bf16 v[68:71], v[24:27], a[12:15], v[68:71]\n"
"ds_read_b64_tr_b16 v[20:21], v11 offset:18432\n"
"ds_read_b64_tr_b16 v[22:23], v12 offset:18432\n"
"v_mfma_f32_16x16x32_bf16 v[132:135], v[24:27], a[68:71], v[132:135]\n"
"buffer_load_dwordx4 %[dmax], s[52:55], s76 offen offset:768 lds\n"
"s_add_u32 m0, s85, 5376\n"
"v_mfma_f32_16x16x32_bf16 v[196:199], v[24:27], a[124:127], v[196:199]\n"
"buffer_load_dwordx4 %[dmax], s[52:55], s79 offen offset:768 lds\n"
"s_waitcnt lgkmcnt(10)\n"
"v_mfma_f32_16x16x32_bf16 v[72:75], v[28:31], a[12:15], v[72:75]\n"
"ds_read_b64_tr_b16 v[24:25], v11 offset:18440\n"
"ds_read_b64_tr_b16 v[26:27], v12 offset:18440\n"
"v_mfma_f32_16x16x32_bf16 v[136:139], v[28:31], a[68:71], v[136:139]\n"
"s_add_u32 m0, s56, 4992\n"
"v_mfma_f32_16x16x32_bf16 v[200:203], v[28:31], a[124:127], v[200:203]\n"
"buffer_load_dwordx4 %[dmaw], s[48:51], s63 offen offset:128 lds\n"
"s_add_u32 m0, s56, 5888\n"
"v_mfma_f32_16x16x32_bf16 v[76:79], v[32:35], a[12:15], v[76:79]\n"
"ds_read_b64_tr_b16 v[28:29], v11 offset:18944\n"
"ds_read_b64_tr_b16 v[30:31], v12 offset:18944\n"
"v_mfma_f32_16x16x32_bf16 v[140:143], v[32:35], a[68:71], v[140:143]\n"
"buffer_load_dwordx4 %[dmaw], s[48:51], s63 offen offset:256 lds\n"
"v_mfma_f32_16x16x32_bf16 v[204:207], v[32:35], a[124:127], v[204:207]\n"
"s_add_u32 m0, s56, 6784\n"
"s_waitcnt lgkmcnt(10)\n"
"v_mfma_f32_16x16x32_bf16 v[80:83], v[36:39], a[12:15], v[80:83]\n"
"ds_read_b64_tr_b16 v[32:33], v11 offset:18952\n"
"ds_read_b64_tr_b16 v[34:35], v12 offset:18952\n"
"v_mfma_f32_16x16x32_bf16 v[144:147], v[36:39], a[68:71], v[144:147]\n"
"v_mfma_f32_16x16x32_bf16 v[208:211], v[36:39], a[124:127], v[208:211]\n"
"v_mfma_f32_16x16x32_bf16 v[84:87], v[40:43], a[12:15], v[84:87]\n"
"ds_read_b64_tr_b16 v[36:37], v11 offset:19456\n"
"ds_read_b64_tr_b16 v[38:39], v12 offset:19456\n"
"v_mfma_f32_16x16x32_bf16 v[148:151], v[40:43], a[68:71], v[148:151]\n"
"buffer_load_dwordx4 %[dmaw], s[48:51], s63 offen offset:384 lds\n"
"v_mfma_f32_16x16x32_bf16 v[212:215], v[40:43], a[124:127], v[212:215]\n"
"s_waitcnt lgkmcnt(10)\n"
"v_mfma_f32_16x16x32_bf16 v[88:91], v[44:47], a[12:15], v[88:91]\n"
"ds_read_b64_tr_b16 v[40:41], v11 offset:19464\n"
"ds_read_b64_tr_b16 v[42:43], v12 offset:19464\n"
"v_mfma_f32_16x16x32_bf16 v[152:155], v[44:47], a[68:71], v[152:155]\n"
"s_add_u32 s48, s48, 0x80000\n"
"s_addc_u32 s49, s49, 0\n"
"v_mfma_f32_16x16x32_bf16 v[216:219], v[44:47], a[124:127], v[216:219]\n"
"v_mfma_f32_16x16x32_bf16 v[92:95], v[48:51], a[12:15], v[92:95]\n"
"ds_read_b64_tr_b16 v[44:45], v11 offset:19968\n"
"ds_read_b64_tr_b16 v[46:47], v12 offset:19968\n"
"v_mfma_f32_16x16x32_bf16 v[156:159], v[48:51], a[68:71], v[156:159]\n"
"v_mfma_f32_16x16x32_bf16 v[220:223], v[48:51], a[124:127], v[220:223]\n"
"s_waitcnt lgkmcnt(10)\n"
"v_mfma_f32_16x16x32_bf16 v[96:99], v[20:23], a[12:15], v[96:99]\n"
"ds_read_b64_tr_b16 v[48:49], v11 offset:19976\n"
"ds_read_b64_tr_b16 v[50:51], v12 offset:19976\n"
"v_mfma_f32_16x16x32_bf16 v[160:163], v[20:23], a[68:71], v[160:163]\n"
"s_sub_u32 s50, s50, 0x80000\n"
"s_cselect_b32 s50, 0, s50\n"
"v_mfma_f32_16x16x32_bf16 v[224:227], v[20:23], a[124:127], v[224:227]\n"
"v_mfma_f32_16x16x32_bf16 v[100:103], v[24:27], a[12:15], v[100:103]\n"
"s_add_u32 s57, s60, 98304\n"
"v_mfma_f32_16x16x32_bf16 v[164:167], v[24:27], a[68:71], v[164:167]\n"
"v_add_u32 v11, s57, %[wl0]\n"
"v_add_u32 v12, s57, %[wl1]\n"
"v_mfma_f32_16x16x32_bf16 v[228:231], v[24:27], a[124:127], v[228:231]\n"
"s_waitcnt lgkmcnt(8)\n"
"v_mfma_f32_16x16x32_bf16 v[104:107], v[28:31], a[12:15], v[104:107]\n"
"v_mfma_f32_16x16x32_bf16 v[168:171], v[28:31], a[68:71], v[168:171]\n"
"v_mfma_f32_16x16x32_bf16 v[232:235], v[28:31], a[124:127], v[232:235]\n"
"s_waitcnt vmcnt(40) lgkmcnt(0)\n"
"s_barrier\n"
"v_mfma_f32_16x16x32_bf16 v[108:111], v[32:35], a[12:15], v[108:111]\n"
"ds_read_b64_tr_b16 v[20:21], v11 offset:0\n"
"ds_read_b64_tr_b16 v[22:23], v12 offset:0\n"
"v_mfma_f32_16x16x32_bf16 v[172:175], v[32:35], a[68:71], v[172:175]\n"
"ds_read_b64_tr_b16 v[24:25], v11 offset:8\n"
"ds_read_b64_tr_b16 v[26:27], v12 offset:8\n"
"v_mfma_f32_16x16x32_bf16 v[236:239], v[32:35], a[124:127], v[236:239]\n"
"ds_read_b64_tr_b16 v[28:29], v11 offset:512\n"
"ds_read_b64_tr_b16 v[30:31], v12 offset:512\n"
"v_mfma_f32_16x16x32_bf16 v[112:115], v[36:39], a[12:15], v[112:115]\n"
"ds_read_b64_tr_b16 v[32:33], v11 offset:520\n"
"ds_read_b64_tr_b16 v[34:35], v12 offset:520\n"
"v_mfma_f32_16x16x32_bf16 v[176:179], v[36:39], a[68:71], v[176:179]\n"
"v_mfma_f32_16x16x32_bf16 v[240:243], v[36:39], a[124:127], v[240:243]\n"
"v_mfma_f32_16x16x32_bf16 v[116:119], v[40:43], a[12:15], v[116:119]\n"
"ds_read_b64_tr_b16 v[36:37], v11 offset:1024\n"
"ds_read_b64_tr_b16 v[38:39], v12 offset:1024\n"
"v_mfma_f32_16x16x32_bf16 v[180:183], v[40:43], a[68:71], v[180:183]\n"
"v_mfma_f32_16x16x32_bf16 v[244:247], v[40:43], a[124:127], v[244:247]\n"
"v_mfma_f32_16x16x32_bf16 v[120:123], v[44:47], a[12:15], v[120:123]\n"
"ds_read_b64_tr_b16 v[40:41], v11 offset:1032\n"
"ds_read_b64_tr_b16 v[42:43], v12 offset:1032\n"
"v_mfma_f32_16x16x32_bf16 v[184:187], v[44:47], a[68:71], v[184:187]\n"
"v_mfma_f32_16x16x32_bf16 v[248:251], v[44:47], a[124:127], v[248:251]\n"
"v_mfma_f32_16x16x32_bf16 v[124:127], v[48:51], a[12:15], v[124:127]\n"
"ds_read_b64_tr_b16 v[44:45], v11 offset:1536\n"
"ds_read_b64_tr_b16 v[46:47], v12 offset:1536\n"
"v_mfma_f32_16x16x32_bf16 v[188:191], v[48:51], a[68:71], v[188:191]\n"
"v_mfma_f32_16x16x32_bf16 v[252:255], v[48:51], a[124:127], v[252:255]\n"
"s_waitcnt lgkmcnt(10)\n"
"v_mfma_f32_16x16x32_bf16 v[64:67], v[20:23], a[16:19], v[64:67]\n"
"ds_read_b64_tr_b16 v[48:49], v11 offset:1544\n"
"ds_read_b64_tr_b16 v[50:51], v12 offset:1544\n"
"v_mfma_f32_16x16x32_bf16 v[128:131], v[20:23], a[72:75], v[128:131]\n"
"s_add_u32 s56, s58, 65536\n"
"v_mfma_f32_16x16x32_bf16 v[192:195], v[20:23], a[128:131], v[192:195]\n"
"v_mfma_f32_16x16x32_bf16 v[68:71], v[24:27], a[16:19], v[68:71]\n"
"ds_read_b64_tr_b16 v[20:21], v11 offset:2048\n"
"ds_read_b64_tr_b16 v[22:23], v12 offset:2048\n"
"v_mfma_f32_16x16x32_bf16 v[132:135], v[24:27], a[72:75], v[132:135]\n"
"s_add_u32 m0, s56, 0\n"
"v_mfma_f32_16x16x32_bf16 v[196:199], v[24:27], a[128:131], v[196:199]\n"
"s_waitcnt lgkmcnt(10)\n"
"v_mfma_f32_16x16x32_bf16 v[72:75], v[28:31], a[16:19], v[72:75]\n"
"ds_read_b64_tr_b16 v[24:25], v11 offset:2056\n"
"ds_read_b64_tr_b16 v[26:27], v12 offset:2056\n"
"v_mfma_f32_16x16x32_bf16 v[136:139], v[28:31], a[72:75], v[136:139]\n"
"buffer_load_dwordx4 %[dmaw], s[48:51], 0 offen offset:0 lds\n"
"v_mfma_f32_16x16x32_bf16 v[200:203], v[28:31], a[128:131], v[200:203]\n"
"v_mfma_f32_16x16x32_bf16 v[76:79], v[32:35], a[16:19], v[76:79]\n"
"ds_read_b64_tr_b16 v[28:29], v11 offset:2560\n"
"ds_read_b64_tr_b16 v[30:31], v12 offset:2560\n"
"v_mfma_f32_16x16x32_bf16 v[140:143], v[32:35], a[72:75], v[140:143]\n"
"v_mfma_f32_16x16x32_bf16 v[204:207], v[32:35], a[128:131], v[204:207]\n"
"s_add_u32 m0, s56, 896\n"
"s_waitcnt lgkmcnt(10)\n"
"v_mfma_f32_16x16x32_bf16 v[80:83], v[36:39], a[16:19], v[80:83]\n"
"ds_read_b64_tr_b16 v[32:33], v11 offset:2568\n"
"ds_read_b64_tr_b16 v[34:35], v12 offset:2568\n"
"v_mfma_f32_16x16x32_bf16 v[144:147], v[36:39], a[72:75], v[144:147]\n"
"s_waitcnt vmcnt(4)\n"
"ds_read_b128 a[48:51], %[xrd0] offset:1024\n"
"v_mfma_f32_16x16x32_bf16 v[208:211], v[36:39], a[128:131], v[208:211]\n"
"ds_read_b128 a[52:55], %[xrd1] offset:1024\n"
"ds_read_b128 a[104:107], %[xrd0] offset:3072\n"
"v_mfma_f32_16x16x32_bf16 v[84:87], v[40:43], a[16:19], v[84:87]\n"
"ds_read_b64_tr_b16 v[36:37], v11 offset:3072\n"
"ds_read_b64_tr_b16 v[38:39], v12 offset:3072\n"
"v_mfma_f32_16x16x32_bf16 v[148:151], v[40:43], a[72:75], v[148:151]\n"
"buffer_load_dwordx4 %[dmaw], s[48:51], 0 offen offset:128 lds\n"
"ds_read_b128 a[108:111], %[xrd1] offset:3072\n"
"v_mfma_f32_16x16x32_bf16 v[212:215], v[40:43], a[128:131], v[212:215]\n"
"ds_read_b128 a[160:163], %[xrd0] offset:5120\n"
"s_waitcnt lgkmcnt(15)\n"
"v_mfma_f32_16x16x32_bf16 v[88:91], v[44:47], a[16:19], v[88:91]\n"
"ds_read_b64_tr_b16 v[40:41], v11 offset:3080\n"
"ds_read_b64_tr_b16 v[42:43], v12 offset:3080\n"
"v_mfma_f32_16x16x32_bf16 v[152:155], v[44:47], a[72:75], v[152:155]\n"
"s_add_u32 m0, s56, 1792\n"
"ds_read_b128 a[164:167], %[xrd1] offset:5120\n"
"v_mfma_f32_16x16x32_bf16 v[216:219], v[44:47], a[128:131], v[216:219]\n"
"v_mfma_f32_16x16x32_bf16 v[92:95], v[48:51], a[16:19], v[92:95]\n"
"ds_read_b64_tr_b16 v[44:45], v11 offset:3584\n"
"ds_read_b64_tr_b16 v[46:47], v12 offset:3584\n"
"v_mfma_f32_16x16x32_bf16 v[156:159], v[48:51], a[72:75], v[156:159]\n"
"v_mfma_f32_16x16x32_bf16 v[220:223], v[48:51], a[128:131], v[220:223]\n"
"buffer_load_dwordx4 %[dmaw], s[48:51], 0 offen offset:256 lds\n"
"s_waitcnt lgkmcnt(15)\n"
"v_mfma_f32_16x16x32_bf16 v[96:99], v[20:23], a[16:19], v[96:99]\n"
"ds_read_b64_tr_b16 v[48:49], v11 offset:3592\n"
"ds_read_b64_tr_b16 v[50:51], v12 offset:3592\n"
"v_mfma_f32_16x16x32_bf16 v[160:163], v[20:23], a[72:75], v[160:163]\n"
"v_mfma_f32_16x16x32_bf16 v[224:227], v[20:23], a[128:131], v[224:227]\n"
"v_mfma_f32_16x16x32_bf16 v[100:103], v[24:27], a[16:19], v[100:103]\n"
"ds_read_b64_tr_b16 v[20:21], v11 offset:16384\n"
"ds_read_b64_tr_b16 v[22:23], v12 offset:16384\n"
"v_mfma_f32_16x16x32_bf16 v[164:167], v[24:27], a[72:75], v[164:167]\n"
"s_add_u32 m0, s56, 2688\n"
"v_mfma_f32_16x16x32_bf16 v[228:231], v[24:27], a[128:131], v[228:231]\n"
"s_waitcnt lgkmcnt(15)\n"
"v_mfma_f32_16x16x32_bf16 v[104:107], v[28:31], a[16:19], v[104:107]\n"
"ds_read_b64_tr_b16 v[24:25], v11 offset:16392\n"
"ds_read_b64_tr_b16 v[26:27], v12 offset:16392\n"
"v_mfma_f32_16x16x32_bf16 v[168:171], v[28:31], a[72:75], v[168:171]\n"
"buffer_load_dwordx4 %[dmaw], s[48:51], 0 offen offset:384 lds\n"
"v_mfma_f32_16x16x32_bf16 v[232:235], v[28:31], a[128:131], v[232:235]\n"
"v_mfma_f32_16x16x32_bf16 v[108:111], v[32:35], a[16:19], v[108:111]\n"
"ds_read_b64_tr_b16 v[28:29], v11 offset:16896\n"
"ds_read_b64_tr_b16 v[30:31], v12 offset:16896\n"
"v_mfma_f32_16x16x32_bf16 v[172:175], v[32:35], a[72:75], v[172:175]\n"
"v_mfma_f32_16x16x32_bf16 v[236:239], v[32:35], a[128:131], v[236:239]\n"
"s_add_u32 m0, s56, 4096\n"
"s_waitcnt lgkmcnt(11)\n"
"v_mfma_f32_16x16x32_bf16 v[112:115], v[36:39], a[16:19], v[112:115]\n"
"ds_read_b64_tr_b16 v[32:33], v11 offset:16904\n"
"ds_read_b64_tr_b16 v[34:35], v12 offset:16904\n"
"v_mfma_f32_16x16x32_bf16 v[176:179], v[36:39], a[72:75], v[176:179]\n"
"v_mfma_f32_16x16x32_bf16 v[240:243], v[36:39], a[128:131], v[240:243]\n"
"v_mfma_f32_16x16x32_bf16 v[116:119], v[40:43], a[16:19], v[116:119]\n"
"ds_read_b64_tr_b16 v[36:37], v11 offset:17408\n"
"ds_read_b64_tr_b16 v[38:39], v12 offset:17408\n"
"v_mfma_f32_16x16x32_bf16 v[180:183], v[40:43], a[72:75], v[180:183]\n"
"buffer_load_dwordx4 %[dmaw], s[48:51], s63 offen offset:0 lds\n"
"v_mfma_f32_16x16x32_bf16 v[244:247], v[40:43], a[128:131], v[244:247]\n"
"s_waitcnt lgkmcnt(10)\n"
"v_mfma_f32_16x16x32_bf16 v[120:123], v[44:47], a[16:19], v[120:123]\n"
"ds_read_b64_tr_b16 v[40:41], v11 offset:17416\n"
"ds_read_b64_tr_b16 v[42:43], v12 offset:17416\n"
"v_mfma_f32_16x16x32_bf16 v[184:187], v[44:47], a[72:75], v[184:187]\n"
"s_add_u32 m0, s56, 4992\n"
"v_mfma_f32_16x16x32_bf16 v[248:251], v[44:47], a[128:131], v[248:251]\n"
"v_mfma_f32_16x16x32_bf16 v[124:127], v[48:51], a[16:19], v[124:127]\n"
"ds_read_b64_tr_b16 v[44:45], v11 offset:17920\n"
"ds_read_b64_tr_b16 v[46:47], v12 offset:17920\n"
"v_mfma_f32_16x16x32_bf16 v[188:191], v[48:51], a[72:75], v[188:191]\n"
"v_mfma_f32_16x16x32_bf16 v[252:255], v[48:51], a[128:131], v[252:255]\n"
"buffer_load_dwordx4 %[dmaw], s[48:51], s63 offen offset:128 lds\n"
"s_waitcnt lgkmcnt(10)\n"
"v_mfma_f32_16x16x32_bf16 v[64:67], v[20:23], a[20:23], v[64:67]\n"
"ds_read_b64_tr_b16 v[48:49], v11 offset:17928\n"
"ds_read_b64_tr_b16 v[50:51], v12 offset:17928\n"
"v_mfma_f32_16x16x32_bf16 v[128:131], v[20:23], a[76:79], v[128:131]\n"
"v_mfma_f32_16x16x32_bf16 v[192:195], v[20:23], a[132:135], v[192:195]\n"
"v_mfma_f32_16x16x32_bf16 v[68:71], v[24:27], a[20:23], v[68:71]\n"
"ds_read_b64_tr_b16 v[20:21], v11 offset:18432\n"
"ds_read_b64_tr_b16 v[22:23], v12 offset:18432\n"
"v_mfma_f32_16x16x32_bf16 v[132:135], v[24:27], a[76:79], v[132:135]\n"
"s_add_u32 m0, s56, 5888\n"
"v_mfma_f32_16x16x32_bf16 v[196:199], v[24:27], a[132:135], v[196:199]\n"
"s_waitcnt lgkmcnt(10)\n"
"v_mfma_f32_16x16x32_bf16 v[72:75], v[28:31], a[20:23], v[72:75]\n"
"ds_read_b64_tr_b16 v[24:25], v11 offset:18440\n"
"ds_read_b64_tr_b16 v[26:27], v12 offset:18440\n"
"v_mfma_f32_16x16x32_bf16 v[136:139], v[28:31], a[76:79], v[136:139]\n"
"buffer_load_dwordx4 %[dmaw], s[48:51], s63 offen offset:256 lds\n"
"v_mfma_f32_16x16x32_bf16 v[200:203], v[28:31], a[132:135], v[200:203]\n"
"v_mfma_f32_16x16x32_bf16 v[76:79], v[32:35], a[20:23], v[76:79]\n"
"ds_read_b64_tr_b16 v[28:29], v11 offset:18944\n"
"ds_read_b64_tr_b16 v[30:31], v12 offset:18944\n"
"v_mfma_f32_16x16x32_bf16 v[140:143], v[32:35], a[76:79], v[140:143]\n"
"v_mfma_f32_16x16x32_bf16 v[204:207], v[32:35], a[132:135], v[204:207]\n"
"s_add_u32 m0, s56, 6784\n"
"s_waitcnt lgkmcnt(10)\n"
"v_mfma_f32_16x16x32_bf16 v[80:83], v[36:39], a[20:23], v[80:83]\n"
"ds_read_b64_tr_b16 v[32:33], v11 offset:18952\n"
"ds_read_b64_tr_b16 v[34:35], v12 offset:18952\n"
"v_mfma_f32_16x16x32_bf16 v[144:147], v[36:39], a[76:79], v[144:147]\n"
"v_mfma_f32_16x16x32_bf16 v[208:211], v[36:39], a[132:135], v[208:211]\n"
"v_mfma_f32_16x16x32_bf16 v[84:87], v[40:43], a[20:23], v[84:87]\n"
"ds_read_b64_tr_b16 v[36:37], v11 offset:19456\n"
"ds_read_b64_tr_b16 v[38:39], v12 offset:19456\n"
"v_mfma_f32_16x16x32_bf16 v[148:151], v[40:43], a[76:79], v[148:151]\n"
"buffer_load_dwordx4 %[dmaw], s[48:51], s63 offen offset:384 lds\n"
"v_mfma_f32_16x16x32_bf16 v[212:215], v[40:43], a[132:135], v[212:215]\n"
"s_waitcnt lgkmcnt(10)\n"
"v_mfma_f32_16x16x32_bf16 v[88:91], v[44:47], a[20:23], v[88:91]\n"
"ds_read_b64_tr_b16 v[40:41], v11 offset:19464\n"
"ds_read_b64_tr_b16 v[42:43], v12 offset:19464\n"
"v_mfma_f32_16x16x32_bf16 v[152:155], v[44:47], a[76:79], v[152:155]\n"
"s_add_u32 s48, s48, 0x80000\n"
"s_addc_u32 s49, s49, 0\n"
"v_mfma_f32_16x16x32_bf16 v[216:219], v[44:47], a[132:135], v[216:219]\n"
"v_mfma_f32_16x16x32_bf16 v[92:95], v[48:51], a[20:23], v[92:95]\n"
"ds_read_b64_tr_b16 v[44:45], v11 offset:19968\n"
"ds_read_b64_tr_b16 v[46:47], v12 offset:19968\n"
"v_mfma_f32_16x16x32_bf16 v[156:159], v[48:51], a[76:79], v[156:159]\n"
"v_mfma_f32_16x16x32_bf16 v[220:223], v[48:51], a[132:135], v[220:223]\n"
"s_waitcnt lgkmcnt(10)\n"
"v_mfma_f32_16x16x32_bf16 v[96:99], v[20:23], a[20:23], v[96:99]\n"
"ds_read_b64_tr_b16 v[48:49], v11 offset:19976\n"
"ds_read_b64_tr_b16 v[50:51], v12 offset:19976\n"
"v_mfma_f32_16x16x32_bf16 v[160:163], v[20:23], a[76:79], v[160:163]\n"
"s_sub_u32 s50, s50, 0x80000\n"
"s_cselect_b32 s50, 0, s50\n"
"v_mfma_f32_16x16x32_bf16 v[224:227], v[20:23], a[132:135], v[224:227]\n"
"v_mfma_f32_16x16x32_bf16 v[100:103], v[24:27], a[20:23], v[100:103]\n"
"s_add_u32 s57, s60, 131072\n"
"v_mfma_f32_16x16x32_bf16 v[164:167], v[24:27], a[76:79], v[164:167]\n"
"v_add_u32 v11, s57, %[wl0]\n"
"v_add_u32 v12, s57, %[wl1]\n"
"v_mfma_f32_16x16x32_bf16 v[228:231], v[24:27], a[132:135], v[228:231]\n"
"s_waitcnt lgkmcnt(8)\n"
"v_mfma_f32_16x16x32_bf16 v[104:107], v[28:31], a[20:23], v[104:107]\n"
"v_mfma_f32_16x16x32_bf16 v[168:171], v[28:31], a[76:79], v[168:171]\n"
"v_mfma_f32_16x16x32_bf16 v[232:235], v[28:31], a[132:135], v[232:235]\n"
"s_waitcnt vmcnt(8) lgkmcnt(0)\n"
"s_barrier\n"
"v_mfma_f32_16x16x32_bf16 v[108:111], v[32:35], a[20:23], v[108:111]\n"
"ds_read_b64_tr_b16 v[20:21], v11 offset:0\n"
"ds_read_b64_tr_b16 v[22:23], v12 offset:0\n"
"v_mfma_f32_16x16x32_bf16 v[172:175], v[32:35], a[76:79], v[172:175]\n"
"ds_read_b64_tr_b16 v[24:25], v11 offset:8\n"
"ds_read_b64_tr_b16 v[26:27], v12 offset:8\n"
"v_mfma_f32_16x16x32_bf16 v[236:239], v[32:35], a[132:135], v[236:239]\n"
"ds_read_b64_tr_b16 v[28:29], v11 offset:512\n"
"ds_read_b64_tr_b16 v[30:31], v12 offset:512\n"
"v_mfma_f32_16x16x32_bf16 v[112:115], v[36:39], a[20:23], v[112:115]\n"
"ds_read_b64_tr_b16 v[32:33], v11 offset:520\n"
"ds_read_b64_tr_b16 v[34:35], v12 offset:520\n"
"v_mfma_f32_16x16x32_bf16 v[176:179], v[36:39], a[76:79], v[176:179]\n"
"v_mfma_f32_16x16x32_bf16 v[240:243], v[36:39], a[132:135], v[240:243]\n"
"v_mfma_f32_16x16x32_bf16 v[116:119], v[40:43], a[20:23], v[116:119]\n"
"ds_read_b64_tr_b16 v[36:37], v11 offset:1024\n"
"ds_read_b64_tr_b16 v[38:39], v12 offset:1024\n"
"v_mfma_f32_16x16x32_bf16 v[180:183], v[40:43], a[76:79], v[180:183]\n"
"v_mfma_f32_16x16x32_bf16 v[244:247], v[40:43], a[132:135], v[244:247]\n"
"v_mfma_f32_16x16x32_bf16 v[120:123], v[44:47], a[20:23], v[120:123]\n"
"ds_read_b64_tr_b16 v[40:41], v11 offset:1032\n"
"ds_read_b64_tr_b16 v[42:43], v12 offset:1032\n"
"v_mfma_f32_16x16x32_bf16 v[184:187], v[44:47], a[76:79], v[184:187]\n"
"v_mfma_f32_16x16x32_bf16 v[248:251], v[44:47], a[132:135], v[248:251]\n"
"v_mfma_f32_16x16x32_bf16 v[124:127], v[48:51], a[20:23], v[124:127]\n"
"ds_read_b64_tr_b16 v[44:45], v11 offset:1536\n"
"ds_read_b64_tr_b16 v[46:47], v12 offset:1536\n"
"v_mfma_f32_16x16x32_bf16 v[188:191], v[48:51], a[76:79], v[188:191]\n"
"v_mfma_f32_16x16x32_bf16 v[252:255], v[48:51], a[132:135], v[252:255]\n"
"s_waitcnt lgkmcnt(10)\n"
"v_mfma_f32_16x16x32_bf16 v[64:67], v[20:23], a[24:27], v[64:67]\n"
"ds_read_b64_tr_b16 v[48:49], v11 offset:1544\n"
"ds_read_b64_tr_b16 v[50:51], v12 offset:1544\n"
"v_mfma_f32_16x16x32_bf16 v[128:131], v[20:23], a[80:83], v[128:131]\n"
"s_add_u32 s56, s58, 98304\n"
"s_add_u32 m0, s56, 0\n"
"v_mfma_f32_16x16x32_bf16 v[192:195], v[20:23], a[136:139], v[192:195]\n"
"v_mfma_f32_16x16x32_bf16 v[68:71], v[24:27], a[24:27], v[68:71]\n"
"ds_read_b64_tr_b16 v[20:21], v11 offset:2048\n"
"ds_read_b64_tr_b16 v[22:23], v12 offset:2048\n"
"v_mfma_f32_16x16x32_bf16 v[132:135], v[24:27], a[80:83], v[132:135]\n"
"buffer_load_dwordx4 %[dmaw], s[48:51], 0 offen offset:0 lds\n"
"v_mfma_f32_16x16x32_bf16 v[196:199], v[24:27], a[136:139], v[196:199]\n"
"s_add_u32 m0, s56, 896\n"
"s_waitcnt lgkmcnt(10)\n"
"v_mfma_f32_16x16x32_bf16 v[72:75], v[28:31], a[24:27], v[72:75]\n"
"ds_read_b64_tr_b16 v[24:25], v11 offset:2056\n"
"ds_read_b64_tr_b16 v[26:27], v12 offset:2056\n"
"v_mfma_f32_16x16x32_bf16 v[136:139], v[28:31], a[80:83], v[136:139]\n"
"buffer_load_dwordx4 %[dmaw], s[48:51], 0 offen offset:128 lds\n"
"v_mfma_f32_16x16x32_bf16 v[200:203], v[28:31], a[136:139], v[200:203]\n"
"v_mfma_f32_16x16x32_bf16 v[76:79], v[32:35], a[24:27], v[76:79]\n"
"ds_read_b64_tr_b16 v[28:29], v11 offset:2560\n"
"ds_read_b64_tr_b16 v[30:31], v12 offset:2560\n"
"v_mfma_f32_16x16x32_bf16 v[140:143], v[32:35], a[80:83], v[140:143]\n"
"s_add_u32 m0, s56, 1792\n"
"v_mfma_f32_16x16x32_bf16 v[204:207], v[32:35], a[136:139], v[204:207]\n"
"buffer_load_dwordx4 %[dmaw], s[48:51], 0 offen offset:256 lds\n"
"s_waitcnt lgkmcnt(10)\n"
"v_mfma_f32_16x16x32_bf16 v[80:83], v[36:39], a[24:27], v[80:83]\n"
"ds_read_b64_tr_b16 v[32:33], v11 offset:2568\n"
"ds_read_b64_tr_b16 v[34:35], v12 offset:2568\n"
"v_mfma_f32_16x16x32_bf16 v[144:147], v[36:39], a[80:83], v[144:147]\n"
"s_add_u32 m0, s56, 2688\n"
"v_mfma_f32_16x16x32_bf16 v[208:211], v[36:39], a[136:139], v[208:211]\n"
"v_mfma_f32_16x16x32_bf16 v[84:87], v[40:43], a[24:27], v[84:87]\n"
"ds_read_b64_tr_b16 v[36:37], v11 offset:3072\n"
"ds_read_b64_tr_b16 v[38:39], v12 offset:3072\n"
"v_mfma_f32_16x16x32_bf16 v[148:151], v[40:43], a[80:83], v[148:151]\n"
"buffer_load_dwordx4 %[dmaw], s[48:51], 0 offen offset:384 lds\n"
"v_mfma_f32_16x16x32_bf16 v[212:215], v[40:43], a[136:139], v[212:215]\n"
"s_add_u32 m0, s56, 4096\n"
"s_waitcnt lgkmcnt(10)\n"
"v_mfma_f32_16x16x32_bf16 v[88:91], v[44:47], a[24:27], v[88:91]\n"
"ds_read_b64_tr_b16 v[40:41], v11 offset:3080\n"
"ds_read_b64_tr_b16 v[42:43], v12 offset:3080\n"
"v_mfma_f32_16x16x32_bf16 v[152:155], v[44:47], a[80:83], v[152:155]\n"
"buffer_load_dwordx4 %[dmaw], s[48:51], s63 offen offset:0 lds\n"
"v_mfma_f32_16x16x32_bf16 v[216:219], v[44:47], a[136:139], v[216:219]\n"
"v_mfma_f32_16x16x32_bf16 v[92:95], v[48:51], a[24:27], v[92:95]\n"
"ds_read_b64_tr_b16 v[44:45], v11 offset:3584\n"
"ds_read_b64_tr_b16 v[46:47], v12 offset:3584\n"
"v_mfma_f32_16x16x32_bf16 v[156:159], v[48:51], a[80:83], v[156:159]\n"
"s_add_u32 m0, s56, 4992\n"
"v_mfma_f32_16x16x32_bf16 v[220:223], v[48:51], a[136:139], v[220:223]\n"
"buffer_load_dwordx4 %[dmaw], s[48:51], s63 offen offset:128 lds\n"
"s_waitcnt lgkmcnt(10)\n"
"v_mfma_f32_16x16x32_bf16 v[96:99], v[20:23], a[24:27], v[96:99]\n"
"ds_read_b64_tr_b16 v[48:49], v11 offset:3592\n"
"ds_read_b64_tr_b16 v[50:51], v12 offset:3592\n"
"v_mfma_f32_16x16x32_bf16 v[160:163], v[20:23], a[80:83], v[160:163]\n"
"s_add_u32 m0, s56, 5888\n"
"v_mfma_f32_16x16x32_bf16 v[224:227], v[20:23], a[136:139], v[224:227]\n"
"v_mfma_f32_16x16x32_bf16 v[100:103], v[24:27], a[24:27], v[100:103]\n"
"ds_read_b64_tr_b16 v[20:21], v11 offset:16384\n"
"ds_read_b64_tr_b16 v[22:23], v12 offset:16384\n"
"v_mfma_f32_16x16x32_bf16 v[164:167], v[24:27], a[80:83], v[164:167]\n"
"buffer_load_dwordx4 %[dmaw], s[48:51], s63 offen offset:256 lds\n"
"v_mfma_f32_16x16x32_bf16 v[228:231], v[24:27], a[136:139], v[228:231]\n"
"s_add_u32 m0, s56, 6784\n"
"s_waitcnt lgkmcnt(10)\n"
"v_mfma_f32_16x16x32_bf16 v[104:107], v[28:31], a[24:27], v[104:107]\n"
"ds_read_b64_tr_b16 v[24:25], v11 offset:16392\n"
"ds_read_b64_tr_b16 v[26:27], v12 offset:16392\n"
"v_mfma_f32_16x16x32_bf16 v[168:171], v[28:31], a[80:83], v[168:171]\n"
"buffer_load_dwordx4 %[dmaw], s[48:51], s63 offen offset:384 lds\n"
"v_mfma_f32_16x16x32_bf16 v[232:235], v[28:31], a[136:139], v[232:235]\n"
"v_mfma_f32_16x16x32_bf16 v[108:111], v[32:35], a[24:27], v[108:111]\n"
"ds_read_b64_tr_b16 v[28:29], v11 offset:16896\n"
"ds_read_b64_tr_b16 v[30:31], v12 offset:16896\n"
"v_mfma_f32_16x16x32_bf16 v[172:175], v[32:35], a[80:83], v[172:175]\n"
"s_add_u32 s48, s48, 0x80000\n"
"s_addc_u32 s49, s49, 0\n"
"v_mfma_f32_16x16x32_bf16 v[236:239], v[32:35], a[136:139], v[236:239]\n"
"s_waitcnt lgkmcnt(10)\n"
"v_mfma_f32_16x16x32_bf16 v[112:115], v[36:39], a[24:27], v[112:115]\n"
"ds_read_b64_tr_b16 v[32:33], v11 offset:16904\n"
"ds_read_b64_tr_b16 v[34:35], v12 offset:16904\n"
"v_mfma_f32_16x16x32_bf16 v[176:179], v[36:39], a[80:83], v[176:179]\n"
"s_sub_u32 s50, s50, 0x80000\n"
"s_cselect_b32 s50, 0, s50\n"
"v_mfma_f32_16x16x32_bf16 v[240:243], v[36:39], a[136:139], v[240:243]\n"
"s_add_u32 s56, s58, 0\n"
"v_mfma_f32_16x16x32_bf16 v[116:119], v[40:43], a[24:27], v[116:119]\n"
"ds_read_b64_tr_b16 v[36:37], v11 offset:17408\n"
"ds_read_b64_tr_b16 v[38:39], v12 offset:17408\n"
"v_mfma_f32_16x16x32_bf16 v[180:183], v[40:43], a[80:83], v[180:183]\n"
"s_add_u32 m0, s56, 0\n"
"v_mfma_f32_16x16x32_bf16 v[244:247], v[40:43], a[136:139], v[244:247]\n"
"buffer_load_dwordx4 %[dmakv], s[40:43], 0 offen offset:0 nt lds\n"
"s_waitcnt lgkmcnt(10)\n"
"v_mfma_f32_16x16x32_bf16 v[120:123], v[44:47], a[24:27], v[120:123]\n"
"ds_read_b64_tr_b16 v[40:41], v11 offset:17416\n"
"ds_read_b64_tr_b16 v[42:43], v12 offset:17416\n"
"v_mfma_f32_16x16x32_bf16 v[184:187], v[44:47], a[80:83], v[184:187]\n"
"s_add_u32 m0, s56, 896\n"
"v_mfma_f32_16x16x32_bf16 v[248:251], v[44:47], a[136:139], v[248:251]\n"
"v_mfma_f32_16x16x32_bf16 v[124:127], v[48:51], a[24:27], v[124:127]\n"
"ds_read_b64_tr_b16 v[44:45], v11 offset:17920\n"
"ds_read_b64_tr_b16 v[46:47], v12 offset:17920\n"
"v_mfma_f32_16x16x32_bf16 v[188:191], v[48:51], a[80:83], v[188:191]\n"
"buffer_load_dwordx4 %[dmakv], s[40:43], 0 offen offset:128 nt lds\n"
"v_mfma_f32_16x16x32_bf16 v[252:255], v[48:51], a[136:139], v[252:255]\n"
"s_add_u32 m0, s56, 1792\n"
"s_waitcnt lgkmcnt(10)\n"
"v_mfma_f32_16x16x32_bf16 v[64:67], v[20:23], a[28:31], v[64:67]\n"
"ds_read_b64_tr_b16 v[48:49], v11 offset:17928\n"
"ds_read_b64_tr_b16 v[50:51], v12 offset:17928\n"
"v_mfma_f32_16x16x32_bf16 v[128:131], v[20:23], a[84:87], v[128:131]\n"
"buffer_load_dwordx4 %[dmakv], s[40:43], 0 offen offset:256 nt lds\n"
"v_mfma_f32_16x16x32_bf16 v[192:195], v[20:23], a[140:143], v[192:195]\n"
"v_mfma_f32_16x16x32_bf16 v[68:71], v[24:27], a[28:31], v[68:71]\n"
"ds_read_b64_tr_b16 v[20:21], v11 offset:18432\n"
"ds_read_b64_tr_b16 v[22:23], v12 offset:18432\n"
"v_mfma_f32_16x16x32_bf16 v[132:135], v[24:27], a[84:87], v[132:135]\n"
"s_add_u32 m0, s56, 2688\n"
"v_mfma_f32_16x16x32_bf16 v[196:199], v[24:27], a[140:143], v[196:199]\n"
"buffer_load_dwordx4 %[dmakv], s[40:43], 0 offen offset:384 nt lds\n"
"s_waitcnt lgkmcnt(10)\n"
"v_mfma_f32_16x16x32_bf16 v[72:75], v[28:31], a[28:31], v[72:75]\n"
"ds_read_b64_tr_b16 v[24:25], v11 offset:18440\n"
"ds_read_b64_tr_b16 v[26:27], v12 offset:18440\n"
"v_mfma_f32_16x16x32_bf16 v[136:139], v[28:31], a[84:87], v[136:139]\n"
"s_add_u32 m0, s56, 4096\n"
"v_mfma_f32_16x16x32_bf16 v[200:203], v[28:31], a[140:143], v[200:203]\n"
"v_mfma_f32_16x16x32_bf16 v[76:79], v[32:35], a[28:31], v[76:79]\n"
"ds_read_b64_tr_b16 v[28:29], v11 offset:18944\n"
"ds_read_b64_tr_b16 v[30:31], v12 offset:18944\n"
"v_mfma_f32_16x16x32_bf16 v[140:143], v[32:35], a[84:87], v[140:143]\n"
"buffer_load_dwordx4 %[dmakv], s[40:43], s62 offen offset:0 nt lds\n"
"v_mfma_f32_16x16x32_bf16 v[204:207], v[32:35], a[140:143], v[204:207]\n"
"s_add_u32 m0, s56, 4992\n"
"s_waitcnt lgkmcnt(10)\n"
"v_mfma_f32_16x16x32_bf16 v[80:83], v[36:39], a[28:31], v[80:83]\n"
"ds_read_b64_tr_b16 v[32:33], v11 offset:18952\n"
"ds_read_b64_tr_b16 v[34:35], v12 offset:18952\n"
"v_mfma_f32_16x16x32_bf16 v[144:147], v[36:39], a[84:87], v[144:147]\n"
"buffer_load_dwordx4 %[dmakv], s[40:43], s62 offen offset:128 nt lds\n"
"v_mfma_f32_16x16x32_bf16 v[208:211], v[36:39], a[140:143], v[208:211]\n"
"v_mfma_f32_16x16x32_bf16 v[84:87], v[40:43], a[28:31], v[84:87]\n"
"ds_read_b64_tr_b16 v[36:37], v11 offset:19456\n"
"ds_read_b64_tr_b16 v[38:39], v12 offset:19456\n"
"v_mfma_f32_16x16x32_bf16 v[148:151], v[40:43], a[84:87], v[148:151]\n"
"s_add_u32 m0, s56, 5888\n"
"v_mfma_f32_16x16x32_bf16 v[212:215], v[40:43], a[140:143], v[212:215]\n"
"buffer_load_dwordx4 %[dmakv], s[40:43], s62 offen offset:256 nt lds\n"
"s_waitcnt lgkmcnt(10)\n"
"v_mfma_f32_16x16x32_bf16 v[88:91], v[44:47], a[28:31], v[88:91]\n"
"ds_read_b64_tr_b16 v[40:41], v11 offset:19464\n"
"ds_read_b64_tr_b16 v[42:43], v12 offset:19464\n"
"v_mfma_f32_16x16x32_bf16 v[152:155], v[44:47], a[84:87], v[152:155]\n"
"s_add_u32 m0, s56, 6784\n"
"v_mfma_f32_16x16x32_bf16 v[216:219], v[44:47], a[140:143], v[216:219]\n"
"v_mfma_f32_16x16x32_bf16 v[92:95], v[48:51], a[28:31], v[92:95]\n"
"ds_read_b64_tr_b16 v[44:45], v11 offset:19968\n"
"ds_read_b64_tr_b16 v[46:47], v12 offset:19968\n"
"v_mfma_f32_16x16x32_bf16 v[156:159], v[48:51], a[84:87], v[156:159]\n"
"buffer_load_dwordx4 %[dmakv], s[40:43], s62 offen offset:384 nt lds\n"
"v_mfma_f32_16x16x32_bf16 v[220:223], v[48:51], a[140:143], v[220:223]\n"
"s_waitcnt lgkmcnt(10)\n"
"v_mfma_f32_16x16x32_bf16 v[96:99], v[20:23], a[28:31], v[96:99]\n"
"ds_read_b64_tr_b16 v[48:49], v11 offset:19976\n"
"ds_read_b64_tr_b16 v[50:51], v12 offset:19976\n"
"v_mfma_f32_16x16x32_bf16 v[160:163], v[20:23], a[84:87], v[160:163]\n"
"s_add_u32 s40, s40, 0x8000\n"
"s_addc_u32 s41, s41, 0\n"
"v_mfma_f32_16x16x32_bf16 v[224:227], v[20:23], a[140:143], v[224:227]\n"
"s_sub_u32 s42, s42, 0x8000\n"
"s_cselect_b32 s42, 0, s42\n"
"v_mfma_f32_16x16x32_bf16 v[100:103], v[24:27], a[28:31], v[100:103]\n"
"s_add_u32 s57, s60, 65536\n"
"v_mfma_f32_16x16x32_bf16 v[164:167], v[24:27], a[84:87], v[164:167]\n"
"v_add_u32 v11, s57, %[wl0]\n"
"v_add_u32 v12, s57, %[wl1]\n"
"v_mfma_f32_16x16x32_bf16 v[228:231], v[24:27], a[140:143], v[228:231]\n"
"s_waitcnt lgkmcnt(8)\n"
"v_mfma_f32_16x16x32_bf16 v[104:107], v[28:31], a[28:31], v[104:107]\n"
"v_mfma_f32_16x16x32_bf16 v[168:171], v[28:31], a[84:87], v[168:171]\n"
"v_mfma_f32_16x16x32_bf16 v[232:235], v[28:31], a[140:143], v[232:235]\n"
"s_waitcnt vmcnt(16) lgkmcnt(0)\n"
"s_barrier\n"
"v_mfma_f32_16x16x32_bf16 v[108:111], v[32:35], a[28:31], v[108:111]\n"
"ds_read_b64_tr_b16 v[20:21], v11 offset:0\n"
"ds_read_b64_tr_b16 v[22:23], v12 offset:0\n"
"v_mfma_f32_16x16x32_bf16 v[172:175], v[32:35], a[84:87], v[172:175]\n"
"ds_read_b64_tr_b16 v[24:25], v11 offset:8\n"
"ds_read_b64_tr_b16 v[26:27], v12 offset:8\n"
"v_mfma_f32_16x16x32_bf16 v[236:239], v[32:35], a[140:143], v[236:239]\n"
"ds_read_b64_tr_b16 v[28:29], v11 offset:512\n"
"ds_read_b64_tr_b16 v[30:31], v12 offset:512\n"
"v_mfma_f32_16x16x32_bf16 v[112:115], v[36:39], a[28:31], v[112:115]\n"
"ds_read_b64_tr_b16 v[32:33], v11 offset:520\n"
"ds_read_b64_tr_b16 v[34:35], v12 offset:520\n"
"v_mfma_f32_16x16x32_bf16 v[176:179], v[36:39], a[84:87], v[176:179]\n"
"v_mfma_f32_16x16x32_bf16 v[240:243], v[36:39], a[140:143], v[240:243]\n"
"v_mfma_f32_16x16x32_bf16 v[116:119], v[40:43], a[28:31], v[116:119]\n"
"ds_read_b64_tr_b16 v[36:37], v11 offset:1024\n"
"ds_read_b64_tr_b16 v[38:39], v12 offset:1024\n"
"v_mfma_f32_16x16x32_bf16 v[180:183], v[40:43], a[84:87], v[180:183]\n"
"v_mfma_f32_16x16x32_bf16 v[244:247], v[40:43], a[140:143], v[244:247]\n"
"v_mfma_f32_16x16x32_bf16 v[120:123], v[44:47], a[28:31], v[120:123]\n"
"ds_read_b64_tr_b16 v[40:41], v11 offset:1032\n"
"ds_read_b64_tr_b16 v[42:43], v12 offset:1032\n"
"v_mfma_f32_16x16x32_bf16 v[184:187], v[44:47], a[84:87], v[184:187]\n"
"v_mfma_f32_16x16x32_bf16 v[248:251], v[44:47], a[140:143], v[248:251]\n"
"v_mfma_f32_16x16x32_bf16 v[124:127], v[48:51], a[28:31], v[124:127]\n"
"ds_read_b64_tr_b16 v[44:45], v11 offset:1536\n"
"ds_read_b64_tr_b16 v[46:47], v12 offset:1536\n"
"v_mfma_f32_16x16x32_bf16 v[188:191], v[48:51], a[84:87], v[188:191]\n"
"v_mfma_f32_16x16x32_bf16 v[252:255], v[48:51], a[140:143], v[252:255]\n"
"s_waitcnt lgkmcnt(10)\n"
"v_mfma_f32_16x16x32_bf16 v[64:67], v[20:23], a[32:35], v[64:67]\n"
"ds_read_b64_tr_b16 v[48:49], v11 offset:1544\n"
"ds_read_b64_tr_b16 v[50:51], v12 offset:1544\n"
"v_mfma_f32_16x16x32_bf16 v[128:131], v[20:23], a[88:91], v[128:131]\n"
"s_add_u32 s56, s58, 131072\n"
"s_add_u32 m0, s56, 0\n"
"v_mfma_f32_16x16x32_bf16 v[192:195], v[20:23], a[144:147], v[192:195]\n"
"v_mfma_f32_16x16x32_bf16 v[68:71], v[24:27], a[32:35], v[68:71]\n"
"ds_read_b64_tr_b16 v[20:21], v11 offset:2048\n"
"ds_read_b64_tr_b16 v[22:23], v12 offset:2048\n"
"v_mfma_f32_16x16x32_bf16 v[132:135], v[24:27], a[88:91], v[132:135]\n"
"buffer_load_dwordx4 %[dmaw], s[48:51], 0 offen offset:0 lds\n"
"v_mfma_f32_16x16x32_bf16 v[196:199], v[24:27], a[144:147], v[196:199]\n"
"s_add_u32 m0, s56, 896\n"
"s_waitcnt lgkmcnt(10)\n"
"v_mfma_f32_16x16x32_bf16 v[72:75], v[28:31], a[32:35], v[72:75]\n"
"ds_read_b64_tr_b16 v[24:25], v11 offset:2056\n"
"ds_read_b64_tr_b16 v[26:27], v12 offset:2056\n"
"v_mfma_f32_16x16x32_bf16 v[136:139], v[28:31], a[88:91], v[136:139]\n"
"buffer_load_dwordx4 %[dmaw], s[48:51], 0 offen offset:128 lds\n"
"v_mfma_f32_16x16x32_bf16 v[200:203], v[28:31], a[144:147], v[200:203]\n"
"v_mfma_f32_16x16x32_bf16 v[76:79], v[32:35], a[32:35], v[76:79]\n"
"ds_read_b64_tr_b16 v[28:29], v11 offset:2560\n"
"ds_read_b64_tr_b16 v[30:31], v12 offset:2560\n"
"v_mfma_f32_16x16x32_bf16 v[140:143], v[32:35], a[88:91], v[140:143]\n"
"s_add_u32 m0, s56, 1792\n"
"v_mfma_f32_16x16x32_bf16 v[204:207], v[32:35], a[144:147], v[204:207]\n"
"buffer_load_dwordx4 %[dmaw], s[48:51], 0 offen offset:256 lds\n"
"s_waitcnt lgkmcnt(10)\n"
"v_mfma_f32_16x16x32_bf16 v[80:83], v[36:39], a[32:35], v[80:83]\n"
"ds_read_b64_tr_b16 v[32:33], v11 offset:2568\n"
"ds_read_b64_tr_b16 v[34:35], v12 offset:2568\n"
"v_mfma_f32_16x16x32_bf16 v[144:147], v[36:39], a[88:91], v[144:147]\n"
"s_add_u32 m0, s56, 2688\n"
"v_mfma_f32_16x16x32_bf16 v[208:211], v[36:39], a[144:147], v[208:211]\n"
"v_mfma_f32_16x16x32_bf16 v[84:87], v[40:43], a[32:35], v[84:87]\n"
"ds_read_b64_tr_b16 v[36:37], v11 offset:3072\n"
"ds_read_b64_tr_b16 v[38:39], v12 offset:3072\n"
"v_mfma_f32_16x16x32_bf16 v[148:151], v[40:43], a[88:91], v[148:151]\n"
"buffer_load_dwordx4 %[dmaw], s[48:51], 0 offen offset:384 lds\n"
"v_mfma_f32_16x16x32_bf16 v[212:215], v[40:43], a[144:147], v[212:215]\n"
"s_add_u32 m0, s56, 4096\n"
"s_waitcnt lgkmcnt(10)\n"
"v_mfma_f32_16x16x32_bf16 v[88:91], v[44:47], a[32:35], v[88:91]\n"
"ds_read_b64_tr_b16 v[40:41], v11 offset:3080\n"
"ds_read_b64_tr_b16 v[42:43], v12 offset:3080\n"
"v_mfma_f32_16x16x32_bf16 v[152:155], v[44:47], a[88:91], v[152:155]\n"
"buffer_load_dwordx4 %[dmaw], s[48:51], s63 offen offset:0 lds\n"
"v_mfma_f32_16x16x32_bf16 v[216:219], v[44:47], a[144:147], v[216:219]\n"
"v_mfma_f32_16x16x32_bf16 v[92:95], v[48:51], a[32:35], v[92:95]\n"
"ds_read_b64_tr_b16 v[44:45], v11 offset:3584\n"
"ds_read_b64_tr_b16 v[46:47], v12 offset:3584\n"
"v_mfma_f32_16x16x32_bf16 v[156:159], v[48:51], a[88:91], v[156:159]\n"
"s_add_u32 m0, s56, 4992\n"
"v_mfma_f32_16x16x32_bf16 v[220:223], v[48:51], a[144:147], v[220:223]\n"
"buffer_load_dwordx4 %[dmaw], s[48:51], s63 offen offset:128 lds\n"
"s_waitcnt lgkmcnt(10)\n"
"v_mfma_f32_16x16x32_bf16 v[96:99], v[20:23], a[32:35], v[96:99]\n"
"ds_read_b64_tr_b16 v[48:49], v11 offset:3592\n"
"ds_read_b64_tr_b16 v[50:51], v12 offset:3592\n"
"v_mfma_f32_16x16x32_bf16 v[160:163], v[20:23], a[88:91], v[160:163]\n"
"s_add_u32 m0, s56, 5888\n"
"v_mfma_f32_16x16x32_bf16 v[224:227], v[20:23], a[144:147], v[224:227]\n"
"v_mfma_f32_16x16x32_bf16 v[100:103], v[24:27], a[32:35], v[100:103]\n"
"ds_read_b64_tr_b16 v[20:21], v11 offset:16384\n"
"ds_read_b64_tr_b16 v[22:23], v12 offset:16384\n"
"v_mfma_f32_16x16x32_bf16 v[164:167], v[24:27], a[88:91], v[164:167]\n"
"buffer_load_dwordx4 %[dmaw], s[48:51], s63 offen offset:256 lds\n"
"v_mfma_f32_16x16x32_bf16 v[228:231], v[24:27], a[144:147], v[228:231]\n"
"s_add_u32 m0, s56, 6784\n"
"s_waitcnt lgkmcnt(10)\n"
"v_mfma_f32_16x16x32_bf16 v[104:107], v[28:31], a[32:35], v[104:107]\n"
"ds_read_b64_tr_b16 v[24:25], v11 offset:16392\n"
"ds_read_b64_tr_b16 v[26:27], v12 offset:16392\n"
"v_mfma_f32_16x16x32_bf16 v[168:171], v[28:31], a[88:91], v[168:171]\n"
"buffer_load_dwordx4 %[dmaw], s[48:51], s63 offen offset:384 lds\n"
"v_mfma_f32_16x16x32_bf16 v[232:235], v[28:31], a[144:147], v[232:235]\n"
"v_mfma_f32_16x16x32_bf16 v[108:111], v[32:35], a[32:35], v[108:111]\n"
"ds_read_b64_tr_b16 v[28:29], v11 offset:16896\n"
"ds_read_b64_tr_b16 v[30:31], v12 offset:16896\n"
"v_mfma_f32_16x16x32_bf16 v[172:175], v[32:35], a[88:91], v[172:175]\n"
"s_add_u32 s48, s48, 0x80000\n"
"s_addc_u32 s49, s49, 0\n"
"v_mfma_f32_16x16x32_bf16 v[236:239], v[32:35], a[144:147], v[236:239]\n"
"s_waitcnt lgkmcnt(10)\n"
"v_mfma_f32_16x16x32_bf16 v[112:115], v[36:39], a[32:35], v[112:115]\n"
"ds_read_b64_tr_b16 v[32:33], v11 offset:16904\n"
"ds_read_b64_tr_b16 v[34:35], v12 offset:16904\n"
"v_mfma_f32_16x16x32_bf16 v[176:179], v[36:39], a[88:91], v[176:179]\n"
"s_sub_u32 s50, s50, 0x80000\n"
"s_cselect_b32 s50, 0, s50\n"
"v_mfma_f32_16x16x32_bf16 v[240:243], v[36:39], a[144:147], v[240:243]\n"
"s_add_u32 s56, s58, 32768\n"
"v_mfma_f32_16x16x32_bf16 v[116:119], v[40:43], a[32:35], v[116:119]\n"
"ds_read_b64_tr_b16 v[36:37], v11 offset:17408\n"
"ds_read_b64_tr_b16 v[38:39], v12 offset:17408\n"
"v_mfma_f32_16x16x32_bf16 v[180:183], v[40:43], a[88:91], v[180:183]\n"
"s_add_u32 m0, s56, 0\n"
"v_mfma_f32_16x16x32_bf16 v[244:247], v[40:43], a[144:147], v[244:247]\n"
"buffer_load_dwordx4 %[dmakv], s[40:43], 0 offen offset:0 nt lds\n"
"s_waitcnt lgkmcnt(10)\n"
"v_mfma_f32_16x16x32_bf16 v[120:123], v[44:47], a[32:35], v[120:123]\n"
"ds_read_b64_tr_b16 v[40:41], v11 offset:17416\n"
"ds_read_b64_tr_b16 v[42:43], v12 offset:17416\n"
"v_mfma_f32_16x16x32_bf16 v[184:187], v[44:47], a[88:91], v[184:187]\n"
"s_add_u32 m0, s56, 896\n"
"v_mfma_f32_16x16x32_bf16 v[248:251], v[44:47], a[144:147], v[248:251]\n"
"v_mfma_f32_16x16x32_bf16 v[124:127], v[48:51], a[32:35], v[124:127]\n"
"ds_read_b64_tr_b16 v[44:45], v11 offset:17920\n"
"ds_read_b64_tr_b16 v[46:47], v12 offset:17920\n"
"v_mfma_f32_16x16x32_bf16 v[188:191], v[48:51], a[88:91], v[188:191]\n"
"buffer_load_dwordx4 %[dmakv], s[40:43], 0 offen offset:128 nt lds\n"
"v_mfma_f32_16x16x32_bf16 v[252:255], v[48:51], a[144:147], v[252:255]\n"
"s_add_u32 m0, s56, 1792\n"
"s_waitcnt lgkmcnt(10)\n"
"v_mfma_f32_16x16x32_bf16 v[64:67], v[20:23], a[36:39], v[64:67]\n"
"ds_read_b64_tr_b16 v[48:49], v11 offset:17928\n"
"ds_read_b64_tr_b16 v[50:51], v12 offset:17928\n"
"v_mfma_f32_16x16x32_bf16 v[128:131], v[20:23], a[92:95], v[128:131]\n"
"buffer_load_dwordx4 %[dmakv], s[40:43], 0 offen offset:256 nt lds\n"
"v_mfma_f32_16x16x32_bf16 v[192:195], v[20:23], a[148:151], v[192:195]\n"
"v_mfma_f32_16x16x32_bf16 v[68:71], v[24:27], a[36:39], v[68:71]\n"
"ds_read_b64_tr_b16 v[20:21], v11 offset:18432\n"
"ds_read_b64_tr_b16 v[22:23], v12 offset:18432\n"
"v_mfma_f32_16x16x32_bf16 v[132:135], v[24:27], a[92:95], v[132:135]\n"
"s_add_u32 m0, s56, 2688\n"
"v_mfma_f32_16x16x32_bf16 v[196:199], v[24:27], a[148:151], v[196:199]\n"
"buffer_load_dwordx4 %[dmakv], s[40:43], 0 offen offset:384 nt lds\n"
"s_waitcnt lgkmcnt(10)\n"
"v_mfma_f32_16x16x32_bf16 v[72:75], v[28:31], a[36:39], v[72:75]\n"
"ds_read_b64_tr_b16 v[24:25], v11 offset:18440\n"
"ds_read_b64_tr_b16 v[26:27], v12 offset:18440\n"
"v_mfma_f32_16x16x32_bf16 v[136:139], v[28:31], a[92:95], v[136:139]\n"
"s_add_u32 m0, s56, 4096\n"
"v_mfma_f32_16x16x32_bf16 v[200:203], v[28:31], a[148:151], v[200:203]\n"
"v_mfma_f32_16x16x32_bf16 v[76:79], v[32:35], a[36:39], v[76:79]\n"
"ds_read_b64_tr_b16 v[28:29], v11 offset:18944\n"
"ds_read_b64_tr_b16 v[30:31], v12 offset:18944\n"
"v_mfma_f32_16x16x32_bf16 v[140:143], v[32:35], a[92:95], v[140:143]\n"
"buffer_load_dwordx4 %[dmakv], s[40:43], s62 offen offset:0 nt lds\n"
"v_mfma_f32_16x16x32_bf16 v[204:207], v[32:35], a[148:151], v[204:207]\n"
"s_add_u32 m0, s56, 4992\n"
"s_waitcnt lgkmcnt(10)\n"
"v_mfma_f32_16x16x32_bf16 v[80:83], v[36:39], a[36:39], v[80:83]\n"
"ds_read_b64_tr_b16 v[32:33], v11 offset:18952\n"
"ds_read_b64_tr_b16 v[34:35], v12 offset:18952\n"
"v_mfma_f32_16x16x32_bf16 v[144:147], v[36:39], a[92:95], v[144:147]\n"
"buffer_load_dwordx4 %[dmakv], s[40:43], s62 offen offset:128 nt lds\n"
"v_mfma_f32_16x16x32_bf16 v[208:211], v[36:39], a[148:151], v[208:211]\n"
"v_mfma_f32_16x16x32_bf16 v[84:87], v[40:43], a[36:39], v[84:87]\n"
"ds_read_b64_tr_b16 v[36:37], v11 offset:19456\n"
"ds_read_b64_tr_b16 v[38:39], v12 offset:19456\n"
"v_mfma_f32_16x16x32_bf16 v[148:151], v[40:43], a[92:95], v[148:151]\n"
"s_add_u32 m0, s56, 5888\n"
"v_mfma_f32_16x16x32_bf16 v[212:215], v[40:43], a[148:151], v[212:215]\n"
"buffer_load_dwordx4 %[dmakv], s[40:43], s62 offen offset:256 nt lds\n"
"s_waitcnt lgkmcnt(10)\n"
"v_mfma_f32_16x16x32_bf16 v[88:91], v[44:47], a[36:39], v[88:91]\n"
"ds_read_b64_tr_b16 v[40:41], v11 offset:19464\n"
"ds_read_b64_tr_b16 v[42:43], v12 offset:19464\n"
"v_mfma_f32_16x16x32_bf16 v[152:155], v[44:47], a[92:95], v[152:155]\n"
"s_add_u32 m0, s56, 6784\n"
"v_mfma_f32_16x16x32_bf16 v[216:219], v[44:47], a[148:151], v[216:219]\n"
"v_mfma_f32_16x16x32_bf16 v[92:95], v[48:51], a[36:39], v[92:95]\n"
"ds_read_b64_tr_b16 v[44:45], v11 offset:19968\n"
"ds_read_b64_tr_b16 v[46:47], v12 offset:19968\n"
"v_mfma_f32_16x16x32_bf16 v[156:159], v[48:51], a[92:95], v[156:159]\n"
"buffer_load_dwordx4 %[dmakv], s[40:43], s62 offen offset:384 nt lds\n"
"v_mfma_f32_16x16x32_bf16 v[220:223], v[48:51], a[148:151], v[220:223]\n"
"s_waitcnt lgkmcnt(10)\n"
"v_mfma_f32_16x16x32_bf16 v[96:99], v[20:23], a[36:39], v[96:99]\n"
"ds_read_b64_tr_b16 v[48:49], v11 offset:19976\n"
"ds_read_b64_tr_b16 v[50:51], v12 offset:19976\n"
"v_mfma_f32_16x16x32_bf16 v[160:163], v[20:23], a[92:95], v[160:163]\n"
"s_add_u32 s40, s40, 0x8000\n"
"s_addc_u32 s41, s41, 0\n"
"v_mfma_f32_16x16x32_bf16 v[224:227], v[20:23], a[148:151], v[224:227]\n"
"s_sub_u32 s42, s42, 0x8000\n"
"s_cselect_b32 s42, 0, s42\n"
"v_mfma_f32_16x16x32_bf16 v[100:103], v[24:27], a[36:39], v[100:103]\n"
"s_add_u32 s57, s60, 98304\n"
"v_mfma_f32_16x16x32_bf16 v[164:167], v[24:27], a[92:95], v[164:167]\n"
"v_add_u32 v11, s57, %[wl0]\n"
"v_add_u32 v12, s57, %[wl1]\n"
"v_mfma_f32_16x16x32_bf16 v[228:231], v[24:27], a[148:151], v[228:231]\n"
"s_waitcnt lgkmcnt(8)\n"
"v_mfma_f32_16x16x32_bf16 v[104:107], v[28:31], a[36:39], v[104:107]\n"
"v_mfma_f32_16x16x32_bf16 v[168:171], v[28:31], a[92:95], v[168:171]\n"
"v_mfma_f32_16x16x32_bf16 v[232:235], v[28:31], a[148:151], v[232:235]\n"
"s_waitcnt vmcnt(24) lgkmcnt(0)\n"
"s_barrier\n"
"v_mfma_f32_16x16x32_bf16 v[108:111], v[32:35], a[36:39], v[108:111]\n"
"ds_read_b64_tr_b16 v[20:21], v11 offset:0\n"
"ds_read_b64_tr_b16 v[22:23], v12 offset:0\n"
"v_mfma_f32_16x16x32_bf16 v[172:175], v[32:35], a[92:95], v[172:175]\n"
"ds_read_b64_tr_b16 v[24:25], v11 offset:8\n"
"ds_read_b64_tr_b16 v[26:27], v12 offset:8\n"
"v_mfma_f32_16x16x32_bf16 v[236:239], v[32:35], a[148:151], v[236:239]\n"
"ds_read_b64_tr_b16 v[28:29], v11 offset:512\n"
"ds_read_b64_tr_b16 v[30:31], v12 offset:512\n"
"v_mfma_f32_16x16x32_bf16 v[112:115], v[36:39], a[36:39], v[112:115]\n"
"ds_read_b64_tr_b16 v[32:33], v11 offset:520\n"
"ds_read_b64_tr_b16 v[34:35], v12 offset:520\n"
"v_mfma_f32_16x16x32_bf16 v[176:179], v[36:39], a[92:95], v[176:179]\n"
"v_mfma_f32_16x16x32_bf16 v[240:243], v[36:39], a[148:151], v[240:243]\n"
"v_mfma_f32_16x16x32_bf16 v[116:119], v[40:43], a[36:39], v[116:119]\n"
"ds_read_b64_tr_b16 v[36:37], v11 offset:1024\n"
"ds_read_b64_tr_b16 v[38:39], v12 offset:1024\n"
"v_mfma_f32_16x16x32_bf16 v[180:183], v[40:43], a[92:95], v[180:183]\n"
"v_mfma_f32_16x16x32_bf16 v[244:247], v[40:43], a[148:151], v[244:247]\n"
"v_mfma_f32_16x16x32_bf16 v[120:123], v[44:47], a[36:39], v[120:123]\n"
"ds_read_b64_tr_b16 v[40:41], v11 offset:1032\n"
"ds_read_b64_tr_b16 v[42:43], v12 offset:1032\n"
"v_mfma_f32_16x16x32_bf16 v[184:187], v[44:47], a[92:95], v[184:187]\n"
"v_mfma_f32_16x16x32_bf16 v[248:251], v[44:47], a[148:151], v[248:251]\n"
"v_mfma_f32_16x16x32_bf16 v[124:127], v[48:51], a[36:39], v[124:127]\n"
"ds_read_b64_tr_b16 v[44:45], v11 offset:1536\n"
"ds_read_b64_tr_b16 v[46:47], v12 offset:1536\n"
"v_mfma_f32_16x16x32_bf16 v[188:191], v[48:51], a[92:95], v[188:191]\n"
"v_mfma_f32_16x16x32_bf16 v[252:255], v[48:51], a[148:151], v[252:255]\n"
"s_waitcnt lgkmcnt(10)\n"
"v_mfma_f32_16x16x32_bf16 v[64:67], v[20:23], a[40:43], v[64:67]\n"
"ds_read_b64_tr_b16 v[48:49], v11 offset:1544\n"
"ds_read_b64_tr_b16 v[50:51], v12 offset:1544\n"
"v_mfma_f32_16x16x32_bf16 v[128:131], v[20:23], a[96:99], v[128:131]\n"
"s_add_u32 s56, s58, 65536\n"
"v_mfma_f32_16x16x32_bf16 v[192:195], v[20:23], a[152:155], v[192:195]\n"
"v_mfma_f32_16x16x32_bf16 v[68:71], v[24:27], a[40:43], v[68:71]\n"
"ds_read_b64_tr_b16 v[20:21], v11 offset:2048\n"
"ds_read_b64_tr_b16 v[22:23], v12 offset:2048\n"
"v_mfma_f32_16x16x32_bf16 v[132:135], v[24:27], a[96:99], v[132:135]\n"
"s_add_u32 m0, s56, 0\n"
"v_mfma_f32_16x16x32_bf16 v[196:199], v[24:27], a[152:155], v[196:199]\n"
"s_waitcnt lgkmcnt(10)\n"
"v_mfma_f32_16x16x32_bf16 v[72:75], v[28:31], a[40:43], v[72:75]\n"
"ds_read_b64_tr_b16 v[24:25], v11 offset:2056\n"
"ds_read_b64_tr_b16 v[26:27], v12 offset:2056\n"
"v_mfma_f32_16x16x32_bf16 v[136:139], v[28:31], a[96:99], v[136:139]\n"
"buffer_load_dwordx4 %[dmakv], s[44:47], 0 offen offset:0 nt lds\n"
"v_mfma_f32_16x16x32_bf16 v[200:203], v[28:31], a[152:155], v[200:203]\n"
"v_mfma_f32_16x16x32_bf16 v[76:79], v[32:35], a[40:43], v[76:79]\n"
"ds_read_b64_tr_b16 v[28:29], v11 offset:2560\n"
"ds_read_b64_tr_b16 v[30:31], v12 offset:2560\n"
"v_mfma_f32_16x16x32_bf16 v[140:143], v[32:35], a[96:99], v[140:143]\n"
"v_mfma_f32_16x16x32_bf16 v[204:207], v[32:35], a[152:155], v[204:207]\n"
"s_add_u32 m0, s56, 896\n"
"s_waitcnt lgkmcnt(10)\n"
"v_mfma_f32_16x16x32_bf16 v[80:83], v[36:39], a[40:43], v[80:83]\n"
"ds_read_b64_tr_b16 v[32:33], v11 offset:2568\n"
"ds_read_b64_tr_b16 v[34:35], v12 offset:2568\n"
"v_mfma_f32_16x16x32_bf16 v[144:147], v[36:39], a[96:99], v[144:147]\n"
"v_mfma_f32_16x16x32_bf16 v[208:211], v[36:39], a[152:155], v[208:211]\n"
"v_mfma_f32_16x16x32_bf16 v[84:87], v[40:43], a[40:43], v[84:87]\n"
"ds_read_b64_tr_b16 v[36:37], v11 offset:3072\n"
"ds_read_b64_tr_b16 v[38:39], v12 offset:3072\n"
"v_mfma_f32_16x16x32_bf16 v[148:151], v[40:43], a[96:99], v[148:151]\n"
"buffer_load_dwordx4 %[dmakv], s[44:47], 0 offen offset:128 nt lds\n"
"v_mfma_f32_16x16x32_bf16 v[212:215], v[40:43], a[152:155], v[212:215]\n"
"s_waitcnt lgkmcnt(10)\n"
"v_mfma_f32_16x16x32_bf16 v[88:91], v[44:47], a[40:43], v[88:91]\n"
"ds_read_b64_tr_b16 v[40:41], v11 offset:3080\n"
"ds_read_b64_tr_b16 v[42:43], v12 offset:3080\n"
"v_mfma_f32_16x16x32_bf16 v[152:155], v[44:47], a[96:99], v[152:155]\n"
"s_add_u32 m0, s56, 1792\n"
"v_mfma_f32_16x16x32_bf16 v[216:219], v[44:47], a[152:155], v[216:219]\n"
"v_mfma_f32_16x16x32_bf16 v[92:95], v[48:51], a[40:43], v[92:95]\n"
"ds_read_b64_tr_b16 v[44:45], v11 offset:3584\n"
"ds_read_b64_tr_b16 v[46:47], v12 offset:3584\n"
"v_mfma_f32_16x16x32_bf16 v[156:159], v[48:51], a[96:99], v[156:159]\n"
"v_mfma_f32_16x16x32_bf16 v[220:223], v[48:51], a[152:155], v[220:223]\n"
"buffer_load_dwordx4 %[dmakv], s[44:47], 0 offen offset:256 nt lds\n"
"s_waitcnt lgkmcnt(10)\n"
"v_mfma_f32_16x16x32_bf16 v[96:99], v[20:23], a[40:43], v[96:99]\n"
"ds_read_b64_tr_b16 v[48:49], v11 offset:3592\n"
"ds_read_b64_tr_b16 v[50:51], v12 offset:3592\n"
"v_mfma_f32_16x16x32_bf16 v[160:163], v[20:23], a[96:99], v[160:163]\n"
"v_mfma_f32_16x16x32_bf16 v[224:227], v[20:23], a[152:155], v[224:227]\n"
"v_mfma_f32_16x16x32_bf16 v[100:103], v[24:27], a[40:43], v[100:103]\n"
"ds_read_b64_tr_b16 v[20:21], v11 offset:16384\n"
"ds_read_b64_tr_b16 v[22:23], v12 offset:16384\n"
"v_mfma_f32_16x16x32_bf16 v[164:167], v[24:27], a[96:99], v[164:167]\n"
"s_add_u32 m0, s56, 2688\n"
"v_mfma_f32_16x16x32_bf16 v[228:231], v[24:27], a[152:155], v[228:231]\n"
"s_waitcnt lgkmcnt(10)\n"
"v_mfma_f32_16x16x32_bf16 v[104:107], v[28:31], a[40:43], v[104:107]\n"
"ds_read_b64_tr_b16 v[24:25], v11 offset:16392\n"
"ds_read_b64_tr_b16 v[26:27], v12 offset:16392\n"
"v_mfma_f32_16x16x32_bf16 v[168:171], v[28:31], a[96:99], v[168:171]\n"
"buffer_load_dwordx4 %[dmakv], s[44:47], 0 offen offset:384 nt lds\n"
"v_mfma_f32_16x16x32_bf16 v[232:235], v[28:31], a[152:155], v[232:235]\n"
"v_mfma_f32_16x16x32_bf16 v[108:111], v[32:35], a[40:43], v[108:111]\n"
"ds_read_b64_tr_b16 v[28:29], v11 offset:16896\n"
"ds_read_b64_tr_b16 v[30:31], v12 offset:16896\n"
"v_mfma_f32_16x16x32_bf16 v[172:175], v[32:35], a[96:99], v[172:175]\n"
"v_mfma_f32_16x16x32_bf16 v[236:239], v[32:35], a[152:155], v[236:239]\n"
"s_add_u32 m0, s56, 4096\n"
"s_waitcnt lgkmcnt(10)\n"
"v_mfma_f32_16x16x32_bf16 v[112:115], v[36:39], a[40:43], v[112:115]\n"
"ds_read_b64_tr_b16 v[32:33], v11 offset:16904\n"
"ds_read_b64_tr_b16 v[34:35], v12 offset:16904\n"
"v_mfma_f32_16x16x32_bf16 v[176:179], v[36:39], a[96:99], v[176:179]\n"
"v_mfma_f32_16x16x32_bf16 v[240:243], v[36:39], a[152:155], v[240:243]\n"
"v_mfma_f32_16x16x32_bf16 v[116:119], v[40:43], a[40:43], v[116:119]\n"
"ds_read_b64_tr_b16 v[36:37], v11 offset:17408\n"
"ds_read_b64_tr_b16 v[38:39], v12 offset:17408\n"
"v_mfma_f32_16x16x32_bf16 v[180:183], v[40:43], a[96:99], v[180:183]\n"
"buffer_load_dwordx4 %[dmakv], s[44:47], s62 offen offset:0 nt lds\n"
"v_mfma_f32_16x16x32_bf16 v[244:247], v[40:43], a[152:155], v[244:247]\n"
"s_waitcnt lgkmcnt(10)\n"
"v_mfma_f32_16x16x32_bf16 v[120:123], v[44:47], a[40:43], v[120:123]\n"
"ds_read_b64_tr_b16 v[40:41], v11 offset:17416\n"
"ds_read_b64_tr_b16 v[42:43], v12 offset:17416\n"
"v_mfma_f32_16x16x32_bf16 v[184:187], v[44:47], a[96:99], v[184:187]\n"
"s_add_u32 m0, s56, 4992\n"
"v_mfma_f32_16x16x32_bf16 v[248:251], v[44:47], a[152:155], v[248:251]\n"
"v_mfma_f32_16x16x32_bf16 v[124:127], v[48:51], a[40:43], v[124:127]\n"
"ds_read_b64_tr_b16 v[44:45], v11 offset:17920\n"
"ds_read_b64_tr_b16 v[46:47], v12 offset:17920\n"
"v_mfma_f32_16x16x32_bf16 v[188:191], v[48:51], a[96:99], v[188:191]\n"
"v_mfma_f32_16x16x32_bf16 v[252:255], v[48:51], a[152:155], v[252:255]\n"
"buffer_load_dwordx4 %[dmakv], s[44:47], s62 offen offset:128 nt lds\n"
"s_waitcnt lgkmcnt(10)\n"
"v_mfma_f32_16x16x32_bf16 v[64:67], v[20:23], a[44:47], v[64:67]\n"
"ds_read_b64_tr_b16 v[48:49], v11 offset:17928\n"
"ds_read_b64_tr_b16 v[50:51], v12 offset:17928\n"
"v_mfma_f32_16x16x32_bf16 v[128:131], v[20:23], a[100:103], v[128:131]\n"
"v_mfma_f32_16x16x32_bf16 v[192:195], v[20:23], a[156:159], v[192:195]\n"
"v_mfma_f32_16x16x32_bf16 v[68:71], v[24:27], a[44:47], v[68:71]\n"
"ds_read_b64_tr_b16 v[20:21], v11 offset:18432\n"
"ds_read_b64_tr_b16 v[22:23], v12 offset:18432\n"
"v_mfma_f32_16x16x32_bf16 v[132:135], v[24:27], a[100:103], v[132:135]\n"
"s_add_u32 m0, s56, 5888\n"
"v_mfma_f32_16x16x32_bf16 v[196:199], v[24:27], a[156:159], v[196:199]\n"
"s_waitcnt lgkmcnt(10)\n"
"v_mfma_f32_16x16x32_bf16 v[72:75], v[28:31], a[44:47], v[72:75]\n"
"ds_read_b64_tr_b16 v[24:25], v11 offset:18440\n"
"ds_read_b64_tr_b16 v[26:27], v12 offset:18440\n"
"v_mfma_f32_16x16x32_bf16 v[136:139], v[28:31], a[100:103], v[136:139]\n"
"buffer_load_dwordx4 %[dmakv], s[44:47], s62 offen offset:256 nt lds\n"
"v_mfma_f32_16x16x32_bf16 v[200:203], v[28:31], a[156:159], v[200:203]\n"
"v_mfma_f32_16x16x32_bf16 v[76:79], v[32:35], a[44:47], v[76:79]\n"
"ds_read_b64_tr_b16 v[28:29], v11 offset:18944\n"
"ds_read_b64_tr_b16 v[30:31], v12 offset:18944\n"
"v_mfma_f32_16x16x32_bf16 v[140:143], v[32:35], a[100:103], v[140:143]\n"
"v_mfma_f32_16x16x32_bf16 v[204:207], v[32:35], a[156:159], v[204:207]\n"
"s_add_u32 m0, s56, 6784\n"
"s_waitcnt lgkmcnt(10)\n"
"v_mfma_f32_16x16x32_bf16 v[80:83], v[36:39], a[44:47], v[80:83]\n"
"ds_read_b64_tr_b16 v[32:33], v11 offset:18952\n"
"ds_read_b64_tr_b16 v[34:35], v12 offset:18952\n"
"v_mfma_f32_16x16x32_bf16 v[144:147], v[36:39], a[100:103], v[144:147]\n"
"v_mfma_f32_16x16x32_bf16 v[208:211], v[36:39], a[156:159], v[208:211]\n"
"v_mfma_f32_16x16x32_bf16 v[84:87], v[40:43], a[44:47], v[84:87]\n"
"ds_read_b64_tr_b16 v[36:37], v11 offset:19456\n"
"ds_read_b64_tr_b16 v[38:39], v12 offset:19456\n"
"v_mfma_f32_16x16x32_bf16 v[148:151], v[40:43], a[100:103], v[148:151]\n"
"buffer_load_dwordx4 %[dmakv], s[44:47], s62 offen offset:384 nt lds\n"
"v_mfma_f32_16x16x32_bf16 v[212:215], v[40:43], a[156:159], v[212:215]\n"
"s_waitcnt lgkmcnt(10)\n"
"v_mfma_f32_16x16x32_bf16 v[88:91], v[44:47], a[44:47], v[88:91]\n"
"ds_read_b64_tr_b16 v[40:41], v11 offset:19464\n"
"ds_read_b64_tr_b16 v[42:43], v12 offset:19464\n"
"v_mfma_f32_16x16x32_bf16 v[152:155], v[44:47], a[100:103], v[152:155]\n"
"s_add_u32 s44, s44, 0x8000\n"
"s_addc_u32 s45, s45, 0\n"
"v_mfma_f32_16x16x32_bf16 v[216:219], v[44:47], a[156:159], v[216:219]\n"
"v_mfma_f32_16x16x32_bf16 v[92:95], v[48:51], a[44:47], v[92:95]\n"
"ds_read_b64_tr_b16 v[44:45], v11 offset:19968\n"
"ds_read_b64_tr_b16 v[46:47], v12 offset:19968\n"
"v_mfma_f32_16x16x32_bf16 v[156:159], v[48:51], a[100:103], v[156:159]\n"
"v_mfma_f32_16x16x32_bf16 v[220:223], v[48:51], a[156:159], v[220:223]\n"
"s_waitcnt lgkmcnt(10)\n"
"v_mfma_f32_16x16x32_bf16 v[96:99], v[20:23], a[44:47], v[96:99]\n"
"ds_read_b64_tr_b16 v[48:49], v11 offset:19976\n"
"ds_read_b64_tr_b16 v[50:51], v12 offset:19976\n"
"v_mfma_f32_16x16x32_bf16 v[160:163], v[20:23], a[100:103], v[160:163]\n"
"s_sub_u32 s46, s46, 0x8000\n"
"s_cselect_b32 s46, 0, s46\n"
"v_mfma_f32_16x16x32_bf16 v[224:227], v[20:23], a[156:159], v[224:227]\n"
"v_mfma_f32_16x16x32_bf16 v[100:103], v[24:27], a[44:47], v[100:103]\n"
"s_add_u32 s57, s60, 131072\n"
"v_mfma_f32_16x16x32_bf16 v[164:167], v[24:27], a[100:103], v[164:167]\n"
"v_add_u32 v11, s57, %[wl0]\n"
"v_add_u32 v12, s57, %[wl1]\n"
"v_mfma_f32_16x16x32_bf16 v[228:231], v[24:27], a[156:159], v[228:231]\n"
"s_waitcnt lgkmcnt(8)\n"
"v_mfma_f32_16x16x32_bf16 v[104:107], v[28:31], a[44:47], v[104:107]\n"
"v_mfma_f32_16x16x32_bf16 v[168:171], v[28:31], a[100:103], v[168:171]\n"
"v_mfma_f32_16x16x32_bf16 v[232:235], v[28:31], a[156:159], v[232:235]\n"
"s_waitcnt vmcnt(16) lgkmcnt(0)\n"
"s_barrier\n"
"v_mfma_f32_16x16x32_bf16 v[108:111], v[32:35], a[44:47], v[108:111]\n"
"ds_read_b64_tr_b16 v[20:21], v11 offset:0\n"
"ds_read_b64_tr_b16 v[22:23], v12 offset:0\n"
"v_mfma_f32_16x16x32_bf16 v[172:175], v[32:35], a[100:103], v[172:175]\n"
"ds_read_b64_tr_b16 v[24:25], v11 offset:8\n"
"ds_read_b64_tr_b16 v[26:27], v12 offset:8\n"
"v_mfma_f32_16x16x32_bf16 v[236:239], v[32:35], a[156:159], v[236:239]\n"
"ds_read_b64_tr_b16 v[28:29], v11 offset:512\n"
"ds_read_b64_tr_b16 v[30:31], v12 offset:512\n"
"v_mfma_f32_16x16x32_bf16 v[112:115], v[36:39], a[44:47], v[112:115]\n"
"ds_read_b64_tr_b16 v[32:33], v11 offset:520\n"
"ds_read_b64_tr_b16 v[34:35], v12 offset:520\n"
"v_mfma_f32_16x16x32_bf16 v[176:179], v[36:39], a[100:103], v[176:179]\n"
"v_mfma_f32_16x16x32_bf16 v[240:243], v[36:39], a[156:159], v[240:243]\n"
"v_mfma_f32_16x16x32_bf16 v[116:119], v[40:43], a[44:47], v[116:119]\n"
"ds_read_b64_tr_b16 v[36:37], v11 offset:1024\n"
"ds_read_b64_tr_b16 v[38:39], v12 offset:1024\n"
"v_mfma_f32_16x16x32_bf16 v[180:183], v[40:43], a[100:103], v[180:183]\n"
"v_mfma_f32_16x16x32_bf16 v[244:247], v[40:43], a[156:159], v[244:247]\n"
"v_mfma_f32_16x16x32_bf16 v[120:123], v[44:47], a[44:47], v[120:123]\n"
"ds_read_b64_tr_b16 v[40:41], v11 offset:1032\n"
"ds_read_b64_tr_b16 v[42:43], v12 offset:1032\n"
"v_mfma_f32_16x16x32_bf16 v[184:187], v[44:47], a[100:103], v[184:187]\n"
"v_mfma_f32_16x16x32_bf16 v[248:251], v[44:47], a[156:159], v[248:251]\n"
"v_mfma_f32_16x16x32_bf16 v[124:127], v[48:51], a[44:47], v[124:127]\n"
"ds_read_b64_tr_b16 v[44:45], v11 offset:1536\n"
"ds_read_b64_tr_b16 v[46:47], v12 offset:1536\n"
"v_mfma_f32_16x16x32_bf16 v[188:191], v[48:51], a[100:103], v[188:191]\n"
"v_mfma_f32_16x16x32_bf16 v[252:255], v[48:51], a[156:159], v[252:255]\n"
"s_waitcnt lgkmcnt(10)\n"
"v_mfma_f32_16x16x32_bf16 v[64:67], v[20:23], a[48:51], v[64:67]\n"
"ds_read_b64_tr_b16 v[48:49], v11 offset:1544\n"
"ds_read_b64_tr_b16 v[50:51], v12 offset:1544\n"
"v_mfma_f32_16x16x32_bf16 v[128:131], v[20:23], a[104:107], v[128:131]\n"
"s_add_u32 s56, s58, 98304\n"
"v_mfma_f32_16x16x32_bf16 v[192:195], v[20:23], a[160:163], v[192:195]\n"
"v_mfma_f32_16x16x32_bf16 v[68:71], v[24:27], a[48:51], v[68:71]\n"
"ds_read_b64_tr_b16 v[20:21], v11 offset:2048\n"
"ds_read_b64_tr_b16 v[22:23], v12 offset:2048\n"
"v_mfma_f32_16x16x32_bf16 v[132:135], v[24:27], a[104:107], v[132:135]\n"
"s_add_u32 m0, s56, 0\n"
"v_mfma_f32_16x16x32_bf16 v[196:199], v[24:27], a[160:163], v[196:199]\n"
"s_waitcnt lgkmcnt(10)\n"
"v_mfma_f32_16x16x32_bf16 v[72:75], v[28:31], a[48:51], v[72:75]\n"
"ds_read_b64_tr_b16 v[24:25], v11 offset:2056\n"
"ds_read_b64_tr_b16 v[26:27], v12 offset:2056\n"
"v_mfma_f32_16x16x32_bf16 v[136:139], v[28:31], a[104:107], v[136:139]\n"
"buffer_load_dwordx4 %[dmakv], s[40:43], 0 offen offset:0 nt lds\n"
"v_mfma_f32_16x16x32_bf16 v[200:203], v[28:31], a[160:163], v[200:203]\n"
"v_mfma_f32_16x16x32_bf16 v[76:79], v[32:35], a[48:51], v[76:79]\n"
"ds_read_b64_tr_b16 v[28:29], v11 offset:2560\n"
"ds_read_b64_tr_b16 v[30:31], v12 offset:2560\n"
"v_mfma_f32_16x16x32_bf16 v[140:143], v[32:35], a[104:107], v[140:143]\n"
"v_mfma_f32_16x16x32_bf16 v[204:207], v[32:35], a[160:163], v[204:207]\n"
"s_add_u32 m0, s56, 896\n"
"s_waitcnt lgkmcnt(10)\n"
"v_mfma_f32_16x16x32_bf16 v[80:83], v[36:39], a[48:51], v[80:83]\n"
"ds_read_b64_tr_b16 v[32:33], v11 offset:2568\n"
"ds_read_b64_tr_b16 v[34:35], v12 offset:2568\n"
"v_mfma_f32_16x16x32_bf16 v[144:147], v[36:39], a[104:107], v[144:147]\n"
"v_mfma_f32_16x16x32_bf16 v[208:211], v[36:39], a[160:163], v[208:211]\n"
"v_mfma_f32_16x16x32_bf16 v[84:87], v[40:43], a[48:51], v[84:87]\n"
"ds_read_b64_tr_b16 v[36:37], v11 offset:3072\n"
"ds_read_b64_tr_b16 v[38:39], v12 offset:3072\n"
"v_mfma_f32_16x16x32_bf16 v[148:151], v[40:43], a[104:107], v[148:151]\n"
"buffer_load_dwordx4 %[dmakv], s[40:43], 0 offen offset:128 nt lds\n"
"v_mfma_f32_16x16x32_bf16 v[212:215], v[40:43], a[160:163], v[212:215]\n"
"s_waitcnt lgkmcnt(10)\n"
"v_mfma_f32_16x16x32_bf16 v[88:91], v[44:47], a[48:51], v[88:91]\n"
"ds_read_b64_tr_b16 v[40:41], v11 offset:3080\n"
"ds_read_b64_tr_b16 v[42:43], v12 offset:3080\n"
"v_mfma_f32_16x16x32_bf16 v[152:155], v[44:47], a[104:107], v[152:155]\n"
"s_add_u32 m0, s56, 1792\n"
"v_mfma_f32_16x16x32_bf16 v[216:219], v[44:47], a[160:163], v[216:219]\n"
"v_mfma_f32_16x16x32_bf16 v[92:95], v[48:51], a[48:51], v[92:95]\n"
"ds_read_b64_tr_b16 v[44:45], v11 offset:3584\n"
"ds_read_b64_tr_b16 v[46:47], v12 offset:3584\n"
"v_mfma_f32_16x16x32_bf16 v[156:159], v[48:51], a[104:107], v[156:159]\n"
"buffer_load_dwordx4 %[dmakv], s[40:43], 0 offen offset:256 nt lds\n"
"v_mfma_f32_16x16x32_bf16 v[220:223], v[48:51], a[160:163], v[220:223]\n"
"s_waitcnt lgkmcnt(10)\n"
"v_mfma_f32_16x16x32_bf16 v[96:99], v[20:23], a[48:51], v[96:99]\n"
"ds_read_b64_tr_b16 v[48:49], v11 offset:3592\n"
"ds_read_b64_tr_b16 v[50:51], v12 offset:3592\n"
"v_mfma_f32_16x16x32_bf16 v[160:163], v[20:23], a[104:107], v[160:163]\n"
"v_mfma_f32_16x16x32_bf16 v[224:227], v[20:23], a[160:163], v[224:227]\n"
"s_add_u32 m0, s56, 2688\n"
"v_mfma_f32_16x16x32_bf16 v[100:103], v[24:27], a[48:51], v[100:103]\n"
"ds_read_b64_tr_b16 v[20:21], v11 offset:16384\n"
"ds_read_b64_tr_b16 v[22:23], v12 offset:16384\n"
"v_mfma_f32_16x16x32_bf16 v[164:167], v[24:27], a[104:107], v[164:167]\n"
"v_mfma_f32_16x16x32_bf16 v[228:231], v[24:27], a[160:163], v[228:231]\n"
"s_waitcnt lgkmcnt(10)\n"
"v_mfma_f32_16x16x32_bf16 v[104:107], v[28:31], a[48:51], v[104:107]\n"
"ds_read_b64_tr_b16 v[24:25], v11 offset:16392\n"
"ds_read_b64_tr_b16 v[26:27], v12 offset:16392\n"
"v_mfma_f32_16x16x32_bf16 v[168:171], v[28:31], a[104:107], v[168:171]\n"
"buffer_load_dwordx4 %[dmakv], s[40:43], 0 offen offset:384 nt lds\n"
"v_mfma_f32_16x16x32_bf16 v[232:235], v[28:31], a[160:163], v[232:235]\n"
"v_mfma_f32_16x16x32_bf16 v[108:111], v[32:35], a[48:51], v[108:111]\n"
"ds_read_b64_tr_b16 v[28:29], v11 offset:16896\n"
"ds_read_b64_tr_b16 v[30:31], v12 offset:16896\n"
"v_mfma_f32_16x16x32_bf16 v[172:175], v[32:35], a[104:107], v[172:175]\n"
"s_add_u32 m0, s56, 4096\n"
"v_mfma_f32_16x16x32_bf16 v[236:239], v[32:35], a[160:163], v[236:239]\n"
"s_waitcnt lgkmcnt(10)\n"
"v_mfma_f32_16x16x32_bf16 v[112:115], v[36:39], a[48:51], v[112:115]\n"
"ds_read_b64_tr_b16 v[32:33], v11 offset:16904\n"
"ds_read_b64_tr_b16 v[34:35], v12 offset:16904\n"
"v_mfma_f32_16x16x32_bf16 v[176:179], v[36:39], a[104:107], v[176:179]\n"
"buffer_load_dwordx4 %[dmakv], s[40:43], s62 offen offset:0 nt lds\n"
"v_mfma_f32_16x16x32_bf16 v[240:243], v[36:39], a[160:163], v[240:243]\n"
"v_mfma_f32_16x16x32_bf16 v[116:119], v[40:43], a[48:51], v[116:119]\n"
"ds_read_b64_tr_b16 v[36:37], v11 offset:17408\n"
"ds_read_b64_tr_b16 v[38:39], v12 offset:17408\n"
"v_mfma_f32_16x16x32_bf16 v[180:183], v[40:43], a[104:107], v[180:183]\n"
"v_mfma_f32_16x16x32_bf16 v[244:247], v[40:43], a[160:163], v[244:247]\n"
"s_add_u32 m0, s56, 4992\n"
"s_waitcnt lgkmcnt(10)\n"
"v_mfma_f32_16x16x32_bf16 v[120:123], v[44:47], a[48:51], v[120:123]\n"
"ds_read_b64_tr_b16 v[40:41], v11 offset:17416\n"
"ds_read_b64_tr_b16 v[42:43], v12 offset:17416\n"
"v_mfma_f32_16x16x32_bf16 v[184:187], v[44:47], a[104:107], v[184:187]\n"
"v_mfma_f32_16x16x32_bf16 v[248:251], v[44:47], a[160:163], v[248:251]\n"
"v_mfma_f32_16x16x32_bf16 v[124:127], v[48:51], a[48:51], v[124:127]\n"
"ds_read_b64_tr_b16 v[44:45], v11 offset:17920\n"
"ds_read_b64_tr_b16 v[46:47], v12 offset:17920\n"
"v_mfma_f32_16x16x32_bf16 v[188:191], v[48:51], a[104:107], v[188:191]\n"
"buffer_load_dwordx4 %[dmakv], s[40:43], s62 offen offset:128 nt lds\n"
"v_mfma_f32_16x16x32_bf16 v[252:255], v[48:51], a[160:163], v[252:255]\n"
"s_waitcnt lgkmcnt(10)\n"
"v_mfma_f32_16x16x32_bf16 v[64:67], v[20:23], a[52:55], v[64:67]\n"
"ds_read_b64_tr_b16 v[48:49], v11 offset:17928\n"
"ds_read_b64_tr_b16 v[50:51], v12 offset:17928\n"
"v_mfma_f32_16x16x32_bf16 v[128:131], v[20:23], a[108:111], v[128:131]\n"
"s_add_u32 m0, s56, 5888\n"
"v_mfma_f32_16x16x32_bf16 v[192:195], v[20:23], a[164:167], v[192:195]\n"
"v_mfma_f32_16x16x32_bf16 v[68:71], v[24:27], a[52:55], v[68:71]\n"
"ds_read_b64_tr_b16 v[20:21], v11 offset:18432\n"
"ds_read_b64_tr_b16 v[22:23], v12 offset:18432\n"
"v_mfma_f32_16x16x32_bf16 v[132:135], v[24:27], a[108:111], v[132:135]\n"
"buffer_load_dwordx4 %[dmakv], s[40:43], s62 offen offset:256 nt lds\n"
"v_mfma_f32_16x16x32_bf16 v[196:199], v[24:27], a[164:167], v[196:199]\n"
"s_waitcnt lgkmcnt(10)\n"
"v_mfma_f32_16x16x32_bf16 v[72:75], v[28:31], a[52:55], v[72:75]\n"
"ds_read_b64_tr_b16 v[24:25], v11 offset:18440\n"
"ds_read_b64_tr_b16 v[26:27], v12 offset:18440\n"
"v_mfma_f32_16x16x32_bf16 v[136:139], v[28:31], a[108:111], v[136:139]\n"
"v_mfma_f32_16x16x32_bf16 v[200:203], v[28:31], a[164:167], v[200:203]\n"
"s_add_u32 m0, s56, 6784\n"
"v_mfma_f32_16x16x32_bf16 v[76:79], v[32:35], a[52:55], v[76:79]\n"
"ds_read_b64_tr_b16 v[28:29], v11 offset:18944\n"
"ds_read_b64_tr_b16 v[30:31], v12 offset:18944\n"
"v_mfma_f32_16x16x32_bf16 v[140:143], v[32:35], a[108:111], v[140:143]\n"
"v_mfma_f32_16x16x32_bf16 v[204:207], v[32:35], a[164:167], v[204:207]\n"
"s_waitcnt lgkmcnt(10)\n"
"v_mfma_f32_16x16x32_bf16 v[80:83], v[36:39], a[52:55], v[80:83]\n"
"ds_read_b64_tr_b16 v[32:33], v11 offset:18952\n"
"ds_read_b64_tr_b16 v[34:35], v12 offset:18952\n"
"v_mfma_f32_16x16x32_bf16 v[144:147], v[36:39], a[108:111], v[144:147]\n"
"buffer_load_dwordx4 %[dmakv], s[40:43], s62 offen offset:384 nt lds\n"
"v_mfma_f32_16x16x32_bf16 v[208:211], v[36:39], a[164:167], v[208:211]\n"
"v_mfma_f32_16x16x32_bf16 v[84:87], v[40:43], a[52:55], v[84:87]\n"
"ds_read_b64_tr_b16 v[36:37], v11 offset:19456\n"
"ds_read_b64_tr_b16 v[38:39], v12 offset:19456\n"
"v_mfma_f32_16x16x32_bf16 v[148:151], v[40:43], a[108:111], v[148:151]\n"
"s_add_u32 s40, s40, 0x8000\n"
"s_addc_u32 s41, s41, 0\n"
"v_mfma_f32_16x16x32_bf16 v[212:215], v[40:43], a[164:167], v[212:215]\n"
"s_waitcnt lgkmcnt(10)\n"
"v_mfma_f32_16x16x32_bf16 v[88:91], v[44:47], a[52:55], v[88:91]\n"
"ds_read_b64_tr_b16 v[40:41], v11 offset:19464\n"
"ds_read_b64_tr_b16 v[42:43], v12 offset:19464\n"
"v_mfma_f32_16x16x32_bf16 v[152:155], v[44:47], a[108:111], v[152:155]\n"
"s_sub_u32 s42, s42, 0x8000\n"
"s_cselect_b32 s42, 0, s42\n"
"v_mfma_f32_16x16x32_bf16 v[216:219], v[44:47], a[164:167], v[216:219]\n"
"v_mfma_f32_16x16x32_bf16 v[92:95], v[48:51], a[52:55], v[92:95]\n"
"ds_read_b64_tr_b16 v[44:45], v11 offset:19968\n"
"ds_read_b64_tr_b16 v[46:47], v12 offset:19968\n"
"v_mfma_f32_16x16x32_bf16 v[156:159], v[48:51], a[108:111], v[156:159]\n"
"v_mfma_f32_16x16x32_bf16 v[220:223], v[48:51], a[164:167], v[220:223]\n"
"s_add_u32 s56, s58, 131072\n"
"s_waitcnt lgkmcnt(10)\n"
"v_mfma_f32_16x16x32_bf16 v[96:99], v[20:23], a[52:55], v[96:99]\n"
"ds_read_b64_tr_b16 v[48:49], v11 offset:19976\n"
"ds_read_b64_tr_b16 v[50:51], v12 offset:19976\n"
"v_mfma_f32_16x16x32_bf16 v[160:163], v[20:23], a[108:111], v[160:163]\n"
"v_mfma_f32_16x16x32_bf16 v[224:227], v[20:23], a[164:167], v[224:227]\n"
"v_mfma_f32_16x16x32_bf16 v[100:103], v[24:27], a[52:55], v[100:103]\n"
"s_add_u32 s57, s60, 0\n"
"v_mfma_f32_16x16x32_bf16 v[164:167], v[24:27], a[108:111], v[164:167]\n"
"v_add_u32 v10, s57, %[klane]\n"
"v_mfma_f32_16x16x32_bf16 v[228:231], v[24:27], a[164:167], v[228:231]\n"
"s_waitcnt lgkmcnt(8)\n"
"v_mfma_f32_16x16x32_bf16 v[104:107], v[28:31], a[52:55], v[104:107]\n"
"v_mfma_f32_16x16x32_bf16 v[168:171], v[28:31], a[108:111], v[168:171]\n"
"v_mfma_f32_16x16x32_bf16 v[232:235], v[28:31], a[164:167], v[232:235]\n"
"s_waitcnt vmcnt(32) lgkmcnt(0)\n"
"s_barrier\n"
"v_mfma_f32_16x16x32_bf16 v[108:111], v[32:35], a[52:55], v[108:111]\n"
"ds_read_b128 v[20:23], v10 offset:0\n"
"ds_read_b128 v[24:27], v10 offset:512\n"
"v_mfma_f32_16x16x32_bf16 v[172:175], v[32:35], a[108:111], v[172:175]\n"
"ds_read_b128 v[28:31], v10 offset:1024\n"
"v_mfma_f32_16x16x32_bf16 v[236:239], v[32:35], a[164:167], v[236:239]\n"
"v_mfma_f32_16x16x32_bf16 v[112:115], v[36:39], a[52:55], v[112:115]\n"
"ds_read_b128 v[32:35], v10 offset:1536\n"
"v_mfma_f32_16x16x32_bf16 v[176:179], v[36:39], a[108:111], v[176:179]\n"
"v_mfma_f32_16x16x32_bf16 v[240:243], v[36:39], a[164:167], v[240:243]\n"
"v_mfma_f32_16x16x32_bf16 v[116:119], v[40:43], a[52:55], v[116:119]\n"
"ds_read_b128 v[36:39], v10 offset:2048\n"
"v_mfma_f32_16x16x32_bf16 v[180:183], v[40:43], a[108:111], v[180:183]\n"
"v_mfma_f32_16x16x32_bf16 v[244:247], v[40:43], a[164:167], v[244:247]\n"
"v_mfma_f32_16x16x32_bf16 v[120:123], v[44:47], a[52:55], v[120:123]\n"
"ds_read_b128 v[40:43], v10 offset:2560\n"
"v_mfma_f32_16x16x32_bf16 v[184:187], v[44:47], a[108:111], v[184:187]\n"
"v_mfma_f32_16x16x32_bf16 v[248:251], v[44:47], a[164:167], v[248:251]\n"
"v_mfma_f32_16x16x32_bf16 v[124:127], v[48:51], a[52:55], v[124:127]\n"
"ds_read_b128 v[44:47], v10 offset:3072\n"
"v_mfma_f32_16x16x32_bf16 v[188:191], v[48:51], a[108:111], v[188:191]\n"
"v_mfma_f32_16x16x32_bf16 v[252:255], v[48:51], a[164:167], v[252:255]\n"
"s_nop 7\n"
"v_pk_mul_f32 v[64:65], v[64:65], s[86:87]\n"
"v_pk_mul_f32 v[66:67], v[66:67], s[86:87]\n"
"v_pk_mul_f32 v[68:69], v[68:69], s[86:87]\n"
"v_pk_mul_f32 v[70:71], v[70:71], s[86:87]\n"
"v_cvt_pk_bf16_f32 v52, v64, v65\n"
"v_cvt_pk_bf16_f32 v53, v66, v67\n"
"v_cvt_pk_bf16_f32 v54, v68, v69\n"
"v_cvt_pk_bf16_f32 v55, v70, v71\n"
"v_accvgpr_write_b32 a208, v52\n"
"v_accvgpr_write_b32 a209, v53\n"
"v_accvgpr_write_b32 a210, v54\n"
"v_accvgpr_write_b32 a211, v55\n"
"v_pk_mul_f32 v[72:73], v[72:73], s[86:87]\n"
"v_pk_mul_f32 v[74:75], v[74:75], s[86:87]\n"
"v_pk_mul_f32 v[76:77], v[76:77], s[86:87]\n"
"v_pk_mul_f32 v[78:79], v[78:79], s[86:87]\n"
"v_cvt_pk_bf16_f32 v52, v72, v73\n"
"v_cvt_pk_bf16_f32 v53, v74, v75\n"
"v_cvt_pk_bf16_f32 v54, v76, v77\n"
"v_cvt_pk_bf16_f32 v55, v78, v79\n"
"v_accvgpr_write_b32 a212, v52\n"
"v_accvgpr_write_b32 a213, v53\n"
"v_accvgpr_write_b32 a214, v54\n"
"v_accvgpr_write_b32 a215, v55\n"
"v_pk_mul_f32 v[80:81], v[80:81], s[86:87]\n"
"v_pk_mul_f32 v[82:83], v[82:83], s[86:87]\n"
"v_pk_mul_f32 v[84:85], v[84:85], s[86:87]\n"
"v_pk_mul_f32 v[86:87], v[86:87], s[86:87]\n"
"v_cvt_pk_bf16_f32 v52, v80, v81\n"
"v_cvt_pk_bf16_f32 v53, v82, v83\n"
"v_cvt_pk_bf16_f32 v54, v84, v85\n"
"v_cvt_pk_bf16_f32 v55, v86, v87\n"
"v_accvgpr_write_b32 a216, v52\n"
"v_accvgpr_write_b32 a217, v53\n"
"v_accvgpr_write_b32 a218, v54\n"
"v_accvgpr_write_b32 a219, v55\n"
"v_pk_mul_f32 v[88:89], v[88:89], s[86:87]\n"
"v_pk_mul_f32 v[90:91], v[90:91], s[86:87]\n"
"v_pk_mul_f32 v[92:93], v[92:93], s[86:87]\n"
"v_pk_mul_f32 v[94:95], v[94:95], s[86:87]\n"
"v_cvt_pk_bf16_f32 v52, v88, v89\n"
"v_cvt_pk_bf16_f32 v53, v90, v91\n"
"v_cvt_pk_bf16_f32 v54, v92, v93\n"
"v_cvt_pk_bf16_f32 v55, v94, v95\n"
"v_accvgpr_write_b32 a220, v52\n"
"v_accvgpr_write_b32 a221, v53\n"
"v_accvgpr_write_b32 a222, v54\n"
"v_accvgpr_write_b32 a223, v55\n"
"v_pk_mul_f32 v[96:97], v[96:97], s[86:87]\n"
"v_pk_mul_f32 v[98:99], v[98:99], s[86:87]\n"
"v_pk_mul_f32 v[100:101], v[100:101], s[86:87]\n"
"v_pk_mul_f32 v[102:103], v[102:103], s[86:87]\n"
"v_cvt_pk_bf16_f32 v52, v96, v97\n"
"v_cvt_pk_bf16_f32 v53, v98, v99\n"
"v_cvt_pk_bf16_f32 v54, v100, v101\n"
"v_cvt_pk_bf16_f32 v55, v102, v103\n"
"v_accvgpr_write_b32 a224, v52\n"
"v_accvgpr_write_b32 a225, v53\n"
"v_accvgpr_write_b32 a226, v54\n"
"v_accvgpr_write_b32 a227, v55\n"
"v_pk_mul_f32 v[104:105], v[104:105], s[86:87]\n"
"v_pk_mul_f32 v[106:107], v[106:107], s[86:87]\n"
"v_pk_mul_f32 v[108:109], v[108:109], s[86:87]\n"
"v_pk_mul_f32 v[110:111], v[110:111], s[86:87]\n"
"v_cvt_pk_bf16_f32 v52, v104, v105\n"
"v_cvt_pk_bf16_f32 v53, v106, v107\n"
"v_cvt_pk_bf16_f32 v54, v108, v109\n"
"v_cvt_pk_bf16_f32 v55, v110, v111\n"
"v_accvgpr_write_b32 a228, v52\n"
"v_accvgpr_write_b32 a229, v53\n"
"v_accvgpr_write_b32 a230, v54\n"
"v_accvgpr_write_b32 a231, v55\n"
"v_pk_mul_f32 v[112:113], v[112:113], s[86:87]\n"
"v_pk_mul_f32 v[114:115], v[114:115], s[86:87]\n"
"v_pk_mul_f32 v[116:117], v[116:117], s[86:87]\n"
"v_pk_mul_f32 v[118:119], v[118:119], s[86:87]\n"
"v_cvt_pk_bf16_f32 v52, v112, v113\n"
"v_cvt_pk_bf16_f32 v53, v114, v115\n"
"v_cvt_pk_bf16_f32 v54, v116, v117\n"
"v_cvt_pk_bf16_f32 v55, v118, v119\n"
"v_accvgpr_write_b32 a232, v52\n"
"v_accvgpr_write_b32 a233, v53\n"
"v_accvgpr_write_b32 a234, v54\n"
"v_accvgpr_write_b32 a235, v55\n"
"v_pk_mul_f32 v[120:121], v[120:121], s[86:87]\n"
"v_pk_mul_f32 v[122:123], v[122:123], s[86:87]\n"
"v_pk_mul_f32 v[124:125], v[124:125], s[86:87]\n"
"v_pk_mul_f32 v[126:127], v[126:127], s[86:87]\n"
"v_cvt_pk_bf16_f32 v52, v120, v121\n"
"v_cvt_pk_bf16_f32 v53, v122, v123\n"
"v_cvt_pk_bf16_f32 v54, v124, v125\n"
"v_cvt_pk_bf16_f32 v55, v126, v127\n"
"v_accvgpr_write_b32 a236, v52\n"
"v_accvgpr_write_b32 a237, v53\n"
"v_accvgpr_write_b32 a238, v54\n"
"v_accvgpr_write_b32 a239, v55\n"
"v_pk_mul_f32 v[128:129], v[128:129], s[86:87]\n"
"v_pk_mul_f32 v[130:131], v[130:131], s[86:87]\n"
"v_pk_mul_f32 v[132:133], v[132:133], s[86:87]\n"
"v_pk_mul_f32 v[134:135], v[134:135], s[86:87]\n"
"v_cvt_pk_bf16_f32 v52, v128, v129\n"
"v_cvt_pk_bf16_f32 v53, v130, v131\n"
"v_cvt_pk_bf16_f32 v54, v132, v133\n"
"v_cvt_pk_bf16_f32 v55, v134, v135\n"
"v_accvgpr_write_b32 a240, v52\n"
"v_accvgpr_write_b32 a241, v53\n"
"v_accvgpr_write_b32 a242, v54\n"
"v_accvgpr_write_b32 a243, v55\n"
"v_pk_mul_f32 v[136:137], v[136:137], s[86:87]\n"
"v_pk_mul_f32 v[138:139], v[138:139], s[86:87]\n"
"v_pk_mul_f32 v[140:141], v[140:141], s[86:87]\n"
"v_pk_mul_f32 v[142:143], v[142:143], s[86:87]\n"
"v_cvt_pk_bf16_f32 v52, v136, v137\n"
"v_cvt_pk_bf16_f32 v53, v138, v139\n"
"v_cvt_pk_bf16_f32 v54, v140, v141\n"
"v_cvt_pk_bf16_f32 v55, v142, v143\n"
"v_accvgpr_write_b32 a244, v52\n"
"v_accvgpr_write_b32 a245, v53\n"
"v_accvgpr_write_b32 a246, v54\n"
"v_accvgpr_write_b32 a247, v55\n"
"v_pk_mul_f32 v[144:145], v[144:145], s[86:87]\n"
"v_pk_mul_f32 v[146:147], v[146:147], s[86:87]\n"
"v_pk_mul_f32 v[148:149], v[148:149], s[86:87]\n"
"v_pk_mul_f32 v[150:151], v[150:151], s[86:87]\n"
"v_cvt_pk_bf16_f32 v52, v144, v145\n"
"v_cvt_pk_bf16_f32 v53, v146, v147\n"
"v_cvt_pk_bf16_f32 v54, v148, v149\n"
"v_cvt_pk_bf16_f32 v55, v150, v151\n"
"v_accvgpr_write_b32 a248, v52\n"
"v_accvgpr_write_b32 a249, v53\n"
"v_accvgpr_write_b32 a250, v54\n"
"v_accvgpr_write_b32 a251, v55\n"
"v_pk_mul_f32 v[152:153], v[152:153], s[86:87]\n"
"v_pk_mul_f32 v[154:155], v[154:155], s[86:87]\n"
"v_pk_mul_f32 v[156:157], v[156:157], s[86:87]\n"
"v_pk_mul_f32 v[158:159], v[158:159], s[86:87]\n"
"v_cvt_pk_bf16_f32 v52, v152, v153\n"
"v_cvt_pk_bf16_f32 v53, v154, v155\n"
"v_cvt_pk_bf16_f32 v54, v156, v157\n"
"v_cvt_pk_bf16_f32 v55, v158, v159\n"
"v_accvgpr_write_b32 a252, v52\n"
"v_accvgpr_write_b32 a253, v53\n"
"v_accvgpr_write_b32 a254, v54\n"
"v_accvgpr_write_b32 a255, v55\n"
"v_pk_mul_f32 v[160:161], v[160:161], s[86:87]\n"
"v_pk_mul_f32 v[162:163], v[162:163], s[86:87]\n"
"v_pk_mul_f32 v[164:165], v[164:165], s[86:87]\n"
"v_pk_mul_f32 v[166:167], v[166:167], s[86:87]\n"
"v_cvt_pk_bf16_f32 v64, v160, v161\n"
"v_cvt_pk_bf16_f32 v65, v162, v163\n"
"v_cvt_pk_bf16_f32 v66, v164, v165\n"
"v_cvt_pk_bf16_f32 v67, v166, v167\n"
"v_pk_mul_f32 v[168:169], v[168:169], s[86:87]\n"
"v_pk_mul_f32 v[170:171], v[170:171], s[86:87]\n"
"v_pk_mul_f32 v[172:173], v[172:173], s[86:87]\n"
"v_pk_mul_f32 v[174:175], v[174:175], s[86:87]\n"
"v_cvt_pk_bf16_f32 v68, v168, v169\n"
"v_cvt_pk_bf16_f32 v69, v170, v171\n"
"v_cvt_pk_bf16_f32 v70, v172, v173\n"
"v_cvt_pk_bf16_f32 v71, v174, v175\n"
"v_pk_mul_f32 v[176:177], v[176:177], s[86:87]\n"
"v_pk_mul_f32 v[178:179], v[178:179], s[86:87]\n"
"v_pk_mul_f32 v[180:181], v[180:181], s[86:87]\n"
"v_pk_mul_f32 v[182:183], v[182:183], s[86:87]\n"
"v_cvt_pk_bf16_f32 v72, v176, v177\n"
"v_cvt_pk_bf16_f32 v73, v178, v179\n"
"v_cvt_pk_bf16_f32 v74, v180, v181\n"
"v_cvt_pk_bf16_f32 v75, v182, v183\n"
"v_pk_mul_f32 v[184:185], v[184:185], s[86:87]\n"
"v_pk_mul_f32 v[186:187], v[186:187], s[86:87]\n"
"v_pk_mul_f32 v[188:189], v[188:189], s[86:87]\n"
"v_pk_mul_f32 v[190:191], v[190:191], s[86:87]\n"
"v_cvt_pk_bf16_f32 v76, v184, v185\n"
"v_cvt_pk_bf16_f32 v77, v186, v187\n"
"v_cvt_pk_bf16_f32 v78, v188, v189\n"
"v_cvt_pk_bf16_f32 v79, v190, v191\n"
"v_pk_mul_f32 v[192:193], v[192:193], s[86:87]\n"
"v_pk_mul_f32 v[194:195], v[194:195], s[86:87]\n"
"v_pk_mul_f32 v[196:197], v[196:197], s[86:87]\n"
"v_pk_mul_f32 v[198:199], v[198:199], s[86:87]\n"
"v_cvt_pk_bf16_f32 v80, v192, v193\n"
"v_cvt_pk_bf16_f32 v81, v194, v195\n"
"v_cvt_pk_bf16_f32 v82, v196, v197\n"
"v_cvt_pk_bf16_f32 v83, v198, v199\n"
"v_pk_mul_f32 v[200:201], v[200:201], s[86:87]\n"
"v_pk_mul_f32 v[202:203], v[202:203], s[86:87]\n"
"v_pk_mul_f32 v[204:205], v[204:205], s[86:87]\n"
"v_pk_mul_f32 v[206:207], v[206:207], s[86:87]\n"
"v_cvt_pk_bf16_f32 v84, v200, v201\n"
"v_cvt_pk_bf16_f32 v85, v202, v203\n"
"v_cvt_pk_bf16_f32 v86, v204, v205\n"
"v_cvt_pk_bf16_f32 v87, v206, v207\n"
"v_pk_mul_f32 v[208:209], v[208:209], s[86:87]\n"
"v_pk_mul_f32 v[210:211], v[210:211], s[86:87]\n"
"v_pk_mul_f32 v[212:213], v[212:213], s[86:87]\n"
"v_pk_mul_f32 v[214:215], v[214:215], s[86:87]\n"
"v_cvt_pk_bf16_f32 v88, v208, v209\n"
"v_cvt_pk_bf16_f32 v89, v210, v211\n"
"v_cvt_pk_bf16_f32 v90, v212, v213\n"
"v_cvt_pk_bf16_f32 v91, v214, v215\n"
"v_pk_mul_f32 v[216:217], v[216:217], s[86:87]\n"
"v_pk_mul_f32 v[218:219], v[218:219], s[86:87]\n"
"v_pk_mul_f32 v[220:221], v[220:221], s[86:87]\n"
"v_pk_mul_f32 v[222:223], v[222:223], s[86:87]\n"
"v_cvt_pk_bf16_f32 v92, v216, v217\n"
"v_cvt_pk_bf16_f32 v93, v218, v219\n"
"v_cvt_pk_bf16_f32 v94, v220, v221\n"
"v_cvt_pk_bf16_f32 v95, v222, v223\n"
"v_pk_mul_f32 v[224:225], v[224:225], s[86:87]\n"
"v_pk_mul_f32 v[226:227], v[226:227], s[86:87]\n"
"v_pk_mul_f32 v[228:229], v[228:229], s[86:87]\n"
"v_pk_mul_f32 v[230:231], v[230:231], s[86:87]\n"
"v_cvt_pk_bf16_f32 v96, v224, v225\n"
"v_cvt_pk_bf16_f32 v97, v226, v227\n"
"v_cvt_pk_bf16_f32 v98, v228, v229\n"
"v_cvt_pk_bf16_f32 v99, v230, v231\n"
"v_pk_mul_f32 v[232:233], v[232:233], s[86:87]\n"
"v_pk_mul_f32 v[234:235], v[234:235], s[86:87]\n"
"v_pk_mul_f32 v[236:237], v[236:237], s[86:87]\n"
"v_pk_mul_f32 v[238:239], v[238:239], s[86:87]\n"
"v_cvt_pk_bf16_f32 v100, v232, v233\n"
"v_cvt_pk_bf16_f32 v101, v234, v235\n"
"v_cvt_pk_bf16_f32 v102, v236, v237\n"
"v_cvt_pk_bf16_f32 v103, v238, v239\n"
"v_pk_mul_f32 v[240:241], v[240:241], s[86:87]\n"
"v_pk_mul_f32 v[242:243], v[242:243], s[86:87]\n"
"v_pk_mul_f32 v[244:245], v[244:245], s[86:87]\n"
"v_pk_mul_f32 v[246:247], v[246:247], s[86:87]\n"
"v_cvt_pk_bf16_f32 v104, v240, v241\n"
"v_cvt_pk_bf16_f32 v105, v242, v243\n"
"v_cvt_pk_bf16_f32 v106, v244, v245\n"
"v_cvt_pk_bf16_f32 v107, v246, v247\n"
"v_pk_mul_f32 v[248:249], v[248:249], s[86:87]\n"
"v_pk_mul_f32 v[250:251], v[250:251], s[86:87]\n"
"v_pk_mul_f32 v[252:253], v[252:253], s[86:87]\n"
"v_pk_mul_f32 v[254:255], v[254:255], s[86:87]\n"
"v_cvt_pk_bf16_f32 v108, v248, v249\n"
"v_cvt_pk_bf16_f32 v109, v250, v251\n"
"v_cvt_pk_bf16_f32 v110, v252, v253\n"
"v_cvt_pk_bf16_f32 v111, v254, v255\n"
"v_mov_b32 v187, 0x3f803f80\n"
"v_accvgpr_write_b32 a204, v187\n"
"v_accvgpr_write_b32 a205, v187\n"
"v_accvgpr_write_b32 a206, v187\n"
"v_accvgpr_write_b32 a207, v187\n"
"v_mbcnt_lo_u32_b32 v200, -1, 0\n"
"v_mbcnt_hi_u32_b32 v200, -1, v200\n"
"v_lshlrev_b32 v201, 4, v200\n"
"v_lshrrev_b32 v188, 5, v200\n"
"v_mul_u32_u24 v188, 3072, v188\n"
"v_sub_u32 v202, %[vl0], v188\n"
"v_sub_u32 v203, %[vl1], v188\n"
"v_bfe_u32 v188, v200, 2, 3\n"
"v_mul_u32_u24 v188, 384, v188\n"
"v_add_u32 v204, %[dmakv], v188\n"
"s_mov_b64 s[48:49], %[wo]\n"
"s_mul_i32 s72, s84, 7168\n"
"s_add_u32 s48, s48, s72\n"
"s_addc_u32 s49, s49, 0\n"
"s_sub_u32 s50, 229376, s72\n"
"s_mov_b32 s83, 0\n"
"s_mov_b64 s[52:53], %[part]\n"
"s_mov_b32 s54, 157696\n"
"s_mul_i32 s77, %[wave], 6144\n"
"s_add_u32 s78, s77, 2048\n"
"s_add_u32 s79, s77, 4096\n"
"s_add_u32 s88, s77, 22528\n"
"s_add_u32 s89, s78, 22528\n"
"s_add_u32 s69, s79, 22528\n"
"s_nop 1\n"
"s_waitcnt lgkmcnt(5)\n"
"v_mfma_f32_16x16x32_bf16 v[112:115], v[20:23], a[208:211], 0\n"
"ds_read_b128 v[48:51], v10 offset:3584\n"
"s_add_u32 m0, s56, 0\n"
"v_mfma_f32_16x16x32_bf16 v[116:119], v[20:23], a[240:243], 0\n"
"v_mfma_f32_16x16x32_bf16 v[120:123], v[20:23], v[80:83], 0\n"
"v_mfma_f32_16x16x32_bf16 v[112:115], v[24:27], a[212:215], v[112:115]\n"
"ds_read_b128 v[20:23], v10 offset:8192\n"
"buffer_load_dwordx4 %[dmakv], s[44:47], 0 offen offset:0 nt lds\n"
"v_mfma_f32_16x16x32_bf16 v[116:119], v[24:27], a[244:247], v[116:119]\n"
"v_mfma_f32_16x16x32_bf16 v[120:123], v[24:27], v[84:87], v[120:123]\n"
"s_waitcnt lgkmcnt(5)\n"
"v_mfma_f32_16x16x32_bf16 v[112:115], v[28:31], a[216:219], v[112:115]\n"
"ds_read_b128 v[24:27], v10 offset:8704\n"
"v_mfma_f32_16x16x32_bf16 v[116:119], v[28:31], a[248:251], v[116:119]\n"
"s_add_u32 m0, s56, 896\n"
"v_mfma_f32_16x16x32_bf16 v[120:123], v[28:31], v[88:91], v[120:123]\n"
"v_mfma_f32_16x16x32_bf16 v[112:115], v[32:35], a[220:223], v[112:115]\n"
"ds_read_b128 v[28:31], v10 offset:9216\n"
"v_mfma_f32_16x16x32_bf16 v[116:119], v[32:35], a[252:255], v[116:119]\n"
"v_mfma_f32_16x16x32_bf16 v[120:123], v[32:35], v[92:95], v[120:123]\n"
"buffer_load_dwordx4 %[dmakv], s[44:47], 0 offen offset:128 nt lds\n"
"s_waitcnt lgkmcnt(5)\n"
"v_mfma_f32_16x16x32_bf16 v[112:115], v[36:39], a[224:227], v[112:115]\n"
"ds_read_b128 v[32:35], v10 offset:9728\n"
"v_mfma_f32_16x16x32_bf16 v[116:119], v[36:39], v[64:67], v[116:119]\n"
"v_mfma_f32_16x16x32_bf16 v[120:123], v[36:39], v[96:99], v[120:123]\n"
"v_mfma_f32_16x16x32_bf16 v[112:115], v[40:43], a[228:231], v[112:115]\n"
"ds_read_b128 v[36:39], v10 offset:10240\n"
"s_add_u32 m0, s56, 1792\n"
"v_mfma_f32_16x16x32_bf16 v[116:119], v[40:43], v[68:71], v[116:119]\n"
"v_mfma_f32_16x16x32_bf16 v[120:123], v[40:43], v[100:103], v[120:123]\n"
"s_waitcnt lgkmcnt(5)\n"
"v_mfma_f32_16x16x32_bf16 v[112:115], v[44:47], a[232:235], v[112:115]\n"
"ds_read_b128 v[40:43], v10 offset:10752\n"
"v_mfma_f32_16x16x32_bf16 v[116:119], v[44:47], v[72:75], v[116:119]\n"
"buffer_load_dwordx4 %[dmakv], s[44:47], 0 offen offset:256 nt lds\n"
"v_mfma_f32_16x16x32_bf16 v[120:123], v[44:47], v[104:107], v[120:123]\n"
"v_mfma_f32_16x16x32_bf16 v[112:115], v[48:51], a[236:239], v[112:115]\n"
"ds_read_b128 v[44:47], v10 offset:11264\n"
"v_mfma_f32_16x16x32_bf16 v[116:119], v[48:51], v[76:79], v[116:119]\n"
"v_mfma_f32_16x16x32_bf16 v[120:123], v[48:51], v[108:111], v[120:123]\n"
"s_add_u32 m0, s56, 2688\n"
"s_waitcnt lgkmcnt(5)\n"
"v_mfma_f32_16x16x32_bf16 v[124:127], v[20:23], a[208:211], 0\n"
"ds_read_b128 v[48:51], v10 offset:11776\n"
"v_mfma_f32_16x16x32_bf16 v[128:131], v[20:23], a[240:243], 0\n"
"v_mfma_f32_16x16x32_bf16 v[132:135], v[20:23], v[80:83], 0\n"
"v_mfma_f32_16x16x32_bf16 v[124:127], v[24:27], a[212:215], v[124:127]\n"
"ds_read_b128 v[20:23], v10 offset:16384\n"
"buffer_load_dwordx4 %[dmakv], s[44:47], 0 offen offset:384 nt lds\n"
"v_mfma_f32_16x16x32_bf16 v[128:131], v[24:27], a[244:247], v[128:131]\n"
"v_max3_f32 v13, v112, v113, v114\n"
"v_max3_f32 v14, v116, v117, v118\n"
"v_mfma_f32_16x16x32_bf16 v[132:135], v[24:27], v[84:87], v[132:135]\n"
"v_max3_f32 v15, v120, v121, v122\n"
"s_waitcnt lgkmcnt(5)\n"
"v_mfma_f32_16x16x32_bf16 v[124:127], v[28:31], a[216:219], v[124:127]\n"
"ds_read_b128 v[24:27], v10 offset:16896\n"
"v_max_f32 v13, v13, v115\n"
"v_mfma_f32_16x16x32_bf16 v[128:131], v[28:31], a[248:251], v[128:131]\n"
"s_add_u32 m0, s56, 4096\n"
"v_max_f32 v14, v14, v119\n"
"v_mfma_f32_16x16x32_bf16 v[132:135], v[28:31], v[88:91], v[132:135]\n"
"v_max_f32 v15, v15, v123\n"
"v_mfma_f32_16x16x32_bf16 v[124:127], v[32:35], a[220:223], v[124:127]\n"
"ds_read_b128 v[28:31], v10 offset:17408\n"
"v_mfma_f32_16x16x32_bf16 v[128:131], v[32:35], a[252:255], v[128:131]\n"
"v_mfma_f32_16x16x32_bf16 v[132:135], v[32:35], v[92:95], v[132:135]\n"
"buffer_load_dwordx4 %[dmakv], s[44:47], s62 offen offset:0 nt lds\n"
"s_waitcnt lgkmcnt(5)\n"
"v_mfma_f32_16x16x32_bf16 v[124:127], v[36:39], a[224:227], v[124:127]\n"
"ds_read_b128 v[32:35], v10 offset:17920\n"
"v_mfma_f32_16x16x32_bf16 v[128:131], v[36:39], v[64:67], v[128:131]\n"
"v_mfma_f32_16x16x32_bf16 v[132:135], v[36:39], v[96:99], v[132:135]\n"
"v_mfma_f32_16x16x32_bf16 v[124:127], v[40:43], a[228:231], v[124:127]\n"
"ds_read_b128 v[36:39], v10 offset:18432\n"
"s_add_u32 m0, s56, 4992\n"
"v_mfma_f32_16x16x32_bf16 v[128:131], v[40:43], v[68:71], v[128:131]\n"
"v_mfma_f32_16x16x32_bf16 v[132:135], v[40:43], v[100:103], v[132:135]\n"
"s_waitcnt lgkmcnt(5)\n"
"v_mfma_f32_16x16x32_bf16 v[124:127], v[44:47], a[232:235], v[124:127]\n"
"ds_read_b128 v[40:43], v10 offset:18944\n"
"v_mfma_f32_16x16x32_bf16 v[128:131], v[44:47], v[72:75], v[128:131]\n"
"buffer_load_dwordx4 %[dmakv], s[44:47], s62 offen offset:128 nt lds\n"
"v_mfma_f32_16x16x32_bf16 v[132:135], v[44:47], v[104:107], v[132:135]\n"
"v_mfma_f32_16x16x32_bf16 v[124:127], v[48:51], a[236:239], v[124:127]\n"
"ds_read_b128 v[44:47], v10 offset:19456\n"
"v_mfma_f32_16x16x32_bf16 v[128:131], v[48:51], v[76:79], v[128:131]\n"
"v_mfma_f32_16x16x32_bf16 v[132:135], v[48:51], v[108:111], v[132:135]\n"
"s_add_u32 m0, s56, 5888\n"
"s_waitcnt lgkmcnt(5)\n"
"v_mfma_f32_16x16x32_bf16 v[136:139], v[20:23], a[208:211], 0\n"
"ds_read_b128 v[48:51], v10 offset:19968\n"
"v_mfma_f32_16x16x32_bf16 v[140:143], v[20:23], a[240:243], 0\n"
"v_mfma_f32_16x16x32_bf16 v[144:147], v[20:23], v[80:83], 0\n"
"v_mfma_f32_16x16x32_bf16 v[136:139], v[24:27], a[212:215], v[136:139]\n"
"ds_read_b128 v[20:23], v10 offset:24576\n"
"buffer_load_dwordx4 %[dmakv], s[44:47], s62 offen offset:256 nt lds\n"
"v_mfma_f32_16x16x32_bf16 v[140:143], v[24:27], a[244:247], v[140:143]\n"
"v_max3_f32 v13, v13, v124, v125\n"
"v_max3_f32 v14, v14, v128, v129\n"
"v_mfma_f32_16x16x32_bf16 v[144:147], v[24:27], v[84:87], v[144:147]\n"
"v_max3_f32 v15, v15, v132, v133\n"
"s_waitcnt lgkmcnt(5)\n"
"v_mfma_f32_16x16x32_bf16 v[136:139], v[28:31], a[216:219], v[136:139]\n"
"ds_read_b128 v[24:27], v10 offset:25088\n"
"v_max3_f32 v13, v13, v126, v127\n"
"v_mfma_f32_16x16x32_bf16 v[140:143], v[28:31], a[248:251], v[140:143]\n"
"s_add_u32 m0, s56, 6784\n"
"v_max3_f32 v14, v14, v130, v131\n"
"v_mfma_f32_16x16x32_bf16 v[144:147], v[28:31], v[88:91], v[144:147]\n"
"v_max3_f32 v15, v15, v134, v135\n"
"v_mfma_f32_16x16x32_bf16 v[136:139], v[32:35], a[220:223], v[136:139]\n"
"ds_read_b128 v[28:31], v10 offset:25600\n"
"v_mfma_f32_16x16x32_bf16 v[140:143], v[32:35], a[252:255], v[140:143]\n"
"v_mfma_f32_16x16x32_bf16 v[144:147], v[32:35], v[92:95], v[144:147]\n"
"buffer_load_dwordx4 %[dmakv], s[44:47], s62 offen offset:384 nt lds\n"
"s_waitcnt lgkmcnt(5)\n"
"v_mfma_f32_16x16x32_bf16 v[136:139], v[36:39], a[224:227], v[136:139]\n"
"ds_read_b128 v[32:35], v10 offset:26112\n"
"v_mfma_f32_16x16x32_bf16 v[140:143], v[36:39], v[64:67], v[140:143]\n"
"v_mfma_f32_16x16x32_bf16 v[144:147], v[36:39], v[96:99], v[144:147]\n"
"v_mfma_f32_16x16x32_bf16 v[136:139], v[40:43], a[228:231], v[136:139]\n"
"ds_read_b128 v[36:39], v10 offset:26624\n"
"v_mfma_f32_16x16x32_bf16 v[140:143], v[40:43], v[68:71], v[140:143]\n"
"s_add_u32 s44, s44, 0x8000\n"
"s_addc_u32 s45, s45, 0\n"
"v_mfma_f32_16x16x32_bf16 v[144:147], v[40:43], v[100:103], v[144:147]\n"
"s_waitcnt lgkmcnt(5)\n"
"v_mfma_f32_16x16x32_bf16 v[136:139], v[44:47], a[232:235], v[136:139]\n"
"ds_read_b128 v[40:43], v10 offset:27136\n"
"v_mfma_f32_16x16x32_bf16 v[140:143], v[44:47], v[72:75], v[140:143]\n"
"s_sub_u32 s46, s46, 0x8000\n"
"s_cselect_b32 s46, 0, s46\n"
"v_mfma_f32_16x16x32_bf16 v[144:147], v[44:47], v[104:107], v[144:147]\n"
"v_mfma_f32_16x16x32_bf16 v[136:139], v[48:51], a[236:239], v[136:139]\n"
"ds_read_b128 v[44:47], v10 offset:27648\n"
"v_mfma_f32_16x16x32_bf16 v[140:143], v[48:51], v[76:79], v[140:143]\n"
"v_mfma_f32_16x16x32_bf16 v[144:147], v[48:51], v[108:111], v[144:147]\n"
"s_waitcnt lgkmcnt(5)\n"
"v_mfma_f32_16x16x32_bf16 v[148:151], v[20:23], a[208:211], 0\n"
"ds_read_b128 v[48:51], v10 offset:28160\n"
"v_mfma_f32_16x16x32_bf16 v[152:155], v[20:23], a[240:243], 0\n"
"s_add_u32 s56, s56, 0x8000\n"
"s_cmp_eq_u32 s56, s59\n"
"s_cselect_b32 s56, s58, s56\n"
"v_mfma_f32_16x16x32_bf16 v[156:159], v[20:23], v[80:83], 0\n"
"v_mfma_f32_16x16x32_bf16 v[148:151], v[24:27], a[212:215], v[148:151]\n"
"s_add_u32 s57, s57, 0x8000\n"
"s_cmp_eq_u32 s57, s61\n"
"s_cselect_b32 s57, s60, s57\n"
"v_mfma_f32_16x16x32_bf16 v[152:155], v[24:27], a[244:247], v[152:155]\n"
"v_add_u32 v10, s57, %[klane]\n"
"v_max3_f32 v13, v13, v136, v137\n"
"v_mfma_f32_16x16x32_bf16 v[156:159], v[24:27], v[84:87], v[156:159]\n"
"v_max3_f32 v14, v14, v140, v141\n"
"s_waitcnt lgkmcnt(4)\n"
"v_mfma_f32_16x16x32_bf16 v[148:151], v[28:31], a[216:219], v[148:151]\n"
"v_max3_f32 v15, v15, v144, v145\n"
"v_max3_f32 v13, v13, v138, v139\n"
"v_mfma_f32_16x16x32_bf16 v[152:155], v[28:31], a[248:251], v[152:155]\n"
"v_max3_f32 v14, v14, v142, v143\n"
"v_max3_f32 v15, v15, v146, v147\n"
"v_mfma_f32_16x16x32_bf16 v[156:159], v[28:31], v[88:91], v[156:159]\n"
"s_waitcnt vmcnt(24) lgkmcnt(0)\n"
"s_barrier\n"
"v_mfma_f32_16x16x32_bf16 v[148:151], v[32:35], a[220:223], v[148:151]\n"
"ds_read_b128 v[20:23], v10 offset:0\n"
"ds_read_b128 v[24:27], v10 offset:512\n"
"v_mfma_f32_16x16x32_bf16 v[152:155], v[32:35], a[252:255], v[152:155]\n"
"ds_read_b128 v[28:31], v10 offset:1024\n"
"v_mfma_f32_16x16x32_bf16 v[156:159], v[32:35], v[92:95], v[156:159]\n"
"v_mfma_f32_16x16x32_bf16 v[148:151], v[36:39], a[224:227], v[148:151]\n"
"ds_read_b128 v[32:35], v10 offset:1536\n"
"v_mfma_f32_16x16x32_bf16 v[152:155], v[36:39], v[64:67], v[152:155]\n"
"v_mfma_f32_16x16x32_bf16 v[156:159], v[36:39], v[96:99], v[156:159]\n"
"v_mfma_f32_16x16x32_bf16 v[148:151], v[40:43], a[228:231], v[148:151]\n"
"ds_read_b128 v[36:39], v10 offset:2048\n"
"v_mfma_f32_16x16x32_bf16 v[152:155], v[40:43], v[68:71], v[152:155]\n"
"v_mfma_f32_16x16x32_bf16 v[156:159], v[40:43], v[100:103], v[156:159]\n"
"v_mfma_f32_16x16x32_bf16 v[148:151], v[44:47], a[232:235], v[148:151]\n"
"ds_read_b128 v[40:43], v10 offset:2560\n"
"v_mfma_f32_16x16x32_bf16 v[152:155], v[44:47], v[72:75], v[152:155]\n"
"v_mfma_f32_16x16x32_bf16 v[156:159], v[44:47], v[104:107], v[156:159]\n"
"v_mfma_f32_16x16x32_bf16 v[148:151], v[48:51], a[236:239], v[148:151]\n"
"ds_read_b128 v[44:47], v10 offset:3072\n"
"v_mfma_f32_16x16x32_bf16 v[152:155], v[48:51], v[76:79], v[152:155]\n"
"v_mfma_f32_16x16x32_bf16 v[156:159], v[48:51], v[108:111], v[156:159]\n"
"s_nop 7\n"
"v_max3_f32 v13, v13, v148, v149\n"
"v_max3_f32 v14, v14, v152, v153\n"
"v_max3_f32 v15, v15, v156, v157\n"
"v_max3_f32 v13, v13, v150, v151\n"
"v_max3_f32 v14, v14, v154, v155\n"
"v_max3_f32 v15, v15, v158, v159\n"
"s_nop 7\n"
"v_mov_b32 v187, v13\n"
"s_nop 1\n"
"v_permlane32_swap_b32 v13, v187\n"
"s_nop 1\n"
"v_max_f32 v13, v13, v187\n"
"v_mov_b32 v187, v13\n"
"s_nop 1\n"
"v_permlane16_swap_b32 v13, v187\n"
"s_nop 1\n"
"v_max_f32 v13, v13, v187\n"
"v_mov_b32 v17, v13\n"
"v_sub_f32 v112, v112, v13\n"
"v_sub_f32 v113, v113, v13\n"
"v_sub_f32 v114, v114, v13\n"
"v_sub_f32 v115, v115, v13\n"
"v_sub_f32 v124, v124, v13\n"
"v_sub_f32 v125, v125, v13\n"
"v_sub_f32 v126, v126, v13\n"
"v_sub_f32 v127, v127, v13\n"
"v_sub_f32 v136, v136, v13\n"
"v_sub_f32 v137, v137, v13\n"
"v_sub_f32 v138, v138, v13\n"
"v_sub_f32 v139, v139, v13\n"
"v_sub_f32 v148, v148, v13\n"
"v_sub_f32 v149, v149, v13\n"
"v_sub_f32 v150, v150, v13\n"
"v_sub_f32 v151, v151, v13\n"
"v_sub_f32 v52, 0, v17\n"
"v_sub_f32 v53, 0, v17\n"
"v_sub_f32 v54, 0, v17\n"
"v_sub_f32 v55, 0, v17\n"
"v_mov_b32 v187, v14\n"
"s_nop 1\n"
"v_permlane32_swap_b32 v14, v187\n"
"s_nop 1\n"
"v_max_f32 v14, v14, v187\n"
"v_mov_b32 v187, v14\n"
"s_nop 1\n"
"v_permlane16_swap_b32 v14, v187\n"
"s_nop 1\n"
"v_max_f32 v14, v14, v187\n"
"v_mov_b32 v18, v14\n"
"v_sub_f32 v116, v116, v14\n"
"v_sub_f32 v117, v117, v14\n"
"v_sub_f32 v118, v118, v14\n"
"v_sub_f32 v119, v119, v14\n"
"v_sub_f32 v128, v128, v14\n"
"v_sub_f32 v129, v129, v14\n"
"v_sub_f32 v130, v130, v14\n"
"v_sub_f32 v131, v131, v14\n"
"v_sub_f32 v140, v140, v14\n"
"v_sub_f32 v141, v141, v14\n"
"v_sub_f32 v142, v142, v14\n"
"v_sub_f32 v143, v143, v14\n"
"v_sub_f32 v152, v152, v14\n"
"v_sub_f32 v153, v153, v14\n"
"v_sub_f32 v154, v154, v14\n"
"v_sub_f32 v155, v155, v14\n"
"v_sub_f32 v56, 0, v18\n"
"v_sub_f32 v57, 0, v18\n"
"v_sub_f32 v58, 0, v18\n"
"v_sub_f32 v59, 0, v18\n"
"v_mov_b32 v187, v15\n"
"s_nop 1\n"
"v_permlane32_swap_b32 v15, v187\n"
"s_nop 1\n"
"v_max_f32 v15, v15, v187\n"
"v_mov_b32 v187, v15\n"
"s_nop 1\n"
"v_permlane16_swap_b32 v15, v187\n"
"s_nop 1\n"
"v_max_f32 v15, v15, v187\n"
"v_mov_b32 v19, v15\n"
"v_sub_f32 v120, v120, v15\n"
"v_sub_f32 v121, v121, v15\n"
"v_sub_f32 v122, v122, v15\n"
"v_sub_f32 v123, v123, v15\n"
"v_sub_f32 v132, v132, v15\n"
"v_sub_f32 v133, v133, v15\n"
"v_sub_f32 v134, v134, v15\n"
"v_sub_f32 v135, v135, v15\n"
"v_sub_f32 v144, v144, v15\n"
"v_sub_f32 v145, v145, v15\n"
"v_sub_f32 v146, v146, v15\n"
"v_sub_f32 v147, v147, v15\n"
"v_sub_f32 v156, v156, v15\n"
"v_sub_f32 v157, v157, v15\n"
"v_sub_f32 v158, v158, v15\n"
"v_sub_f32 v159, v159, v15\n"
"v_sub_f32 v60, 0, v19\n"
"v_sub_f32 v61, 0, v19\n"
"v_sub_f32 v62, 0, v19\n"
"v_sub_f32 v63, 0, v19\n"
"s_nop 1\n"
"v_exp_f32 v112, v112\n"
"v_exp_f32 v113, v113\n"
"v_exp_f32 v114, v114\n"
"v_exp_f32 v115, v115\n"
"v_exp_f32 v116, v116\n"
"v_exp_f32 v117, v117\n"
"v_exp_f32 v118, v118\n"
"v_exp_f32 v119, v119\n"
"v_exp_f32 v120, v120\n"
"v_exp_f32 v121, v121\n"
"v_exp_f32 v122, v122\n"
"v_exp_f32 v123, v123\n"
"s_nop 0\n"
"v_cvt_pk_bf16_f32 v160, v112, v113\n"
"v_cvt_pk_bf16_f32 v161, v114, v115\n"
"v_cvt_pk_bf16_f32 v168, v116, v117\n"
"v_cvt_pk_bf16_f32 v169, v118, v119\n"
"v_cvt_pk_bf16_f32 v176, v120, v121\n"
"v_cvt_pk_bf16_f32 v177, v122, v123\n"
"s_waitcnt lgkmcnt(5)\n"
"v_mfma_f32_16x16x32_bf16 v[112:115], v[20:23], a[208:211], v[52:55]\n"
"ds_read_b128 v[48:51], v10 offset:3584\n"
"s_add_u32 m0, s56, 0\n"
"v_mfma_f32_16x16x32_bf16 v[116:119], v[20:23], a[240:243], v[56:59]\n"
"v_exp_f32 v124, v124\n"
"v_mfma_f32_16x16x32_bf16 v[120:123], v[20:23], v[80:83], v[60:63]\n"
"v_exp_f32 v125, v125\n"
"v_mfma_f32_16x16x32_bf16 v[112:115], v[24:27], a[212:215], v[112:115]\n"
"ds_read_b128 v[20:23], v10 offset:8192\n"
"v_cvt_pk_bf16_f32 v162, v124, v125\n"
"v_mfma_f32_16x16x32_bf16 v[116:119], v[24:27], a[244:247], v[116:119]\n"
"v_exp_f32 v126, v126\n"
"v_mfma_f32_16x16x32_bf16 v[120:123], v[24:27], v[84:87], v[120:123]\n"
"buffer_load_dwordx4 %[dmakv], s[40:43], 0 offen offset:0 nt lds\n"
"s_waitcnt lgkmcnt(5)\n"
"v_mfma_f32_16x16x32_bf16 v[112:115], v[28:31], a[216:219], v[112:115]\n"
"ds_read_b128 v[24:27], v10 offset:8704\n"
"v_mfma_f32_16x16x32_bf16 v[116:119], v[28:31], a[248:251], v[116:119]\n"
"v_exp_f32 v127, v127\n"
"v_mfma_f32_16x16x32_bf16 v[120:123], v[28:31], v[88:91], v[120:123]\n"
"v_exp_f32 v128, v128\n"
"v_mfma_f32_16x16x32_bf16 v[112:115], v[32:35], a[220:223], v[112:115]\n"
"ds_read_b128 v[28:31], v10 offset:9216\n"
"v_cvt_pk_bf16_f32 v163, v126, v127\n"
"v_mfma_f32_16x16x32_bf16 v[116:119], v[32:35], a[252:255], v[116:119]\n"
"v_exp_f32 v129, v129\n"
"v_mfma_f32_16x16x32_bf16 v[120:123], v[32:35], v[92:95], v[120:123]\n"
"v_cvt_pk_bf16_f32 v170, v128, v129\n"
"s_waitcnt lgkmcnt(5)\n"
"v_mfma_f32_16x16x32_bf16 v[112:115], v[36:39], a[224:227], v[112:115]\n"
"ds_read_b128 v[32:35], v10 offset:9728\n"
"s_add_u32 m0, s56, 896\n"
"v_mfma_f32_16x16x32_bf16 v[116:119], v[36:39], v[64:67], v[116:119]\n"
"v_exp_f32 v130, v130\n"
"v_mfma_f32_16x16x32_bf16 v[120:123], v[36:39], v[96:99], v[120:123]\n"
"v_exp_f32 v131, v131\n"
"v_mfma_f32_16x16x32_bf16 v[112:115], v[40:43], a[228:231], v[112:115]\n"
"ds_read_b128 v[36:39], v10 offset:10240\n"
"v_cvt_pk_bf16_f32 v171, v130, v131\n"
"v_mfma_f32_16x16x32_bf16 v[116:119], v[40:43], v[68:71], v[116:119]\n"
"v_exp_f32 v132, v132\n"
"v_mfma_f32_16x16x32_bf16 v[120:123], v[40:43], v[100:103], v[120:123]\n"
"buffer_load_dwordx4 %[dmakv], s[40:43], 0 offen offset:128 nt lds\n"
"s_waitcnt lgkmcnt(5)\n"
"v_mfma_f32_16x16x32_bf16 v[112:115], v[44:47], a[232:235], v[112:115]\n"
"ds_read_b128 v[40:43], v10 offset:10752\n"
"s_add_u32 m0, s56, 1792\n"
"v_mfma_f32_16x16x32_bf16 v[116:119], v[44:47], v[72:75], v[116:119]\n"
"v_exp_f32 v133, v133\n"
"v_mfma_f32_16x16x32_bf16 v[120:123], v[44:47], v[104:107], v[120:123]\n"
"v_exp_f32 v134, v134\n"
"v_mfma_f32_16x16x32_bf16 v[112:115], v[48:51], a[236:239], v[112:115]\n"
"ds_read_b128 v[44:47], v10 offset:11264\n"
"v_cvt_pk_bf16_f32 v178, v132, v133\n"
"v_mfma_f32_16x16x32_bf16 v[116:119], v[48:51], v[76:79], v[116:119]\n"
"v_exp_f32 v135, v135\n"
"v_mfma_f32_16x16x32_bf16 v[120:123], v[48:51], v[108:111], v[120:123]\n"
"v_cvt_pk_bf16_f32 v179, v134, v135\n"
"s_waitcnt lgkmcnt(5)\n"
"v_mfma_f32_16x16x32_bf16 v[124:127], v[20:23], a[208:211], v[52:55]\n"
"ds_read_b128 v[48:51], v10 offset:11776\n"
"buffer_load_dwordx4 %[dmakv], s[40:43], 0 offen offset:256 nt lds\n"
"v_mfma_f32_16x16x32_bf16 v[128:131], v[20:23], a[240:243], v[56:59]\n"
"v_exp_f32 v136, v136\n"
"v_mfma_f32_16x16x32_bf16 v[132:135], v[20:23], v[80:83], v[60:63]\n"
"v_exp_f32 v137, v137\n"
"v_mfma_f32_16x16x32_bf16 v[124:127], v[24:27], a[212:215], v[124:127]\n"
"ds_read_b128 v[20:23], v10 offset:16384\n"
"v_cvt_pk_bf16_f32 v164, v136, v137\n"
"v_mfma_f32_16x16x32_bf16 v[128:131], v[24:27], a[244:247], v[128:131]\n"
"v_exp_f32 v138, v138\n"
"v_mfma_f32_16x16x32_bf16 v[132:135], v[24:27], v[84:87], v[132:135]\n"
"s_add_u32 m0, s56, 2688\n"
"s_waitcnt lgkmcnt(5)\n"
"v_mfma_f32_16x16x32_bf16 v[124:127], v[28:31], a[216:219], v[124:127]\n"
"ds_read_b128 v[24:27], v10 offset:16896\n"
"buffer_load_dwordx4 %[dmakv], s[40:43], 0 offen offset:384 nt lds\n"
"v_mfma_f32_16x16x32_bf16 v[128:131], v[28:31], a[248:251], v[128:131]\n"
"v_exp_f32 v139, v139\n"
"v_mfma_f32_16x16x32_bf16 v[132:135], v[28:31], v[88:91], v[132:135]\n"
"v_exp_f32 v140, v140\n"
"v_mfma_f32_16x16x32_bf16 v[124:127], v[32:35], a[220:223], v[124:127]\n"
"ds_read_b128 v[28:31], v10 offset:17408\n"
"v_cvt_pk_bf16_f32 v165, v138, v139\n"
"v_mfma_f32_16x16x32_bf16 v[128:131], v[32:35], a[252:255], v[128:131]\n"
"v_exp_f32 v141, v141\n"
"v_mfma_f32_16x16x32_bf16 v[132:135], v[32:35], v[92:95], v[132:135]\n"
"v_cvt_pk_bf16_f32 v172, v140, v141\n"
"s_waitcnt lgkmcnt(5)\n"
"v_mfma_f32_16x16x32_bf16 v[124:127], v[36:39], a[224:227], v[124:127]\n"
"ds_read_b128 v[32:35], v10 offset:17920\n"
"s_add_u32 m0, s56, 4096\n"
"v_mfma_f32_16x16x32_bf16 v[128:131], v[36:39], v[64:67], v[128:131]\n"
"v_exp_f32 v142, v142\n"
"v_mfma_f32_16x16x32_bf16 v[132:135], v[36:39], v[96:99], v[132:135]\n"
"v_exp_f32 v143, v143\n"
"v_mfma_f32_16x16x32_bf16 v[124:127], v[40:43], a[228:231], v[124:127]\n"
"ds_read_b128 v[36:39], v10 offset:18432\n"
"v_cvt_pk_bf16_f32 v173, v142, v143\n"
"v_mfma_f32_16x16x32_bf16 v[128:131], v[40:43], v[68:71], v[128:131]\n"
"v_exp_f32 v144, v144\n"
"v_mfma_f32_16x16x32_bf16 v[132:135], v[40:43], v[100:103], v[132:135]\n"
"buffer_load_dwordx4 %[dmakv], s[40:43], s62 offen offset:0 nt lds\n"
"s_waitcnt lgkmcnt(5)\n"
"v_mfma_f32_16x16x32_bf16 v[124:127], v[44:47], a[232:235], v[124:127]\n"
"ds_read_b128 v[40:43], v10 offset:18944\n"
"s_add_u32 m0, s56, 4992\n"
"v_mfma_f32_16x16x32_bf16 v[128:131], v[44:47], v[72:75], v[128:131]\n"
"v_exp_f32 v145, v145\n"
"v_mfma_f32_16x16x32_bf16 v[132:135], v[44:47], v[104:107], v[132:135]\n"
"v_exp_f32 v146, v146\n"
"v_mfma_f32_16x16x32_bf16 v[124:127], v[48:51], a[236:239], v[124:127]\n"
"ds_read_b128 v[44:47], v10 offset:19456\n"
"v_cvt_pk_bf16_f32 v180, v144, v145\n"
"v_mfma_f32_16x16x32_bf16 v[128:131], v[48:51], v[76:79], v[128:131]\n"
"v_exp_f32 v147, v147\n"
"v_mfma_f32_16x16x32_bf16 v[132:135], v[48:51], v[108:111], v[132:135]\n"
"v_cvt_pk_bf16_f32 v181, v146, v147\n"
"s_waitcnt lgkmcnt(5)\n"
"v_mfma_f32_16x16x32_bf16 v[136:139], v[20:23], a[208:211], v[52:55]\n"
"ds_read_b128 v[48:51], v10 offset:19968\n"
"buffer_load_dwordx4 %[dmakv], s[40:43], s62 offen offset:128 nt lds\n"
"v_mfma_f32_16x16x32_bf16 v[140:143], v[20:23], a[240:243], v[56:59]\n"
"v_exp_f32 v148, v148\n"
"v_mfma_f32_16x16x32_bf16 v[144:147], v[20:23], v[80:83], v[60:63]\n"
"v_exp_f32 v149, v149\n"
"v_mfma_f32_16x16x32_bf16 v[136:139], v[24:27], a[212:215], v[136:139]\n"
"ds_read_b128 v[20:23], v10 offset:24576\n"
"v_cvt_pk_bf16_f32 v166, v148, v149\n"
"v_mfma_f32_16x16x32_bf16 v[140:143], v[24:27], a[244:247], v[140:143]\n"
"v_exp_f32 v150, v150\n"
"v_mfma_f32_16x16x32_bf16 v[144:147], v[24:27], v[84:87], v[144:147]\n"
"s_add_u32 m0, s56, 5888\n"
"s_waitcnt lgkmcnt(5)\n"
"v_mfma_f32_16x16x32_bf16 v[136:139], v[28:31], a[216:219], v[136:139]\n"
"ds_read_b128 v[24:27], v10 offset:25088\n"
"buffer_load_dwordx4 %[dmakv], s[40:43], s62 offen offset:256 nt lds\n"
"v_mfma_f32_16x16x32_bf16 v[140:143], v[28:31], a[248:251], v[140:143]\n"
"v_exp_f32 v151, v151\n"
"v_mfma_f32_16x16x32_bf16 v[144:147], v[28:31], v[88:91], v[144:147]\n"
"v_exp_f32 v152, v152\n"
"v_mfma_f32_16x16x32_bf16 v[136:139], v[32:35], a[220:223], v[136:139]\n"
"ds_read_b128 v[28:31], v10 offset:25600\n"
"v_cvt_pk_bf16_f32 v167, v150, v151\n"
"v_mfma_f32_16x16x32_bf16 v[140:143], v[32:35], a[252:255], v[140:143]\n"
"v_exp_f32 v153, v153\n"
"v_mfma_f32_16x16x32_bf16 v[144:147], v[32:35], v[92:95], v[144:147]\n"
"v_cvt_pk_bf16_f32 v174, v152, v153\n"
"s_waitcnt lgkmcnt(5)\n"
"v_mfma_f32_16x16x32_bf16 v[136:139], v[36:39], a[224:227], v[136:139]\n"
"ds_read_b128 v[32:35], v10 offset:26112\n"
"s_add_u32 m0, s56, 6784\n"
"v_mfma_f32_16x16x32_bf16 v[140:143], v[36:39], v[64:67], v[140:143]\n"
"v_exp_f32 v154, v154\n"
"v_mfma_f32_16x16x32_bf16 v[144:147], v[36:39], v[96:99], v[144:147]\n"
"v_exp_f32 v155, v155\n"
"v_mfma_f32_16x16x32_bf16 v[136:139], v[40:43], a[228:231], v[136:139]\n"
"ds_read_b128 v[36:39], v10 offset:26624\n"
"v_cvt_pk_bf16_f32 v175, v154, v155\n"
"v_mfma_f32_16x16x32_bf16 v[140:143], v[40:43], v[68:71], v[140:143]\n"
"v_exp_f32 v156, v156\n"
"v_mfma_f32_16x16x32_bf16 v[144:147], v[40:43], v[100:103], v[144:147]\n"
"buffer_load_dwordx4 %[dmakv], s[40:43], s62 offen offset:384 nt lds\n"
"s_waitcnt lgkmcnt(5)\n"
"v_mfma_f32_16x16x32_bf16 v[136:139], v[44:47], a[232:235], v[136:139]\n"
"ds_read_b128 v[40:43], v10 offset:27136\n"
"v_max3_f32 v13, v112, v113, v114\n"
"v_mfma_f32_16x16x32_bf16 v[140:143], v[44:47], v[72:75], v[140:143]\n"
"v_exp_f32 v157, v157\n"
"v_mfma_f32_16x16x32_bf16 v[144:147], v[44:47], v[104:107], v[144:147]\n"
"v_exp_f32 v158, v158\n"
"v_mfma_f32_16x16x32_bf16 v[136:139], v[48:51], a[236:239], v[136:139]\n"
"ds_read_b128 v[44:47], v10 offset:27648\n"
"v_cvt_pk_bf16_f32 v182, v156, v157\n"
"v_mfma_f32_16x16x32_bf16 v[140:143], v[48:51], v[76:79], v[140:143]\n"
"v_exp_f32 v159, v159\n"
"v_mfma_f32_16x16x32_bf16 v[144:147], v[48:51], v[108:111], v[144:147]\n"
"v_cvt_pk_bf16_f32 v183, v158, v159\n"
"s_waitcnt lgkmcnt(5)\n"
"v_mfma_f32_16x16x32_bf16 v[148:151], v[20:23], a[208:211], v[52:55]\n"
"ds_read_b128 v[48:51], v10 offset:28160\n"
"v_max3_f32 v14, v116, v117, v118\n"
"v_mfma_f32_16x16x32_bf16 v[152:155], v[20:23], a[240:243], v[56:59]\n"
"s_add_u32 s40, s40, 0x8000\n"
"s_addc_u32 s41, s41, 0\n"
"v_mfma_f32_16x16x32_bf16 v[156:159], v[20:23], v[80:83], v[60:63]\n"
"s_sub_u32 s42, s42, 0x8000\n"
"s_cselect_b32 s42, 0, s42\n"
"v_mfma_f32_16x16x32_bf16 v[148:151], v[24:27], a[212:215], v[148:151]\n"
"s_add_u32 s57, s57, 0x8000\n"
"s_cmp_eq_u32 s57, s61\n"
"s_cselect_b32 s57, s60, s57\n"
"v_mfma_f32_16x16x32_bf16 v[152:155], v[24:27], a[244:247], v[152:155]\n"
"v_add_u32 v11, s57, %[vl0]\n"
"v_add_u32 v12, s57, %[vl1]\n"
"v_mfma_f32_16x16x32_bf16 v[156:159], v[24:27], v[84:87], v[156:159]\n"
"s_add_u32 s56, s56, 0x8000\n"
"s_cmp_eq_u32 s56, s59\n"
"s_cselect_b32 s56, s58, s56\n"
"s_waitcnt lgkmcnt(4)\n"
"v_mfma_f32_16x16x32_bf16 v[148:151], v[28:31], a[216:219], v[148:151]\n"
"v_max3_f32 v15, v120, v121, v122\n"
"v_max_f32 v13, v13, v115\n"
"v_mfma_f32_16x16x32_bf16 v[152:155], v[28:31], a[248:251], v[152:155]\n"
"v_max_f32 v14, v14, v119\n"
"v_max_f32 v15, v15, v123\n"
"v_mfma_f32_16x16x32_bf16 v[156:159], v[28:31], v[88:91], v[156:159]\n"
"s_waitcnt vmcnt(24) lgkmcnt(0)\n"
"s_barrier\n"
"v_mfma_f32_16x16x32_bf16 v[148:151], v[32:35], a[220:223], v[148:151]\n"
"ds_read_b64_tr_b16 v[20:21], v11 offset:0\n"
"ds_read_b64_tr_b16 v[22:23], v11 offset:8192\n"
"v_mfma_f32_16x16x32_bf16 v[152:155], v[32:35], a[252:255], v[152:155]\n"
"ds_read_b64_tr_b16 v[24:25], v12 offset:0\n"
"ds_read_b64_tr_b16 v[26:27], v12 offset:8192\n"
"v_mfma_f32_16x16x32_bf16 v[156:159], v[32:35], v[92:95], v[156:159]\n"
"ds_read_b64_tr_b16 v[28:29], v11 offset:512\n"
"ds_read_b64_tr_b16 v[30:31], v11 offset:8704\n"
"v_mfma_f32_16x16x32_bf16 v[148:151], v[36:39], a[224:227], v[148:151]\n"
"ds_read_b64_tr_b16 v[32:33], v12 offset:512\n"
"ds_read_b64_tr_b16 v[34:35], v12 offset:8704\n"
"v_mfma_f32_16x16x32_bf16 v[152:155], v[36:39], v[64:67], v[152:155]\n"
"v_max3_f32 v13, v13, v124, v125\n"
"v_max3_f32 v14, v14, v128, v129\n"
"v_mfma_f32_16x16x32_bf16 v[156:159], v[36:39], v[96:99], v[156:159]\n"
"v_max3_f32 v15, v15, v132, v133\n"
"v_max3_f32 v13, v13, v126, v127\n"
"v_mfma_f32_16x16x32_bf16 v[148:151], v[40:43], a[228:231], v[148:151]\n"
"ds_read_b64_tr_b16 v[36:37], v11 offset:1024\n"
"ds_read_b64_tr_b16 v[38:39], v11 offset:9216\n"
"v_mfma_f32_16x16x32_bf16 v[152:155], v[40:43], v[68:71], v[152:155]\n"
"v_max3_f32 v14, v14, v130, v131\n"
"v_max3_f32 v15, v15, v134, v135\n"
"v_mfma_f32_16x16x32_bf16 v[156:159], v[40:43], v[100:103], v[156:159]\n"
"v_max3_f32 v13, v13, v136, v137\n"
"v_max3_f32 v14, v14, v140, v141\n"
"v_mfma_f32_16x16x32_bf16 v[148:151], v[44:47], a[232:235], v[148:151]\n"
"ds_read_b64_tr_b16 v[40:41], v12 offset:1024\n"
"ds_read_b64_tr_b16 v[42:43], v12 offset:9216\n"
"v_mfma_f32_16x16x32_bf16 v[152:155], v[44:47], v[72:75], v[152:155]\n"
"v_max3_f32 v15, v15, v144, v145\n"
"v_max3_f32 v13, v13, v138, v139\n"
"v_mfma_f32_16x16x32_bf16 v[156:159], v[44:47], v[104:107], v[156:159]\n"
"v_max3_f32 v14, v14, v142, v143\n"
"v_max3_f32 v15, v15, v146, v147\n"
"v_mfma_f32_16x16x32_bf16 v[148:151], v[48:51], a[236:239], v[148:151]\n"
"ds_read_b64_tr_b16 v[44:45], v11 offset:1536\n"
"ds_read_b64_tr_b16 v[46:47], v11 offset:9728\n"
"v_mfma_f32_16x16x32_bf16 v[152:155], v[48:51], v[76:79], v[152:155]\n"
"v_mfma_f32_16x16x32_bf16 v[156:159], v[48:51], v[108:111], v[156:159]\n"
"s_waitcnt lgkmcnt(10)\n"
"v_mfma_f32_16x16x32_bf16 a[0:3], v[20:23], v[160:163], 0\n"
"ds_read_b64_tr_b16 v[48:49], v12 offset:1536\n"
"ds_read_b64_tr_b16 v[50:51], v12 offset:9728\n"
"v_mfma_f32_16x16x32_bf16 a[64:67], v[20:23], v[168:171], 0\n"
"s_add_u32 m0, s56, 0\n"
"v_mfma_f32_16x16x32_bf16 a[128:131], v[20:23], v[176:179], 0\n"
"v_mfma_f32_16x16x32_bf16 a[4:7], v[24:27], v[160:163], 0\n"
"ds_read_b64_tr_b16 v[20:21], v11 offset:2048\n"
"ds_read_b64_tr_b16 v[22:23], v11 offset:10240\n"
"v_mfma_f32_16x16x32_bf16 a[68:71], v[24:27], v[168:171], 0\n"
"buffer_load_dwordx4 %[dmakv], s[44:47], 0 offen offset:0 nt lds\n"
"v_mfma_f32_16x16x32_bf16 a[132:135], v[24:27], v[176:179], 0\n"
"v_max3_f32 v13, v13, v148, v149\n"
"v_max3_f32 v14, v14, v152, v153\n"
"v_mfma_f32_16x16x32_bf16 a[192:195], a[204:207], v[160:163], 0\n"
"v_max3_f32 v15, v15, v156, v157\n"
"s_waitcnt lgkmcnt(10)\n"
"v_mfma_f32_16x16x32_bf16 a[8:11], v[28:31], v[160:163], 0\n"
"ds_read_b64_tr_b16 v[24:25], v12 offset:2048\n"
"ds_read_b64_tr_b16 v[26:27], v12 offset:10240\n"
"v_mfma_f32_16x16x32_bf16 a[72:75], v[28:31], v[168:171], 0\n"
"s_add_u32 m0, s56, 896\n"
"v_max3_f32 v13, v13, v150, v151\n"
"v_mfma_f32_16x16x32_bf16 a[136:139], v[28:31], v[176:179], 0\n"
"v_max3_f32 v14, v14, v154, v155\n"
"v_max3_f32 v15, v15, v158, v159\n"
"v_mfma_f32_16x16x32_bf16 a[196:199], a[204:207], v[168:171], 0\n"
"v_max3_f32 v16, v13, v14, v15\n"
"v_mfma_f32_16x16x32_bf16 a[12:15], v[32:35], v[160:163], 0\n"
"ds_read_b64_tr_b16 v[28:29], v11 offset:2560\n"
"ds_read_b64_tr_b16 v[30:31], v11 offset:10752\n"
"v_mfma_f32_16x16x32_bf16 a[76:79], v[32:35], v[168:171], 0\n"
"v_cmp_lt_f32 vcc, s73, v16\n"
"s_cbranch_vccnz L_rare_w0f\n"
"L_back_w0f:\n"
"v_mfma_f32_16x16x32_bf16 a[140:143], v[32:35], v[176:179], 0\n"
"v_exp_f32 v112, v112\n"
"v_mfma_f32_16x16x32_bf16 a[200:203], a[204:207], v[176:179], 0\n"
"buffer_load_dwordx4 %[dmakv], s[44:47], 0 offen offset:128 nt lds\n"
"s_waitcnt lgkmcnt(10)\n"
"v_mfma_f32_16x16x32_bf16 a[16:19], v[36:39], v[160:163], 0\n"
"ds_read_b64_tr_b16 v[32:33], v12 offset:2560\n"
"ds_read_b64_tr_b16 v[34:35], v12 offset:10752\n"
"v_mfma_f32_16x16x32_bf16 a[80:83], v[36:39], v[168:171], 0\n"
"v_exp_f32 v113, v113\n"
"v_mfma_f32_16x16x32_bf16 a[144:147], v[36:39], v[176:179], 0\n"
"v_exp_f32 v114, v114\n"
"v_mfma_f32_16x16x32_bf16 a[20:23], v[40:43], v[160:163], 0\n"
"ds_read_b64_tr_b16 v[36:37], v11 offset:3072\n"
"ds_read_b64_tr_b16 v[38:39], v11 offset:11264\n"
"v_mfma_f32_16x16x32_bf16 a[84:87], v[40:43], v[168:171], 0\n"
"v_exp_f32 v115, v115\n"
"v_mfma_f32_16x16x32_bf16 a[148:151], v[40:43], v[176:179], 0\n"
"s_add_u32 m0, s56, 1792\n"
"s_waitcnt lgkmcnt(10)\n"
"v_mfma_f32_16x16x32_bf16 a[24:27], v[44:47], v[160:163], 0\n"
"ds_read_b64_tr_b16 v[40:41], v12 offset:3072\n"
"ds_read_b64_tr_b16 v[42:43], v12 offset:11264\n"
"v_mfma_f32_16x16x32_bf16 a[88:91], v[44:47], v[168:171], 0\n"
"v_exp_f32 v116, v116\n"
"v_mfma_f32_16x16x32_bf16 a[152:155], v[44:47], v[176:179], 0\n"
"v_exp_f32 v117, v117\n"
"v_mfma_f32_16x16x32_bf16 a[28:31], v[48:51], v[160:163], 0\n"
"ds_read_b64_tr_b16 v[44:45], v11 offset:3584\n"
"ds_read_b64_tr_b16 v[46:47], v11 offset:11776\n"
"v_mfma_f32_16x16x32_bf16 a[92:95], v[48:51], v[168:171], 0\n"
"v_exp_f32 v118, v118\n"
"v_mfma_f32_16x16x32_bf16 a[156:159], v[48:51], v[176:179], 0\n"
"buffer_load_dwordx4 %[dmakv], s[44:47], 0 offen offset:256 nt lds\n"
"s_waitcnt lgkmcnt(10)\n"
"v_mfma_f32_16x16x32_bf16 a[32:35], v[20:23], v[160:163], 0\n"
"ds_read_b64_tr_b16 v[48:49], v12 offset:3584\n"
"ds_read_b64_tr_b16 v[50:51], v12 offset:11776\n"
"v_mfma_f32_16x16x32_bf16 a[96:99], v[20:23], v[168:171], 0\n"
"v_exp_f32 v119, v119\n"
"v_mfma_f32_16x16x32_bf16 a[160:163], v[20:23], v[176:179], 0\n"
"v_exp_f32 v120, v120\n"
"v_mfma_f32_16x16x32_bf16 a[36:39], v[24:27], v[160:163], 0\n"
"ds_read_b64_tr_b16 v[20:21], v11 offset:16384\n"
"ds_read_b64_tr_b16 v[22:23], v11 offset:24576\n"
"v_mfma_f32_16x16x32_bf16 a[100:103], v[24:27], v[168:171], 0\n"
"v_exp_f32 v121, v121\n"
"v_mfma_f32_16x16x32_bf16 a[164:167], v[24:27], v[176:179], 0\n"
"s_add_u32 m0, s56, 2688\n"
"s_waitcnt lgkmcnt(10)\n"
"v_mfma_f32_16x16x32_bf16 a[40:43], v[28:31], v[160:163], 0\n"
"ds_read_b64_tr_b16 v[24:25], v12 offset:16384\n"
"ds_read_b64_tr_b16 v[26:27], v12 offset:24576\n"
"v_mfma_f32_16x16x32_bf16 a[104:107], v[28:31], v[168:171], 0\n"
"v_exp_f32 v122, v122\n"
"v_mfma_f32_16x16x32_bf16 a[168:171], v[28:31], v[176:179], 0\n"
"v_exp_f32 v123, v123\n"
"v_mfma_f32_16x16x32_bf16 a[44:47], v[32:35], v[160:163], 0\n"
"ds_read_b64_tr_b16 v[28:29], v11 offset:16896\n"
"ds_read_b64_tr_b16 v[30:31], v11 offset:25088\n"
"v_mfma_f32_16x16x32_bf16 a[108:111], v[32:35], v[168:171], 0\n"
"buffer_load_dwordx4 %[dmakv], s[44:47], 0 offen offset:384 nt lds\n"
"s_add_u32 m0, s56, 4096\n"
"v_mfma_f32_16x16x32_bf16 a[172:175], v[32:35], v[176:179], 0\n"
"buffer_load_dwordx4 %[dmakv], s[44:47], s62 offen offset:0 nt lds\n"
"s_waitcnt lgkmcnt(10)\n"
"v_mfma_f32_16x16x32_bf16 a[48:51], v[36:39], v[160:163], 0\n"
"ds_read_b64_tr_b16 v[32:33], v12 offset:16896\n"
"ds_read_b64_tr_b16 v[34:35], v12 offset:25088\n"
"v_mfma_f32_16x16x32_bf16 a[112:115], v[36:39], v[168:171], 0\n"
"v_mfma_f32_16x16x32_bf16 a[176:179], v[36:39], v[176:179], 0\n"
"v_mfma_f32_16x16x32_bf16 a[52:55], v[40:43], v[160:163], 0\n"
"ds_read_b64_tr_b16 v[36:37], v11 offset:17408\n"
"ds_read_b64_tr_b16 v[38:39], v11 offset:25600\n"
"v_mfma_f32_16x16x32_bf16 a[116:119], v[40:43], v[168:171], 0\n"
"s_add_u32 m0, s56, 4992\n"
"v_mfma_f32_16x16x32_bf16 a[180:183], v[40:43], v[176:179], 0\n"
"s_waitcnt lgkmcnt(10)\n"
"v_mfma_f32_16x16x32_bf16 a[56:59], v[44:47], v[160:163], 0\n"
"ds_read_b64_tr_b16 v[40:41], v12 offset:17408\n"
"ds_read_b64_tr_b16 v[42:43], v12 offset:25600\n"
"v_mfma_f32_16x16x32_bf16 a[120:123], v[44:47], v[168:171], 0\n"
"buffer_load_dwordx4 %[dmakv], s[44:47], s62 offen offset:128 nt lds\n"
"v_mfma_f32_16x16x32_bf16 a[184:187], v[44:47], v[176:179], 0\n"
"v_mfma_f32_16x16x32_bf16 a[60:63], v[48:51], v[160:163], 0\n"
"ds_read_b64_tr_b16 v[44:45], v11 offset:17920\n"
"ds_read_b64_tr_b16 v[46:47], v11 offset:26112\n"
"v_mfma_f32_16x16x32_bf16 a[124:127], v[48:51], v[168:171], 0\n"
"v_mfma_f32_16x16x32_bf16 a[188:191], v[48:51], v[176:179], 0\n"
"s_waitcnt lgkmcnt(10)\n"
"v_mfma_f32_16x16x32_bf16 a[0:3], v[20:23], v[164:167], a[0:3]\n"
"ds_read_b64_tr_b16 v[48:49], v12 offset:17920\n"
"ds_read_b64_tr_b16 v[50:51], v12 offset:26112\n"
"v_mfma_f32_16x16x32_bf16 a[64:67], v[20:23], v[172:175], a[64:67]\n"
"s_add_u32 m0, s56, 5888\n"
"v_cvt_pk_bf16_f32 v160, v112, v113\n"
"v_mfma_f32_16x16x32_bf16 a[128:131], v[20:23], v[180:183], a[128:131]\n"
"v_cvt_pk_bf16_f32 v161, v114, v115\n"
"v_cvt_pk_bf16_f32 v168, v116, v117\n"
"v_mfma_f32_16x16x32_bf16 a[4:7], v[24:27], v[164:167], a[4:7]\n"
"ds_read_b64_tr_b16 v[20:21], v11 offset:18432\n"
"ds_read_b64_tr_b16 v[22:23], v11 offset:26624\n"
"v_mfma_f32_16x16x32_bf16 a[68:71], v[24:27], v[172:175], a[68:71]\n"
"buffer_load_dwordx4 %[dmakv], s[44:47], s62 offen offset:256 nt lds\n"
"v_cvt_pk_bf16_f32 v169, v118, v119\n"
"v_mfma_f32_16x16x32_bf16 a[132:135], v[24:27], v[180:183], a[132:135]\n"
"v_cvt_pk_bf16_f32 v176, v120, v121\n"
"v_cvt_pk_bf16_f32 v177, v122, v123\n"
"v_mfma_f32_16x16x32_bf16 a[192:195], a[204:207], v[164:167], a[192:195]\n"
"s_waitcnt lgkmcnt(10)\n"
"v_mfma_f32_16x16x32_bf16 a[8:11], v[28:31], v[164:167], a[8:11]\n"
"ds_read_b64_tr_b16 v[24:25], v12 offset:18432\n"
"ds_read_b64_tr_b16 v[26:27], v12 offset:26624\n"
"v_mfma_f32_16x16x32_bf16 a[72:75], v[28:31], v[172:175], a[72:75]\n"
"s_add_u32 m0, s56, 6784\n"
"v_mfma_f32_16x16x32_bf16 a[136:139], v[28:31], v[180:183], a[136:139]\n"
"v_mfma_f32_16x16x32_bf16 a[196:199], a[204:207], v[172:175], a[196:199]\n"
"v_mfma_f32_16x16x32_bf16 a[12:15], v[32:35], v[164:167], a[12:15]\n"
"ds_read_b64_tr_b16 v[28:29], v11 offset:18944\n"
"ds_read_b64_tr_b16 v[30:31], v11 offset:27136\n"
"v_mfma_f32_16x16x32_bf16 a[76:79], v[32:35], v[172:175], a[76:79]\n"
"buffer_load_dwordx4 %[dmakv], s[44:47], s62 offen offset:384 nt lds\n"
"v_mfma_f32_16x16x32_bf16 a[140:143], v[32:35], v[180:183], a[140:143]\n"
"v_mfma_f32_16x16x32_bf16 a[200:203], a[204:207], v[180:183], a[200:203]\n"
"s_waitcnt lgkmcnt(10)\n"
"v_mfma_f32_16x16x32_bf16 a[16:19], v[36:39], v[164:167], a[16:19]\n"
"ds_read_b64_tr_b16 v[32:33], v12 offset:18944\n"
"ds_read_b64_tr_b16 v[34:35], v12 offset:27136\n"
"v_mfma_f32_16x16x32_bf16 a[80:83], v[36:39], v[172:175], a[80:83]\n"
"v_mfma_f32_16x16x32_bf16 a[144:147], v[36:39], v[180:183], a[144:147]\n"
"s_add_u32 s44, s44, 0x8000\n"
"s_addc_u32 s45, s45, 0\n"
"v_mfma_f32_16x16x32_bf16 a[20:23], v[40:43], v[164:167], a[20:23]\n"
"ds_read_b64_tr_b16 v[36:37], v11 offset:19456\n"
"ds_read_b64_tr_b16 v[38:39], v11 offset:27648\n"
"v_mfma_f32_16x16x32_bf16 a[84:87], v[40:43], v[172:175], a[84:87]\n"
"v_mfma_f32_16x16x32_bf16 a[148:151], v[40:43], v[180:183], a[148:151]\n"
"s_waitcnt lgkmcnt(10)\n"
"v_mfma_f32_16x16x32_bf16 a[24:27], v[44:47], v[164:167], a[24:27]\n"
"ds_read_b64_tr_b16 v[40:41], v12 offset:19456\n"
"ds_read_b64_tr_b16 v[42:43], v12 offset:27648\n"
"v_mfma_f32_16x16x32_bf16 a[88:91], v[44:47], v[172:175], a[88:91]\n"
"s_sub_u32 s46, s46, 0x8000\n"
"s_cselect_b32 s46, 0, s46\n"
"v_mfma_f32_16x16x32_bf16 a[152:155], v[44:47], v[180:183], a[152:155]\n"
"v_mfma_f32_16x16x32_bf16 a[28:31], v[48:51], v[164:167], a[28:31]\n"
"ds_read_b64_tr_b16 v[44:45], v11 offset:19968\n"
"ds_read_b64_tr_b16 v[46:47], v11 offset:28160\n"
"v_mfma_f32_16x16x32_bf16 a[92:95], v[48:51], v[172:175], a[92:95]\n"
"s_add_u32 s56, s56, 0x8000\n"
"s_cmp_eq_u32 s56, s59\n"
"s_cselect_b32 s56, s58, s56\n"
"v_mfma_f32_16x16x32_bf16 a[156:159], v[48:51], v[180:183], a[156:159]\n"
"s_waitcnt lgkmcnt(10)\n"
"v_mfma_f32_16x16x32_bf16 a[32:35], v[20:23], v[164:167], a[32:35]\n"
"ds_read_b64_tr_b16 v[48:49], v12 offset:19968\n"
"ds_read_b64_tr_b16 v[50:51], v12 offset:28160\n"
"v_mfma_f32_16x16x32_bf16 a[96:99], v[20:23], v[172:175], a[96:99]\n"
"v_mfma_f32_16x16x32_bf16 a[160:163], v[20:23], v[180:183], a[160:163]\n"
"v_mfma_f32_16x16x32_bf16 a[36:39], v[24:27], v[164:167], a[36:39]\n"
"s_add_u32 s57, s57, 0x8000\n"
"s_cmp_eq_u32 s57, s61\n"
"s_cselect_b32 s57, s60, s57\n"
"v_mfma_f32_16x16x32_bf16 a[100:103], v[24:27], v[172:175], a[100:103]\n"
"v_add_u32 v10, s57, %[klane]\n"
"v_mfma_f32_16x16x32_bf16 a[164:167], v[24:27], v[180:183], a[164:167]\n"
"s_waitcnt lgkmcnt(8)\n"
"v_mfma_f32_16x16x32_bf16 a[40:43], v[28:31], v[164:167], a[40:43]\n"
"v_mfma_f32_16x16x32_bf16 a[104:107], v[28:31], v[172:175], a[104:107]\n"
"v_mfma_f32_16x16x32_bf16 a[168:171], v[28:31], v[180:183], a[168:171]\n"
"s_waitcnt vmcnt(24) lgkmcnt(0)\n"
"s_barrier\n"
"v_mfma_f32_16x16x32_bf16 a[44:47], v[32:35], v[164:167], a[44:47]\n"
"ds_read_b128 v[20:23], v10 offset:0\n"
"ds_read_b128 v[24:27], v10 offset:512\n"
"v_mfma_f32_16x16x32_bf16 a[108:111], v[32:35], v[172:175], a[108:111]\n"
"ds_read_b128 v[28:31], v10 offset:1024\n"
"v_mfma_f32_16x16x32_bf16 a[172:175], v[32:35], v[180:183], a[172:175]\n"
"v_mfma_f32_16x16x32_bf16 a[48:51], v[36:39], v[164:167], a[48:51]\n"
"ds_read_b128 v[32:35], v10 offset:1536\n"
"v_mfma_f32_16x16x32_bf16 a[112:115], v[36:39], v[172:175], a[112:115]\n"
"v_mfma_f32_16x16x32_bf16 a[176:179], v[36:39], v[180:183], a[176:179]\n"
"v_mfma_f32_16x16x32_bf16 a[52:55], v[40:43], v[164:167], a[52:55]\n"
"ds_read_b128 v[36:39], v10 offset:2048\n"
"v_mfma_f32_16x16x32_bf16 a[116:119], v[40:43], v[172:175], a[116:119]\n"
"v_mfma_f32_16x16x32_bf16 a[180:183], v[40:43], v[180:183], a[180:183]\n"
"v_mfma_f32_16x16x32_bf16 a[56:59], v[44:47], v[164:167], a[56:59]\n"
"ds_read_b128 v[40:43], v10 offset:2560\n"
"v_mfma_f32_16x16x32_bf16 a[120:123], v[44:47], v[172:175], a[120:123]\n"
"v_mfma_f32_16x16x32_bf16 a[184:187], v[44:47], v[180:183], a[184:187]\n"
"v_mfma_f32_16x16x32_bf16 a[60:63], v[48:51], v[164:167], a[60:63]\n"
"ds_read_b128 v[44:47], v10 offset:3072\n"
"v_mfma_f32_16x16x32_bf16 a[124:127], v[48:51], v[172:175], a[124:127]\n"
"v_mfma_f32_16x16x32_bf16 a[188:191], v[48:51], v[180:183], a[188:191]\n"
"s_cmp_lg_u32 s71, 0\n"
"s_cbranch_scc1 L_resc_w0f\n"
"L_rescback_w0f:\n"
"s_mov_b32 s70, 29\n"
"L_loop_w0:\n"
"s_waitcnt lgkmcnt(5)\n"
"v_mfma_f32_16x16x32_bf16 v[112:115], v[20:23], a[208:211], v[52:55]\n"
"ds_read_b128 v[48:51], v10 offset:3584\n"
"s_add_u32 m0, s56, 0\n"
"v_mfma_f32_16x16x32_bf16 v[116:119], v[20:23], a[240:243], v[56:59]\n"
"v_exp_f32 v124, v124\n"
"v_mfma_f32_16x16x32_bf16 v[120:123], v[20:23], v[80:83], v[60:63]\n"
"v_exp_f32 v125, v125\n"
"v_mfma_f32_16x16x32_bf16 v[112:115], v[24:27], a[212:215], v[112:115]\n"
"ds_read_b128 v[20:23], v10 offset:8192\n"
"v_cvt_pk_bf16_f32 v162, v124, v125\n"
"v_mfma_f32_16x16x32_bf16 v[116:119], v[24:27], a[244:247], v[116:119]\n"
"v_exp_f32 v126, v126\n"
"v_mfma_f32_16x16x32_bf16 v[120:123], v[24:27], v[84:87], v[120:123]\n"
"buffer_load_dwordx4 %[dmakv], s[40:43], 0 offen offset:0 nt lds\n"
"s_waitcnt lgkmcnt(5)\n"
"v_mfma_f32_16x16x32_bf16 v[112:115], v[28:31], a[216:219], v[112:115]\n"
"ds_read_b128 v[24:27], v10 offset:8704\n"
"v_mfma_f32_16x16x32_bf16 v[116:119], v[28:31], a[248:251], v[116:119]\n"
"v_exp_f32 v127, v127\n"
"v_mfma_f32_16x16x32_bf16 v[120:123], v[28:31], v[88:91], v[120:123]\n"
"v_exp_f32 v128, v128\n"
"v_mfma_f32_16x16x32_bf16 v[112:115], v[32:35], a[220:223], v[112:115]\n"
"ds_read_b128 v[28:31], v10 offset:9216\n"
"v_cvt_pk_bf16_f32 v163, v126, v127\n"
"v_mfma_f32_16x16x32_bf16 v[116:119], v[32:35], a[252:255], v[116:119]\n"
"v_exp_f32 v129, v129\n"
"v_mfma_f32_16x16x32_bf16 v[120:123], v[32:35], v[92:95], v[120:123]\n"
"v_cvt_pk_bf16_f32 v170, v128, v129\n"
"s_waitcnt lgkmcnt(5)\n"
"v_mfma_f32_16x16x32_bf16 v[112:115], v[36:39], a[224:227], v[112:115]\n"
"ds_read_b128 v[32:35], v10 offset:9728\n"
"s_add_u32 m0, s56, 896\n"
"v_mfma_f32_16x16x32_bf16 v[116:119], v[36:39], v[64:67], v[116:119]\n"
"v_exp_f32 v130, v130\n"
"v_mfma_f32_16x16x32_bf16 v[120:123], v[36:39], v[96:99], v[120:123]\n"
"v_exp_f32 v131, v131\n"
"v_mfma_f32_16x16x32_bf16 v[112:115], v[40:43], a[228:231], v[112:115]\n"
"ds_read_b128 v[36:39], v10 offset:10240\n"
"v_cvt_pk_bf16_f32 v171, v130, v131\n"
"v_mfma_f32_16x16x32_bf16 v[116:119], v[40:43], v[68:71], v[116:119]\n"
"v_exp_f32 v132, v132\n"
"v_mfma_f32_16x16x32_bf16 v[120:123], v[40:43], v[100:103], v[120:123]\n"
"buffer_load_dwordx4 %[dmakv], s[40:43], 0 offen offset:128 nt lds\n"
"s_waitcnt lgkmcnt(5)\n"
"v_mfma_f32_16x16x32_bf16 v[112:115], v[44:47], a[232:235], v[112:115]\n"
"ds_read_b128 v[40:43], v10 offset:10752\n"
"s_add_u32 m0, s56, 1792\n"
"v_mfma_f32_16x16x32_bf16 v[116:119], v[44:47], v[72:75], v[116:119]\n"
"v_exp_f32 v133, v133\n"
"v_mfma_f32_16x16x32_bf16 v[120:123], v[44:47], v[104:107], v[120:123]\n"
"v_exp_f32 v134, v134\n"
"v_mfma_f32_16x16x32_bf16 v[112:115], v[48:51], a[236:239], v[112:115]\n"
"ds_read_b128 v[44:47], v10 offset:11264\n"
"v_cvt_pk_bf16_f32 v178, v132, v133\n"
"v_mfma_f32_16x16x32_bf16 v[116:119], v[48:51], v[76:79], v[116:119]\n"
"v_exp_f32 v135, v135\n"
"v_mfma_f32_16x16x32_bf16 v[120:123], v[48:51], v[108:111], v[120:123]\n"
"v_cvt_pk_bf16_f32 v179, v134, v135\n"
"s_waitcnt lgkmcnt(5)\n"
"v_mfma_f32_16x16x32_bf16 v[124:127], v[20:23], a[208:211], v[52:55]\n"
"ds_read_b128 v[48:51], v10 offset:11776\n"
"buffer_load_dwordx4 %[dmakv], s[40:43], 0 offen offset:256 nt lds\n"
"v_mfma_f32_16x16x32_bf16 v[128:131], v[20:23], a[240:243], v[56:59]\n"
"v_exp_f32 v136, v136\n"
"v_mfma_f32_16x16x32_bf16 v[132:135], v[20:23], v[80:83], v[60:63]\n"
"v_exp_f32 v137, v137\n"
"v_mfma_f32_16x16x32_bf16 v[124:127], v[24:27], a[212:215], v[124:127]\n"
"ds_read_b128 v[20:23], v10 offset:16384\n"
"v_cvt_pk_bf16_f32 v164, v136, v137\n"
"v_mfma_f32_16x16x32_bf16 v[128:131], v[24:27], a[244:247], v[128:131]\n"
"v_exp_f32 v138, v138\n"
"v_mfma_f32_16x16x32_bf16 v[132:135], v[24:27], v[84:87], v[132:135]\n"
"s_add_u32 m0, s56, 2688\n"
"s_waitcnt lgkmcnt(5)\n"
"v_mfma_f32_16x16x32_bf16 v[124:127], v[28:31], a[216:219], v[124:127]\n"
"ds_read_b128 v[24:27], v10 offset:16896\n"
"buffer_load_dwordx4 %[dmakv], s[40:43], 0 offen offset:384 nt lds\n"
"v_mfma_f32_16x16x32_bf16 v[128:131], v[28:31], a[248:251], v[128:131]\n"
"v_exp_f32 v139, v139\n"
"v_mfma_f32_16x16x32_bf16 v[132:135], v[28:31], v[88:91], v[132:135]\n"
"v_exp_f32 v140, v140\n"
"v_mfma_f32_16x16x32_bf16 v[124:127], v[32:35], a[220:223], v[124:127]\n"
"ds_read_b128 v[28:31], v10 offset:17408\n"
"v_cvt_pk_bf16_f32 v165, v138, v139\n"
"v_mfma_f32_16x16x32_bf16 v[128:131], v[32:35], a[252:255], v[128:131]\n"
"v_exp_f32 v141, v141\n"
"v_mfma_f32_16x16x32_bf16 v[132:135], v[32:35], v[92:95], v[132:135]\n"
"v_cvt_pk_bf16_f32 v172, v140, v141\n"
"s_waitcnt lgkmcnt(5)\n"
"v_mfma_f32_16x16x32_bf16 v[124:127], v[36:39], a[224:227], v[124:127]\n"
"ds_read_b128 v[32:35], v10 offset:17920\n"
"s_add_u32 m0, s56, 4096\n"
"v_mfma_f32_16x16x32_bf16 v[128:131], v[36:39], v[64:67], v[128:131]\n"
"v_exp_f32 v142, v142\n"
"v_mfma_f32_16x16x32_bf16 v[132:135], v[36:39], v[96:99], v[132:135]\n"
"v_exp_f32 v143, v143\n"
"v_mfma_f32_16x16x32_bf16 v[124:127], v[40:43], a[228:231], v[124:127]\n"
"ds_read_b128 v[36:39], v10 offset:18432\n"
"v_cvt_pk_bf16_f32 v173, v142, v143\n"
"v_mfma_f32_16x16x32_bf16 v[128:131], v[40:43], v[68:71], v[128:131]\n"
"v_exp_f32 v144, v144\n"
"v_mfma_f32_16x16x32_bf16 v[132:135], v[40:43], v[100:103], v[132:135]\n"
"buffer_load_dwordx4 %[dmakv], s[40:43], s62 offen offset:0 nt lds\n"
"s_waitcnt lgkmcnt(5)\n"
"v_mfma_f32_16x16x32_bf16 v[124:127], v[44:47], a[232:235], v[124:127]\n"
"ds_read_b128 v[40:43], v10 offset:18944\n"
"s_add_u32 m0, s56, 4992\n"
"v_mfma_f32_16x16x32_bf16 v[128:131], v[44:47], v[72:75], v[128:131]\n"
"v_exp_f32 v145, v145\n"
"v_mfma_f32_16x16x32_bf16 v[132:135], v[44:47], v[104:107], v[132:135]\n"
"v_exp_f32 v146, v146\n"
"v_mfma_f32_16x16x32_bf16 v[124:127], v[48:51], a[236:239], v[124:127]\n"
"ds_read_b128 v[44:47], v10 offset:19456\n"
"v_cvt_pk_bf16_f32 v180, v144, v145\n"
"v_mfma_f32_16x16x32_bf16 v[128:131], v[48:51], v[76:79], v[128:131]\n"
"v_exp_f32 v147, v147\n"
"v_mfma_f32_16x16x32_bf16 v[132:135], v[48:51], v[108:111], v[132:135]\n"
"v_cvt_pk_bf16_f32 v181, v146, v147\n"
"s_waitcnt lgkmcnt(5)\n"
"v_mfma_f32_16x16x32_bf16 v[136:139], v[20:23], a[208:211], v[52:55]\n"
"ds_read_b128 v[48:51], v10 offset:19968\n"
"buffer_load_dwordx4 %[dmakv], s[40:43], s62 offen offset:128 nt lds\n"
"v_mfma_f32_16x16x32_bf16 v[140:143], v[20:23], a[240:243], v[56:59]\n"
"v_exp_f32 v148, v148\n"
"v_mfma_f32_16x16x32_bf16 v[144:147], v[20:23], v[80:83], v[60:63]\n"
"v_exp_f32 v149, v149\n"
"v_mfma_f32_16x16x32_bf16 v[136:139], v[24:27], a[212:215], v[136:139]\n"
"ds_read_b128 v[20:23], v10 offset:24576\n"
"v_cvt_pk_bf16_f32 v166, v148, v149\n"
"v_mfma_f32_16x16x32_bf16 v[140:143], v[24:27], a[244:247], v[140:143]\n"
"v_exp_f32 v150, v150\n"
"v_mfma_f32_16x16x32_bf16 v[144:147], v[24:27], v[84:87], v[144:147]\n"
"s_add_u32 m0, s56, 5888\n"
"s_waitcnt lgkmcnt(5)\n"
"v_mfma_f32_16x16x32_bf16 v[136:139], v[28:31], a[216:219], v[136:139]\n"
"ds_read_b128 v[24:27], v10 offset:25088\n"
"buffer_load_dwordx4 %[dmakv], s[40:43], s62 offen offset:256 nt lds\n"
"v_mfma_f32_16x16x32_bf16 v[140:143], v[28:31], a[248:251], v[140:143]\n"
"v_exp_f32 v151, v151\n"
"v_mfma_f32_16x16x32_bf16 v[144:147], v[28:31], v[88:91], v[144:147]\n"
"v_exp_f32 v152, v152\n"
"v_mfma_f32_16x16x32_bf16 v[136:139], v[32:35], a[220:223], v[136:139]\n"
"ds_read_b128 v[28:31], v10 offset:25600\n"
"v_cvt_pk_bf16_f32 v167, v150, v151\n"
"v_mfma_f32_16x16x32_bf16 v[140:143], v[32:35], a[252:255], v[140:143]\n"
"v_exp_f32 v153, v153\n"
"v_mfma_f32_16x16x32_bf16 v[144:147], v[32:35], v[92:95], v[144:147]\n"
"v_cvt_pk_bf16_f32 v174, v152, v153\n"
"s_waitcnt lgkmcnt(5)\n"
"v_mfma_f32_16x16x32_bf16 v[136:139], v[36:39], a[224:227], v[136:139]\n"
"ds_read_b128 v[32:35], v10 offset:26112\n"
"s_add_u32 m0, s56, 6784\n"
"v_mfma_f32_16x16x32_bf16 v[140:143], v[36:39], v[64:67], v[140:143]\n"
"v_exp_f32 v154, v154\n"
"v_mfma_f32_16x16x32_bf16 v[144:147], v[36:39], v[96:99], v[144:147]\n"
"v_exp_f32 v155, v155\n"
"v_mfma_f32_16x16x32_bf16 v[136:139], v[40:43], a[228:231], v[136:139]\n"
"ds_read_b128 v[36:39], v10 offset:26624\n"
"v_cvt_pk_bf16_f32 v175, v154, v155\n"
"v_mfma_f32_16x16x32_bf16 v[140:143], v[40:43], v[68:71], v[140:143]\n"
"v_exp_f32 v156, v156\n"
"v_mfma_f32_16x16x32_bf16 v[144:147], v[40:43], v[100:103], v[144:147]\n"
"buffer_load_dwordx4 %[dmakv], s[40:43], s62 offen offset:384 nt lds\n"
"s_waitcnt lgkmcnt(5)\n"
"v_mfma_f32_16x16x32_bf16 v[136:139], v[44:47], a[232:235], v[136:139]\n"
"ds_read_b128 v[40:43], v10 offset:27136\n"
"v_max3_f32 v13, v112, v113, v114\n"
"v_mfma_f32_16x16x32_bf16 v[140:143], v[44:47], v[72:75], v[140:143]\n"
"v_exp_f32 v157, v157\n"
"v_mfma_f32_16x16x32_bf16 v[144:147], v[44:47], v[104:107], v[144:147]\n"
"v_exp_f32 v158, v158\n"
"v_mfma_f32_16x16x32_bf16 v[136:139], v[48:51], a[236:239], v[136:139]\n"
"ds_read_b128 v[44:47], v10 offset:27648\n"
"v_cvt_pk_bf16_f32 v182, v156, v157\n"
"v_mfma_f32_16x16x32_bf16 v[140:143], v[48:51], v[76:79], v[140:143]\n"
"v_exp_f32 v159, v159\n"
"v_mfma_f32_16x16x32_bf16 v[144:147], v[48:51], v[108:111], v[144:147]\n"
"v_cvt_pk_bf16_f32 v183, v158, v159\n"
"s_waitcnt lgkmcnt(5)\n"
"v_mfma_f32_16x16x32_bf16 v[148:151], v[20:23], a[208:211], v[52:55]\n"
"ds_read_b128 v[48:51], v10 offset:28160\n"
"v_max3_f32 v14, v116, v117, v118\n"
"v_mfma_f32_16x16x32_bf16 v[152:155], v[20:23], a[240:243], v[56:59]\n"
"s_add_u32 s40, s40, 0x8000\n"
"s_addc_u32 s41, s41, 0\n"
"v_mfma_f32_16x16x32_bf16 v[156:159], v[20:23], v[80:83], v[60:63]\n"
"s_sub_u32 s42, s42, 0x8000\n"
"s_cselect_b32 s42, 0, s42\n"
"v_mfma_f32_16x16x32_bf16 v[148:151], v[24:27], a[212:215], v[148:151]\n"
"s_add_u32 s57, s57, 0x8000\n"
"s_cmp_eq_u32 s57, s61\n"
"s_cselect_b32 s57, s60, s57\n"
"v_mfma_f32_16x16x32_bf16 v[152:155], v[24:27], a[244:247], v[152:155]\n"
"v_add_u32 v11, s57, %[vl0]\n"
"v_add_u32 v12, s57, %[vl1]\n"
"v_mfma_f32_16x16x32_bf16 v[156:159], v[24:27], v[84:87], v[156:159]\n"
"s_add_u32 s56, s56, 0x8000\n"
"s_cmp_eq_u32 s56, s59\n"
"s_cselect_b32 s56, s58, s56\n"
"s_waitcnt lgkmcnt(4)\n"
"v_mfma_f32_16x16x32_bf16 v[148:151], v[28:31], a[216:219], v[148:151]\n"
"v_max3_f32 v15, v120, v121, v122\n"
"v_max_f32 v13, v13, v115\n"
"v_mfma_f32_16x16x32_bf16 v[152:155], v[28:31], a[248:251], v[152:155]\n"
"v_max_f32 v14, v14, v119\n"
"v_max_f32 v15, v15, v123\n"
"v_mfma_f32_16x16x32_bf16 v[156:159], v[28:31], v[88:91], v[156:159]\n"
"s_waitcnt vmcnt(24) lgkmcnt(0)\n"
"s_barrier\n"
"v_mfma_f32_16x16x32_bf16 v[148:151], v[32:35], a[220:223], v[148:151]\n"
"ds_read_b64_tr_b16 v[20:21], v11 offset:0\n"
"ds_read_b64_tr_b16 v[22:23], v11 offset:8192\n"
"v_mfma_f32_16x16x32_bf16 v[152:155], v[32:35], a[252:255], v[152:155]\n"
"ds_read_b64_tr_b16 v[24:25], v12 offset:0\n"
"ds_read_b64_tr_b16 v[26:27], v12 offset:8192\n"
"v_mfma_f32_16x16x32_bf16 v[156:159], v[32:35], v[92:95], v[156:159]\n"
"ds_read_b64_tr_b16 v[28:29], v11 offset:512\n"
"ds_read_b64_tr_b16 v[30:31], v11 offset:8704\n"
"v_mfma_f32_16x16x32_bf16 v[148:151], v[36:39], a[224:227], v[148:151]\n"
"ds_read_b64_tr_b16 v[32:33], v12 offset:512\n"
"ds_read_b64_tr_b16 v[34:35], v12 offset:8704\n"
"v_mfma_f32_16x16x32_bf16 v[152:155], v[36:39], v[64:67], v[152:155]\n"
"v_max3_f32 v13, v13, v124, v125\n"
"v_max3_f32 v14, v14, v128, v129\n"
"v_mfma_f32_16x16x32_bf16 v[156:159], v[36:39], v[96:99], v[156:159]\n"
"v_max3_f32 v15, v15, v132, v133\n"
"v_max3_f32 v13, v13, v126, v127\n"
"v_mfma_f32_16x16x32_bf16 v[148:151], v[40:43], a[228:231], v[148:151]\n"
"ds_read_b64_tr_b16 v[36:37], v11 offset:1024\n"
"ds_read_b64_tr_b16 v[38:39], v11 offset:9216\n"
"v_mfma_f32_16x16x32_bf16 v[152:155], v[40:43], v[68:71], v[152:155]\n"
"v_max3_f32 v14, v14, v130, v131\n"
"v_max3_f32 v15, v15, v134, v135\n"
"v_mfma_f32_16x16x32_bf16 v[156:159], v[40:43], v[100:103], v[156:159]\n"
"v_max3_f32 v13, v13, v136, v137\n"
"v_max3_f32 v14, v14, v140, v141\n"
"v_mfma_f32_16x16x32_bf16 v[148:151], v[44:47], a[232:235], v[148:151]\n"
"ds_read_b64_tr_b16 v[40:41], v12 offset:1024\n"
"ds_read_b64_tr_b16 v[42:43], v12 offset:9216\n"
"v_mfma_f32_16x16x32_bf16 v[152:155], v[44:47], v[72:75], v[152:155]\n"
"v_max3_f32 v15, v15, v144, v145\n"
"v_max3_f32 v13, v13, v138, v139\n"
"v_mfma_f32_16x16x32_bf16 v[156:159], v[44:47], v[104:107], v[156:159]\n"
"v_max3_f32 v14, v14, v142, v143\n"
"v_max3_f32 v15, v15, v146, v147\n"
"v_mfma_f32_16x16x32_bf16 v[148:151], v[48:51], a[236:239], v[148:151]\n"
"ds_read_b64_tr_b16 v[44:45], v11 offset:1536\n"
"ds_read_b64_tr_b16 v[46:47], v11 offset:9728\n"
"v_mfma_f32_16x16x32_bf16 v[152:155], v[48:51], v[76:79], v[152:155]\n"
"v_mfma_f32_16x16x32_bf16 v[156:159], v[48:51], v[108:111], v[156:159]\n"
"s_waitcnt lgkmcnt(10)\n"
"v_mfma_f32_16x16x32_bf16 a[0:3], v[20:23], v[160:163], a[0:3]\n"
"ds_read_b64_tr_b16 v[48:49], v12 offset:1536\n"
"ds_read_b64_tr_b16 v[50:51], v12 offset:9728\n"
"v_mfma_f32_16x16x32_bf16 a[64:67], v[20:23], v[168:171], a[64:67]\n"
"s_add_u32 m0, s56, 0\n"
"v_mfma_f32_16x16x32_bf16 a[128:131], v[20:23], v[176:179], a[128:131]\n"
"v_mfma_f32_16x16x32_bf16 a[4:7], v[24:27], v[160:163], a[4:7]\n"
"ds_read_b64_tr_b16 v[20:21], v11 offset:2048\n"
"ds_read_b64_tr_b16 v[22:23], v11 offset:10240\n"
"v_mfma_f32_16x16x32_bf16 a[68:71], v[24:27], v[168:171], a[68:71]\n"
"buffer_load_dwordx4 %[dmakv], s[44:47], 0 offen offset:0 nt lds\n"
"v_mfma_f32_16x16x32_bf16 a[132:135], v[24:27], v[176:179], a[132:135]\n"
"v_max3_f32 v13, v13, v148, v149\n"
"v_max3_f32 v14, v14, v152, v153\n"
"v_mfma_f32_16x16x32_bf16 a[192:195], a[204:207], v[160:163], a[192:195]\n"
"v_max3_f32 v15, v15, v156, v157\n"
"s_waitcnt lgkmcnt(10)\n"
"v_mfma_f32_16x16x32_bf16 a[8:11], v[28:31], v[160:163], a[8:11]\n"
"ds_read_b64_tr_b16 v[24:25], v12 offset:2048\n"
"ds_read_b64_tr_b16 v[26:27], v12 offset:10240\n"
"v_mfma_f32_16x16x32_bf16 a[72:75], v[28:31], v[168:171], a[72:75]\n"
"s_add_u32 m0, s56, 896\n"
"v_max3_f32 v13, v13, v150, v151\n"
"v_mfma_f32_16x16x32_bf16 a[136:139], v[28:31], v[176:179], a[136:139]\n"
"v_max3_f32 v14, v14, v154, v155\n"
"v_max3_f32 v15, v15, v158, v159\n"
"v_mfma_f32_16x16x32_bf16 a[196:199], a[204:207], v[168:171], a[196:199]\n"
"v_max3_f32 v16, v13, v14, v15\n"
"v_mfma_f32_16x16x32_bf16 a[12:15], v[32:35], v[160:163], a[12:15]\n"
"ds_read_b64_tr_b16 v[28:29], v11 offset:2560\n"
"ds_read_b64_tr_b16 v[30:31], v11 offset:10752\n"
"v_mfma_f32_16x16x32_bf16 a[76:79], v[32:35], v[168:171], a[76:79]\n"
"v_cmp_lt_f32 vcc, s73, v16\n"
"s_cbranch_vccnz L_rare_w0l\n"
"L_back_w0l:\n"
"v_mfma_f32_16x16x32_bf16 a[140:143], v[32:35], v[176:179], a[140:143]\n"
"v_exp_f32 v112, v112\n"
"v_mfma_f32_16x16x32_bf16 a[200:203], a[204:207], v[176:179], a[200:203]\n"
"buffer_load_dwordx4 %[dmakv], s[44:47], 0 offen offset:128 nt lds\n"
"s_waitcnt lgkmcnt(10)\n"
"v_mfma_f32_16x16x32_bf16 a[16:19], v[36:39], v[160:163], a[16:19]\n"
"ds_read_b64_tr_b16 v[32:33], v12 offset:2560\n"
"ds_read_b64_tr_b16 v[34:35], v12 offset:10752\n"
"v_mfma_f32_16x16x32_bf16 a[80:83], v[36:39], v[168:171], a[80:83]\n"
"v_exp_f32 v113, v113\n"
"v_mfma_f32_16x16x32_bf16 a[144:147], v[36:39], v[176:179], a[144:147]\n"
"v_exp_f32 v114, v114\n"
"v_mfma_f32_16x16x32_bf16 a[20:23], v[40:43], v[160:163], a[20:23]\n"
"ds_read_b64_tr_b16 v[36:37], v11 offset:3072\n"
"ds_read_b64_tr_b16 v[38:39], v11 offset:11264\n"
"v_mfma_f32_16x16x32_bf16 a[84:87], v[40:43], v[168:171], a[84:87]\n"
"v_exp_f32 v115, v115\n"
"v_mfma_f32_16x16x32_bf16 a[148:151], v[40:43], v[176:179], a[148:151]\n"
"s_add_u32 m0, s56, 1792\n"
"s_waitcnt lgkmcnt(10)\n"
"v_mfma_f32_16x16x32_bf16 a[24:27], v[44:47], v[160:163], a[24:27]\n"
"ds_read_b64_tr_b16 v[40:41], v12 offset:3072\n"
"ds_read_b64_tr_b16 v[42:43], v12 offset:11264\n"
"v_mfma_f32_16x16x32_bf16 a[88:91], v[44:47], v[168:171], a[88:91]\n"
"v_exp_f32 v116, v116\n"
"v_mfma_f32_16x16x32_bf16 a[152:155], v[44:47], v[176:179], a[152:155]\n"
"v_exp_f32 v117, v117\n"
"v_mfma_f32_16x16x32_bf16 a[28:31], v[48:51], v[160:163], a[28:31]\n"
"ds_read_b64_tr_b16 v[44:45], v11 offset:3584\n"
"ds_read_b64_tr_b16 v[46:47], v11 offset:11776\n"
"v_mfma_f32_16x16x32_bf16 a[92:95], v[48:51], v[168:171], a[92:95]\n"
"v_exp_f32 v118, v118\n"
"v_mfma_f32_16x16x32_bf16 a[156:159], v[48:51], v[176:179], a[156:159]\n"
"buffer_load_dwordx4 %[dmakv], s[44:47], 0 offen offset:256 nt lds\n"
"s_waitcnt lgkmcnt(10)\n"
"v_mfma_f32_16x16x32_bf16 a[32:35], v[20:23], v[160:163], a[32:35]\n"
"ds_read_b64_tr_b16 v[48:49], v12 offset:3584\n"
"ds_read_b64_tr_b16 v[50:51], v12 offset:11776\n"
"v_mfma_f32_16x16x32_bf16 a[96:99], v[20:23], v[168:171], a[96:99]\n"
"v_exp_f32 v119, v119\n"
"v_mfma_f32_16x16x32_bf16 a[160:163], v[20:23], v[176:179], a[160:163]\n"
"v_exp_f32 v120, v120\n"
"v_mfma_f32_16x16x32_bf16 a[36:39], v[24:27], v[160:163], a[36:39]\n"
"ds_read_b64_tr_b16 v[20:21], v11 offset:16384\n"
"ds_read_b64_tr_b16 v[22:23], v11 offset:24576\n"
"v_mfma_f32_16x16x32_bf16 a[100:103], v[24:27], v[168:171], a[100:103]\n"
"v_exp_f32 v121, v121\n"
"v_mfma_f32_16x16x32_bf16 a[164:167], v[24:27], v[176:179], a[164:167]\n"
"s_add_u32 m0, s56, 2688\n"
"s_waitcnt lgkmcnt(10)\n"
"v_mfma_f32_16x16x32_bf16 a[40:43], v[28:31], v[160:163], a[40:43]\n"
"ds_read_b64_tr_b16 v[24:25], v12 offset:16384\n"
"ds_read_b64_tr_b16 v[26:27], v12 offset:24576\n"
"v_mfma_f32_16x16x32_bf16 a[104:107], v[28:31], v[168:171], a[104:107]\n"
"v_exp_f32 v122, v122\n"
"v_mfma_f32_16x16x32_bf16 a[168:171], v[28:31], v[176:179], a[168:171]\n"
"v_exp_f32 v123, v123\n"
"v_mfma_f32_16x16x32_bf16 a[44:47], v[32:35], v[160:163], a[44:47]\n"
"ds_read_b64_tr_b16 v[28:29], v11 offset:16896\n"
"ds_read_b64_tr_b16 v[30:31], v11 offset:25088\n"
"v_mfma_f32_16x16x32_bf16 a[108:111], v[32:35], v[168:171], a[108:111]\n"
"buffer_load_dwordx4 %[dmakv], s[44:47], 0 offen offset:384 nt lds\n"
"s_add_u32 m0, s56, 4096\n"
"v_mfma_f32_16x16x32_bf16 a[172:175], v[32:35], v[176:179], a[172:175]\n"
"buffer_load_dwordx4 %[dmakv], s[44:47], s62 offen offset:0 nt lds\n"
"s_waitcnt lgkmcnt(10)\n"
"v_mfma_f32_16x16x32_bf16 a[48:51], v[36:39], v[160:163], a[48:51]\n"
"ds_read_b64_tr_b16 v[32:33], v12 offset:16896\n"
"ds_read_b64_tr_b16 v[34:35], v12 offset:25088\n"
"v_mfma_f32_16x16x32_bf16 a[112:115], v[36:39], v[168:171], a[112:115]\n"
"v_mfma_f32_16x16x32_bf16 a[176:179], v[36:39], v[176:179], a[176:179]\n"
"v_mfma_f32_16x16x32_bf16 a[52:55], v[40:43], v[160:163], a[52:55]\n"
"ds_read_b64_tr_b16 v[36:37], v11 offset:17408\n"
"ds_read_b64_tr_b16 v[38:39], v11 offset:25600\n"
"v_mfma_f32_16x16x32_bf16 a[116:119], v[40:43], v[168:171], a[116:119]\n"
"s_add_u32 m0, s56, 4992\n"
"v_mfma_f32_16x16x32_bf16 a[180:183], v[40:43], v[176:179], a[180:183]\n"
"s_waitcnt lgkmcnt(10)\n"
"v_mfma_f32_16x16x32_bf16 a[56:59], v[44:47], v[160:163], a[56:59]\n"
"ds_read_b64_tr_b16 v[40:41], v12 offset:17408\n"
"ds_read_b64_tr_b16 v[42:43], v12 offset:25600\n"
"v_mfma_f32_16x16x32_bf16 a[120:123], v[44:47], v[168:171], a[120:123]\n"
"buffer_load_dwordx4 %[dmakv], s[44:47], s62 offen offset:128 nt lds\n"
"v_mfma_f32_16x16x32_bf16 a[184:187], v[44:47], v[176:179], a[184:187]\n"
"v_mfma_f32_16x16x32_bf16 a[60:63], v[48:51], v[160:163], a[60:63]\n"
"ds_read_b64_tr_b16 v[44:45], v11 offset:17920\n"
"ds_read_b64_tr_b16 v[46:47], v11 offset:26112\n"
"v_mfma_f32_16x16x32_bf16 a[124:127], v[48:51], v[168:171], a[124:127]\n"
"v_mfma_f32_16x16x32_bf16 a[188:191], v[48:51], v[176:179], a[188:191]\n"
"s_waitcnt lgkmcnt(10)\n"
"v_mfma_f32_16x16x32_bf16 a[0:3], v[20:23], v[164:167], a[0:3]\n"
"ds_read_b64_tr_b16 v[48:49], v12 offset:17920\n"
"ds_read_b64_tr_b16 v[50:51], v12 offset:26112\n"
"v_mfma_f32_16x16x32_bf16 a[64:67], v[20:23], v[172:175], a[64:67]\n"
"s_add_u32 m0, s56, 5888\n"
"v_cvt_pk_bf16_f32 v160, v112, v113\n"
"v_mfma_f32_16x16x32_bf16 a[128:131], v[20:23], v[180:183], a[128:131]\n"
"v_cvt_pk_bf16_f32 v161, v114, v115\n"
"v_cvt_pk_bf16_f32 v168, v116, v117\n"
"v_mfma_f32_16x16x32_bf16 a[4:7], v[24:27], v[164:167], a[4:7]\n"
"ds_read_b64_tr_b16 v[20:21], v11 offset:18432\n"
"ds_read_b64_tr_b16 v[22:23], v11 offset:26624\n"
"v_mfma_f32_16x16x32_bf16 a[68:71], v[24:27], v[172:175], a[68:71]\n"
"buffer_load_dwordx4 %[dmakv], s[44:47], s62 offen offset:256 nt lds\n"
"v_cvt_pk_bf16_f32 v169, v118, v119\n"
"v_mfma_f32_16x16x32_bf16 a[132:135], v[24:27], v[180:183], a[132:135]\n"
"v_cvt_pk_bf16_f32 v176, v120, v121\n"
"v_cvt_pk_bf16_f32 v177, v122, v123\n"
"v_mfma_f32_16x16x32_bf16 a[192:195], a[204:207], v[164:167], a[192:195]\n"
"s_waitcnt lgkmcnt(10)\n"
"v_mfma_f32_16x16x32_bf16 a[8:11], v[28:31], v[164:167], a[8:11]\n"
"ds_read_b64_tr_b16 v[24:25], v12 offset:18432\n"
"ds_read_b64_tr_b16 v[26:27], v12 offset:26624\n"
"v_mfma_f32_16x16x32_bf16 a[72:75], v[28:31], v[172:175], a[72:75]\n"
"s_add_u32 m0, s56, 6784\n"
"v_mfma_f32_16x16x32_bf16 a[136:139], v[28:31], v[180:183], a[136:139]\n"
"v_mfma_f32_16x16x32_bf16 a[196:199], a[204:207], v[172:175], a[196:199]\n"
"v_mfma_f32_16x16x32_bf16 a[12:15], v[32:35], v[164:167], a[12:15]\n"
"ds_read_b64_tr_b16 v[28:29], v11 offset:18944\n"
"ds_read_b64_tr_b16 v[30:31], v11 offset:27136\n"
"v_mfma_f32_16x16x32_bf16 a[76:79], v[32:35], v[172:175], a[76:79]\n"
"buffer_load_dwordx4 %[dmakv], s[44:47], s62 offen offset:384 nt lds\n"
"v_mfma_f32_16x16x32_bf16 a[140:143], v[32:35], v[180:183], a[140:143]\n"
"v_mfma_f32_16x16x32_bf16 a[200:203], a[204:207], v[180:183], a[200:203]\n"
"s_waitcnt lgkmcnt(10)\n"
"v_mfma_f32_16x16x32_bf16 a[16:19], v[36:39], v[164:167], a[16:19]\n"
"ds_read_b64_tr_b16 v[32:33], v12 offset:18944\n"
"ds_read_b64_tr_b16 v[34:35], v12 offset:27136\n"
"v_mfma_f32_16x16x32_bf16 a[80:83], v[36:39], v[172:175], a[80:83]\n"
"v_mfma_f32_16x16x32_bf16 a[144:147], v[36:39], v[180:183], a[144:147]\n"
"s_add_u32 s44, s44, 0x8000\n"
"s_addc_u32 s45, s45, 0\n"
"v_mfma_f32_16x16x32_bf16 a[20:23], v[40:43], v[164:167], a[20:23]\n"
"ds_read_b64_tr_b16 v[36:37], v11 offset:19456\n"
"ds_read_b64_tr_b16 v[38:39], v11 offset:27648\n"
"v_mfma_f32_16x16x32_bf16 a[84:87], v[40:43], v[172:175], a[84:87]\n"
"v_mfma_f32_16x16x32_bf16 a[148:151], v[40:43], v[180:183], a[148:151]\n"
"s_waitcnt lgkmcnt(10)\n"
"v_mfma_f32_16x16x32_bf16 a[24:27], v[44:47], v[164:167], a[24:27]\n"
"ds_read_b64_tr_b16 v[40:41], v12 offset:19456\n"
"ds_read_b64_tr_b16 v[42:43], v12 offset:27648\n"
"v_mfma_f32_16x16x32_bf16 a[88:91], v[44:47], v[172:175], a[88:91]\n"
"s_sub_u32 s46, s46, 0x8000\n"
"s_cselect_b32 s46, 0, s46\n"
"v_mfma_f32_16x16x32_bf16 a[152:155], v[44:47], v[180:183], a[152:155]\n"
"v_mfma_f32_16x16x32_bf16 a[28:31], v[48:51], v[164:167], a[28:31]\n"
"ds_read_b64_tr_b16 v[44:45], v11 offset:19968\n"
"ds_read_b64_tr_b16 v[46:47], v11 offset:28160\n"
"v_mfma_f32_16x16x32_bf16 a[92:95], v[48:51], v[172:175], a[92:95]\n"
"s_add_u32 s56, s56, 0x8000\n"
"s_cmp_eq_u32 s56, s59\n"
"s_cselect_b32 s56, s58, s56\n"
"v_mfma_f32_16x16x32_bf16 a[156:159], v[48:51], v[180:183], a[156:159]\n"
"s_waitcnt lgkmcnt(10)\n"
"v_mfma_f32_16x16x32_bf16 a[32:35], v[20:23], v[164:167], a[32:35]\n"
"ds_read_b64_tr_b16 v[48:49], v12 offset:19968\n"
"ds_read_b64_tr_b16 v[50:51], v12 offset:28160\n"
"v_mfma_f32_16x16x32_bf16 a[96:99], v[20:23], v[172:175], a[96:99]\n"
"v_mfma_f32_16x16x32_bf16 a[160:163], v[20:23], v[180:183], a[160:163]\n"
"v_mfma_f32_16x16x32_bf16 a[36:39], v[24:27], v[164:167], a[36:39]\n"
"s_add_u32 s57, s57, 0x8000\n"
"s_cmp_eq_u32 s57, s61\n"
"s_cselect_b32 s57, s60, s57\n"
"v_mfma_f32_16x16x32_bf16 a[100:103], v[24:27], v[172:175], a[100:103]\n"
"v_add_u32 v10, s57, %[klane]\n"
"v_mfma_f32_16x16x32_bf16 a[164:167], v[24:27], v[180:183], a[164:167]\n"
"s_waitcnt lgkmcnt(8)\n"
"v_mfma_f32_16x16x32_bf16 a[40:43], v[28:31], v[164:167], a[40:43]\n"
"v_mfma_f32_16x16x32_bf16 a[104:107], v[28:31], v[172:175], a[104:107]\n"
"v_mfma_f32_16x16x32_bf16 a[168:171], v[28:31], v[180:183], a[168:171]\n"
"s_waitcnt vmcnt(24) lgkmcnt(0)\n"
"s_barrier\n"
"v_mfma_f32_16x16x32_bf16 a[44:47], v[32:35], v[164:167], a[44:47]\n"
"ds_read_b128 v[20:23], v10 offset:0\n"
"ds_read_b128 v[24:27], v10 offset:512\n"
"v_mfma_f32_16x16x32_bf16 a[108:111], v[32:35], v[172:175], a[108:111]\n"
"ds_read_b128 v[28:31], v10 offset:1024\n"
"v_mfma_f32_16x16x32_bf16 a[172:175], v[32:35], v[180:183], a[172:175]\n"
"v_mfma_f32_16x16x32_bf16 a[48:51], v[36:39], v[164:167], a[48:51]\n"
"ds_read_b128 v[32:35], v10 offset:1536\n"
"v_mfma_f32_16x16x32_bf16 a[112:115], v[36:39], v[172:175], a[112:115]\n"
"v_mfma_f32_16x16x32_bf16 a[176:179], v[36:39], v[180:183], a[176:179]\n"
"v_mfma_f32_16x16x32_bf16 a[52:55], v[40:43], v[164:167], a[52:55]\n"
"ds_read_b128 v[36:39], v10 offset:2048\n"
"v_mfma_f32_16x16x32_bf16 a[116:119], v[40:43], v[172:175], a[116:119]\n"
"v_mfma_f32_16x16x32_bf16 a[180:183], v[40:43], v[180:183], a[180:183]\n"
"v_mfma_f32_16x16x32_bf16 a[56:59], v[44:47], v[164:167], a[56:59]\n"
"ds_read_b128 v[40:43], v10 offset:2560\n"
"v_mfma_f32_16x16x32_bf16 a[120:123], v[44:47], v[172:175], a[120:123]\n"
"v_mfma_f32_16x16x32_bf16 a[184:187], v[44:47], v[180:183], a[184:187]\n"
"v_mfma_f32_16x16x32_bf16 a[60:63], v[48:51], v[164:167], a[60:63]\n"
"ds_read_b128 v[44:47], v10 offset:3072\n"
"v_mfma_f32_16x16x32_bf16 a[124:127], v[48:51], v[172:175], a[124:127]\n"
"v_mfma_f32_16x16x32_bf16 a[188:191], v[48:51], v[180:183], a[188:191]\n"
"s_cmp_lg_u32 s71, 0\n"
"s_cbranch_scc1 L_resc_w0l\n"
"L_rescback_w0l:\n"
"s_sub_u32 s70, s70, 1\n"
"s_cmp_lg_u32 s70, 0\n"
"s_cbranch_scc1 L_loop_w0\n"
"s_getpc_b64 s[64:65]\n"
"L_pc_L_tail_w0:\n"
"s_add_u32 s64, s64, L_tail_w0-L_pc_L_tail_w0\n"
"s_addc_u32 s65, s65, 0\n"
"v_mbcnt_lo_u32_b32 v206, -1, 0\n"
"v_mbcnt_hi_u32_b32 v206, -1, v206\n"
"v_lshlrev_b32 v206, 7, v206\n"
"v_add_u32 v206, 0, v206\n"
"v_min_u32 v206, L_fin_w0-L_tail_w0-4, v206\n"
"global_load_dword v205, v206, s[64:65]\n"
"v_mbcnt_lo_u32_b32 v206, -1, 0\n"
"v_mbcnt_hi_u32_b32 v206, -1, v206\n"
"v_lshlrev_b32 v206, 7, v206\n"
"v_add_u32 v206, 8192, v206\n"
"v_min_u32 v206, L_fin_w0-L_tail_w0-4, v206\n"
"global_load_dword v205, v206, s[64:65]\n"
"v_mbcnt_lo_u32_b32 v206, -1, 0\n"
"v_mbcnt_hi_u32_b32 v206, -1, v206\n"
"v_lshlrev_b32 v206, 7, v206\n"
"v_add_u32 v206, 16384, v206\n"
"v_min_u32 v206, L_fin_w0-L_tail_w0-4, v206\n"
"global_load_dword v205, v206, s[64:65]\n"
"v_mbcnt_lo_u32_b32 v206, -1, 0\n"
"v_mbcnt_hi_u32_b32 v206, -1, v206\n"
"v_lshlrev_b32 v206, 7, v206\n"
"v_add_u32 v206, 24576, v206\n"
"v_min_u32 v206, L_fin_w0-L_tail_w0-4, v206\n"
"global_load_dword v205, v206, s[64:65]\n"
"L_tail_w0:\n"
"s_waitcnt lgkmcnt(5)\n"
"v_mfma_f32_16x16x32_bf16 v[112:115], v[20:23], a[208:211], v[52:55]\n"
"ds_read_b128 v[48:51], v10 offset:3584\n"
"s_add_u32 m0, s56, 0\n"
"v_mfma_f32_16x16x32_bf16 v[116:119], v[20:23], a[240:243], v[56:59]\n"
"v_exp_f32 v124, v124\n"
"v_mfma_f32_16x16x32_bf16 v[120:123], v[20:23], v[80:83], v[60:63]\n"
"v_exp_f32 v125, v125\n"
"v_mfma_f32_16x16x32_bf16 v[112:115], v[24:27], a[212:215], v[112:115]\n"
"ds_read_b128 v[20:23], v10 offset:8192\n"
"v_cvt_pk_bf16_f32 v162, v124, v125\n"
"v_mfma_f32_16x16x32_bf16 v[116:119], v[24:27], a[244:247], v[116:119]\n"
"v_exp_f32 v126, v126\n"
"v_mfma_f32_16x16x32_bf16 v[120:123], v[24:27], v[84:87], v[120:123]\n"
"buffer_load_dwordx4 v204, s[48:51], s83 offen lds\n"
"s_waitcnt lgkmcnt(5)\n"
"v_mfma_f32_16x16x32_bf16 v[112:115], v[28:31], a[216:219], v[112:115]\n"
"ds_read_b128 v[24:27], v10 offset:8704\n"
"s_add_u32 s83, s83, 7168\n"
"v_mfma_f32_16x16x32_bf16 v[116:119], v[28:31], a[248:251], v[116:119]\n"
"v_exp_f32 v127, v127\n"
"v_mfma_f32_16x16x32_bf16 v[120:123], v[28:31], v[88:91], v[120:123]\n"
"v_exp_f32 v128, v128\n"
"v_mfma_f32_16x16x32_bf16 v[112:115], v[32:35], a[220:223], v[112:115]\n"
"ds_read_b128 v[28:31], v10 offset:9216\n"
"v_cvt_pk_bf16_f32 v163, v126, v127\n"
"v_mfma_f32_16x16x32_bf16 v[116:119], v[32:35], a[252:255], v[116:119]\n"
"v_exp_f32 v129, v129\n"
"v_mfma_f32_16x16x32_bf16 v[120:123], v[32:35], v[92:95], v[120:123]\n"
"v_cvt_pk_bf16_f32 v170, v128, v129\n"
"s_waitcnt lgkmcnt(5)\n"
"v_mfma_f32_16x16x32_bf16 v[112:115], v[36:39], a[224:227], v[112:115]\n"
"ds_read_b128 v[32:35], v10 offset:9728\n"
"s_add_u32 m0, s56, 1024\n"
"v_mfma_f32_16x16x32_bf16 v[116:119], v[36:39], v[64:67], v[116:119]\n"
"v_exp_f32 v130, v130\n"
"v_mfma_f32_16x16x32_bf16 v[120:123], v[36:39], v[96:99], v[120:123]\n"
"v_exp_f32 v131, v131\n"
"v_mfma_f32_16x16x32_bf16 v[112:115], v[40:43], a[228:231], v[112:115]\n"
"ds_read_b128 v[36:39], v10 offset:10240\n"
"v_cvt_pk_bf16_f32 v171, v130, v131\n"
"v_mfma_f32_16x16x32_bf16 v[116:119], v[40:43], v[68:71], v[116:119]\n"
"v_exp_f32 v132, v132\n"
"v_mfma_f32_16x16x32_bf16 v[120:123], v[40:43], v[100:103], v[120:123]\n"
"buffer_load_dwordx4 v204, s[48:51], s83 offen lds\n"
"s_waitcnt lgkmcnt(5)\n"
"v_mfma_f32_16x16x32_bf16 v[112:115], v[44:47], a[232:235], v[112:115]\n"
"ds_read_b128 v[40:43], v10 offset:10752\n"
"s_add_u32 s83, s83, 7168\n"
"v_mfma_f32_16x16x32_bf16 v[116:119], v[44:47], v[72:75], v[116:119]\n"
"v_exp_f32 v133, v133\n"
"v_mfma_f32_16x16x32_bf16 v[120:123], v[44:47], v[104:107], v[120:123]\n"
"v_exp_f32 v134, v134\n"
"v_mfma_f32_16x16x32_bf16 v[112:115], v[48:51], a[236:239], v[112:115]\n"
"ds_read_b128 v[44:47], v10 offset:11264\n"
"v_cvt_pk_bf16_f32 v178, v132, v133\n"
"v_mfma_f32_16x16x32_bf16 v[116:119], v[48:51], v[76:79], v[116:119]\n"
"v_exp_f32 v135, v135\n"
"v_mfma_f32_16x16x32_bf16 v[120:123], v[48:51], v[108:111], v[120:123]\n"
"v_cvt_pk_bf16_f32 v179, v134, v135\n"
"s_waitcnt lgkmcnt(5)\n"
"v_mfma_f32_16x16x32_bf16 v[124:127], v[20:23], a[208:211], v[52:55]\n"
"ds_read_b128 v[48:51], v10 offset:11776\n"
"s_add_u32 m0, s56, 2048\n"
"v_mfma_f32_16x16x32_bf16 v[128:131], v[20:23], a[240:243], v[56:59]\n"
"v_exp_f32 v136, v136\n"
"v_mfma_f32_16x16x32_bf16 v[132:135], v[20:23], v[80:83], v[60:63]\n"
"v_exp_f32 v137, v137\n"
"v_mfma_f32_16x16x32_bf16 v[124:127], v[24:27], a[212:215], v[124:127]\n"
"ds_read_b128 v[20:23], v10 offset:16384\n"
"v_cvt_pk_bf16_f32 v164, v136, v137\n"
"v_mfma_f32_16x16x32_bf16 v[128:131], v[24:27], a[244:247], v[128:131]\n"
"v_exp_f32 v138, v138\n"
"v_mfma_f32_16x16x32_bf16 v[132:135], v[24:27], v[84:87], v[132:135]\n"
"buffer_load_dwordx4 v204, s[48:51], s83 offen lds\n"
"s_waitcnt lgkmcnt(5)\n"
"v_mfma_f32_16x16x32_bf16 v[124:127], v[28:31], a[216:219], v[124:127]\n"
"ds_read_b128 v[24:27], v10 offset:16896\n"
"s_add_u32 s83, s83, 7168\n"
"v_mfma_f32_16x16x32_bf16 v[128:131], v[28:31], a[248:251], v[128:131]\n"
"v_exp_f32 v139, v139\n"
"v_mfma_f32_16x16x32_bf16 v[132:135], v[28:31], v[88:91], v[132:135]\n"
"v_exp_f32 v140, v140\n"
"v_mfma_f32_16x16x32_bf16 v[124:127], v[32:35], a[220:223], v[124:127]\n"
"ds_read_b128 v[28:31], v10 offset:17408\n"
"v_cvt_pk_bf16_f32 v165, v138, v139\n"
"v_mfma_f32_16x16x32_bf16 v[128:131], v[32:35], a[252:255], v[128:131]\n"
"v_exp_f32 v141, v141\n"
"v_mfma_f32_16x16x32_bf16 v[132:135], v[32:35], v[92:95], v[132:135]\n"
"v_cvt_pk_bf16_f32 v172, v140, v141\n"
"s_waitcnt lgkmcnt(5)\n"
"v_mfma_f32_16x16x32_bf16 v[124:127], v[36:39], a[224:227], v[124:127]\n"
"ds_read_b128 v[32:35], v10 offset:17920\n"
"s_add_u32 m0, s56, 3072\n"
"v_mfma_f32_16x16x32_bf16 v[128:131], v[36:39], v[64:67], v[128:131]\n"
"v_exp_f32 v142, v142\n"
"v_mfma_f32_16x16x32_bf16 v[132:135], v[36:39], v[96:99], v[132:135]\n"
"v_exp_f32 v143, v143\n"
"v_mfma_f32_16x16x32_bf16 v[124:127], v[40:43], a[228:231], v[124:127]\n"
"ds_read_b128 v[36:39], v10 offset:18432\n"
"v_cvt_pk_bf16_f32 v173, v142, v143\n"
"v_mfma_f32_16x16x32_bf16 v[128:131], v[40:43], v[68:71], v[128:131]\n"
"v_exp_f32 v144, v144\n"
"v_mfma_f32_16x16x32_bf16 v[132:135], v[40:43], v[100:103], v[132:135]\n"
"buffer_load_dwordx4 v204, s[48:51], s83 offen lds\n"
"s_waitcnt lgkmcnt(5)\n"
"v_mfma_f32_16x16x32_bf16 v[124:127], v[44:47], a[232:235], v[124:127]\n"
"ds_read_b128 v[40:43], v10 offset:18944\n"
"s_add_u32 s83, s83, 7168\n"
"v_mfma_f32_16x16x32_bf16 v[128:131], v[44:47], v[72:75], v[128:131]\n"
"v_exp_f32 v145, v145\n"
"v_mfma_f32_16x16x32_bf16 v[132:135], v[44:47], v[104:107], v[132:135]\n"
"v_exp_f32 v146, v146\n"
"v_mfma_f32_16x16x32_bf16 v[124:127], v[48:51], a[236:239], v[124:127]\n"
"ds_read_b128 v[44:47], v10 offset:19456\n"
"v_cvt_pk_bf16_f32 v180, v144, v145\n"
"v_mfma_f32_16x16x32_bf16 v[128:131], v[48:51], v[76:79], v[128:131]\n"
"v_exp_f32 v147, v147\n"
"v_mfma_f32_16x16x32_bf16 v[132:135], v[48:51], v[108:111], v[132:135]\n"
"v_cvt_pk_bf16_f32 v181, v146, v147\n"
"s_waitcnt lgkmcnt(5)\n"
"v_mfma_f32_16x16x32_bf16 v[136:139], v[20:23], a[208:211], v[52:55]\n"
"ds_read_b128 v[48:51], v10 offset:19968\n"
"s_add_u32 m0, s56, 4096\n"
"v_mfma_f32_16x16x32_bf16 v[140:143], v[20:23], a[240:243], v[56:59]\n"
"v_exp_f32 v148, v148\n"
"v_mfma_f32_16x16x32_bf16 v[144:147], v[20:23], v[80:83], v[60:63]\n"
"v_exp_f32 v149, v149\n"
"v_mfma_f32_16x16x32_bf16 v[136:139], v[24:27], a[212:215], v[136:139]\n"
"ds_read_b128 v[20:23], v10 offset:24576\n"
"v_cvt_pk_bf16_f32 v166, v148, v149\n"
"v_mfma_f32_16x16x32_bf16 v[140:143], v[24:27], a[244:247], v[140:143]\n"
"v_exp_f32 v150, v150\n"
"v_mfma_f32_16x16x32_bf16 v[144:147], v[24:27], v[84:87], v[144:147]\n"
"buffer_load_dwordx4 v204, s[48:51], s83 offen lds\n"
"s_waitcnt lgkmcnt(5)\n"
"v_mfma_f32_16x16x32_bf16 v[136:139], v[28:31], a[216:219], v[136:139]\n"
"ds_read_b128 v[24:27], v10 offset:25088\n"
"s_add_u32 s83, s83, 7168\n"
"v_mfma_f32_16x16x32_bf16 v[140:143], v[28:31], a[248:251], v[140:143]\n"
"v_exp_f32 v151, v151\n"
"v_mfma_f32_16x16x32_bf16 v[144:147], v[28:31], v[88:91], v[144:147]\n"
"v_exp_f32 v152, v152\n"
"v_mfma_f32_16x16x32_bf16 v[136:139], v[32:35], a[220:223], v[136:139]\n"
"ds_read_b128 v[28:31], v10 offset:25600\n"
"v_cvt_pk_bf16_f32 v167, v150, v151\n"
"v_mfma_f32_16x16x32_bf16 v[140:143], v[32:35], a[252:255], v[140:143]\n"
"v_exp_f32 v153, v153\n"
"v_mfma_f32_16x16x32_bf16 v[144:147], v[32:35], v[92:95], v[144:147]\n"
"v_cvt_pk_bf16_f32 v174, v152, v153\n"
"s_waitcnt lgkmcnt(5)\n"
"v_mfma_f32_16x16x32_bf16 v[136:139], v[36:39], a[224:227], v[136:139]\n"
"ds_read_b128 v[32:35], v10 offset:26112\n"
"s_add_u32 m0, s56, 5120\n"
"v_mfma_f32_16x16x32_bf16 v[140:143], v[36:39], v[64:67], v[140:143]\n"
"v_exp_f32 v154, v154\n"
"v_mfma_f32_16x16x32_bf16 v[144:147], v[36:39], v[96:99], v[144:147]\n"
"v_exp_f32 v155, v155\n"
"v_mfma_f32_16x16x32_bf16 v[136:139], v[40:43], a[228:231], v[136:139]\n"
"ds_read_b128 v[36:39], v10 offset:26624\n"
"v_cvt_pk_bf16_f32 v175, v154, v155\n"
"v_mfma_f32_16x16x32_bf16 v[140:143], v[40:43], v[68:71], v[140:143]\n"
"v_exp_f32 v156, v156\n"
"v_mfma_f32_16x16x32_bf16 v[144:147], v[40:43], v[100:103], v[144:147]\n"
"buffer_load_dwordx4 v204, s[48:51], s83 offen lds\n"
"s_waitcnt lgkmcnt(5)\n"
"v_mfma_f32_16x16x32_bf16 v[136:139], v[44:47], a[232:235], v[136:139]\n"
"ds_read_b128 v[40:43], v10 offset:27136\n"
"s_add_u32 s83, s83, 7168\n"
"v_mfma_f32_16x16x32_bf16 v[140:143], v[44:47], v[72:75], v[140:143]\n"
"v_exp_f32 v157, v157\n"
"v_mfma_f32_16x16x32_bf16 v[144:147], v[44:47], v[104:107], v[144:147]\n"
"v_exp_f32 v158, v158\n"
"v_mfma_f32_16x16x32_bf16 v[136:139], v[48:51], a[236:239], v[136:139]\n"
"ds_read_b128 v[44:47], v10 offset:27648\n"
"v_cvt_pk_bf16_f32 v182, v156, v157\n"
"v_mfma_f32_16x16x32_bf16 v[140:143], v[48:51], v[76:79], v[140:143]\n"
"v_exp_f32 v159, v159\n"
"v_mfma_f32_16x16x32_bf16 v[144:147], v[48:51], v[108:111], v[144:147]\n"
"v_cvt_pk_bf16_f32 v183, v158, v159\n"
"s_waitcnt lgkmcnt(5)\n"
"v_mfma_f32_16x16x32_bf16 v[148:151], v[20:23], a[208:211], v[52:55]\n"
"ds_read_b128 v[48:51], v10 offset:28160\n"
"s_add_u32 m0, s56, 6144\n"
"v_mfma_f32_16x16x32_bf16 v[152:155], v[20:23], a[240:243], v[56:59]\n"
"buffer_load_dwordx4 v204, s[48:51], s83 offen lds\n"
"s_add_u32 s83, s83, 7168\n"
"v_mfma_f32_16x16x32_bf16 v[156:159], v[20:23], v[80:83], v[60:63]\n"
"s_add_u32 m0, s56, 7168\n"
"v_max3_f32 v13, v112, v113, v114\n"
"v_mfma_f32_16x16x32_bf16 v[148:151], v[24:27], a[212:215], v[148:151]\n"
"s_add_u32 s57, s57, 0x8000\n"
"s_cmp_eq_u32 s57, s61\n"
"s_cselect_b32 s57, s60, s57\n"
"v_mfma_f32_16x16x32_bf16 v[152:155], v[24:27], a[244:247], v[152:155]\n"
"v_add_u32 v11, s57, %[vl0]\n"
"v_add_u32 v12, s57, %[vl1]\n"
"v_mfma_f32_16x16x32_bf16 v[156:159], v[24:27], v[84:87], v[156:159]\n"
"buffer_load_dwordx4 v204, s[48:51], s83 offen lds\n"
"s_add_u32 s83, s83, 7168\n"
"s_mov_b32 s83, 0\n"
"s_add_u32 s48, s48, 128\n"
"s_addc_u32 s49, s49, 0\n"
"s_sub_u32 s50, s50, 128\n"
"s_add_u32 s56, s56, 0x8000\n"
"s_cmp_eq_u32 s56, s59\n"
"s_cselect_b32 s56, s58, s56\n"
"s_waitcnt lgkmcnt(4)\n"
"v_mfma_f32_16x16x32_bf16 v[148:151], v[28:31], a[216:219], v[148:151]\n"
"v_max3_f32 v14, v116, v117, v118\n"
"v_max3_f32 v15, v120, v121, v122\n"
"v_mfma_f32_16x16x32_bf16 v[152:155], v[28:31], a[248:251], v[152:155]\n"
"v_max_f32 v13, v13, v115\n"
"v_max_f32 v14, v14, v119\n"
"v_mfma_f32_16x16x32_bf16 v[156:159], v[28:31], v[88:91], v[156:159]\n"
"s_waitcnt vmcnt(28) lgkmcnt(0)\n"
"s_barrier\n"
"v_mfma_f32_16x16x32_bf16 v[148:151], v[32:35], a[220:223], v[148:151]\n"
"ds_read_b64_tr_b16 v[20:21], v11 offset:0\n"
"ds_read_b64_tr_b16 v[22:23], v11 offset:8192\n"
"v_mfma_f32_16x16x32_bf16 v[152:155], v[32:35], a[252:255], v[152:155]\n"
"ds_read_b64_tr_b16 v[24:25], v12 offset:0\n"
"ds_read_b64_tr_b16 v[26:27], v12 offset:8192\n"
"v_mfma_f32_16x16x32_bf16 v[156:159], v[32:35], v[92:95], v[156:159]\n"
"ds_read_b64_tr_b16 v[28:29], v11 offset:512\n"
"ds_read_b64_tr_b16 v[30:31], v11 offset:8704\n"
"v_mfma_f32_16x16x32_bf16 v[148:151], v[36:39], a[224:227], v[148:151]\n"
"ds_read_b64_tr_b16 v[32:33], v12 offset:512\n"
"ds_read_b64_tr_b16 v[34:35], v12 offset:8704\n"
"v_mfma_f32_16x16x32_bf16 v[152:155], v[36:39], v[64:67], v[152:155]\n"
"v_max_f32 v15, v15, v123\n"
"v_max3_f32 v13, v13, v124, v125\n"
"v_mfma_f32_16x16x32_bf16 v[156:159], v[36:39], v[96:99], v[156:159]\n"
"v_max3_f32 v14, v14, v128, v129\n"
"v_max3_f32 v15, v15, v132, v133\n"
"v_mfma_f32_16x16x32_bf16 v[148:151], v[40:43], a[228:231], v[148:151]\n"
"ds_read_b64_tr_b16 v[36:37], v11 offset:1024\n"
"ds_read_b64_tr_b16 v[38:39], v11 offset:9216\n"
"v_mfma_f32_16x16x32_bf16 v[152:155], v[40:43], v[68:71], v[152:155]\n"
"v_max3_f32 v13, v13, v126, v127\n"
"v_max3_f32 v14, v14, v130, v131\n"
"v_mfma_f32_16x16x32_bf16 v[156:159], v[40:43], v[100:103], v[156:159]\n"
"v_max3_f32 v15, v15, v134, v135\n"
"v_max3_f32 v13, v13, v136, v137\n"
"v_mfma_f32_16x16x32_bf16 v[148:151], v[44:47], a[232:235], v[148:151]\n"
"ds_read_b64_tr_b16 v[40:41], v12 offset:1024\n"
"ds_read_b64_tr_b16 v[42:43], v12 offset:9216\n"
"v_mfma_f32_16x16x32_bf16 v[152:155], v[44:47], v[72:75], v[152:155]\n"
"v_max3_f32 v14, v14, v140, v141\n"
"v_max3_f32 v15, v15, v144, v145\n"
"v_mfma_f32_16x16x32_bf16 v[156:159], v[44:47], v[104:107], v[156:159]\n"
"v_max3_f32 v13, v13, v138, v139\n"
"v_max3_f32 v14, v14, v142, v143\n"
"v_mfma_f32_16x16x32_bf16 v[148:151], v[48:51], a[236:239], v[148:151]\n"
"ds_read_b64_tr_b16 v[44:45], v11 offset:1536\n"
"ds_read_b64_tr_b16 v[46:47], v11 offset:9728\n"
"v_mfma_f32_16x16x32_bf16 v[152:155], v[48:51], v[76:79], v[152:155]\n"
"v_max3_f32 v15, v15, v146, v147\n"
"v_mfma_f32_16x16x32_bf16 v[156:159], v[48:51], v[108:111], v[156:159]\n"
"s_waitcnt lgkmcnt(10)\n"
"v_mfma_f32_16x16x32_bf16 a[0:3], v[20:23], v[160:163], a[0:3]\n"
"ds_read_b64_tr_b16 v[48:49], v12 offset:1536\n"
"ds_read_b64_tr_b16 v[50:51], v12 offset:9728\n"
"v_mfma_f32_16x16x32_bf16 a[64:67], v[20:23], v[168:171], a[64:67]\n"
"s_add_u32 m0, s56, 0\n"
"v_mfma_f32_16x16x32_bf16 a[128:131], v[20:23], v[176:179], a[128:131]\n"
"v_mfma_f32_16x16x32_bf16 a[4:7], v[24:27], v[160:163], a[4:7]\n"
"ds_read_b64_tr_b16 v[20:21], v11 offset:2048\n"
"ds_read_b64_tr_b16 v[22:23], v11 offset:10240\n"
"v_mfma_f32_16x16x32_bf16 a[68:71], v[24:27], v[168:171], a[68:71]\n"
"buffer_load_dwordx4 v204, s[48:51], s83 offen lds\n"
"v_mfma_f32_16x16x32_bf16 a[132:135], v[24:27], v[176:179], a[132:135]\n"
"v_max3_f32 v13, v13, v148, v149\n"
"v_max3_f32 v14, v14, v152, v153\n"
"v_mfma_f32_16x16x32_bf16 a[192:195], a[204:207], v[160:163], a[192:195]\n"
"s_add_u32 s83, s83, 7168\n"
"s_waitcnt lgkmcnt(10)\n"
"v_mfma_f32_16x16x32_bf16 a[8:11], v[28:31], v[160:163], a[8:11]\n"
"ds_read_b64_tr_b16 v[24:25], v12 offset:2048\n"
"ds_read_b64_tr_b16 v[26:27], v12 offset:10240\n"
"v_mfma_f32_16x16x32_bf16 a[72:75], v[28:31], v[168:171], a[72:75]\n"
"v_max3_f32 v15, v15, v156, v157\n"
"v_max3_f32 v13, v13, v150, v151\n"
"v_mfma_f32_16x16x32_bf16 a[136:139], v[28:31], v[176:179], a[136:139]\n"
"s_add_u32 m0, s56, 1024\n"
"v_max3_f32 v14, v14, v154, v155\n"
"v_mfma_f32_16x16x32_bf16 a[196:199], a[204:207], v[168:171], a[196:199]\n"
"v_max3_f32 v15, v15, v158, v159\n"
"v_max3_f32 v16, v13, v14, v15\n"
"v_mfma_f32_16x16x32_bf16 a[12:15], v[32:35], v[160:163], a[12:15]\n"
"ds_read_b64_tr_b16 v[28:29], v11 offset:2560\n"
"ds_read_b64_tr_b16 v[30:31], v11 offset:10752\n"
"v_mfma_f32_16x16x32_bf16 a[76:79], v[32:35], v[168:171], a[76:79]\n"
"v_cmp_lt_f32 vcc, s73, v16\n"
"s_cbranch_vccnz L_rare_w0e\n"
"L_back_w0e:\n"
"v_mfma_f32_16x16x32_bf16 a[140:143], v[32:35], v[176:179], a[140:143]\n"
"v_exp_f32 v112, v112\n"
"v_mfma_f32_16x16x32_bf16 a[200:203], a[204:207], v[176:179], a[200:203]\n"
"buffer_load_dwordx4 v204, s[48:51], s83 offen lds\n"
"s_waitcnt lgkmcnt(10)\n"
"v_mfma_f32_16x16x32_bf16 a[16:19], v[36:39], v[160:163], a[16:19]\n"
"ds_read_b64_tr_b16 v[32:33], v12 offset:2560\n"
"ds_read_b64_tr_b16 v[34:35], v12 offset:10752\n"
"v_mfma_f32_16x16x32_bf16 a[80:83], v[36:39], v[168:171], a[80:83]\n"
"v_exp_f32 v113, v113\n"
"v_mfma_f32_16x16x32_bf16 a[144:147], v[36:39], v[176:179], a[144:147]\n"
"v_exp_f32 v114, v114\n"
"v_mfma_f32_16x16x32_bf16 a[20:23], v[40:43], v[160:163], a[20:23]\n"
"ds_read_b64_tr_b16 v[36:37], v11 offset:3072\n"
"ds_read_b64_tr_b16 v[38:39], v11 offset:11264\n"
"v_mfma_f32_16x16x32_bf16 a[84:87], v[40:43], v[168:171], a[84:87]\n"
"v_exp_f32 v115, v115\n"
"v_mfma_f32_16x16x32_bf16 a[148:151], v[40:43], v[176:179], a[148:151]\n"
"s_add_u32 s83, s83, 7168\n"
"s_waitcnt lgkmcnt(10)\n"
"v_mfma_f32_16x16x32_bf16 a[24:27], v[44:47], v[160:163], a[24:27]\n"
"ds_read_b64_tr_b16 v[40:41], v12 offset:3072\n"
"ds_read_b64_tr_b16 v[42:43], v12 offset:11264\n"
"v_mfma_f32_16x16x32_bf16 a[88:91], v[44:47], v[168:171], a[88:91]\n"
"v_exp_f32 v116, v116\n"
"v_mfma_f32_16x16x32_bf16 a[152:155], v[44:47], v[176:179], a[152:155]\n"
"v_exp_f32 v117, v117\n"
"v_mfma_f32_16x16x32_bf16 a[28:31], v[48:51], v[160:163], a[28:31]\n"
"ds_read_b64_tr_b16 v[44:45], v11 offset:3584\n"
"ds_read_b64_tr_b16 v[46:47], v11 offset:11776\n"
"v_mfma_f32_16x16x32_bf16 a[92:95], v[48:51], v[168:171], a[92:95]\n"
"v_exp_f32 v118, v118\n"
"v_mfma_f32_16x16x32_bf16 a[156:159], v[48:51], v[176:179], a[156:159]\n"
"s_add_u32 m0, s56, 2048\n"
"s_waitcnt lgkmcnt(10)\n"
"v_mfma_f32_16x16x32_bf16 a[32:35], v[20:23], v[160:163], a[32:35]\n"
"ds_read_b64_tr_b16 v[48:49], v12 offset:3584\n"
"ds_read_b64_tr_b16 v[50:51], v12 offset:11776\n"
"v_mfma_f32_16x16x32_bf16 a[96:99], v[20:23], v[168:171], a[96:99]\n"
"v_exp_f32 v119, v119\n"
"v_mfma_f32_16x16x32_bf16 a[160:163], v[20:23], v[176:179], a[160:163]\n"
"v_exp_f32 v120, v120\n"
"v_mfma_f32_16x16x32_bf16 a[36:39], v[24:27], v[160:163], a[36:39]\n"
"ds_read_b64_tr_b16 v[20:21], v11 offset:16384\n"
"ds_read_b64_tr_b16 v[22:23], v11 offset:24576\n"
"v_mfma_f32_16x16x32_bf16 a[100:103], v[24:27], v[168:171], a[100:103]\n"
"v_exp_f32 v121, v121\n"
"v_mfma_f32_16x16x32_bf16 a[164:167], v[24:27], v[176:179], a[164:167]\n"
"buffer_load_dwordx4 v204, s[48:51], s83 offen lds\n"
"s_waitcnt lgkmcnt(10)\n"
"v_mfma_f32_16x16x32_bf16 a[40:43], v[28:31], v[160:163], a[40:43]\n"
"ds_read_b64_tr_b16 v[24:25], v12 offset:16384\n"
"ds_read_b64_tr_b16 v[26:27], v12 offset:24576\n"
"v_mfma_f32_16x16x32_bf16 a[104:107], v[28:31], v[168:171], a[104:107]\n"
"v_exp_f32 v122, v122\n"
"v_mfma_f32_16x16x32_bf16 a[168:171], v[28:31], v[176:179], a[168:171]\n"
"v_exp_f32 v123, v123\n"
"v_mfma_f32_16x16x32_bf16 a[44:47], v[32:35], v[160:163], a[44:47]\n"
"ds_read_b64_tr_b16 v[28:29], v11 offset:16896\n"
"ds_read_b64_tr_b16 v[30:31], v11 offset:25088\n"
"v_mfma_f32_16x16x32_bf16 a[108:111], v[32:35], v[168:171], a[108:111]\n"
"s_add_u32 s83, s83, 7168\n"
"s_add_u32 m0, s56, 3072\n"
"v_mfma_f32_16x16x32_bf16 a[172:175], v[32:35], v[176:179], a[172:175]\n"
"buffer_load_dwordx4 v204, s[48:51], s83 offen lds\n"
"s_waitcnt lgkmcnt(10)\n"
"v_mfma_f32_16x16x32_bf16 a[48:51], v[36:39], v[160:163], a[48:51]\n"
"ds_read_b64_tr_b16 v[32:33], v12 offset:16896\n"
"ds_read_b64_tr_b16 v[34:35], v12 offset:25088\n"
"v_mfma_f32_16x16x32_bf16 a[112:115], v[36:39], v[168:171], a[112:115]\n"
"s_add_u32 s83, s83, 7168\n"
"s_add_u32 m0, s56, 4096\n"
"v_mfma_f32_16x16x32_bf16 a[176:179], v[36:39], v[176:179], a[176:179]\n"
"buffer_load_dwordx4 v204, s[48:51], s83 offen lds\n"
"v_mfma_f32_16x16x32_bf16 a[52:55], v[40:43], v[160:163], a[52:55]\n"
"ds_read_b64_tr_b16 v[36:37], v11 offset:17408\n"
"ds_read_b64_tr_b16 v[38:39], v11 offset:25600\n"
"v_mfma_f32_16x16x32_bf16 a[116:119], v[40:43], v[168:171], a[116:119]\n"
"s_add_u32 s83, s83, 7168\n"
"v_mfma_f32_16x16x32_bf16 a[180:183], v[40:43], v[176:179], a[180:183]\n"
"s_waitcnt lgkmcnt(10)\n"
"v_mfma_f32_16x16x32_bf16 a[56:59], v[44:47], v[160:163], a[56:59]\n"
"ds_read_b64_tr_b16 v[40:41], v12 offset:17408\n"
"ds_read_b64_tr_b16 v[42:43], v12 offset:25600\n"
"v_mfma_f32_16x16x32_bf16 a[120:123], v[44:47], v[168:171], a[120:123]\n"
"s_add_u32 m0, s56, 5120\n"
"v_mfma_f32_16x16x32_bf16 a[184:187], v[44:47], v[176:179], a[184:187]\n"
"v_mfma_f32_16x16x32_bf16 a[60:63], v[48:51], v[160:163], a[60:63]\n"
"ds_read_b64_tr_b16 v[44:45], v11 offset:17920\n"
"ds_read_b64_tr_b16 v[46:47], v11 offset:26112\n"
"v_mfma_f32_16x16x32_bf16 a[124:127], v[48:51], v[168:171], a[124:127]\n"
"buffer_load_dwordx4 v204, s[48:51], s83 offen lds\n"
"v_mfma_f32_16x16x32_bf16 a[188:191], v[48:51], v[176:179], a[188:191]\n"
"s_waitcnt lgkmcnt(10)\n"
"v_mfma_f32_16x16x32_bf16 a[0:3], v[20:23], v[164:167], a[0:3]\n"
"ds_read_b64_tr_b16 v[48:49], v12 offset:17920\n"
"ds_read_b64_tr_b16 v[50:51], v12 offset:26112\n"
"v_mfma_f32_16x16x32_bf16 a[64:67], v[20:23], v[172:175], a[64:67]\n"
"s_add_u32 s83, s83, 7168\n"
"v_cvt_pk_bf16_f32 v160, v112, v113\n"
"v_mfma_f32_16x16x32_bf16 a[128:131], v[20:23], v[180:183], a[128:131]\n"
"v_cvt_pk_bf16_f32 v161, v114, v115\n"
"v_cvt_pk_bf16_f32 v168, v116, v117\n"
"v_mfma_f32_16x16x32_bf16 a[4:7], v[24:27], v[164:167], a[4:7]\n"
"ds_read_b64_tr_b16 v[20:21], v11 offset:18432\n"
"ds_read_b64_tr_b16 v[22:23], v11 offset:26624\n"
"v_mfma_f32_16x16x32_bf16 a[68:71], v[24:27], v[172:175], a[68:71]\n"
"s_add_u32 m0, s56, 6144\n"
"v_cvt_pk_bf16_f32 v169, v118, v119\n"
"v_mfma_f32_16x16x32_bf16 a[132:135], v[24:27], v[180:183], a[132:135]\n"
"v_cvt_pk_bf16_f32 v176, v120, v121\n"
"v_cvt_pk_bf16_f32 v177, v122, v123\n"
"v_mfma_f32_16x16x32_bf16 a[192:195], a[204:207], v[164:167], a[192:195]\n"
"buffer_load_dwordx4 v204, s[48:51], s83 offen lds\n"
"s_waitcnt lgkmcnt(10)\n"
"v_mfma_f32_16x16x32_bf16 a[8:11], v[28:31], v[164:167], a[8:11]\n"
"ds_read_b64_tr_b16 v[24:25], v12 offset:18432\n"
"ds_read_b64_tr_b16 v[26:27], v12 offset:26624\n"
"v_mfma_f32_16x16x32_bf16 a[72:75], v[28:31], v[172:175], a[72:75]\n"
"v_mfma_f32_16x16x32_bf16 a[136:139], v[28:31], v[180:183], a[136:139]\n"
"s_add_u32 s83, s83, 7168\n"
"v_mfma_f32_16x16x32_bf16 a[196:199], a[204:207], v[172:175], a[196:199]\n"
"v_mfma_f32_16x16x32_bf16 a[12:15], v[32:35], v[164:167], a[12:15]\n"
"ds_read_b64_tr_b16 v[28:29], v11 offset:18944\n"
"ds_read_b64_tr_b16 v[30:31], v11 offset:27136\n"
"v_mfma_f32_16x16x32_bf16 a[76:79], v[32:35], v[172:175], a[76:79]\n"
"s_add_u32 m0, s56, 7168\n"
"v_mfma_f32_16x16x32_bf16 a[140:143], v[32:35], v[180:183], a[140:143]\n"
"v_mfma_f32_16x16x32_bf16 a[200:203], a[204:207], v[180:183], a[200:203]\n"
"s_waitcnt lgkmcnt(10)\n"
"v_mfma_f32_16x16x32_bf16 a[16:19], v[36:39], v[164:167], a[16:19]\n"
"ds_read_b64_tr_b16 v[32:33], v12 offset:18944\n"
"ds_read_b64_tr_b16 v[34:35], v12 offset:27136\n"
"v_mfma_f32_16x16x32_bf16 a[80:83], v[36:39], v[172:175], a[80:83]\n"
"buffer_load_dwordx4 v204, s[48:51], s83 offen lds\n"
"v_mfma_f32_16x16x32_bf16 a[144:147], v[36:39], v[180:183], a[144:147]\n"
"v_mfma_f32_16x16x32_bf16 a[20:23], v[40:43], v[164:167], a[20:23]\n"
"ds_read_b64_tr_b16 v[36:37], v11 offset:19456\n"
"ds_read_b64_tr_b16 v[38:39], v11 offset:27648\n"
"v_mfma_f32_16x16x32_bf16 a[84:87], v[40:43], v[172:175], a[84:87]\n"
"s_add_u32 s83, s83, 7168\n"
"v_mfma_f32_16x16x32_bf16 a[148:151], v[40:43], v[180:183], a[148:151]\n"
"s_waitcnt lgkmcnt(10)\n"
"v_mfma_f32_16x16x32_bf16 a[24:27], v[44:47], v[164:167], a[24:27]\n"
"ds_read_b64_tr_b16 v[40:41], v12 offset:19456\n"
"ds_read_b64_tr_b16 v[42:43], v12 offset:27648\n"
"v_mfma_f32_16x16x32_bf16 a[88:91], v[44:47], v[172:175], a[88:91]\n"
"s_mov_b32 s83, 0\n"
"s_add_u32 s48, s48, 128\n"
"s_addc_u32 s49, s49, 0\n"
"v_mfma_f32_16x16x32_bf16 a[152:155], v[44:47], v[180:183], a[152:155]\n"
"v_mfma_f32_16x16x32_bf16 a[28:31], v[48:51], v[164:167], a[28:31]\n"
"ds_read_b64_tr_b16 v[44:45], v11 offset:19968\n"
"ds_read_b64_tr_b16 v[46:47], v11 offset:28160\n"
"v_mfma_f32_16x16x32_bf16 a[92:95], v[48:51], v[172:175], a[92:95]\n"
"s_sub_u32 s50, s50, 128\n"
"v_mfma_f32_16x16x32_bf16 a[156:159], v[48:51], v[180:183], a[156:159]\n"
"s_waitcnt lgkmcnt(10)\n"
"v_mfma_f32_16x16x32_bf16 a[32:35], v[20:23], v[164:167], a[32:35]\n"
"ds_read_b64_tr_b16 v[48:49], v12 offset:19968\n"
"ds_read_b64_tr_b16 v[50:51], v12 offset:28160\n"
"v_mfma_f32_16x16x32_bf16 a[96:99], v[20:23], v[172:175], a[96:99]\n"
"s_add_u32 s56, s56, 0x8000\n"
"s_cmp_eq_u32 s56, s59\n"
"s_cselect_b32 s56, s58, s56\n"
"v_mfma_f32_16x16x32_bf16 a[160:163], v[20:23], v[180:183], a[160:163]\n"
"v_mfma_f32_16x16x32_bf16 a[36:39], v[24:27], v[164:167], a[36:39]\n"
"s_add_u32 s57, s57, 0x8000\n"
"s_cmp_eq_u32 s57, s61\n"
"s_cselect_b32 s57, s60, s57\n"
"v_mfma_f32_16x16x32_bf16 a[100:103], v[24:27], v[172:175], a[100:103]\n"
"v_add_u32 v10, s57, %[klane]\n"
"v_mfma_f32_16x16x32_bf16 a[164:167], v[24:27], v[180:183], a[164:167]\n"
"s_waitcnt lgkmcnt(8)\n"
"v_mfma_f32_16x16x32_bf16 a[40:43], v[28:31], v[164:167], a[40:43]\n"
"v_mfma_f32_16x16x32_bf16 a[104:107], v[28:31], v[172:175], a[104:107]\n"
"v_mfma_f32_16x16x32_bf16 a[168:171], v[28:31], v[180:183], a[168:171]\n"
"s_waitcnt vmcnt(28) lgkmcnt(0)\n"
"s_barrier\n"
"v_mfma_f32_16x16x32_bf16 a[44:47], v[32:35], v[164:167], a[44:47]\n"
"ds_read_b128 v[20:23], v10 offset:0\n"
"ds_read_b128 v[24:27], v10 offset:512\n"
"v_mfma_f32_16x16x32_bf16 a[108:111], v[32:35], v[172:175], a[108:111]\n"
"ds_read_b128 v[28:31], v10 offset:1024\n"
"v_mfma_f32_16x16x32_bf16 a[172:175], v[32:35], v[180:183], a[172:175]\n"
"v_mfma_f32_16x16x32_bf16 a[48:51], v[36:39], v[164:167], a[48:51]\n"
"ds_read_b128 v[32:35], v10 offset:1536\n"
"v_mfma_f32_16x16x32_bf16 a[112:115], v[36:39], v[172:175], a[112:115]\n"
"v_mfma_f32_16x16x32_bf16 a[176:179], v[36:39], v[180:183], a[176:179]\n"
"v_mfma_f32_16x16x32_bf16 a[52:55], v[40:43], v[164:167], a[52:55]\n"
"ds_read_b128 v[36:39], v10 offset:2048\n"
"v_mfma_f32_16x16x32_bf16 a[116:119], v[40:43], v[172:175], a[116:119]\n"
"v_mfma_f32_16x16x32_bf16 a[180:183], v[40:43], v[180:183], a[180:183]\n"
"v_mfma_f32_16x16x32_bf16 a[56:59], v[44:47], v[164:167], a[56:59]\n"
"ds_read_b128 v[40:43], v10 offset:2560\n"
"v_mfma_f32_16x16x32_bf16 a[120:123], v[44:47], v[172:175], a[120:123]\n"
"v_mfma_f32_16x16x32_bf16 a[184:187], v[44:47], v[180:183], a[184:187]\n"
"v_mfma_f32_16x16x32_bf16 a[60:63], v[48:51], v[164:167], a[60:63]\n"
"ds_read_b128 v[44:47], v10 offset:3072\n"
"v_mfma_f32_16x16x32_bf16 a[124:127], v[48:51], v[172:175], a[124:127]\n"
"v_mfma_f32_16x16x32_bf16 a[188:191], v[48:51], v[180:183], a[188:191]\n"
"s_cmp_lg_u32 s71, 0\n"
"s_cbranch_scc1 L_resc_w0e\n"
"L_rescback_w0e:\n"
"s_add_u32 m0, s56, 0\n"
"s_nop 0\n"
"buffer_load_dwordx4 v204, s[48:51], s83 offen lds\n"
"s_add_u32 s83, s83, 7168\n"
"s_add_u32 m0, s56, 1024\n"
"s_nop 0\n"
"buffer_load_dwordx4 v204, s[48:51], s83 offen lds\n"
"s_add_u32 s83, s83, 7168\n"
"s_add_u32 m0, s56, 2048\n"
"s_nop 0\n"
"buffer_load_dwordx4 v204, s[48:51], s83 offen lds\n"
"s_add_u32 s83, s83, 7168\n"
"s_add_u32 m0, s56, 3072\n"
"s_nop 0\n"
"buffer_load_dwordx4 v204, s[48:51], s83 offen lds\n"
"s_add_u32 s83, s83, 7168\n"
"s_add_u32 m0, s56, 4096\n"
"s_nop 0\n"
"buffer_load_dwordx4 v204, s[48:51], s83 offen lds\n"
"s_add_u32 s83, s83, 7168\n"
"s_add_u32 m0, s56, 5120\n"
"s_nop 0\n"
"buffer_load_dwordx4 v204, s[48:51], s83 offen lds\n"
"s_add_u32 s83, s83, 7168\n"
"s_add_u32 m0, s56, 6144\n"
"s_nop 0\n"
"buffer_load_dwordx4 v204, s[48:51], s83 offen lds\n"
"s_add_u32 s83, s83, 7168\n"
"s_add_u32 m0, s56, 7168\n"
"s_nop 0\n"
"buffer_load_dwordx4 v204, s[48:51], s83 offen lds\n"
"s_add_u32 s83, s83, 7168\n"
"s_mov_b32 s83, 0\n"
"s_add_u32 s48, s48, 128\n"
"s_addc_u32 s49, s49, 0\n"
"s_sub_u32 s50, s50, 128\n"
"s_add_u32 s56, s56, 0x8000\n"
"s_cmp_eq_u32 s56, s59\n"
"s_cselect_b32 s56, s58, s56\n"
"v_exp_f32 v124, v124\n"
"v_exp_f32 v125, v125\n"
"v_exp_f32 v126, v126\n"
"v_exp_f32 v127, v127\n"
"v_exp_f32 v128, v128\n"
"v_exp_f32 v129, v129\n"
"v_exp_f32 v130, v130\n"
"v_exp_f32 v131, v131\n"
"v_exp_f32 v132, v132\n"
"v_exp_f32 v133, v133\n"
"v_exp_f32 v134, v134\n"
"v_exp_f32 v135, v135\n"
"v_exp_f32 v136, v136\n"
"v_exp_f32 v137, v137\n"
"v_exp_f32 v138, v138\n"
"v_exp_f32 v139, v139\n"
"v_exp_f32 v140, v140\n"
"v_exp_f32 v141, v141\n"
"v_exp_f32 v142, v142\n"
"v_exp_f32 v143, v143\n"
"v_exp_f32 v144, v144\n"
"v_exp_f32 v145, v145\n"
"v_exp_f32 v146, v146\n"
"v_exp_f32 v147, v147\n"
"v_exp_f32 v148, v148\n"
"v_exp_f32 v149, v149\n"
"v_exp_f32 v150, v150\n"
"v_exp_f32 v151, v151\n"
"v_exp_f32 v152, v152\n"
"v_exp_f32 v153, v153\n"
"v_exp_f32 v154, v154\n"
"v_exp_f32 v155, v155\n"
"v_exp_f32 v156, v156\n"
"v_exp_f32 v157, v157\n"
"v_exp_f32 v158, v158\n"
"v_exp_f32 v159, v159\n"
"s_nop 0\n"
"v_cvt_pk_bf16_f32 v162, v124, v125\n"
"v_cvt_pk_bf16_f32 v163, v126, v127\n"
"v_cvt_pk_bf16_f32 v170, v128, v129\n"
"v_cvt_pk_bf16_f32 v171, v130, v131\n"
"v_cvt_pk_bf16_f32 v178, v132, v133\n"
"v_cvt_pk_bf16_f32 v179, v134, v135\n"
"v_cvt_pk_bf16_f32 v164, v136, v137\n"
"v_cvt_pk_bf16_f32 v165, v138, v139\n"
"v_cvt_pk_bf16_f32 v172, v140, v141\n"
"v_cvt_pk_bf16_f32 v173, v142, v143\n"
"v_cvt_pk_bf16_f32 v180, v144, v145\n"
"v_cvt_pk_bf16_f32 v181, v146, v147\n"
"v_cvt_pk_bf16_f32 v166, v148, v149\n"
"v_cvt_pk_bf16_f32 v167, v150, v151\n"
"v_cvt_pk_bf16_f32 v174, v152, v153\n"
"v_cvt_pk_bf16_f32 v175, v154, v155\n"
"v_cvt_pk_bf16_f32 v182, v156, v157\n"
"v_cvt_pk_bf16_f32 v183, v158, v159\n"
"s_waitcnt vmcnt(28) lgkmcnt(0)\n"
"s_barrier\n"
"s_add_u32 s57, s57, 0x8000\n"
"s_cmp_eq_u32 s57, s61\n"
"s_cselect_b32 s57, s60, s57\n"
"v_add_u32 v11, s57, %[vl0]\n"
"v_add_u32 v12, s57, %[vl1]\n"
"ds_read_b64_tr_b16 v[20:21], v11 offset:0\n"
"ds_read_b64_tr_b16 v[22:23], v11 offset:8192\n"
"ds_read_b64_tr_b16 v[24:25], v12 offset:0\n"
"ds_read_b64_tr_b16 v[26:27], v12 offset:8192\n"
"ds_read_b64_tr_b16 v[28:29], v11 offset:512\n"
"ds_read_b64_tr_b16 v[30:31], v11 offset:8704\n"
"ds_read_b64_tr_b16 v[32:33], v12 offset:512\n"
"ds_read_b64_tr_b16 v[34:35], v12 offset:8704\n"
"ds_read_b64_tr_b16 v[36:37], v11 offset:1024\n"
"ds_read_b64_tr_b16 v[38:39], v11 offset:9216\n"
"ds_read_b64_tr_b16 v[40:41], v12 offset:1024\n"
"ds_read_b64_tr_b16 v[42:43], v12 offset:9216\n"
"ds_read_b64_tr_b16 v[44:45], v11 offset:1536\n"
"ds_read_b64_tr_b16 v[46:47], v11 offset:9728\n"
"s_waitcnt lgkmcnt(10)\n"
"v_mfma_f32_16x16x32_bf16 a[0:3], v[20:23], v[160:163], a[0:3]\n"
"ds_read_b64_tr_b16 v[48:49], v12 offset:1536\n"
"ds_read_b64_tr_b16 v[50:51], v12 offset:9728\n"
"v_mfma_f32_16x16x32_bf16 a[64:67], v[20:23], v[168:171], a[64:67]\n"
"s_add_u32 m0, s56, 0\n"
"v_mfma_f32_16x16x32_bf16 a[128:131], v[20:23], v[176:179], a[128:131]\n"
"v_mfma_f32_16x16x32_bf16 a[4:7], v[24:27], v[160:163], a[4:7]\n"
"ds_read_b64_tr_b16 v[20:21], v11 offset:2048\n"
"ds_read_b64_tr_b16 v[22:23], v11 offset:10240\n"
"v_mfma_f32_16x16x32_bf16 a[68:71], v[24:27], v[168:171], a[68:71]\n"
"buffer_load_dwordx4 v204, s[48:51], s83 offen lds\n"
"v_mfma_f32_16x16x32_bf16 a[132:135], v[24:27], v[176:179], a[132:135]\n"
"v_mfma_f32_16x16x32_bf16 a[192:195], a[204:207], v[160:163], a[192:195]\n"
"s_add_u32 s83, s83, 7168\n"
"s_waitcnt lgkmcnt(10)\n"
"v_mfma_f32_16x16x32_bf16 a[8:11], v[28:31], v[160:163], a[8:11]\n"
"ds_read_b64_tr_b16 v[24:25], v12 offset:2048\n"
"ds_read_b64_tr_b16 v[26:27], v12 offset:10240\n"
"v_mfma_f32_16x16x32_bf16 a[72:75], v[28:31], v[168:171], a[72:75]\n"
"v_mfma_f32_16x16x32_bf16 a[136:139], v[28:31], v[176:179], a[136:139]\n"
"s_add_u32 m0, s56, 1024\n"
"v_mfma_f32_16x16x32_bf16 a[196:199], a[204:207], v[168:171], a[196:199]\n"
"v_mfma_f32_16x16x32_bf16 a[12:15], v[32:35], v[160:163], a[12:15]\n"
"ds_read_b64_tr_b16 v[28:29], v11 offset:2560\n"
"ds_read_b64_tr_b16 v[30:31], v11 offset:10752\n"
"v_mfma_f32_16x16x32_bf16 a[76:79], v[32:35], v[168:171], a[76:79]\n"
"buffer_load_dwordx4 v204, s[48:51], s83 offen lds\n"
"v_mfma_f32_16x16x32_bf16 a[140:143], v[32:35], v[176:179], a[140:143]\n"
"v_mfma_f32_16x16x32_bf16 a[200:203], a[204:207], v[176:179], a[200:203]\n"
"s_waitcnt lgkmcnt(10)\n"
"v_mfma_f32_16x16x32_bf16 a[16:19], v[36:39], v[160:163], a[16:19]\n"
"ds_read_b64_tr_b16 v[32:33], v12 offset:2560\n"
"ds_read_b64_tr_b16 v[34:35], v12 offset:10752\n"
"v_mfma_f32_16x16x32_bf16 a[80:83], v[36:39], v[168:171], a[80:83]\n"
"s_add_u32 s83, s83, 7168\n"
"v_mfma_f32_16x16x32_bf16 a[144:147], v[36:39], v[176:179], a[144:147]\n"
"v_mfma_f32_16x16x32_bf16 a[20:23], v[40:43], v[160:163], a[20:23]\n"
"ds_read_b64_tr_b16 v[36:37], v11 offset:3072\n"
"ds_read_b64_tr_b16 v[38:39], v11 offset:11264\n"
"v_mfma_f32_16x16x32_bf16 a[84:87], v[40:43], v[168:171], a[84:87]\n"
"s_add_u32 m0, s56, 2048\n"
"v_mfma_f32_16x16x32_bf16 a[148:151], v[40:43], v[176:179], a[148:151]\n"
"s_waitcnt lgkmcnt(10)\n"
"v_mfma_f32_16x16x32_bf16 a[24:27], v[44:47], v[160:163], a[24:27]\n"
"ds_read_b64_tr_b16 v[40:41], v12 offset:3072\n"
"ds_read_b64_tr_b16 v[42:43], v12 offset:11264\n"
"v_mfma_f32_16x16x32_bf16 a[88:91], v[44:47], v[168:171], a[88:91]\n"
"buffer_load_dwordx4 v204, s[48:51], s83 offen lds\n"
"v_mfma_f32_16x16x32_bf16 a[152:155], v[44:47], v[176:179], a[152:155]\n"
"v_mfma_f32_16x16x32_bf16 a[28:31], v[48:51], v[160:163], a[28:31]\n"
"ds_read_b64_tr_b16 v[44:45], v11 offset:3584\n"
"ds_read_b64_tr_b16 v[46:47], v11 offset:11776\n"
"v_mfma_f32_16x16x32_bf16 a[92:95], v[48:51], v[168:171], a[92:95]\n"
"s_add_u32 s83, s83, 7168\n"
"v_mfma_f32_16x16x32_bf16 a[156:159], v[48:51], v[176:179], a[156:159]\n"
"s_waitcnt lgkmcnt(10)\n"
"v_mfma_f32_16x16x32_bf16 a[32:35], v[20:23], v[160:163], a[32:35]\n"
"ds_read_b64_tr_b16 v[48:49], v12 offset:3584\n"
"ds_read_b64_tr_b16 v[50:51], v12 offset:11776\n"
"v_mfma_f32_16x16x32_bf16 a[96:99], v[20:23], v[168:171], a[96:99]\n"
"s_add_u32 m0, s56, 3072\n"
"v_mfma_f32_16x16x32_bf16 a[160:163], v[20:23], v[176:179], a[160:163]\n"
"v_mfma_f32_16x16x32_bf16 a[36:39], v[24:27], v[160:163], a[36:39]\n"
"ds_read_b64_tr_b16 v[20:21], v11 offset:16384\n"
"ds_read_b64_tr_b16 v[22:23], v11 offset:24576\n"
"v_mfma_f32_16x16x32_bf16 a[100:103], v[24:27], v[168:171], a[100:103]\n"
"buffer_load_dwordx4 v204, s[48:51], s83 offen lds\n"
"v_mfma_f32_16x16x32_bf16 a[164:167], v[24:27], v[176:179], a[164:167]\n"
"s_waitcnt lgkmcnt(10)\n"
"v_mfma_f32_16x16x32_bf16 a[40:43], v[28:31], v[160:163], a[40:43]\n"
"ds_read_b64_tr_b16 v[24:25], v12 offset:16384\n"
"ds_read_b64_tr_b16 v[26:27], v12 offset:24576\n"
"v_mfma_f32_16x16x32_bf16 a[104:107], v[28:31], v[168:171], a[104:107]\n"
"s_add_u32 s83, s83, 7168\n"
"v_mfma_f32_16x16x32_bf16 a[168:171], v[28:31], v[176:179], a[168:171]\n"
"v_mfma_f32_16x16x32_bf16 a[44:47], v[32:35], v[160:163], a[44:47]\n"
"ds_read_b64_tr_b16 v[28:29], v11 offset:16896\n"
"ds_read_b64_tr_b16 v[30:31], v11 offset:25088\n"
"v_mfma_f32_16x16x32_bf16 a[108:111], v[32:35], v[168:171], a[108:111]\n"
"s_add_u32 m0, s56, 4096\n"
"v_mfma_f32_16x16x32_bf16 a[172:175], v[32:35], v[176:179], a[172:175]\n"
"s_waitcnt lgkmcnt(10)\n"
"v_mfma_f32_16x16x32_bf16 a[48:51], v[36:39], v[160:163], a[48:51]\n"
"ds_read_b64_tr_b16 v[32:33], v12 offset:16896\n"
"ds_read_b64_tr_b16 v[34:35], v12 offset:25088\n"
"v_mfma_f32_16x16x32_bf16 a[112:115], v[36:39], v[168:171], a[112:115]\n"
"buffer_load_dwordx4 v204, s[48:51], s83 offen lds\n"
"v_mfma_f32_16x16x32_bf16 a[176:179], v[36:39], v[176:179], a[176:179]\n"
"v_mfma_f32_16x16x32_bf16 a[52:55], v[40:43], v[160:163], a[52:55]\n"
"ds_read_b64_tr_b16 v[36:37], v11 offset:17408\n"
"ds_read_b64_tr_b16 v[38:39], v11 offset:25600\n"
"v_mfma_f32_16x16x32_bf16 a[116:119], v[40:43], v[168:171], a[116:119]\n"
"s_add_u32 s83, s83, 7168\n"
"v_mfma_f32_16x16x32_bf16 a[180:183], v[40:43], v[176:179], a[180:183]\n"
"s_waitcnt lgkmcnt(10)\n"
"v_mfma_f32_16x16x32_bf16 a[56:59], v[44:47], v[160:163], a[56:59]\n"
"ds_read_b64_tr_b16 v[40:41], v12 offset:17408\n"
"ds_read_b64_tr_b16 v[42:43], v12 offset:25600\n"
"v_mfma_f32_16x16x32_bf16 a[120:123], v[44:47], v[168:171], a[120:123]\n"
"s_add_u32 m0, s56, 5120\n"
"v_mfma_f32_16x16x32_bf16 a[184:187], v[44:47], v[176:179], a[184:187]\n"
"v_mfma_f32_16x16x32_bf16 a[60:63], v[48:51], v[160:163], a[60:63]\n"
"ds_read_b64_tr_b16 v[44:45], v11 offset:17920\n"
"ds_read_b64_tr_b16 v[46:47], v11 offset:26112\n"
"v_mfma_f32_16x16x32_bf16 a[124:127], v[48:51], v[168:171], a[124:127]\n"
"buffer_load_dwordx4 v204, s[48:51], s83 offen lds\n"
"v_mfma_f32_16x16x32_bf16 a[188:191], v[48:51], v[176:179], a[188:191]\n"
"s_waitcnt lgkmcnt(10)\n"
"v_mfma_f32_16x16x32_bf16 a[0:3], v[20:23], v[164:167], a[0:3]\n"
"ds_read_b64_tr_b16 v[48:49], v12 offset:17920\n"
"ds_read_b64_tr_b16 v[50:51], v12 offset:26112\n"
"v_mfma_f32_16x16x32_bf16 a[64:67], v[20:23], v[172:175], a[64:67]\n"
"s_add_u32 s83, s83, 7168\n"
"v_mfma_f32_16x16x32_bf16 a[128:131], v[20:23], v[180:183], a[128:131]\n"
"v_mfma_f32_16x16x32_bf16 a[4:7], v[24:27], v[164:167], a[4:7]\n"
"ds_read_b64_tr_b16 v[20:21], v11 offset:18432\n"
"ds_read_b64_tr_b16 v[22:23], v11 offset:26624\n"
"v_mfma_f32_16x16x32_bf16 a[68:71], v[24:27], v[172:175], a[68:71]\n"
"s_add_u32 m0, s56, 6144\n"
"v_mfma_f32_16x16x32_bf16 a[132:135], v[24:27], v[180:183], a[132:135]\n"
"v_mfma_f32_16x16x32_bf16 a[192:195], a[204:207], v[164:167], a[192:195]\n"
"buffer_load_dwordx4 v204, s[48:51], s83 offen lds\n"
"s_waitcnt lgkmcnt(10)\n"
"v_mfma_f32_16x16x32_bf16 a[8:11], v[28:31], v[164:167], a[8:11]\n"
"ds_read_b64_tr_b16 v[24:25], v12 offset:18432\n"
"ds_read_b64_tr_b16 v[26:27], v12 offset:26624\n"
"v_mfma_f32_16x16x32_bf16 a[72:75], v[28:31], v[172:175], a[72:75]\n"
"v_mfma_f32_16x16x32_bf16 a[136:139], v[28:31], v[180:183], a[136:139]\n"
"s_add_u32 s83, s83, 7168\n"
"v_mfma_f32_16x16x32_bf16 a[196:199], a[204:207], v[172:175], a[196:199]\n"
"v_mfma_f32_16x16x32_bf16 a[12:15], v[32:35], v[164:167], a[12:15]\n"
"ds_read_b64_tr_b16 v[28:29], v11 offset:18944\n"
"ds_read_b64_tr_b16 v[30:31], v11 offset:27136\n"
"v_mfma_f32_16x16x32_bf16 a[76:79], v[32:35], v[172:175], a[76:79]\n"
"s_add_u32 m0, s56, 7168\n"
"v_mfma_f32_16x16x32_bf16 a[140:143], v[32:35], v[180:183], a[140:143]\n"
"v_mfma_f32_16x16x32_bf16 a[200:203], a[204:207], v[180:183], a[200:203]\n"
"s_waitcnt lgkmcnt(10)\n"
"v_mfma_f32_16x16x32_bf16 a[16:19], v[36:39], v[164:167], a[16:19]\n"
"ds_read_b64_tr_b16 v[32:33], v12 offset:18944\n"
"ds_read_b64_tr_b16 v[34:35], v12 offset:27136\n"
"v_mfma_f32_16x16x32_bf16 a[80:83], v[36:39], v[172:175], a[80:83]\n"
"buffer_load_dwordx4 v204, s[48:51], s83 offen lds\n"
"v_mfma_f32_16x16x32_bf16 a[144:147], v[36:39], v[180:183], a[144:147]\n"
"v_mfma_f32_16x16x32_bf16 a[20:23], v[40:43], v[164:167], a[20:23]\n"
"ds_read_b64_tr_b16 v[36:37], v11 offset:19456\n"
"ds_read_b64_tr_b16 v[38:39], v11 offset:27648\n"
"v_mfma_f32_16x16x32_bf16 a[84:87], v[40:43], v[172:175], a[84:87]\n"
"s_add_u32 s83, s83, 7168\n"
"v_mfma_f32_16x16x32_bf16 a[148:151], v[40:43], v[180:183], a[148:151]\n"
"s_waitcnt lgkmcnt(10)\n"
"v_mfma_f32_16x16x32_bf16 a[24:27], v[44:47], v[164:167], a[24:27]\n"
"ds_read_b64_tr_b16 v[40:41], v12 offset:19456\n"
"ds_read_b64_tr_b16 v[42:43], v12 offset:27648\n"
"v_mfma_f32_16x16x32_bf16 a[88:91], v[44:47], v[172:175], a[88:91]\n"
"s_mov_b32 s83, 0\n"
"s_add_u32 s48, s48, 128\n"
"s_addc_u32 s49, s49, 0\n"
"v_mfma_f32_16x16x32_bf16 a[152:155], v[44:47], v[180:183], a[152:155]\n"
"v_mfma_f32_16x16x32_bf16 a[28:31], v[48:51], v[164:167], a[28:31]\n"
"ds_read_b64_tr_b16 v[44:45], v11 offset:19968\n"
"ds_read_b64_tr_b16 v[46:47], v11 offset:28160\n"
"v_mfma_f32_16x16x32_bf16 a[92:95], v[48:51], v[172:175], a[92:95]\n"
"s_sub_u32 s50, s50, 128\n"
"v_mfma_f32_16x16x32_bf16 a[156:159], v[48:51], v[180:183], a[156:159]\n"
"s_waitcnt lgkmcnt(10)\n"
"v_mfma_f32_16x16x32_bf16 a[32:35], v[20:23], v[164:167], a[32:35]\n"
"ds_read_b64_tr_b16 v[48:49], v12 offset:19968\n"
"ds_read_b64_tr_b16 v[50:51], v12 offset:28160\n"
"v_mfma_f32_16x16x32_bf16 a[96:99], v[20:23], v[172:175], a[96:99]\n"
"s_add_u32 s56, s56, 0x8000\n"
"s_cmp_eq_u32 s56, s59\n"
"s_cselect_b32 s56, s58, s56\n"
"v_mfma_f32_16x16x32_bf16 a[160:163], v[20:23], v[180:183], a[160:163]\n"
"v_mfma_f32_16x16x32_bf16 a[36:39], v[24:27], v[164:167], a[36:39]\n"
"s_add_u32 s57, s57, 0x8000\n"
"s_cmp_eq_u32 s57, s61\n"
"s_cselect_b32 s57, s60, s57\n"
"v_mfma_f32_16x16x32_bf16 a[100:103], v[24:27], v[172:175], a[100:103]\n"
"v_add_u32 v11, s57, v202\n"
"v_add_u32 v12, s57, v203\n"
"v_mfma_f32_16x16x32_bf16 a[164:167], v[24:27], v[180:183], a[164:167]\n"
"s_waitcnt lgkmcnt(8)\n"
"v_mfma_f32_16x16x32_bf16 a[40:43], v[28:31], v[164:167], a[40:43]\n"
"v_mfma_f32_16x16x32_bf16 a[104:107], v[28:31], v[172:175], a[104:107]\n"
"v_mfma_f32_16x16x32_bf16 a[168:171], v[28:31], v[180:183], a[168:171]\n"
"s_waitcnt vmcnt(24) lgkmcnt(0)\n"
"s_barrier\n"
"v_mfma_f32_16x16x32_bf16 a[44:47], v[32:35], v[164:167], a[44:47]\n"
"ds_read_b64_tr_b16 v[20:21], v11 offset:0\n"
"ds_read_b64_tr_b16 v[22:23], v11 offset:2048\n"
"v_mfma_f32_16x16x32_bf16 a[108:111], v[32:35], v[172:175], a[108:111]\n"
"ds_read_b64_tr_b16 v[24:25], v11 offset:4096\n"
"ds_read_b64_tr_b16 v[26:27], v11 offset:6144\n"
"v_mfma_f32_16x16x32_bf16 a[172:175], v[32:35], v[180:183], a[172:175]\n"
"ds_read_b64_tr_b16 v[28:29], v11 offset:8192\n"
"ds_read_b64_tr_b16 v[30:31], v11 offset:10240\n"
"v_mfma_f32_16x16x32_bf16 a[48:51], v[36:39], v[164:167], a[48:51]\n"
"ds_read_b64_tr_b16 v[32:33], v11 offset:12288\n"
"ds_read_b64_tr_b16 v[34:35], v11 offset:14336\n"
"v_mfma_f32_16x16x32_bf16 a[112:115], v[36:39], v[172:175], a[112:115]\n"
"v_mfma_f32_16x16x32_bf16 a[176:179], v[36:39], v[180:183], a[176:179]\n"
"v_mfma_f32_16x16x32_bf16 a[52:55], v[40:43], v[164:167], a[52:55]\n"
"ds_read_b64_tr_b16 v[36:37], v11 offset:16384\n"
"ds_read_b64_tr_b16 v[38:39], v11 offset:18432\n"
"v_mfma_f32_16x16x32_bf16 a[116:119], v[40:43], v[172:175], a[116:119]\n"
"v_mfma_f32_16x16x32_bf16 a[180:183], v[40:43], v[180:183], a[180:183]\n"
"v_mfma_f32_16x16x32_bf16 a[56:59], v[44:47], v[164:167], a[56:59]\n"
"ds_read_b64_tr_b16 v[40:41], v11 offset:20480\n"
"ds_read_b64_tr_b16 v[42:43], v11 offset:22528\n"
"v_mfma_f32_16x16x32_bf16 a[120:123], v[44:47], v[172:175], a[120:123]\n"
"v_mfma_f32_16x16x32_bf16 a[184:187], v[44:47], v[180:183], a[184:187]\n"
"v_mfma_f32_16x16x32_bf16 a[60:63], v[48:51], v[164:167], a[60:63]\n"
"ds_read_b64_tr_b16 v[44:45], v11 offset:24576\n"
"ds_read_b64_tr_b16 v[46:47], v11 offset:26624\n"
"v_mfma_f32_16x16x32_bf16 a[124:127], v[48:51], v[172:175], a[124:127]\n"
"v_mfma_f32_16x16x32_bf16 a[188:191], v[48:51], v[180:183], a[188:191]\n"
"s_nop 7\n"
"v_accvgpr_read_b32 v184, a192\n"
"v_accvgpr_read_b32 v185, a196\n"
"v_accvgpr_read_b32 v186, a200\n"
"v_rcp_f32 v184, v184\n"
"v_rcp_f32 v185, v185\n"
"v_rcp_f32 v186, v186\n"
"s_mov_b64 s[64:65], %[linv]\n"
"s_mov_b32 s66, 704\n"
"s_mov_b32 s67, 0x00020000\n"
"s_mul_i32 s68, %[wave], 192\n"
"v_lshlrev_b32 v187, 2, v200\n"
"s_mov_b64 exec, 0xffff\n"
"buffer_store_dword v184, v187, s[64:67], s68 offen offset:0 sc1\n"
"buffer_store_dword v185, v187, s[64:67], s68 offen offset:64 sc1\n"
"buffer_store_dword v186, v187, s[64:67], s68 offen offset:128 sc1\n"
"s_mov_b64 exec, -1\n"
"v_accvgpr_read_b32 v160, a0\n"
"v_accvgpr_read_b32 v161, a1\n"
"v_accvgpr_read_b32 v162, a2\n"
"v_accvgpr_read_b32 v163, a3\n"
"v_accvgpr_read_b32 v164, a4\n"
"v_accvgpr_read_b32 v165, a5\n"
"v_accvgpr_read_b32 v166, a6\n"
"v_accvgpr_read_b32 v167, a7\n"
"v_cvt_pk_bf16_f32 v64, v160, v161\n"
"v_cvt_pk_bf16_f32 v65, v162, v163\n"
"v_cvt_pk_bf16_f32 v66, v164, v165\n"
"v_cvt_pk_bf16_f32 v67, v166, v167\n"
"v_accvgpr_read_b32 v168, a64\n"
"v_accvgpr_read_b32 v169, a65\n"
"v_accvgpr_read_b32 v170, a66\n"
"v_accvgpr_read_b32 v171, a67\n"
"v_accvgpr_read_b32 v172, a68\n"
"v_accvgpr_read_b32 v173, a69\n"
"v_accvgpr_read_b32 v174, a70\n"
"v_accvgpr_read_b32 v175, a71\n"
"v_cvt_pk_bf16_f32 v96, v168, v169\n"
"v_cvt_pk_bf16_f32 v97, v170, v171\n"
"v_cvt_pk_bf16_f32 v98, v172, v173\n"
"v_cvt_pk_bf16_f32 v99, v174, v175\n"
"v_accvgpr_read_b32 v176, a128\n"
"v_accvgpr_read_b32 v177, a129\n"
"v_accvgpr_read_b32 v178, a130\n"
"v_accvgpr_read_b32 v179, a131\n"
"v_accvgpr_read_b32 v180, a132\n"
"v_accvgpr_read_b32 v181, a133\n"
"v_accvgpr_read_b32 v182, a134\n"
"v_accvgpr_read_b32 v183, a135\n"
"v_cvt_pk_bf16_f32 v224, v176, v177\n"
"v_cvt_pk_bf16_f32 v225, v178, v179\n"
"v_cvt_pk_bf16_f32 v226, v180, v181\n"
"v_cvt_pk_bf16_f32 v227, v182, v183\n"
"v_accvgpr_read_b32 v160, a8\n"
"v_accvgpr_read_b32 v161, a9\n"
"v_accvgpr_read_b32 v162, a10\n"
"v_accvgpr_read_b32 v163, a11\n"
"v_accvgpr_read_b32 v164, a12\n"
"v_accvgpr_read_b32 v165, a13\n"
"v_accvgpr_read_b32 v166, a14\n"
"v_accvgpr_read_b32 v167, a15\n"
"v_cvt_pk_bf16_f32 v68, v160, v161\n"
"v_cvt_pk_bf16_f32 v69, v162, v163\n"
"v_cvt_pk_bf16_f32 v70, v164, v165\n"
"v_cvt_pk_bf16_f32 v71, v166, v167\n"
"v_accvgpr_read_b32 v168, a72\n"
"v_accvgpr_read_b32 v169, a73\n"
"v_accvgpr_read_b32 v170, a74\n"
"v_accvgpr_read_b32 v171, a75\n"
"v_accvgpr_read_b32 v172, a76\n"
"v_accvgpr_read_b32 v173, a77\n"
"v_accvgpr_read_b32 v174, a78\n"
"v_accvgpr_read_b32 v175, a79\n"
"v_cvt_pk_bf16_f32 v100, v168, v169\n"
"v_cvt_pk_bf16_f32 v101, v170, v171\n"
"v_cvt_pk_bf16_f32 v102, v172, v173\n"
"v_cvt_pk_bf16_f32 v103, v174, v175\n"
"v_accvgpr_read_b32 v176, a136\n"
"v_accvgpr_read_b32 v177, a137\n"
"v_accvgpr_read_b32 v178, a138\n"
"v_accvgpr_read_b32 v179, a139\n"
"v_accvgpr_read_b32 v180, a140\n"
"v_accvgpr_read_b32 v181, a141\n"
"v_accvgpr_read_b32 v182, a142\n"
"v_accvgpr_read_b32 v183, a143\n"
"v_cvt_pk_bf16_f32 v228, v176, v177\n"
"v_cvt_pk_bf16_f32 v229, v178, v179\n"
"v_cvt_pk_bf16_f32 v230, v180, v181\n"
"v_cvt_pk_bf16_f32 v231, v182, v183\n"
"v_accvgpr_read_b32 v160, a16\n"
"v_accvgpr_read_b32 v161, a17\n"
"v_accvgpr_read_b32 v162, a18\n"
"v_accvgpr_read_b32 v163, a19\n"
"v_accvgpr_read_b32 v164, a20\n"
"v_accvgpr_read_b32 v165, a21\n"
"v_accvgpr_read_b32 v166, a22\n"
"v_accvgpr_read_b32 v167, a23\n"
"v_cvt_pk_bf16_f32 v72, v160, v161\n"
"v_cvt_pk_bf16_f32 v73, v162, v163\n"
"v_cvt_pk_bf16_f32 v74, v164, v165\n"
"v_cvt_pk_bf16_f32 v75, v166, v167\n"
"v_accvgpr_read_b32 v168, a80\n"
"v_accvgpr_read_b32 v169, a81\n"
"v_accvgpr_read_b32 v170, a82\n"
"v_accvgpr_read_b32 v171, a83\n"
"v_accvgpr_read_b32 v172, a84\n"
"v_accvgpr_read_b32 v173, a85\n"
"v_accvgpr_read_b32 v174, a86\n"
"v_accvgpr_read_b32 v175, a87\n"
"v_cvt_pk_bf16_f32 v104, v168, v169\n"
"v_cvt_pk_bf16_f32 v105, v170, v171\n"
"v_cvt_pk_bf16_f32 v106, v172, v173\n"
"v_cvt_pk_bf16_f32 v107, v174, v175\n"
"v_accvgpr_read_b32 v176, a144\n"
"v_accvgpr_read_b32 v177, a145\n"
"v_accvgpr_read_b32 v178, a146\n"
"v_accvgpr_read_b32 v179, a147\n"
"v_accvgpr_read_b32 v180, a148\n"
"v_accvgpr_read_b32 v181, a149\n"
"v_accvgpr_read_b32 v182, a150\n"
"v_accvgpr_read_b32 v183, a151\n"
"v_cvt_pk_bf16_f32 v232, v176, v177\n"
"v_cvt_pk_bf16_f32 v233, v178, v179\n"
"v_cvt_pk_bf16_f32 v234, v180, v181\n"
"v_cvt_pk_bf16_f32 v235, v182, v183\n"
"v_accvgpr_read_b32 v160, a24\n"
"v_accvgpr_read_b32 v161, a25\n"
"v_accvgpr_read_b32 v162, a26\n"
"v_accvgpr_read_b32 v163, a27\n"
"v_accvgpr_read_b32 v164, a28\n"
"v_accvgpr_read_b32 v165, a29\n"
"v_accvgpr_read_b32 v166, a30\n"
"v_accvgpr_read_b32 v167, a31\n"
"v_cvt_pk_bf16_f32 v76, v160, v161\n"
"v_cvt_pk_bf16_f32 v77, v162, v163\n"
"v_cvt_pk_bf16_f32 v78, v164, v165\n"
"v_cvt_pk_bf16_f32 v79, v166, v167\n"
"v_accvgpr_read_b32 v168, a88\n"
"v_accvgpr_read_b32 v169, a89\n"
"v_accvgpr_read_b32 v170, a90\n"
"v_accvgpr_read_b32 v171, a91\n"
"v_accvgpr_read_b32 v172, a92\n"
"v_accvgpr_read_b32 v173, a93\n"
"v_accvgpr_read_b32 v174, a94\n"
"v_accvgpr_read_b32 v175, a95\n"
"v_cvt_pk_bf16_f32 v108, v168, v169\n"
"v_cvt_pk_bf16_f32 v109, v170, v171\n"
"v_cvt_pk_bf16_f32 v110, v172, v173\n"
"v_cvt_pk_bf16_f32 v111, v174, v175\n"
"v_accvgpr_read_b32 v176, a152\n"
"v_accvgpr_read_b32 v177, a153\n"
"v_accvgpr_read_b32 v178, a154\n"
"v_accvgpr_read_b32 v179, a155\n"
"v_accvgpr_read_b32 v180, a156\n"
"v_accvgpr_read_b32 v181, a157\n"
"v_accvgpr_read_b32 v182, a158\n"
"v_accvgpr_read_b32 v183, a159\n"
"v_cvt_pk_bf16_f32 v236, v176, v177\n"
"v_cvt_pk_bf16_f32 v237, v178, v179\n"
"v_cvt_pk_bf16_f32 v238, v180, v181\n"
"v_cvt_pk_bf16_f32 v239, v182, v183\n"
"v_accvgpr_read_b32 v160, a32\n"
"v_accvgpr_read_b32 v161, a33\n"
"v_accvgpr_read_b32 v162, a34\n"
"v_accvgpr_read_b32 v163, a35\n"
"v_accvgpr_read_b32 v164, a36\n"
"v_accvgpr_read_b32 v165, a37\n"
"v_accvgpr_read_b32 v166, a38\n"
"v_accvgpr_read_b32 v167, a39\n"
"v_cvt_pk_bf16_f32 v80, v160, v161\n"
"v_cvt_pk_bf16_f32 v81, v162, v163\n"
"v_cvt_pk_bf16_f32 v82, v164, v165\n"
"v_cvt_pk_bf16_f32 v83, v166, v167\n"
"v_accvgpr_read_b32 v168, a96\n"
"v_accvgpr_read_b32 v169, a97\n"
"v_accvgpr_read_b32 v170, a98\n"
"v_accvgpr_read_b32 v171, a99\n"
"v_accvgpr_read_b32 v172, a100\n"
"v_accvgpr_read_b32 v173, a101\n"
"v_accvgpr_read_b32 v174, a102\n"
"v_accvgpr_read_b32 v175, a103\n"
"v_cvt_pk_bf16_f32 v208, v168, v169\n"
"v_cvt_pk_bf16_f32 v209, v170, v171\n"
"v_cvt_pk_bf16_f32 v210, v172, v173\n"
"v_cvt_pk_bf16_f32 v211, v174, v175\n"
"v_accvgpr_read_b32 v176, a160\n"
"v_accvgpr_read_b32 v177, a161\n"
"v_accvgpr_read_b32 v178, a162\n"
"v_accvgpr_read_b32 v179, a163\n"
"v_accvgpr_read_b32 v180, a164\n"
"v_accvgpr_read_b32 v181, a165\n"
"v_accvgpr_read_b32 v182, a166\n"
"v_accvgpr_read_b32 v183, a167\n"
"v_cvt_pk_bf16_f32 v240, v176, v177\n"
"v_cvt_pk_bf16_f32 v241, v178, v179\n"
"v_cvt_pk_bf16_f32 v242, v180, v181\n"
"v_cvt_pk_bf16_f32 v243, v182, v183\n"
"v_accvgpr_read_b32 v160, a40\n"
"v_accvgpr_read_b32 v161, a41\n"
"v_accvgpr_read_b32 v162, a42\n"
"v_accvgpr_read_b32 v163, a43\n"
"v_accvgpr_read_b32 v164, a44\n"
"v_accvgpr_read_b32 v165, a45\n"
"v_accvgpr_read_b32 v166, a46\n"
"v_accvgpr_read_b32 v167, a47\n"
"v_cvt_pk_bf16_f32 v84, v160, v161\n"
"v_cvt_pk_bf16_f32 v85, v162, v163\n"
"v_cvt_pk_bf16_f32 v86, v164, v165\n"
"v_cvt_pk_bf16_f32 v87, v166, v167\n"
"v_accvgpr_read_b32 v168, a104\n"
"v_accvgpr_read_b32 v169, a105\n"
"v_accvgpr_read_b32 v170, a106\n"
"v_accvgpr_read_b32 v171, a107\n"
"v_accvgpr_read_b32 v172, a108\n"
"v_accvgpr_read_b32 v173, a109\n"
"v_accvgpr_read_b32 v174, a110\n"
"v_accvgpr_read_b32 v175, a111\n"
"v_cvt_pk_bf16_f32 v212, v168, v169\n"
"v_cvt_pk_bf16_f32 v213, v170, v171\n"
"v_cvt_pk_bf16_f32 v214, v172, v173\n"
"v_cvt_pk_bf16_f32 v215, v174, v175\n"
"v_accvgpr_read_b32 v176, a168\n"
"v_accvgpr_read_b32 v177, a169\n"
"v_accvgpr_read_b32 v178, a170\n"
"v_accvgpr_read_b32 v179, a171\n"
"v_accvgpr_read_b32 v180, a172\n"
"v_accvgpr_read_b32 v181, a173\n"
"v_accvgpr_read_b32 v182, a174\n"
"v_accvgpr_read_b32 v183, a175\n"
"v_cvt_pk_bf16_f32 v244, v176, v177\n"
"v_cvt_pk_bf16_f32 v245, v178, v179\n"
"v_cvt_pk_bf16_f32 v246, v180, v181\n"
"v_cvt_pk_bf16_f32 v247, v182, v183\n"
"v_accvgpr_read_b32 v160, a48\n"
"v_accvgpr_read_b32 v161, a49\n"
"v_accvgpr_read_b32 v162, a50\n"
"v_accvgpr_read_b32 v163, a51\n"
"v_accvgpr_read_b32 v164, a52\n"
"v_accvgpr_read_b32 v165, a53\n"
"v_accvgpr_read_b32 v166, a54\n"
"v_accvgpr_read_b32 v167, a55\n"
"v_cvt_pk_bf16_f32 v88, v160, v161\n"
"v_cvt_pk_bf16_f32 v89, v162, v163\n"
"v_cvt_pk_bf16_f32 v90, v164, v165\n"
"v_cvt_pk_bf16_f32 v91, v166, v167\n"
"v_accvgpr_read_b32 v168, a112\n"
"v_accvgpr_read_b32 v169, a113\n"
"v_accvgpr_read_b32 v170, a114\n"
"v_accvgpr_read_b32 v171, a115\n"
"v_accvgpr_read_b32 v172, a116\n"
"v_accvgpr_read_b32 v173, a117\n"
"v_accvgpr_read_b32 v174, a118\n"
"v_accvgpr_read_b32 v175, a119\n"
"v_cvt_pk_bf16_f32 v216, v168, v169\n"
"v_cvt_pk_bf16_f32 v217, v170, v171\n"
"v_cvt_pk_bf16_f32 v218, v172, v173\n"
"v_cvt_pk_bf16_f32 v219, v174, v175\n"
"v_accvgpr_read_b32 v176, a176\n"
"v_accvgpr_read_b32 v177, a177\n"
"v_accvgpr_read_b32 v178, a178\n"
"v_accvgpr_read_b32 v179, a179\n"
"v_accvgpr_read_b32 v180, a180\n"
"v_accvgpr_read_b32 v181, a181\n"
"v_accvgpr_read_b32 v182, a182\n"
"v_accvgpr_read_b32 v183, a183\n"
"v_cvt_pk_bf16_f32 v248, v176, v177\n"
"v_cvt_pk_bf16_f32 v249, v178, v179\n"
"v_cvt_pk_bf16_f32 v250, v180, v181\n"
"v_cvt_pk_bf16_f32 v251, v182, v183\n"
"v_accvgpr_read_b32 v160, a56\n"
"v_accvgpr_read_b32 v161, a57\n"
"v_accvgpr_read_b32 v162, a58\n"
"v_accvgpr_read_b32 v163, a59\n"
"v_accvgpr_read_b32 v164, a60\n"
"v_accvgpr_read_b32 v165, a61\n"
"v_accvgpr_read_b32 v166, a62\n"
"v_accvgpr_read_b32 v167, a63\n"
"v_cvt_pk_bf16_f32 v92, v160, v161\n"
"v_cvt_pk_bf16_f32 v93, v162, v163\n"
"v_cvt_pk_bf16_f32 v94, v164, v165\n"
"v_cvt_pk_bf16_f32 v95, v166, v167\n"
"v_accvgpr_read_b32 v168, a120\n"
"v_accvgpr_read_b32 v169, a121\n"
"v_accvgpr_read_b32 v170, a122\n"
"v_accvgpr_read_b32 v171, a123\n"
"v_accvgpr_read_b32 v172, a124\n"
"v_accvgpr_read_b32 v173, a125\n"
"v_accvgpr_read_b32 v174, a126\n"
"v_accvgpr_read_b32 v175, a127\n"
"v_cvt_pk_bf16_f32 v220, v168, v169\n"
"v_cvt_pk_bf16_f32 v221, v170, v171\n"
"v_cvt_pk_bf16_f32 v222, v172, v173\n"
"v_cvt_pk_bf16_f32 v223, v174, v175\n"
"v_accvgpr_read_b32 v176, a184\n"
"v_accvgpr_read_b32 v177, a185\n"
"v_accvgpr_read_b32 v178, a186\n"
"v_accvgpr_read_b32 v179, a187\n"
"v_accvgpr_read_b32 v180, a188\n"
"v_accvgpr_read_b32 v181, a189\n"
"v_accvgpr_read_b32 v182, a190\n"
"v_accvgpr_read_b32 v183, a191\n"
"v_cvt_pk_bf16_f32 v252, v176, v177\n"
"v_cvt_pk_bf16_f32 v253, v178, v179\n"
"v_cvt_pk_bf16_f32 v254, v180, v181\n"
"v_cvt_pk_bf16_f32 v255, v182, v183\n"
"s_nop 1\n"
"s_waitcnt lgkmcnt(10)\n"
"v_mfma_f32_16x16x32_bf16 v[112:115], v[20:23], v[64:67], 0\n"
"ds_read_b64_tr_b16 v[48:49], v11 offset:28672\n"
"ds_read_b64_tr_b16 v[50:51], v11 offset:30720\n"
"v_mfma_f32_16x16x32_bf16 v[116:119], v[20:23], v[96:99], 0\n"
"s_add_u32 m0, s56, 0\n"
"v_mfma_f32_16x16x32_bf16 v[120:123], v[20:23], v[224:227], 0\n"
"buffer_load_dwordx4 v204, s[48:51], s83 offen lds\n"
"s_add_u32 s83, s83, 7168\n"
"v_mfma_f32_16x16x32_bf16 v[112:115], v[24:27], v[68:71], v[112:115]\n"
"ds_read_b64_tr_b16 v[20:21], v12 offset:0\n"
"ds_read_b64_tr_b16 v[22:23], v12 offset:2048\n"
"v_mfma_f32_16x16x32_bf16 v[116:119], v[24:27], v[100:103], v[116:119]\n"
"s_add_u32 m0, s56, 1024\n"
"v_mfma_f32_16x16x32_bf16 v[120:123], v[24:27], v[228:231], v[120:123]\n"
"buffer_load_dwordx4 v204, s[48:51], s83 offen lds\n"
"s_waitcnt lgkmcnt(10)\n"
"v_mfma_f32_16x16x32_bf16 v[112:115], v[28:31], v[72:75], v[112:115]\n"
"ds_read_b64_tr_b16 v[24:25], v12 offset:4096\n"
"ds_read_b64_tr_b16 v[26:27], v12 offset:6144\n"
"v_mfma_f32_16x16x32_bf16 v[116:119], v[28:31], v[104:107], v[116:119]\n"
"s_add_u32 s83, s83, 7168\n"
"s_add_u32 m0, s56, 2048\n"
"v_mfma_f32_16x16x32_bf16 v[120:123], v[28:31], v[232:235], v[120:123]\n"
"buffer_load_dwordx4 v204, s[48:51], s83 offen lds\n"
"s_add_u32 s83, s83, 7168\n"
"v_mfma_f32_16x16x32_bf16 v[112:115], v[32:35], v[76:79], v[112:115]\n"
"ds_read_b64_tr_b16 v[28:29], v12 offset:8192\n"
"ds_read_b64_tr_b16 v[30:31], v12 offset:10240\n"
"v_mfma_f32_16x16x32_bf16 v[116:119], v[32:35], v[108:111], v[116:119]\n"
"s_add_u32 m0, s56, 3072\n"
"v_mfma_f32_16x16x32_bf16 v[120:123], v[32:35], v[236:239], v[120:123]\n"
"buffer_load_dwordx4 v204, s[48:51], s83 offen lds\n"
"s_waitcnt lgkmcnt(10)\n"
"v_mfma_f32_16x16x32_bf16 v[112:115], v[36:39], v[80:83], v[112:115]\n"
"ds_read_b64_tr_b16 v[32:33], v12 offset:12288\n"
"ds_read_b64_tr_b16 v[34:35], v12 offset:14336\n"
"v_mfma_f32_16x16x32_bf16 v[116:119], v[36:39], v[208:211], v[116:119]\n"
"s_add_u32 s83, s83, 7168\n"
"s_add_u32 m0, s56, 4096\n"
"v_mfma_f32_16x16x32_bf16 v[120:123], v[36:39], v[240:243], v[120:123]\n"
"buffer_load_dwordx4 v204, s[48:51], s83 offen lds\n"
"s_add_u32 s83, s83, 7168\n"
"v_mfma_f32_16x16x32_bf16 v[112:115], v[40:43], v[84:87], v[112:115]\n"
"ds_read_b64_tr_b16 v[36:37], v12 offset:16384\n"
"ds_read_b64_tr_b16 v[38:39], v12 offset:18432\n"
"v_mfma_f32_16x16x32_bf16 v[116:119], v[40:43], v[212:215], v[116:119]\n"
"s_add_u32 m0, s56, 5120\n"
"v_mfma_f32_16x16x32_bf16 v[120:123], v[40:43], v[244:247], v[120:123]\n"
"buffer_load_dwordx4 v204, s[48:51], s83 offen lds\n"
"s_waitcnt lgkmcnt(10)\n"
"v_mfma_f32_16x16x32_bf16 v[112:115], v[44:47], v[88:91], v[112:115]\n"
"ds_read_b64_tr_b16 v[40:41], v12 offset:20480\n"
"ds_read_b64_tr_b16 v[42:43], v12 offset:22528\n"
"v_mfma_f32_16x16x32_bf16 v[116:119], v[44:47], v[216:219], v[116:119]\n"
"s_add_u32 s83, s83, 7168\n"
"s_add_u32 m0, s56, 6144\n"
"v_mfma_f32_16x16x32_bf16 v[120:123], v[44:47], v[248:251], v[120:123]\n"
"buffer_load_dwordx4 v204, s[48:51], s83 offen lds\n"
"s_add_u32 s83, s83, 7168\n"
"v_mfma_f32_16x16x32_bf16 v[112:115], v[48:51], v[92:95], v[112:115]\n"
"ds_read_b64_tr_b16 v[44:45], v12 offset:24576\n"
"ds_read_b64_tr_b16 v[46:47], v12 offset:26624\n"
"v_mfma_f32_16x16x32_bf16 v[116:119], v[48:51], v[220:223], v[116:119]\n"
"s_add_u32 m0, s56, 7168\n"
"v_mfma_f32_16x16x32_bf16 v[120:123], v[48:51], v[252:255], v[120:123]\n"
"buffer_load_dwordx4 v204, s[48:51], s83 offen lds\n"
"s_waitcnt lgkmcnt(10)\n"
"v_mfma_f32_16x16x32_bf16 v[124:127], v[20:23], v[64:67], 0\n"
"ds_read_b64_tr_b16 v[48:49], v12 offset:28672\n"
"ds_read_b64_tr_b16 v[50:51], v12 offset:30720\n"
"v_mfma_f32_16x16x32_bf16 v[128:131], v[20:23], v[96:99], 0\n"
"s_add_u32 s83, s83, 7168\n"
"v_mfma_f32_16x16x32_bf16 v[132:135], v[20:23], v[224:227], 0\n"
"s_mov_b32 s83, 0\n"
"s_add_u32 s48, s48, 128\n"
"s_addc_u32 s49, s49, 0\n"
"v_mfma_f32_16x16x32_bf16 v[124:127], v[24:27], v[68:71], v[124:127]\n"
"ds_read_b64_tr_b16 v[20:21], v11 offset:512\n"
"ds_read_b64_tr_b16 v[22:23], v11 offset:2560\n"
"v_mfma_f32_16x16x32_bf16 v[128:131], v[24:27], v[100:103], v[128:131]\n"
"s_sub_u32 s50, s50, 128\n"
"v_mfma_f32_16x16x32_bf16 v[132:135], v[24:27], v[228:231], v[132:135]\n"
"s_waitcnt lgkmcnt(10)\n"
"v_mfma_f32_16x16x32_bf16 v[124:127], v[28:31], v[72:75], v[124:127]\n"
"ds_read_b64_tr_b16 v[24:25], v11 offset:4608\n"
"ds_read_b64_tr_b16 v[26:27], v11 offset:6656\n"
"v_mfma_f32_16x16x32_bf16 v[128:131], v[28:31], v[104:107], v[128:131]\n"
"s_add_u32 s56, s56, 0x8000\n"
"s_cmp_eq_u32 s56, s59\n"
"s_cselect_b32 s56, s58, s56\n"
"v_mfma_f32_16x16x32_bf16 v[132:135], v[28:31], v[232:235], v[132:135]\n"
"v_mfma_f32_16x16x32_bf16 v[124:127], v[32:35], v[76:79], v[124:127]\n"
"ds_read_b64_tr_b16 v[28:29], v11 offset:8704\n"
"ds_read_b64_tr_b16 v[30:31], v11 offset:10752\n"
"v_mfma_f32_16x16x32_bf16 v[128:131], v[32:35], v[108:111], v[128:131]\n"
"v_mfma_f32_16x16x32_bf16 v[132:135], v[32:35], v[236:239], v[132:135]\n"
"s_waitcnt lgkmcnt(10)\n"
"v_mfma_f32_16x16x32_bf16 v[124:127], v[36:39], v[80:83], v[124:127]\n"
"ds_read_b64_tr_b16 v[32:33], v11 offset:12800\n"
"ds_read_b64_tr_b16 v[34:35], v11 offset:14848\n"
"v_mfma_f32_16x16x32_bf16 v[128:131], v[36:39], v[208:211], v[128:131]\n"
"v_mfma_f32_16x16x32_bf16 v[132:135], v[36:39], v[240:243], v[132:135]\n"
"v_mfma_f32_16x16x32_bf16 v[124:127], v[40:43], v[84:87], v[124:127]\n"
"ds_read_b64_tr_b16 v[36:37], v11 offset:16896\n"
"ds_read_b64_tr_b16 v[38:39], v11 offset:18944\n"
"v_mfma_f32_16x16x32_bf16 v[128:131], v[40:43], v[212:215], v[128:131]\n"
"v_mfma_f32_16x16x32_bf16 v[132:135], v[40:43], v[244:247], v[132:135]\n"
"s_waitcnt lgkmcnt(10)\n"
"v_mfma_f32_16x16x32_bf16 v[124:127], v[44:47], v[88:91], v[124:127]\n"
"ds_read_b64_tr_b16 v[40:41], v11 offset:20992\n"
"ds_read_b64_tr_b16 v[42:43], v11 offset:23040\n"
"v_mfma_f32_16x16x32_bf16 v[128:131], v[44:47], v[216:219], v[128:131]\n"
"v_mfma_f32_16x16x32_bf16 v[132:135], v[44:47], v[248:251], v[132:135]\n"
"v_mfma_f32_16x16x32_bf16 v[124:127], v[48:51], v[92:95], v[124:127]\n"
"ds_read_b64_tr_b16 v[44:45], v11 offset:25088\n"
"ds_read_b64_tr_b16 v[46:47], v11 offset:27136\n"
"v_mfma_f32_16x16x32_bf16 v[128:131], v[48:51], v[220:223], v[128:131]\n"
"v_mfma_f32_16x16x32_bf16 v[132:135], v[48:51], v[252:255], v[132:135]\n"
"s_waitcnt lgkmcnt(10)\n"
"v_mfma_f32_16x16x32_bf16 v[136:139], v[20:23], v[64:67], 0\n"
"ds_read_b64_tr_b16 v[48:49], v11 offset:29184\n"
"ds_read_b64_tr_b16 v[50:51], v11 offset:31232\n"
"v_mfma_f32_16x16x32_bf16 v[140:143], v[20:23], v[96:99], 0\n"
"v_mfma_f32_16x16x32_bf16 v[144:147], v[20:23], v[224:227], 0\n"
"v_mfma_f32_16x16x32_bf16 v[136:139], v[24:27], v[68:71], v[136:139]\n"
"ds_read_b64_tr_b16 v[20:21], v12 offset:512\n"
"ds_read_b64_tr_b16 v[22:23], v12 offset:2560\n"
"v_mfma_f32_16x16x32_bf16 v[140:143], v[24:27], v[100:103], v[140:143]\n"
"v_cvt_pk_bf16_f32 v160, v112, v113\n"
"v_cvt_pk_bf16_f32 v161, v114, v115\n"
"v_mfma_f32_16x16x32_bf16 v[144:147], v[24:27], v[228:231], v[144:147]\n"
"v_cvt_pk_bf16_f32 v162, v124, v125\n"
"s_waitcnt lgkmcnt(10)\n"
"v_mfma_f32_16x16x32_bf16 v[136:139], v[28:31], v[72:75], v[136:139]\n"
"ds_read_b64_tr_b16 v[24:25], v12 offset:4608\n"
"ds_read_b64_tr_b16 v[26:27], v12 offset:6656\n"
"v_mfma_f32_16x16x32_bf16 v[140:143], v[28:31], v[104:107], v[140:143]\n"
"v_cvt_pk_bf16_f32 v163, v126, v127\n"
"v_permlane16_swap_b32 v160, v162\n"
"v_mfma_f32_16x16x32_bf16 v[144:147], v[28:31], v[232:235], v[144:147]\n"
"v_permlane16_swap_b32 v161, v163\n"
"buffer_store_dwordx4 v[160:163], v201, s[52:55], s77 offen offset:0 sc1\n"
"v_mfma_f32_16x16x32_bf16 v[136:139], v[32:35], v[76:79], v[136:139]\n"
"ds_read_b64_tr_b16 v[28:29], v12 offset:8704\n"
"ds_read_b64_tr_b16 v[30:31], v12 offset:10752\n"
"v_mfma_f32_16x16x32_bf16 v[140:143], v[32:35], v[108:111], v[140:143]\n"
"v_cvt_pk_bf16_f32 v168, v116, v117\n"
"v_cvt_pk_bf16_f32 v169, v118, v119\n"
"v_mfma_f32_16x16x32_bf16 v[144:147], v[32:35], v[236:239], v[144:147]\n"
"v_cvt_pk_bf16_f32 v170, v128, v129\n"
"s_waitcnt lgkmcnt(10)\n"
"v_mfma_f32_16x16x32_bf16 v[136:139], v[36:39], v[80:83], v[136:139]\n"
"ds_read_b64_tr_b16 v[32:33], v12 offset:12800\n"
"ds_read_b64_tr_b16 v[34:35], v12 offset:14848\n"
"v_mfma_f32_16x16x32_bf16 v[140:143], v[36:39], v[208:211], v[140:143]\n"
"v_cvt_pk_bf16_f32 v171, v130, v131\n"
"v_permlane16_swap_b32 v168, v170\n"
"v_mfma_f32_16x16x32_bf16 v[144:147], v[36:39], v[240:243], v[144:147]\n"
"v_permlane16_swap_b32 v169, v171\n"
"buffer_store_dwordx4 v[168:171], v201, s[52:55], s78 offen offset:0 sc1\n"
"v_mfma_f32_16x16x32_bf16 v[136:139], v[40:43], v[84:87], v[136:139]\n"
"ds_read_b64_tr_b16 v[36:37], v12 offset:16896\n"
"ds_read_b64_tr_b16 v[38:39], v12 offset:18944\n"
"v_mfma_f32_16x16x32_bf16 v[140:143], v[40:43], v[212:215], v[140:143]\n"
"v_cvt_pk_bf16_f32 v176, v120, v121\n"
"v_cvt_pk_bf16_f32 v177, v122, v123\n"
"v_mfma_f32_16x16x32_bf16 v[144:147], v[40:43], v[244:247], v[144:147]\n"
"v_cvt_pk_bf16_f32 v178, v132, v133\n"
"s_waitcnt lgkmcnt(10)\n"
"v_mfma_f32_16x16x32_bf16 v[136:139], v[44:47], v[88:91], v[136:139]\n"
"ds_read_b64_tr_b16 v[40:41], v12 offset:20992\n"
"ds_read_b64_tr_b16 v[42:43], v12 offset:23040\n"
"v_mfma_f32_16x16x32_bf16 v[140:143], v[44:47], v[216:219], v[140:143]\n"
"v_cvt_pk_bf16_f32 v179, v134, v135\n"
"v_permlane16_swap_b32 v176, v178\n"
"v_mfma_f32_16x16x32_bf16 v[144:147], v[44:47], v[248:251], v[144:147]\n"
"v_permlane16_swap_b32 v177, v179\n"
"buffer_store_dwordx4 v[176:179], v201, s[52:55], s79 offen offset:0 sc1\n"
"v_mfma_f32_16x16x32_bf16 v[136:139], v[48:51], v[92:95], v[136:139]\n"
"ds_read_b64_tr_b16 v[44:45], v12 offset:25088\n"
"ds_read_b64_tr_b16 v[46:47], v12 offset:27136\n"
"v_mfma_f32_16x16x32_bf16 v[140:143], v[48:51], v[220:223], v[140:143]\n"
"v_mfma_f32_16x16x32_bf16 v[144:147], v[48:51], v[252:255], v[144:147]\n"
"s_waitcnt lgkmcnt(10)\n"
"v_mfma_f32_16x16x32_bf16 v[148:151], v[20:23], v[64:67], 0\n"
"ds_read_b64_tr_b16 v[48:49], v12 offset:29184\n"
"ds_read_b64_tr_b16 v[50:51], v12 offset:31232\n"
"v_mfma_f32_16x16x32_bf16 v[152:155], v[20:23], v[96:99], 0\n"
"v_mfma_f32_16x16x32_bf16 v[156:159], v[20:23], v[224:227], 0\n"
"v_mfma_f32_16x16x32_bf16 v[148:151], v[24:27], v[68:71], v[148:151]\n"
"s_add_u32 s57, s57, 0x8000\n"
"s_cmp_eq_u32 s57, s61\n"
"s_cselect_b32 s57, s60, s57\n"
"v_mfma_f32_16x16x32_bf16 v[152:155], v[24:27], v[100:103], v[152:155]\n"
"v_add_u32 v11, s57, v202\n"
"v_add_u32 v12, s57, v203\n"
"v_mfma_f32_16x16x32_bf16 v[156:159], v[24:27], v[228:231], v[156:159]\n"
"s_waitcnt lgkmcnt(8)\n"
"v_mfma_f32_16x16x32_bf16 v[148:151], v[28:31], v[72:75], v[148:151]\n"
"v_mfma_f32_16x16x32_bf16 v[152:155], v[28:31], v[104:107], v[152:155]\n"
"v_mfma_f32_16x16x32_bf16 v[156:159], v[28:31], v[232:235], v[156:159]\n"
"s_waitcnt vmcnt(30) lgkmcnt(0)\n"
"s_barrier\n"
"v_mfma_f32_16x16x32_bf16 v[148:151], v[32:35], v[76:79], v[148:151]\n"
"ds_read_b64_tr_b16 v[20:21], v11 offset:0\n"
"ds_read_b64_tr_b16 v[22:23], v11 offset:2048\n"
"v_mfma_f32_16x16x32_bf16 v[152:155], v[32:35], v[108:111], v[152:155]\n"
"ds_read_b64_tr_b16 v[24:25], v11 offset:4096\n"
"ds_read_b64_tr_b16 v[26:27], v11 offset:6144\n"
"v_mfma_f32_16x16x32_bf16 v[156:159], v[32:35], v[236:239], v[156:159]\n"
"ds_read_b64_tr_b16 v[28:29], v11 offset:8192\n"
"ds_read_b64_tr_b16 v[30:31], v11 offset:10240\n"
"v_mfma_f32_16x16x32_bf16 v[148:151], v[36:39], v[80:83], v[148:151]\n"
"ds_read_b64_tr_b16 v[32:33], v11 offset:12288\n"
"ds_read_b64_tr_b16 v[34:35], v11 offset:14336\n"
"v_mfma_f32_16x16x32_bf16 v[152:155], v[36:39], v[208:211], v[152:155]\n"
"v_mfma_f32_16x16x32_bf16 v[156:159], v[36:39], v[240:243], v[156:159]\n"
"v_mfma_f32_16x16x32_bf16 v[148:151], v[40:43], v[84:87], v[148:151]\n"
"ds_read_b64_tr_b16 v[36:37], v11 offset:16384\n"
"ds_read_b64_tr_b16 v[38:39], v11 offset:18432\n"
"v_mfma_f32_16x16x32_bf16 v[152:155], v[40:43], v[212:215], v[152:155]\n"
"v_mfma_f32_16x16x32_bf16 v[156:159], v[40:43], v[244:247], v[156:159]\n"
"v_mfma_f32_16x16x32_bf16 v[148:151], v[44:47], v[88:91], v[148:151]\n"
"ds_read_b64_tr_b16 v[40:41], v11 offset:20480\n"
"ds_read_b64_tr_b16 v[42:43], v11 offset:22528\n"
"v_mfma_f32_16x16x32_bf16 v[152:155], v[44:47], v[216:219], v[152:155]\n"
"v_mfma_f32_16x16x32_bf16 v[156:159], v[44:47], v[248:251], v[156:159]\n"
"v_mfma_f32_16x16x32_bf16 v[148:151], v[48:51], v[92:95], v[148:151]\n"
"ds_read_b64_tr_b16 v[44:45], v11 offset:24576\n"
"ds_read_b64_tr_b16 v[46:47], v11 offset:26624\n"
"v_mfma_f32_16x16x32_bf16 v[152:155], v[48:51], v[220:223], v[152:155]\n"
"v_mfma_f32_16x16x32_bf16 v[156:159], v[48:51], v[252:255], v[156:159]\n"
"s_waitcnt lgkmcnt(10)\n"
"v_mfma_f32_16x16x32_bf16 v[112:115], v[20:23], v[64:67], 0\n"
"ds_read_b64_tr_b16 v[48:49], v11 offset:28672\n"
"ds_read_b64_tr_b16 v[50:51], v11 offset:30720\n"
"v_mfma_f32_16x16x32_bf16 v[116:119], v[20:23], v[96:99], 0\n"
"s_add_u32 m0, s56, 0\n"
"v_mfma_f32_16x16x32_bf16 v[120:123], v[20:23], v[224:227], 0\n"
"buffer_load_dwordx4 v204, s[48:51], s83 offen lds\n"
"s_add_u32 s83, s83, 7168\n"
"v_mfma_f32_16x16x32_bf16 v[112:115], v[24:27], v[68:71], v[112:115]\n"
"ds_read_b64_tr_b16 v[20:21], v12 offset:0\n"
"ds_read_b64_tr_b16 v[22:23], v12 offset:2048\n"
"v_mfma_f32_16x16x32_bf16 v[116:119], v[24:27], v[100:103], v[116:119]\n"
"v_cvt_pk_bf16_f32 v164, v136, v137\n"
"v_cvt_pk_bf16_f32 v165, v138, v139\n"
"v_mfma_f32_16x16x32_bf16 v[120:123], v[24:27], v[228:231], v[120:123]\n"
"v_cvt_pk_bf16_f32 v166, v148, v149\n"
"s_waitcnt lgkmcnt(10)\n"
"v_mfma_f32_16x16x32_bf16 v[112:115], v[28:31], v[72:75], v[112:115]\n"
"ds_read_b64_tr_b16 v[24:25], v12 offset:4096\n"
"ds_read_b64_tr_b16 v[26:27], v12 offset:6144\n"
"v_mfma_f32_16x16x32_bf16 v[116:119], v[28:31], v[104:107], v[116:119]\n"
"v_cvt_pk_bf16_f32 v167, v150, v151\n"
"v_permlane16_swap_b32 v164, v166\n"
"v_mfma_f32_16x16x32_bf16 v[120:123], v[28:31], v[232:235], v[120:123]\n"
"v_permlane16_swap_b32 v165, v167\n"
"buffer_store_dwordx4 v[164:167], v201, s[52:55], s77 offen offset:1024 sc1\n"
"v_mfma_f32_16x16x32_bf16 v[112:115], v[32:35], v[76:79], v[112:115]\n"
"ds_read_b64_tr_b16 v[28:29], v12 offset:8192\n"
"ds_read_b64_tr_b16 v[30:31], v12 offset:10240\n"
"v_mfma_f32_16x16x32_bf16 v[116:119], v[32:35], v[108:111], v[116:119]\n"
"v_cvt_pk_bf16_f32 v172, v140, v141\n"
"v_cvt_pk_bf16_f32 v173, v142, v143\n"
"v_mfma_f32_16x16x32_bf16 v[120:123], v[32:35], v[236:239], v[120:123]\n"
"v_cvt_pk_bf16_f32 v174, v152, v153\n"
"s_waitcnt lgkmcnt(10)\n"
"v_mfma_f32_16x16x32_bf16 v[112:115], v[36:39], v[80:83], v[112:115]\n"
"ds_read_b64_tr_b16 v[32:33], v12 offset:12288\n"
"ds_read_b64_tr_b16 v[34:35], v12 offset:14336\n"
"v_mfma_f32_16x16x32_bf16 v[116:119], v[36:39], v[208:211], v[116:119]\n"
"v_cvt_pk_bf16_f32 v175, v154, v155\n"
"v_permlane16_swap_b32 v172, v174\n"
"v_mfma_f32_16x16x32_bf16 v[120:123], v[36:39], v[240:243], v[120:123]\n"
"v_permlane16_swap_b32 v173, v175\n"
"buffer_store_dwordx4 v[172:175], v201, s[52:55], s78 offen offset:1024 sc1\n"
"v_mfma_f32_16x16x32_bf16 v[112:115], v[40:43], v[84:87], v[112:115]\n"
"ds_read_b64_tr_b16 v[36:37], v12 offset:16384\n"
"ds_read_b64_tr_b16 v[38:39], v12 offset:18432\n"
"v_mfma_f32_16x16x32_bf16 v[116:119], v[40:43], v[212:215], v[116:119]\n"
"v_cvt_pk_bf16_f32 v180, v144, v145\n"
"v_cvt_pk_bf16_f32 v181, v146, v147\n"
"v_mfma_f32_16x16x32_bf16 v[120:123], v[40:43], v[244:247], v[120:123]\n"
"v_cvt_pk_bf16_f32 v182, v156, v157\n"
"s_waitcnt lgkmcnt(10)\n"
"v_mfma_f32_16x16x32_bf16 v[112:115], v[44:47], v[88:91], v[112:115]\n"
"ds_read_b64_tr_b16 v[40:41], v12 offset:20480\n"
"ds_read_b64_tr_b16 v[42:43], v12 offset:22528\n"
"v_mfma_f32_16x16x32_bf16 v[116:119], v[44:47], v[216:219], v[116:119]\n"
"v_cvt_pk_bf16_f32 v183, v158, v159\n"
"v_permlane16_swap_b32 v180, v182\n"
"v_mfma_f32_16x16x32_bf16 v[120:123], v[44:47], v[248:251], v[120:123]\n"
"v_permlane16_swap_b32 v181, v183\n"
"buffer_store_dwordx4 v[180:183], v201, s[52:55], s79 offen offset:1024 sc1\n"
"v_mfma_f32_16x16x32_bf16 v[112:115], v[48:51], v[92:95], v[112:115]\n"
"ds_read_b64_tr_b16 v[44:45], v12 offset:24576\n"
"ds_read_b64_tr_b16 v[46:47], v12 offset:26624\n"
"v_mfma_f32_16x16x32_bf16 v[116:119], v[48:51], v[220:223], v[116:119]\n"
"s_add_u32 s52, s52, 22528\n"
"s_addc_u32 s53, s53, 0\n"
"s_sub_u32 s54, s54, 22528\n"
"v_mfma_f32_16x16x32_bf16 v[120:123], v[48:51], v[252:255], v[120:123]\n"
"s_add_u32 m0, s56, 1024\n"
"s_waitcnt lgkmcnt(10)\n"
"v_mfma_f32_16x16x32_bf16 v[124:127], v[20:23], v[64:67], 0\n"
"ds_read_b64_tr_b16 v[48:49], v12 offset:28672\n"
"ds_read_b64_tr_b16 v[50:51], v12 offset:30720\n"
"v_mfma_f32_16x16x32_bf16 v[128:131], v[20:23], v[96:99], 0\n"
"buffer_load_dwordx4 v204, s[48:51], s83 offen lds\n"
"s_add_u32 s83, s83, 7168\n"
"v_mfma_f32_16x16x32_bf16 v[132:135], v[20:23], v[224:227], 0\n"
"s_add_u32 m0, s56, 2048\n"
"v_mfma_f32_16x16x32_bf16 v[124:127], v[24:27], v[68:71], v[124:127]\n"
"ds_read_b64_tr_b16 v[20:21], v11 offset:512\n"
"ds_read_b64_tr_b16 v[22:23], v11 offset:2560\n"
"v_mfma_f32_16x16x32_bf16 v[128:131], v[24:27], v[100:103], v[128:131]\n"
"buffer_load_dwordx4 v204, s[48:51], s83 offen lds\n"
"s_add_u32 s83, s83, 7168\n"
"v_mfma_f32_16x16x32_bf16 v[132:135], v[24:27], v[228:231], v[132:135]\n"
"s_add_u32 m0, s56, 3072\n"
"s_waitcnt lgkmcnt(10)\n"
"v_mfma_f32_16x16x32_bf16 v[124:127], v[28:31], v[72:75], v[124:127]\n"
"ds_read_b64_tr_b16 v[24:25], v11 offset:4608\n"
"ds_read_b64_tr_b16 v[26:27], v11 offset:6656\n"
"v_mfma_f32_16x16x32_bf16 v[128:131], v[28:31], v[104:107], v[128:131]\n"
"buffer_load_dwordx4 v204, s[48:51], s83 offen lds\n"
"s_add_u32 s83, s83, 7168\n"
"v_mfma_f32_16x16x32_bf16 v[132:135], v[28:31], v[232:235], v[132:135]\n"
"s_add_u32 m0, s56, 4096\n"
"v_mfma_f32_16x16x32_bf16 v[124:127], v[32:35], v[76:79], v[124:127]\n"
"ds_read_b64_tr_b16 v[28:29], v11 offset:8704\n"
"ds_read_b64_tr_b16 v[30:31], v11 offset:10752\n"
"v_mfma_f32_16x16x32_bf16 v[128:131], v[32:35], v[108:111], v[128:131]\n"
"buffer_load_dwordx4 v204, s[48:51], s83 offen lds\n"
"s_add_u32 s83, s83, 7168\n"
"v_mfma_f32_16x16x32_bf16 v[132:135], v[32:35], v[236:239], v[132:135]\n"
"s_add_u32 m0, s56, 5120\n"
"s_waitcnt lgkmcnt(10)\n"
"v_mfma_f32_16x16x32_bf16 v[124:127], v[36:39], v[80:83], v[124:127]\n"
"ds_read_b64_tr_b16 v[32:33], v11 offset:12800\n"
"ds_read_b64_tr_b16 v[34:35], v11 offset:14848\n"
"v_mfma_f32_16x16x32_bf16 v[128:131], v[36:39], v[208:211], v[128:131]\n"
"buffer_load_dwordx4 v204, s[48:51], s83 offen lds\n"
"s_add_u32 s83, s83, 7168\n"
"v_mfma_f32_16x16x32_bf16 v[132:135], v[36:39], v[240:243], v[132:135]\n"
"s_add_u32 m0, s56, 6144\n"
"v_mfma_f32_16x16x32_bf16 v[124:127], v[40:43], v[84:87], v[124:127]\n"
"ds_read_b64_tr_b16 v[36:37], v11 offset:16896\n"
"ds_read_b64_tr_b16 v[38:39], v11 offset:18944\n"
"v_mfma_f32_16x16x32_bf16 v[128:131], v[40:43], v[212:215], v[128:131]\n"
"buffer_load_dwordx4 v204, s[48:51], s83 offen lds\n"
"s_add_u32 s83, s83, 7168\n"
"v_mfma_f32_16x16x32_bf16 v[132:135], v[40:43], v[244:247], v[132:135]\n"
"s_add_u32 m0, s56, 7168\n"
"s_waitcnt lgkmcnt(10)\n"
"v_mfma_f32_16x16x32_bf16 v[124:127], v[44:47], v[88:91], v[124:127]\n"
"ds_read_b64_tr_b16 v[40:41], v11 offset:20992\n"
"ds_read_b64_tr_b16 v[42:43], v11 offset:23040\n"
"v_mfma_f32_16x16x32_bf16 v[128:131], v[44:47], v[216:219], v[128:131]\n"
"buffer_load_dwordx4 v204, s[48:51], s83 offen lds\n"
"s_add_u32 s83, s83, 7168\n"
"v_mfma_f32_16x16x32_bf16 v[132:135], v[44:47], v[248:251], v[132:135]\n"
"s_mov_b32 s83, 0\n"
"s_add_u32 s48, s48, 128\n"
"s_addc_u32 s49, s49, 0\n"
"v_mfma_f32_16x16x32_bf16 v[124:127], v[48:51], v[92:95], v[124:127]\n"
"ds_read_b64_tr_b16 v[44:45], v11 offset:25088\n"
"ds_read_b64_tr_b16 v[46:47], v11 offset:27136\n"
"v_mfma_f32_16x16x32_bf16 v[128:131], v[48:51], v[220:223], v[128:131]\n"
"s_sub_u32 s50, s50, 128\n"
"v_mfma_f32_16x16x32_bf16 v[132:135], v[48:51], v[252:255], v[132:135]\n"
"s_waitcnt lgkmcnt(10)\n"
"v_mfma_f32_16x16x32_bf16 v[136:139], v[20:23], v[64:67], 0\n"
"ds_read_b64_tr_b16 v[48:49], v11 offset:29184\n"
"ds_read_b64_tr_b16 v[50:51], v11 offset:31232\n"
"v_mfma_f32_16x16x32_bf16 v[140:143], v[20:23], v[96:99], 0\n"
"s_add_u32 s56, s56, 0x8000\n"
"s_cmp_eq_u32 s56, s59\n"
"s_cselect_b32 s56, s58, s56\n"
"v_mfma_f32_16x16x32_bf16 v[144:147], v[20:23], v[224:227], 0\n"
"v_mfma_f32_16x16x32_bf16 v[136:139], v[24:27], v[68:71], v[136:139]\n"
"ds_read_b64_tr_b16 v[20:21], v12 offset:512\n"
"ds_read_b64_tr_b16 v[22:23], v12 offset:2560\n"
"v_mfma_f32_16x16x32_bf16 v[140:143], v[24:27], v[100:103], v[140:143]\n"
"v_cvt_pk_bf16_f32 v160, v112, v113\n"
"v_cvt_pk_bf16_f32 v161, v114, v115\n"
"v_mfma_f32_16x16x32_bf16 v[144:147], v[24:27], v[228:231], v[144:147]\n"
"v_cvt_pk_bf16_f32 v162, v124, v125\n"
"s_waitcnt lgkmcnt(10)\n"
"v_mfma_f32_16x16x32_bf16 v[136:139], v[28:31], v[72:75], v[136:139]\n"
"ds_read_b64_tr_b16 v[24:25], v12 offset:4608\n"
"ds_read_b64_tr_b16 v[26:27], v12 offset:6656\n"
"v_mfma_f32_16x16x32_bf16 v[140:143], v[28:31], v[104:107], v[140:143]\n"
"v_cvt_pk_bf16_f32 v163, v126, v127\n"
"v_permlane16_swap_b32 v160, v162\n"
"v_mfma_f32_16x16x32_bf16 v[144:147], v[28:31], v[232:235], v[144:147]\n"
"v_permlane16_swap_b32 v161, v163\n"
"buffer_store_dwordx4 v[160:163], v201, s[52:55], s77 offen offset:0 sc1\n"
"v_mfma_f32_16x16x32_bf16 v[136:139], v[32:35], v[76:79], v[136:139]\n"
"ds_read_b64_tr_b16 v[28:29], v12 offset:8704\n"
"ds_read_b64_tr_b16 v[30:31], v12 offset:10752\n"
"v_mfma_f32_16x16x32_bf16 v[140:143], v[32:35], v[108:111], v[140:143]\n"
"v_cvt_pk_bf16_f32 v168, v116, v117\n"
"v_cvt_pk_bf16_f32 v169, v118, v119\n"
"v_mfma_f32_16x16x32_bf16 v[144:147], v[32:35], v[236:239], v[144:147]\n"
"v_cvt_pk_bf16_f32 v170, v128, v129\n"
"s_waitcnt lgkmcnt(10)\n"
"v_mfma_f32_16x16x32_bf16 v[136:139], v[36:39], v[80:83], v[136:139]\n"
"ds_read_b64_tr_b16 v[32:33], v12 offset:12800\n"
"ds_read_b64_tr_b16 v[34:35], v12 offset:14848\n"
"v_mfma_f32_16x16x32_bf16 v[140:143], v[36:39], v[208:211], v[140:143]\n"
"v_cvt_pk_bf16_f32 v171, v130, v131\n"
"v_permlane16_swap_b32 v168, v170\n"
"v_mfma_f32_16x16x32_bf16 v[144:147], v[36:39], v[240:243], v[144:147]\n"
"v_permlane16_swap_b32 v169, v171\n"
"buffer_store_dwordx4 v[168:171], v201, s[52:55], s78 offen offset:0 sc1\n"
"v_mfma_f32_16x16x32_bf16 v[136:139], v[40:43], v[84:87], v[136:139]\n"
"ds_read_b64_tr_b16 v[36:37], v12 offset:16896\n"
"ds_read_b64_tr_b16 v[38:39], v12 offset:18944\n"
"v_mfma_f32_16x16x32_bf16 v[140:143], v[40:43], v[212:215], v[140:143]\n"
"v_cvt_pk_bf16_f32 v176, v120, v121\n"
"v_cvt_pk_bf16_f32 v177, v122, v123\n"
"v_mfma_f32_16x16x32_bf16 v[144:147], v[40:43], v[244:247], v[144:147]\n"
"v_cvt_pk_bf16_f32 v178, v132, v133\n"
"s_waitcnt lgkmcnt(10)\n"
"v_mfma_f32_16x16x32_bf16 v[136:139], v[44:47], v[88:91], v[136:139]\n"
"ds_read_b64_tr_b16 v[40:41], v12 offset:20992\n"
"ds_read_b64_tr_b16 v[42:43], v12 offset:23040\n"
"v_mfma_f32_16x16x32_bf16 v[140:143], v[44:47], v[216:219], v[140:143]\n"
"v_cvt_pk_bf16_f32 v179, v134, v135\n"
"v_permlane16_swap_b32 v176, v178\n"
"v_mfma_f32_16x16x32_bf16 v[144:147], v[44:47], v[248:251], v[144:147]\n"
"v_permlane16_swap_b32 v177, v179\n"
"buffer_store_dwordx4 v[176:179], v201, s[52:55], s79 offen offset:0 sc1\n"
"v_mfma_f32_16x16x32_bf16 v[136:139], v[48:51], v[92:95], v[136:139]\n"
"ds_read_b64_tr_b16 v[44:45], v12 offset:25088\n"
"ds_read_b64_tr_b16 v[46:47], v12 offset:27136\n"
"v_mfma_f32_16x16x32_bf16 v[140:143], v[48:51], v[220:223], v[140:143]\n"
"v_mfma_f32_16x16x32_bf16 v[144:147], v[48:51], v[252:255], v[144:147]\n"
"s_waitcnt lgkmcnt(10)\n"
"v_mfma_f32_16x16x32_bf16 v[148:151], v[20:23], v[64:67], 0\n"
"ds_read_b64_tr_b16 v[48:49], v12 offset:29184\n"
"ds_read_b64_tr_b16 v[50:51], v12 offset:31232\n"
"v_mfma_f32_16x16x32_bf16 v[152:155], v[20:23], v[96:99], 0\n"
"v_mfma_f32_16x16x32_bf16 v[156:159], v[20:23], v[224:227], 0\n"
"v_mfma_f32_16x16x32_bf16 v[148:151], v[24:27], v[68:71], v[148:151]\n"
"s_add_u32 s57, s57, 0x8000\n"
"s_cmp_eq_u32 s57, s61\n"
"s_cselect_b32 s57, s60, s57\n"
"v_mfma_f32_16x16x32_bf16 v[152:155], v[24:27], v[100:103], v[152:155]\n"
"v_add_u32 v11, s57, v202\n"
"v_add_u32 v12, s57, v203\n"
"v_mfma_f32_16x16x32_bf16 v[156:159], v[24:27], v[228:231], v[156:159]\n"
"s_waitcnt lgkmcnt(8)\n"
"v_mfma_f32_16x16x32_bf16 v[148:151], v[28:31], v[72:75], v[148:151]\n"
"v_mfma_f32_16x16x32_bf16 v[152:155], v[28:31], v[104:107], v[152:155]\n"
"v_mfma_f32_16x16x32_bf16 v[156:159], v[28:31], v[232:235], v[156:159]\n"
"s_waitcnt vmcnt(36) lgkmcnt(0)\n"
"s_barrier\n"
"v_mfma_f32_16x16x32_bf16 v[148:151], v[32:35], v[76:79], v[148:151]\n"
"ds_read_b64_tr_b16 v[20:21], v11 offset:0\n"
"ds_read_b64_tr_b16 v[22:23], v11 offset:2048\n"
"v_mfma_f32_16x16x32_bf16 v[152:155], v[32:35], v[108:111], v[152:155]\n"
"ds_read_b64_tr_b16 v[24:25], v11 offset:4096\n"
"ds_read_b64_tr_b16 v[26:27], v11 offset:6144\n"
"v_mfma_f32_16x16x32_bf16 v[156:159], v[32:35], v[236:239], v[156:159]\n"
"ds_read_b64_tr_b16 v[28:29], v11 offset:8192\n"
"ds_read_b64_tr_b16 v[30:31], v11 offset:10240\n"
"v_mfma_f32_16x16x32_bf16 v[148:151], v[36:39], v[80:83], v[148:151]\n"
"ds_read_b64_tr_b16 v[32:33], v11 offset:12288\n"
"ds_read_b64_tr_b16 v[34:35], v11 offset:14336\n"
"v_mfma_f32_16x16x32_bf16 v[152:155], v[36:39], v[208:211], v[152:155]\n"
"v_mfma_f32_16x16x32_bf16 v[156:159], v[36:39], v[240:243], v[156:159]\n"
"v_mfma_f32_16x16x32_bf16 v[148:151], v[40:43], v[84:87], v[148:151]\n"
"ds_read_b64_tr_b16 v[36:37], v11 offset:16384\n"
"ds_read_b64_tr_b16 v[38:39], v11 offset:18432\n"
"v_mfma_f32_16x16x32_bf16 v[152:155], v[40:43], v[212:215], v[152:155]\n"
"v_mfma_f32_16x16x32_bf16 v[156:159], v[40:43], v[244:247], v[156:159]\n"
"v_mfma_f32_16x16x32_bf16 v[148:151], v[44:47], v[88:91], v[148:151]\n"
"ds_read_b64_tr_b16 v[40:41], v11 offset:20480\n"
"ds_read_b64_tr_b16 v[42:43], v11 offset:22528\n"
"v_mfma_f32_16x16x32_bf16 v[152:155], v[44:47], v[216:219], v[152:155]\n"
"v_mfma_f32_16x16x32_bf16 v[156:159], v[44:47], v[248:251], v[156:159]\n"
"v_mfma_f32_16x16x32_bf16 v[148:151], v[48:51], v[92:95], v[148:151]\n"
"ds_read_b64_tr_b16 v[44:45], v11 offset:24576\n"
"ds_read_b64_tr_b16 v[46:47], v11 offset:26624\n"
"v_mfma_f32_16x16x32_bf16 v[152:155], v[48:51], v[220:223], v[152:155]\n"
"v_mfma_f32_16x16x32_bf16 v[156:159], v[48:51], v[252:255], v[156:159]\n"
"s_waitcnt lgkmcnt(10)\n"
"v_mfma_f32_16x16x32_bf16 v[112:115], v[20:23], v[64:67], 0\n"
"ds_read_b64_tr_b16 v[48:49], v11 offset:28672\n"
"ds_read_b64_tr_b16 v[50:51], v11 offset:30720\n"
"v_mfma_f32_16x16x32_bf16 v[116:119], v[20:23], v[96:99], 0\n"
"s_add_u32 m0, s56, 0\n"
"v_mfma_f32_16x16x32_bf16 v[120:123], v[20:23], v[224:227], 0\n"
"buffer_load_dwordx4 v204, s[48:51], s83 offen lds\n"
"s_add_u32 s83, s83, 7168\n"
"v_mfma_f32_16x16x32_bf16 v[112:115], v[24:27], v[68:71], v[112:115]\n"
"ds_read_b64_tr_b16 v[20:21], v12 offset:0\n"
"ds_read_b64_tr_b16 v[22:23], v12 offset:2048\n"
"v_mfma_f32_16x16x32_bf16 v[116:119], v[24:27], v[100:103], v[116:119]\n"
"v_cvt_pk_bf16_f32 v164, v136, v137\n"
"v_cvt_pk_bf16_f32 v165, v138, v139\n"
"v_mfma_f32_16x16x32_bf16 v[120:123], v[24:27], v[228:231], v[120:123]\n"
"v_cvt_pk_bf16_f32 v166, v148, v149\n"
"s_waitcnt lgkmcnt(10)\n"
"v_mfma_f32_16x16x32_bf16 v[112:115], v[28:31], v[72:75], v[112:115]\n"
"ds_read_b64_tr_b16 v[24:25], v12 offset:4096\n"
"ds_read_b64_tr_b16 v[26:27], v12 offset:6144\n"
"v_mfma_f32_16x16x32_bf16 v[116:119], v[28:31], v[104:107], v[116:119]\n"
"v_cvt_pk_bf16_f32 v167, v150, v151\n"
"v_permlane16_swap_b32 v164, v166\n"
"v_mfma_f32_16x16x32_bf16 v[120:123], v[28:31], v[232:235], v[120:123]\n"
"v_permlane16_swap_b32 v165, v167\n"
"buffer_store_dwordx4 v[164:167], v201, s[52:55], s77 offen offset:1024 sc1\n"
"v_mfma_f32_16x16x32_bf16 v[112:115], v[32:35], v[76:79], v[112:115]\n"
"ds_read_b64_tr_b16 v[28:29], v12 offset:8192\n"
"ds_read_b64_tr_b16 v[30:31], v12 offset:10240\n"
"v_mfma_f32_16x16x32_bf16 v[116:119], v[32:35], v[108:111], v[116:119]\n"
"v_cvt_pk_bf16_f32 v172, v140, v141\n"
"v_cvt_pk_bf16_f32 v173, v142, v143\n"
"v_mfma_f32_16x16x32_bf16 v[120:123], v[32:35], v[236:239], v[120:123]\n"
"v_cvt_pk_bf16_f32 v174, v152, v153\n"
"s_waitcnt lgkmcnt(10)\n"
"v_mfma_f32_16x16x32_bf16 v[112:115], v[36:39], v[80:83], v[112:115]\n"
"ds_read_b64_tr_b16 v[32:33], v12 offset:12288\n"
"ds_read_b64_tr_b16 v[34:35], v12 offset:14336\n"
"v_mfma_f32_16x16x32_bf16 v[116:119], v[36:39], v[208:211], v[116:119]\n"
"v_cvt_pk_bf16_f32 v175, v154, v155\n"
"v_permlane16_swap_b32 v172, v174\n"
"v_mfma_f32_16x16x32_bf16 v[120:123], v[36:39], v[240:243], v[120:123]\n"
"v_permlane16_swap_b32 v173, v175\n"
"buffer_store_dwordx4 v[172:175], v201, s[52:55], s78 offen offset:1024 sc1\n"
"v_mfma_f32_16x16x32_bf16 v[112:115], v[40:43], v[84:87], v[112:115]\n"
"ds_read_b64_tr_b16 v[36:37], v12 offset:16384\n"
"ds_read_b64_tr_b16 v[38:39], v12 offset:18432\n"
"v_mfma_f32_16x16x32_bf16 v[116:119], v[40:43], v[212:215], v[116:119]\n"
"v_cvt_pk_bf16_f32 v180, v144, v145\n"
"v_cvt_pk_bf16_f32 v181, v146, v147\n"
"v_mfma_f32_16x16x32_bf16 v[120:123], v[40:43], v[244:247], v[120:123]\n"
"v_cvt_pk_bf16_f32 v182, v156, v157\n"
"s_waitcnt lgkmcnt(10)\n"
"v_mfma_f32_16x16x32_bf16 v[112:115], v[44:47], v[88:91], v[112:115]\n"
"ds_read_b64_tr_b16 v[40:41], v12 offset:20480\n"
"ds_read_b64_tr_b16 v[42:43], v12 offset:22528\n"
"v_mfma_f32_16x16x32_bf16 v[116:119], v[44:47], v[216:219], v[116:119]\n"
"v_cvt_pk_bf16_f32 v183, v158, v159\n"
"v_permlane16_swap_b32 v180, v182\n"
"v_mfma_f32_16x16x32_bf16 v[120:123], v[44:47], v[248:251], v[120:123]\n"
"v_permlane16_swap_b32 v181, v183\n"
"buffer_store_dwordx4 v[180:183], v201, s[52:55], s79 offen offset:1024 sc1\n"
"v_mfma_f32_16x16x32_bf16 v[112:115], v[48:51], v[92:95], v[112:115]\n"
"ds_read_b64_tr_b16 v[44:45], v12 offset:24576\n"
"ds_read_b64_tr_b16 v[46:47], v12 offset:26624\n"
"v_mfma_f32_16x16x32_bf16 v[116:119], v[48:51], v[220:223], v[116:119]\n"
"s_add_u32 s52, s52, 22528\n"
"s_addc_u32 s53, s53, 0\n"
"s_sub_u32 s54, s54, 22528\n"
"v_mfma_f32_16x16x32_bf16 v[120:123], v[48:51], v[252:255], v[120:123]\n"
"s_add_u32 m0, s56, 1024\n"
"s_waitcnt lgkmcnt(10)\n"
"v_mfma_f32_16x16x32_bf16 v[124:127], v[20:23], v[64:67], 0\n"
"ds_read_b64_tr_b16 v[48:49], v12 offset:28672\n"
"ds_read_b64_tr_b16 v[50:51], v12 offset:30720\n"
"v_mfma_f32_16x16x32_bf16 v[128:131], v[20:23], v[96:99], 0\n"
"buffer_load_dwordx4 v204, s[48:51], s83 offen lds\n"
"s_add_u32 s83, s83, 7168\n"
"v_mfma_f32_16x16x32_bf16 v[132:135], v[20:23], v[224:227], 0\n"
"s_add_u32 m0, s56, 2048\n"
"v_mfma_f32_16x16x32_bf16 v[124:127], v[24:27], v[68:71], v[124:127]\n"
"ds_read_b64_tr_b16 v[20:21], v11 offset:512\n"
"ds_read_b64_tr_b16 v[22:23], v11 offset:2560\n"
"v_mfma_f32_16x16x32_bf16 v[128:131], v[24:27], v[100:103], v[128:131]\n"
"buffer_load_dwordx4 v204, s[48:51], s83 offen lds\n"
"s_add_u32 s83, s83, 7168\n"
"v_mfma_f32_16x16x32_bf16 v[132:135], v[24:27], v[228:231], v[132:135]\n"
"s_add_u32 m0, s56, 3072\n"
"s_waitcnt lgkmcnt(10)\n"
"v_mfma_f32_16x16x32_bf16 v[124:127], v[28:31], v[72:75], v[124:127]\n"
"ds_read_b64_tr_b16 v[24:25], v11 offset:4608\n"
"ds_read_b64_tr_b16 v[26:27], v11 offset:6656\n"
"v_mfma_f32_16x16x32_bf16 v[128:131], v[28:31], v[104:107], v[128:131]\n"
"buffer_load_dwordx4 v204, s[48:51], s83 offen lds\n"
"s_add_u32 s83, s83, 7168\n"
"v_mfma_f32_16x16x32_bf16 v[132:135], v[28:31], v[232:235], v[132:135]\n"
"s_add_u32 m0, s56, 4096\n"
"v_mfma_f32_16x16x32_bf16 v[124:127], v[32:35], v[76:79], v[124:127]\n"
"ds_read_b64_tr_b16 v[28:29], v11 offset:8704\n"
"ds_read_b64_tr_b16 v[30:31], v11 offset:10752\n"
"v_mfma_f32_16x16x32_bf16 v[128:131], v[32:35], v[108:111], v[128:131]\n"
"buffer_load_dwordx4 v204, s[48:51], s83 offen lds\n"
"s_add_u32 s83, s83, 7168\n"
"v_mfma_f32_16x16x32_bf16 v[132:135], v[32:35], v[236:239], v[132:135]\n"
"s_add_u32 m0, s56, 5120\n"
"s_waitcnt lgkmcnt(10)\n"
"v_mfma_f32_16x16x32_bf16 v[124:127], v[36:39], v[80:83], v[124:127]\n"
"ds_read_b64_tr_b16 v[32:33], v11 offset:12800\n"
"ds_read_b64_tr_b16 v[34:35], v11 offset:14848\n"
"v_mfma_f32_16x16x32_bf16 v[128:131], v[36:39], v[208:211], v[128:131]\n"
"buffer_load_dwordx4 v204, s[48:51], s83 offen lds\n"
"s_add_u32 s83, s83, 7168\n"
"v_mfma_f32_16x16x32_bf16 v[132:135], v[36:39], v[240:243], v[132:135]\n"
"s_add_u32 m0, s56, 6144\n"
"v_mfma_f32_16x16x32_bf16 v[124:127], v[40:43], v[84:87], v[124:127]\n"
"ds_read_b64_tr_b16 v[36:37], v11 offset:16896\n"
"ds_read_b64_tr_b16 v[38:39], v11 offset:18944\n"
"v_mfma_f32_16x16x32_bf16 v[128:131], v[40:43], v[212:215], v[128:131]\n"
"buffer_load_dwordx4 v204, s[48:51], s83 offen lds\n"
"s_add_u32 s83, s83, 7168\n"
"v_mfma_f32_16x16x32_bf16 v[132:135], v[40:43], v[244:247], v[132:135]\n"
"s_add_u32 m0, s56, 7168\n"
"s_waitcnt lgkmcnt(10)\n"
"v_mfma_f32_16x16x32_bf16 v[124:127], v[44:47], v[88:91], v[124:127]\n"
"ds_read_b64_tr_b16 v[40:41], v11 offset:20992\n"
"ds_read_b64_tr_b16 v[42:43], v11 offset:23040\n"
"v_mfma_f32_16x16x32_bf16 v[128:131], v[44:47], v[216:219], v[128:131]\n"
"buffer_load_dwordx4 v204, s[48:51], s83 offen lds\n"
"s_add_u32 s83, s83, 7168\n"
"v_mfma_f32_16x16x32_bf16 v[132:135], v[44:47], v[248:251], v[132:135]\n"
"s_mov_b32 s83, 0\n"
"s_add_u32 s48, s48, 128\n"
"s_addc_u32 s49, s49, 0\n"
"v_mfma_f32_16x16x32_bf16 v[124:127], v[48:51], v[92:95], v[124:127]\n"
"ds_read_b64_tr_b16 v[44:45], v11 offset:25088\n"
"ds_read_b64_tr_b16 v[46:47], v11 offset:27136\n"
"v_mfma_f32_16x16x32_bf16 v[128:131], v[48:51], v[220:223], v[128:131]\n"
"s_sub_u32 s50, s50, 128\n"
"v_mfma_f32_16x16x32_bf16 v[132:135], v[48:51], v[252:255], v[132:135]\n"
"s_waitcnt lgkmcnt(10)\n"
"v_mfma_f32_16x16x32_bf16 v[136:139], v[20:23], v[64:67], 0\n"
"ds_read_b64_tr_b16 v[48:49], v11 offset:29184\n"
"ds_read_b64_tr_b16 v[50:51], v11 offset:31232\n"
"v_mfma_f32_16x16x32_bf16 v[140:143], v[20:23], v[96:99], 0\n"
"s_add_u32 s56, s56, 0x8000\n"
"s_cmp_eq_u32 s56, s59\n"
"s_cselect_b32 s56, s58, s56\n"
"v_mfma_f32_16x16x32_bf16 v[144:147], v[20:23], v[224:227], 0\n"
"v_mfma_f32_16x16x32_bf16 v[136:139], v[24:27], v[68:71], v[136:139]\n"
"ds_read_b64_tr_b16 v[20:21], v12 offset:512\n"
"ds_read_b64_tr_b16 v[22:23], v12 offset:2560\n"
"v_mfma_f32_16x16x32_bf16 v[140:143], v[24:27], v[100:103], v[140:143]\n"
"v_cvt_pk_bf16_f32 v160, v112, v113\n"
"v_cvt_pk_bf16_f32 v161, v114, v115\n"
"v_mfma_f32_16x16x32_bf16 v[144:147], v[24:27], v[228:231], v[144:147]\n"
"v_cvt_pk_bf16_f32 v162, v124, v125\n"
"s_waitcnt lgkmcnt(10)\n"
"v_mfma_f32_16x16x32_bf16 v[136:139], v[28:31], v[72:75], v[136:139]\n"
"ds_read_b64_tr_b16 v[24:25], v12 offset:4608\n"
"ds_read_b64_tr_b16 v[26:27], v12 offset:6656\n"
"v_mfma_f32_16x16x32_bf16 v[140:143], v[28:31], v[104:107], v[140:143]\n"
"v_cvt_pk_bf16_f32 v163, v126, v127\n"
"v_permlane16_swap_b32 v160, v162\n"
"v_mfma_f32_16x16x32_bf16 v[144:147], v[28:31], v[232:235], v[144:147]\n"
"v_permlane16_swap_b32 v161, v163\n"
"buffer_store_dwordx4 v[160:163], v201, s[52:55], s77 offen offset:0 sc1\n"
"v_mfma_f32_16x16x32_bf16 v[136:139], v[32:35], v[76:79], v[136:139]\n"
"ds_read_b64_tr_b16 v[28:29], v12 offset:8704\n"
"ds_read_b64_tr_b16 v[30:31], v12 offset:10752\n"
"v_mfma_f32_16x16x32_bf16 v[140:143], v[32:35], v[108:111], v[140:143]\n"
"v_cvt_pk_bf16_f32 v168, v116, v117\n"
"v_cvt_pk_bf16_f32 v169, v118, v119\n"
"v_mfma_f32_16x16x32_bf16 v[144:147], v[32:35], v[236:239], v[144:147]\n"
"v_cvt_pk_bf16_f32 v170, v128, v129\n"
"s_waitcnt lgkmcnt(10)\n"
"v_mfma_f32_16x16x32_bf16 v[136:139], v[36:39], v[80:83], v[136:139]\n"
"ds_read_b64_tr_b16 v[32:33], v12 offset:12800\n"
"ds_read_b64_tr_b16 v[34:35], v12 offset:14848\n"
"v_mfma_f32_16x16x32_bf16 v[140:143], v[36:39], v[208:211], v[140:143]\n"
"v_cvt_pk_bf16_f32 v171, v130, v131\n"
"v_permlane16_swap_b32 v168, v170\n"
"v_mfma_f32_16x16x32_bf16 v[144:147], v[36:39], v[240:243], v[144:147]\n"
"v_permlane16_swap_b32 v169, v171\n"
"buffer_store_dwordx4 v[168:171], v201, s[52:55], s78 offen offset:0 sc1\n"
"v_mfma_f32_16x16x32_bf16 v[136:139], v[40:43], v[84:87], v[136:139]\n"
"ds_read_b64_tr_b16 v[36:37], v12 offset:16896\n"
"ds_read_b64_tr_b16 v[38:39], v12 offset:18944\n"
"v_mfma_f32_16x16x32_bf16 v[140:143], v[40:43], v[212:215], v[140:143]\n"
"v_cvt_pk_bf16_f32 v176, v120, v121\n"
"v_cvt_pk_bf16_f32 v177, v122, v123\n"
"v_mfma_f32_16x16x32_bf16 v[144:147], v[40:43], v[244:247], v[144:147]\n"
"v_cvt_pk_bf16_f32 v178, v132, v133\n"
"s_waitcnt lgkmcnt(10)\n"
"v_mfma_f32_16x16x32_bf16 v[136:139], v[44:47], v[88:91], v[136:139]\n"
"ds_read_b64_tr_b16 v[40:41], v12 offset:20992\n"
"ds_read_b64_tr_b16 v[42:43], v12 offset:23040\n"
"v_mfma_f32_16x16x32_bf16 v[140:143], v[44:47], v[216:219], v[140:143]\n"
"v_cvt_pk_bf16_f32 v179, v134, v135\n"
"v_permlane16_swap_b32 v176, v178\n"
"v_mfma_f32_16x16x32_bf16 v[144:147], v[44:47], v[248:251], v[144:147]\n"
"v_permlane16_swap_b32 v177, v179\n"
"buffer_store_dwordx4 v[176:179], v201, s[52:55], s79 offen offset:0 sc1\n"
"v_mfma_f32_16x16x32_bf16 v[136:139], v[48:51], v[92:95], v[136:139]\n"
"ds_read_b64_tr_b16 v[44:45], v12 offset:25088\n"
"ds_read_b64_tr_b16 v[46:47], v12 offset:27136\n"
"v_mfma_f32_16x16x32_bf16 v[140:143], v[48:51], v[220:223], v[140:143]\n"
"v_mfma_f32_16x16x32_bf16 v[144:147], v[48:51], v[252:255], v[144:147]\n"
"s_waitcnt lgkmcnt(10)\n"
"v_mfma_f32_16x16x32_bf16 v[148:151], v[20:23], v[64:67], 0\n"
"ds_read_b64_tr_b16 v[48:49], v12 offset:29184\n"
"ds_read_b64_tr_b16 v[50:51], v12 offset:31232\n"
"v_mfma_f32_16x16x32_bf16 v[152:155], v[20:23], v[96:99], 0\n"
"v_mfma_f32_16x16x32_bf16 v[156:159], v[20:23], v[224:227], 0\n"
"v_mfma_f32_16x16x32_bf16 v[148:151], v[24:27], v[68:71], v[148:151]\n"
"s_add_u32 s57, s57, 0x8000\n"
"s_cmp_eq_u32 s57, s61\n"
"s_cselect_b32 s57, s60, s57\n"
"v_mfma_f32_16x16x32_bf16 v[152:155], v[24:27], v[100:103], v[152:155]\n"
"v_add_u32 v11, s57, v202\n"
"v_add_u32 v12, s57, v203\n"
"v_mfma_f32_16x16x32_bf16 v[156:159], v[24:27], v[228:231], v[156:159]\n"
"s_waitcnt lgkmcnt(8)\n"
"v_mfma_f32_16x16x32_bf16 v[148:151], v[28:31], v[72:75], v[148:151]\n"
"v_mfma_f32_16x16x32_bf16 v[152:155], v[28:31], v[104:107], v[152:155]\n"
"v_mfma_f32_16x16x32_bf16 v[156:159], v[28:31], v[232:235], v[156:159]\n"
"s_waitcnt vmcnt(42) lgkmcnt(0)\n"
"s_barrier\n"
"v_mfma_f32_16x16x32_bf16 v[148:151], v[32:35], v[76:79], v[148:151]\n"
"ds_read_b64_tr_b16 v[20:21], v11 offset:0\n"
"ds_read_b64_tr_b16 v[22:23], v11 offset:2048\n"
"v_mfma_f32_16x16x32_bf16 v[152:155], v[32:35], v[108:111], v[152:155]\n"
"ds_read_b64_tr_b16 v[24:25], v11 offset:4096\n"
"ds_read_b64_tr_b16 v[26:27], v11 offset:6144\n"
"v_mfma_f32_16x16x32_bf16 v[156:159], v[32:35], v[236:239], v[156:159]\n"
"ds_read_b64_tr_b16 v[28:29], v11 offset:8192\n"
"ds_read_b64_tr_b16 v[30:31], v11 offset:10240\n"
"v_mfma_f32_16x16x32_bf16 v[148:151], v[36:39], v[80:83], v[148:151]\n"
"ds_read_b64_tr_b16 v[32:33], v11 offset:12288\n"
"ds_read_b64_tr_b16 v[34:35], v11 offset:14336\n"
"v_mfma_f32_16x16x32_bf16 v[152:155], v[36:39], v[208:211], v[152:155]\n"
"v_mfma_f32_16x16x32_bf16 v[156:159], v[36:39], v[240:243], v[156:159]\n"
"v_mfma_f32_16x16x32_bf16 v[148:151], v[40:43], v[84:87], v[148:151]\n"
"ds_read_b64_tr_b16 v[36:37], v11 offset:16384\n"
"ds_read_b64_tr_b16 v[38:39], v11 offset:18432\n"
"v_mfma_f32_16x16x32_bf16 v[152:155], v[40:43], v[212:215], v[152:155]\n"
"v_mfma_f32_16x16x32_bf16 v[156:159], v[40:43], v[244:247], v[156:159]\n"
"v_mfma_f32_16x16x32_bf16 v[148:151], v[44:47], v[88:91], v[148:151]\n"
"ds_read_b64_tr_b16 v[40:41], v11 offset:20480\n"
"ds_read_b64_tr_b16 v[42:43], v11 offset:22528\n"
"v_mfma_f32_16x16x32_bf16 v[152:155], v[44:47], v[216:219], v[152:155]\n"
"v_mfma_f32_16x16x32_bf16 v[156:159], v[44:47], v[248:251], v[156:159]\n"
"v_mfma_f32_16x16x32_bf16 v[148:151], v[48:51], v[92:95], v[148:151]\n"
"ds_read_b64_tr_b16 v[44:45], v11 offset:24576\n"
"ds_read_b64_tr_b16 v[46:47], v11 offset:26624\n"
"v_mfma_f32_16x16x32_bf16 v[152:155], v[48:51], v[220:223], v[152:155]\n"
"v_mfma_f32_16x16x32_bf16 v[156:159], v[48:51], v[252:255], v[156:159]\n"
"s_waitcnt lgkmcnt(10)\n"
"v_mfma_f32_16x16x32_bf16 v[112:115], v[20:23], v[64:67], 0\n"
"ds_read_b64_tr_b16 v[48:49], v11 offset:28672\n"
"ds_read_b64_tr_b16 v[50:51], v11 offset:30720\n"
"v_mfma_f32_16x16x32_bf16 v[116:119], v[20:23], v[96:99], 0\n"
"v_mfma_f32_16x16x32_bf16 v[120:123], v[20:23], v[224:227], 0\n"
"v_mfma_f32_16x16x32_bf16 v[112:115], v[24:27], v[68:71], v[112:115]\n"
"ds_read_b64_tr_b16 v[20:21], v12 offset:0\n"
"ds_read_b64_tr_b16 v[22:23], v12 offset:2048\n"
"v_mfma_f32_16x16x32_bf16 v[116:119], v[24:27], v[100:103], v[116:119]\n"
"v_cvt_pk_bf16_f32 v164, v136, v137\n"
"v_cvt_pk_bf16_f32 v165, v138, v139\n"
"v_mfma_f32_16x16x32_bf16 v[120:123], v[24:27], v[228:231], v[120:123]\n"
"v_cvt_pk_bf16_f32 v166, v148, v149\n"
"s_waitcnt lgkmcnt(10)\n"
"v_mfma_f32_16x16x32_bf16 v[112:115], v[28:31], v[72:75], v[112:115]\n"
"ds_read_b64_tr_b16 v[24:25], v12 offset:4096\n"
"ds_read_b64_tr_b16 v[26:27], v12 offset:6144\n"
"v_mfma_f32_16x16x32_bf16 v[116:119], v[28:31], v[104:107], v[116:119]\n"
"v_cvt_pk_bf16_f32 v167, v150, v151\n"
"v_permlane16_swap_b32 v164, v166\n"
"v_mfma_f32_16x16x32_bf16 v[120:123], v[28:31], v[232:235], v[120:123]\n"
"v_permlane16_swap_b32 v165, v167\n"
"buffer_store_dwordx4 v[164:167], v201, s[52:55], s77 offen offset:1024 sc1\n"
"v_mfma_f32_16x16x32_bf16 v[112:115], v[32:35], v[76:79], v[112:115]\n"
"ds_read_b64_tr_b16 v[28:29], v12 offset:8192\n"
"ds_read_b64_tr_b16 v[30:31], v12 offset:10240\n"
"v_mfma_f32_16x16x32_bf16 v[116:119], v[32:35], v[108:111], v[116:119]\n"
"v_cvt_pk_bf16_f32 v172, v140, v141\n"
"v_cvt_pk_bf16_f32 v173, v142, v143\n"
"v_mfma_f32_16x16x32_bf16 v[120:123], v[32:35], v[236:239], v[120:123]\n"
"v_cvt_pk_bf16_f32 v174, v152, v153\n"
"s_waitcnt lgkmcnt(10)\n"
"v_mfma_f32_16x16x32_bf16 v[112:115], v[36:39], v[80:83], v[112:115]\n"
"ds_read_b64_tr_b16 v[32:33], v12 offset:12288\n"
"ds_read_b64_tr_b16 v[34:35], v12 offset:14336\n"
"v_mfma_f32_16x16x32_bf16 v[116:119], v[36:39], v[208:211], v[116:119]\n"
"v_cvt_pk_bf16_f32 v175, v154, v155\n"
"v_permlane16_swap_b32 v172, v174\n"
"v_mfma_f32_16x16x32_bf16 v[120:123], v[36:39], v[240:243], v[120:123]\n"
"v_permlane16_swap_b32 v173, v175\n"
"buffer_store_dwordx4 v[172:175], v201, s[52:55], s78 offen offset:1024 sc1\n"
"v_mfma_f32_16x16x32_bf16 v[112:115], v[40:43], v[84:87], v[112:115]\n"
"ds_read_b64_tr_b16 v[36:37], v12 offset:16384\n"
"ds_read_b64_tr_b16 v[38:39], v12 offset:18432\n"
"v_mfma_f32_16x16x32_bf16 v[116:119], v[40:43], v[212:215], v[116:119]\n"
"v_cvt_pk_bf16_f32 v180, v144, v145\n"
"v_cvt_pk_bf16_f32 v181, v146, v147\n"
"v_mfma_f32_16x16x32_bf16 v[120:123], v[40:43], v[244:247], v[120:123]\n"
"v_cvt_pk_bf16_f32 v182, v156, v157\n"
"s_waitcnt lgkmcnt(10)\n"
"v_mfma_f32_16x16x32_bf16 v[112:115], v[44:47], v[88:91], v[112:115]\n"
"ds_read_b64_tr_b16 v[40:41], v12 offset:20480\n"
"ds_read_b64_tr_b16 v[42:43], v12 offset:22528\n"
"v_mfma_f32_16x16x32_bf16 v[116:119], v[44:47], v[216:219], v[116:119]\n"
"v_cvt_pk_bf16_f32 v183, v158, v159\n"
"v_permlane16_swap_b32 v180, v182\n"
"v_mfma_f32_16x16x32_bf16 v[120:123], v[44:47], v[248:251], v[120:123]\n"
"v_permlane16_swap_b32 v181, v183\n"
"buffer_store_dwordx4 v[180:183], v201, s[52:55], s79 offen offset:1024 sc1\n"
"v_mfma_f32_16x16x32_bf16 v[112:115], v[48:51], v[92:95], v[112:115]\n"
"ds_read_b64_tr_b16 v[44:45], v12 offset:24576\n"
"ds_read_b64_tr_b16 v[46:47], v12 offset:26624\n"
"v_mfma_f32_16x16x32_bf16 v[116:119], v[48:51], v[220:223], v[116:119]\n"
"s_add_u32 s52, s52, 22528\n"
"s_addc_u32 s53, s53, 0\n"
"s_sub_u32 s54, s54, 22528\n"
"v_mfma_f32_16x16x32_bf16 v[120:123], v[48:51], v[252:255], v[120:123]\n"
"s_waitcnt lgkmcnt(10)\n"
"v_mfma_f32_16x16x32_bf16 v[124:127], v[20:23], v[64:67], 0\n"
"ds_read_b64_tr_b16 v[48:49], v12 offset:28672\n"
"ds_read_b64_tr_b16 v[50:51], v12 offset:30720\n"
"v_mfma_f32_16x16x32_bf16 v[128:131], v[20:23], v[96:99], 0\n"
"v_mfma_f32_16x16x32_bf16 v[132:135], v[20:23], v[224:227], 0\n"
"v_mfma_f32_16x16x32_bf16 v[124:127], v[24:27], v[68:71], v[124:127]\n"
"ds_read_b64_tr_b16 v[20:21], v11 offset:512\n"
"ds_read_b64_tr_b16 v[22:23], v11 offset:2560\n"
"v_mfma_f32_16x16x32_bf16 v[128:131], v[24:27], v[100:103], v[128:131]\n"
"v_mfma_f32_16x16x32_bf16 v[132:135], v[24:27], v[228:231], v[132:135]\n"
"s_waitcnt lgkmcnt(10)\n"
"v_mfma_f32_16x16x32_bf16 v[124:127], v[28:31], v[72:75], v[124:127]\n"
"ds_read_b64_tr_b16 v[24:25], v11 offset:4608\n"
"ds_read_b64_tr_b16 v[26:27], v11 offset:6656\n"
"v_mfma_f32_16x16x32_bf16 v[128:131], v[28:31], v[104:107], v[128:131]\n"
"v_mfma_f32_16x16x32_bf16 v[132:135], v[28:31], v[232:235], v[132:135]\n"
"v_mfma_f32_16x16x32_bf16 v[124:127], v[32:35], v[76:79], v[124:127]\n"
"ds_read_b64_tr_b16 v[28:29], v11 offset:8704\n"
"ds_read_b64_tr_b16 v[30:31], v11 offset:10752\n"
"v_mfma_f32_16x16x32_bf16 v[128:131], v[32:35], v[108:111], v[128:131]\n"
"v_mfma_f32_16x16x32_bf16 v[132:135], v[32:35], v[236:239], v[132:135]\n"
"s_waitcnt lgkmcnt(10)\n"
"v_mfma_f32_16x16x32_bf16 v[124:127], v[36:39], v[80:83], v[124:127]\n"
"ds_read_b64_tr_b16 v[32:33], v11 offset:12800\n"
"ds_read_b64_tr_b16 v[34:35], v11 offset:14848\n"
"v_mfma_f32_16x16x32_bf16 v[128:131], v[36:39], v[208:211], v[128:131]\n"
"v_mfma_f32_16x16x32_bf16 v[132:135], v[36:39], v[240:243], v[132:135]\n"
"v_mfma_f32_16x16x32_bf16 v[124:127], v[40:43], v[84:87], v[124:127]\n"
"ds_read_b64_tr_b16 v[36:37], v11 offset:16896\n"
"ds_read_b64_tr_b16 v[38:39], v11 offset:18944\n"
"v_mfma_f32_16x16x32_bf16 v[128:131], v[40:43], v[212:215], v[128:131]\n"
"v_mfma_f32_16x16x32_bf16 v[132:135], v[40:43], v[244:247], v[132:135]\n"
"s_waitcnt lgkmcnt(10)\n"
"v_mfma_f32_16x16x32_bf16 v[124:127], v[44:47], v[88:91], v[124:127]\n"
"ds_read_b64_tr_b16 v[40:41], v11 offset:20992\n"
"ds_read_b64_tr_b16 v[42:43], v11 offset:23040\n"
"v_mfma_f32_16x16x32_bf16 v[128:131], v[44:47], v[216:219], v[128:131]\n"
"v_mfma_f32_16x16x32_bf16 v[132:135], v[44:47], v[248:251], v[132:135]\n"
"v_mfma_f32_16x16x32_bf16 v[124:127], v[48:51], v[92:95], v[124:127]\n"
"ds_read_b64_tr_b16 v[44:45], v11 offset:25088\n"
"ds_read_b64_tr_b16 v[46:47], v11 offset:27136\n"
"v_mfma_f32_16x16x32_bf16 v[128:131], v[48:51], v[220:223], v[128:131]\n"
"v_mfma_f32_16x16x32_bf16 v[132:135], v[48:51], v[252:255], v[132:135]\n"
"s_waitcnt lgkmcnt(10)\n"
"v_mfma_f32_16x16x32_bf16 v[136:139], v[20:23], v[64:67], 0\n"
"ds_read_b64_tr_b16 v[48:49], v11 offset:29184\n"
"ds_read_b64_tr_b16 v[50:51], v11 offset:31232\n"
"v_mfma_f32_16x16x32_bf16 v[140:143], v[20:23], v[96:99], 0\n"
"v_mfma_f32_16x16x32_bf16 v[144:147], v[20:23], v[224:227], 0\n"
"v_mfma_f32_16x16x32_bf16 v[136:139], v[24:27], v[68:71], v[136:139]\n"
"ds_read_b64_tr_b16 v[20:21], v12 offset:512\n"
"ds_read_b64_tr_b16 v[22:23], v12 offset:2560\n"
"v_mfma_f32_16x16x32_bf16 v[140:143], v[24:27], v[100:103], v[140:143]\n"
"v_cvt_pk_bf16_f32 v160, v112, v113\n"
"v_cvt_pk_bf16_f32 v161, v114, v115\n"
"v_mfma_f32_16x16x32_bf16 v[144:147], v[24:27], v[228:231], v[144:147]\n"
"v_cvt_pk_bf16_f32 v162, v124, v125\n"
"s_waitcnt lgkmcnt(10)\n"
"v_mfma_f32_16x16x32_bf16 v[136:139], v[28:31], v[72:75], v[136:139]\n"
"ds_read_b64_tr_b16 v[24:25], v12 offset:4608\n"
"ds_read_b64_tr_b16 v[26:27], v12 offset:6656\n"
"v_mfma_f32_16x16x32_bf16 v[140:143], v[28:31], v[104:107], v[140:143]\n"
"v_cvt_pk_bf16_f32 v163, v126, v127\n"
"v_permlane16_swap_b32 v160, v162\n"
"v_mfma_f32_16x16x32_bf16 v[144:147], v[28:31], v[232:235], v[144:147]\n"
"v_permlane16_swap_b32 v161, v163\n"
"buffer_store_dwordx4 v[160:163], v201, s[52:55], s77 offen offset:0 sc1\n"
"v_mfma_f32_16x16x32_bf16 v[136:139], v[32:35], v[76:79], v[136:139]\n"
"ds_read_b64_tr_b16 v[28:29], v12 offset:8704\n"
"ds_read_b64_tr_b16 v[30:31], v12 offset:10752\n"
"v_mfma_f32_16x16x32_bf16 v[140:143], v[32:35], v[108:111], v[140:143]\n"
"v_cvt_pk_bf16_f32 v168, v116, v117\n"
"v_cvt_pk_bf16_f32 v169, v118, v119\n"
"v_mfma_f32_16x16x32_bf16 v[144:147], v[32:35], v[236:239], v[144:147]\n"
"v_cvt_pk_bf16_f32 v170, v128, v129\n"
"s_waitcnt lgkmcnt(10)\n"
"v_mfma_f32_16x16x32_bf16 v[136:139], v[36:39], v[80:83], v[136:139]\n"
"ds_read_b64_tr_b16 v[32:33], v12 offset:12800\n"
"ds_read_b64_tr_b16 v[34:35], v12 offset:14848\n"
"v_mfma_f32_16x16x32_bf16 v[140:143], v[36:39], v[208:211], v[140:143]\n"
"v_cvt_pk_bf16_f32 v171, v130, v131\n"
"v_permlane16_swap_b32 v168, v170\n"
"v_mfma_f32_16x16x32_bf16 v[144:147], v[36:39], v[240:243], v[144:147]\n"
"v_permlane16_swap_b32 v169, v171\n"
"buffer_store_dwordx4 v[168:171], v201, s[52:55], s78 offen offset:0 sc1\n"
"v_mfma_f32_16x16x32_bf16 v[136:139], v[40:43], v[84:87], v[136:139]\n"
"ds_read_b64_tr_b16 v[36:37], v12 offset:16896\n"
"ds_read_b64_tr_b16 v[38:39], v12 offset:18944\n"
"v_mfma_f32_16x16x32_bf16 v[140:143], v[40:43], v[212:215], v[140:143]\n"
"v_cvt_pk_bf16_f32 v176, v120, v121\n"
"v_cvt_pk_bf16_f32 v177, v122, v123\n"
"v_mfma_f32_16x16x32_bf16 v[144:147], v[40:43], v[244:247], v[144:147]\n"
"v_cvt_pk_bf16_f32 v178, v132, v133\n"
"s_waitcnt lgkmcnt(10)\n"
"v_mfma_f32_16x16x32_bf16 v[136:139], v[44:47], v[88:91], v[136:139]\n"
"ds_read_b64_tr_b16 v[40:41], v12 offset:20992\n"
"ds_read_b64_tr_b16 v[42:43], v12 offset:23040\n"
"v_mfma_f32_16x16x32_bf16 v[140:143], v[44:47], v[216:219], v[140:143]\n"
"v_cvt_pk_bf16_f32 v179, v134, v135\n"
"v_permlane16_swap_b32 v176, v178\n"
"v_mfma_f32_16x16x32_bf16 v[144:147], v[44:47], v[248:251], v[144:147]\n"
"v_permlane16_swap_b32 v177, v179\n"
"buffer_store_dwordx4 v[176:179], v201, s[52:55], s79 offen offset:0 sc1\n"
"v_mfma_f32_16x16x32_bf16 v[136:139], v[48:51], v[92:95], v[136:139]\n"
"ds_read_b64_tr_b16 v[44:45], v12 offset:25088\n"
"ds_read_b64_tr_b16 v[46:47], v12 offset:27136\n"
"v_mfma_f32_16x16x32_bf16 v[140:143], v[48:51], v[220:223], v[140:143]\n"
"v_mfma_f32_16x16x32_bf16 v[144:147], v[48:51], v[252:255], v[144:147]\n"
"s_waitcnt lgkmcnt(10)\n"
"v_mfma_f32_16x16x32_bf16 v[148:151], v[20:23], v[64:67], 0\n"
"ds_read_b64_tr_b16 v[48:49], v12 offset:29184\n"
"ds_read_b64_tr_b16 v[50:51], v12 offset:31232\n"
"v_mfma_f32_16x16x32_bf16 v[152:155], v[20:23], v[96:99], 0\n"
"v_mfma_f32_16x16x32_bf16 v[156:159], v[20:23], v[224:227], 0\n"
"v_mfma_f32_16x16x32_bf16 v[148:151], v[24:27], v[68:71], v[148:151]\n"
"s_add_u32 s57, s57, 0x8000\n"
"s_cmp_eq_u32 s57, s61\n"
"s_cselect_b32 s57, s60, s57\n"
"v_mfma_f32_16x16x32_bf16 v[152:155], v[24:27], v[100:103], v[152:155]\n"
"v_add_u32 v11, s57, v202\n"
"v_add_u32 v12, s57, v203\n"
"v_mfma_f32_16x16x32_bf16 v[156:159], v[24:27], v[228:231], v[156:159]\n"
"s_waitcnt lgkmcnt(8)\n"
"v_mfma_f32_16x16x32_bf16 v[148:151], v[28:31], v[72:75], v[148:151]\n"
"v_mfma_f32_16x16x32_bf16 v[152:155], v[28:31], v[104:107], v[152:155]\n"
"v_mfma_f32_16x16x32_bf16 v[156:159], v[28:31], v[232:235], v[156:159]\n"
"s_waitcnt vmcnt(37) lgkmcnt(0)\n"
"s_barrier\n"
"v_mfma_f32_16x16x32_bf16 v[148:151], v[32:35], v[76:79], v[148:151]\n"
"ds_read_b64_tr_b16 v[20:21], v11 offset:0\n"
"ds_read_b64_tr_b16 v[22:23], v11 offset:2048\n"
"v_mfma_f32_16x16x32_bf16 v[152:155], v[32:35], v[108:111], v[152:155]\n"
"ds_read_b64_tr_b16 v[24:25], v11 offset:4096\n"
"ds_read_b64_tr_b16 v[26:27], v11 offset:6144\n"
"v_mfma_f32_16x16x32_bf16 v[156:159], v[32:35], v[236:239], v[156:159]\n"
"ds_read_b64_tr_b16 v[28:29], v11 offset:8192\n"
"ds_read_b64_tr_b16 v[30:31], v11 offset:10240\n"
"v_mfma_f32_16x16x32_bf16 v[148:151], v[36:39], v[80:83], v[148:151]\n"
"ds_read_b64_tr_b16 v[32:33], v11 offset:12288\n"
"ds_read_b64_tr_b16 v[34:35], v11 offset:14336\n"
"v_mfma_f32_16x16x32_bf16 v[152:155], v[36:39], v[208:211], v[152:155]\n"
"v_mfma_f32_16x16x32_bf16 v[156:159], v[36:39], v[240:243], v[156:159]\n"
"v_mfma_f32_16x16x32_bf16 v[148:151], v[40:43], v[84:87], v[148:151]\n"
"ds_read_b64_tr_b16 v[36:37], v11 offset:16384\n"
"ds_read_b64_tr_b16 v[38:39], v11 offset:18432\n"
"v_mfma_f32_16x16x32_bf16 v[152:155], v[40:43], v[212:215], v[152:155]\n"
"v_mfma_f32_16x16x32_bf16 v[156:159], v[40:43], v[244:247], v[156:159]\n"
"v_mfma_f32_16x16x32_bf16 v[148:151], v[44:47], v[88:91], v[148:151]\n"
"ds_read_b64_tr_b16 v[40:41], v11 offset:20480\n"
"ds_read_b64_tr_b16 v[42:43], v11 offset:22528\n"
"v_mfma_f32_16x16x32_bf16 v[152:155], v[44:47], v[216:219], v[152:155]\n"
"v_mfma_f32_16x16x32_bf16 v[156:159], v[44:47], v[248:251], v[156:159]\n"
"v_mfma_f32_16x16x32_bf16 v[148:151], v[48:51], v[92:95], v[148:151]\n"
"ds_read_b64_tr_b16 v[44:45], v11 offset:24576\n"
"ds_read_b64_tr_b16 v[46:47], v11 offset:26624\n"
"v_mfma_f32_16x16x32_bf16 v[152:155], v[48:51], v[220:223], v[152:155]\n"
"v_mfma_f32_16x16x32_bf16 v[156:159], v[48:51], v[252:255], v[156:159]\n"
"s_waitcnt lgkmcnt(10)\n"
"v_mfma_f32_16x16x32_bf16 v[112:115], v[20:23], v[64:67], 0\n"
"ds_read_b64_tr_b16 v[48:49], v11 offset:28672\n"
"ds_read_b64_tr_b16 v[50:51], v11 offset:30720\n"
"v_mfma_f32_16x16x32_bf16 v[116:119], v[20:23], v[96:99], 0\n"
"v_mfma_f32_16x16x32_bf16 v[120:123], v[20:23], v[224:227], 0\n"
"v_mfma_f32_16x16x32_bf16 v[112:115], v[24:27], v[68:71], v[112:115]\n"
"ds_read_b64_tr_b16 v[20:21], v12 offset:0\n"
"ds_read_b64_tr_b16 v[22:23], v12 offset:2048\n"
"v_mfma_f32_16x16x32_bf16 v[116:119], v[24:27], v[100:103], v[116:119]\n"
"v_cvt_pk_bf16_f32 v164, v136, v137\n"
"v_cvt_pk_bf16_f32 v165, v138, v139\n"
"v_mfma_f32_16x16x32_bf16 v[120:123], v[24:27], v[228:231], v[120:123]\n"
"v_cvt_pk_bf16_f32 v166, v148, v149\n"
"s_waitcnt lgkmcnt(10)\n"
"v_mfma_f32_16x16x32_bf16 v[112:115], v[28:31], v[72:75], v[112:115]\n"
"ds_read_b64_tr_b16 v[24:25], v12 offset:4096\n"
"ds_read_b64_tr_b16 v[26:27], v12 offset:6144\n"
"v_mfma_f32_16x16x32_bf16 v[116:119], v[28:31], v[104:107], v[116:119]\n"
"v_cvt_pk_bf16_f32 v167, v150, v151\n"
"v_permlane16_swap_b32 v164, v166\n"
"v_mfma_f32_16x16x32_bf16 v[120:123], v[28:31], v[232:235], v[120:123]\n"
"v_permlane16_swap_b32 v165, v167\n"
"buffer_store_dwordx4 v[164:167], v201, s[52:55], s77 offen offset:1024 sc1\n"
"v_mfma_f32_16x16x32_bf16 v[112:115], v[32:35], v[76:79], v[112:115]\n"
"ds_read_b64_tr_b16 v[28:29], v12 offset:8192\n"
"ds_read_b64_tr_b16 v[30:31], v12 offset:10240\n"
"v_mfma_f32_16x16x32_bf16 v[116:119], v[32:35], v[108:111], v[116:119]\n"
"v_cvt_pk_bf16_f32 v172, v140, v141\n"
"v_cvt_pk_bf16_f32 v173, v142, v143\n"
"v_mfma_f32_16x16x32_bf16 v[120:123], v[32:35], v[236:239], v[120:123]\n"
"v_cvt_pk_bf16_f32 v174, v152, v153\n"
"s_waitcnt lgkmcnt(10)\n"
"v_mfma_f32_16x16x32_bf16 v[112:115], v[36:39], v[80:83], v[112:115]\n"
"ds_read_b64_tr_b16 v[32:33], v12 offset:12288\n"
"ds_read_b64_tr_b16 v[34:35], v12 offset:14336\n"
"v_mfma_f32_16x16x32_bf16 v[116:119], v[36:39], v[208:211], v[116:119]\n"
"v_cvt_pk_bf16_f32 v175, v154, v155\n"
"v_permlane16_swap_b32 v172, v174\n"
"v_mfma_f32_16x16x32_bf16 v[120:123], v[36:39], v[240:243], v[120:123]\n"
"v_permlane16_swap_b32 v173, v175\n"
"buffer_store_dwordx4 v[172:175], v201, s[52:55], s78 offen offset:1024 sc1\n"
"v_mfma_f32_16x16x32_bf16 v[112:115], v[40:43], v[84:87], v[112:115]\n"
"ds_read_b64_tr_b16 v[36:37], v12 offset:16384\n"
"ds_read_b64_tr_b16 v[38:39], v12 offset:18432\n"
"v_mfma_f32_16x16x32_bf16 v[116:119], v[40:43], v[212:215], v[116:119]\n"
"v_cvt_pk_bf16_f32 v180, v144, v145\n"
"v_cvt_pk_bf16_f32 v181, v146, v147\n"
"v_mfma_f32_16x16x32_bf16 v[120:123], v[40:43], v[244:247], v[120:123]\n"
"v_cvt_pk_bf16_f32 v182, v156, v157\n"
"s_waitcnt lgkmcnt(10)\n"
"v_mfma_f32_16x16x32_bf16 v[112:115], v[44:47], v[88:91], v[112:115]\n"
"ds_read_b64_tr_b16 v[40:41], v12 offset:20480\n"
"ds_read_b64_tr_b16 v[42:43], v12 offset:22528\n"
"v_mfma_f32_16x16x32_bf16 v[116:119], v[44:47], v[216:219], v[116:119]\n"
"v_cvt_pk_bf16_f32 v183, v158, v159\n"
"v_permlane16_swap_b32 v180, v182\n"
"v_mfma_f32_16x16x32_bf16 v[120:123], v[44:47], v[248:251], v[120:123]\n"
"v_permlane16_swap_b32 v181, v183\n"
"buffer_store_dwordx4 v[180:183], v201, s[52:55], s79 offen offset:1024 sc1\n"
"v_mfma_f32_16x16x32_bf16 v[112:115], v[48:51], v[92:95], v[112:115]\n"
"ds_read_b64_tr_b16 v[44:45], v12 offset:24576\n"
"ds_read_b64_tr_b16 v[46:47], v12 offset:26624\n"
"v_mfma_f32_16x16x32_bf16 v[116:119], v[48:51], v[220:223], v[116:119]\n"
"s_add_u32 s52, s52, 22528\n"
"s_addc_u32 s53, s53, 0\n"
"s_sub_u32 s54, s54, 22528\n"
"v_mfma_f32_16x16x32_bf16 v[120:123], v[48:51], v[252:255], v[120:123]\n"
"s_waitcnt lgkmcnt(10)\n"
"v_mfma_f32_16x16x32_bf16 v[124:127], v[20:23], v[64:67], 0\n"
"ds_read_b64_tr_b16 v[48:49], v12 offset:28672\n"
"ds_read_b64_tr_b16 v[50:51], v12 offset:30720\n"
"v_mfma_f32_16x16x32_bf16 v[128:131], v[20:23], v[96:99], 0\n"
"v_mfma_f32_16x16x32_bf16 v[132:135], v[20:23], v[224:227], 0\n"
"v_mfma_f32_16x16x32_bf16 v[124:127], v[24:27], v[68:71], v[124:127]\n"
"ds_read_b64_tr_b16 v[20:21], v11 offset:512\n"
"ds_read_b64_tr_b16 v[22:23], v11 offset:2560\n"
"v_mfma_f32_16x16x32_bf16 v[128:131], v[24:27], v[100:103], v[128:131]\n"
"v_mfma_f32_16x16x32_bf16 v[132:135], v[24:27], v[228:231], v[132:135]\n"
"s_waitcnt lgkmcnt(10)\n"
"v_mfma_f32_16x16x32_bf16 v[124:127], v[28:31], v[72:75], v[124:127]\n"
"ds_read_b64_tr_b16 v[24:25], v11 offset:4608\n"
"ds_read_b64_tr_b16 v[26:27], v11 offset:6656\n"
"v_mfma_f32_16x16x32_bf16 v[128:131], v[28:31], v[104:107], v[128:131]\n"
"v_mfma_f32_16x16x32_bf16 v[132:135], v[28:31], v[232:235], v[132:135]\n"
"v_mfma_f32_16x16x32_bf16 v[124:127], v[32:35], v[76:79], v[124:127]\n"
"ds_read_b64_tr_b16 v[28:29], v11 offset:8704\n"
"ds_read_b64_tr_b16 v[30:31], v11 offset:10752\n"
"v_mfma_f32_16x16x32_bf16 v[128:131], v[32:35], v[108:111], v[128:131]\n"
"v_mfma_f32_16x16x32_bf16 v[132:135], v[32:35], v[236:239], v[132:135]\n"
"s_waitcnt lgkmcnt(10)\n"
"v_mfma_f32_16x16x32_bf16 v[124:127], v[36:39], v[80:83], v[124:127]\n"
"ds_read_b64_tr_b16 v[32:33], v11 offset:12800\n"
"ds_read_b64_tr_b16 v[34:35], v11 offset:14848\n"
"v_mfma_f32_16x16x32_bf16 v[128:131], v[36:39], v[208:211], v[128:131]\n"
"v_mfma_f32_16x16x32_bf16 v[132:135], v[36:39], v[240:243], v[132:135]\n"
"v_mfma_f32_16x16x32_bf16 v[124:127], v[40:43], v[84:87], v[124:127]\n"
"ds_read_b64_tr_b16 v[36:37], v11 offset:16896\n"
"ds_read_b64_tr_b16 v[38:39], v11 offset:18944\n"
"v_mfma_f32_16x16x32_bf16 v[128:131], v[40:43], v[212:215], v[128:131]\n"
"v_mfma_f32_16x16x32_bf16 v[132:135], v[40:43], v[244:247], v[132:135]\n"
"s_waitcnt lgkmcnt(10)\n"
"v_mfma_f32_16x16x32_bf16 v[124:127], v[44:47], v[88:91], v[124:127]\n"
"ds_read_b64_tr_b16 v[40:41], v11 offset:20992\n"
"ds_read_b64_tr_b16 v[42:43], v11 offset:23040\n"
"v_mfma_f32_16x16x32_bf16 v[128:131], v[44:47], v[216:219], v[128:131]\n"
"v_mfma_f32_16x16x32_bf16 v[132:135], v[44:47], v[248:251], v[132:135]\n"
"v_mfma_f32_16x16x32_bf16 v[124:127], v[48:51], v[92:95], v[124:127]\n"
"ds_read_b64_tr_b16 v[44:45], v11 offset:25088\n"
"ds_read_b64_tr_b16 v[46:47], v11 offset:27136\n"
"v_mfma_f32_16x16x32_bf16 v[128:131], v[48:51], v[220:223], v[128:131]\n"
"v_mfma_f32_16x16x32_bf16 v[132:135], v[48:51], v[252:255], v[132:135]\n"
"s_waitcnt lgkmcnt(10)\n"
"v_mfma_f32_16x16x32_bf16 v[136:139], v[20:23], v[64:67], 0\n"
"ds_read_b64_tr_b16 v[48:49], v11 offset:29184\n"
"ds_read_b64_tr_b16 v[50:51], v11 offset:31232\n"
"v_mfma_f32_16x16x32_bf16 v[140:143], v[20:23], v[96:99], 0\n"
"v_mfma_f32_16x16x32_bf16 v[144:147], v[20:23], v[224:227], 0\n"
"v_mfma_f32_16x16x32_bf16 v[136:139], v[24:27], v[68:71], v[136:139]\n"
"ds_read_b64_tr_b16 v[20:21], v12 offset:512\n"
"ds_read_b64_tr_b16 v[22:23], v12 offset:2560\n"
"v_mfma_f32_16x16x32_bf16 v[140:143], v[24:27], v[100:103], v[140:143]\n"
"v_cvt_pk_bf16_f32 v160, v112, v113\n"
"v_cvt_pk_bf16_f32 v161, v114, v115\n"
"v_mfma_f32_16x16x32_bf16 v[144:147], v[24:27], v[228:231], v[144:147]\n"
"v_cvt_pk_bf16_f32 v162, v124, v125\n"
"s_waitcnt lgkmcnt(10)\n"
"v_mfma_f32_16x16x32_bf16 v[136:139], v[28:31], v[72:75], v[136:139]\n"
"ds_read_b64_tr_b16 v[24:25], v12 offset:4608\n"
"ds_read_b64_tr_b16 v[26:27], v12 offset:6656\n"
"v_mfma_f32_16x16x32_bf16 v[140:143], v[28:31], v[104:107], v[140:143]\n"
"v_cvt_pk_bf16_f32 v163, v126, v127\n"
"v_permlane16_swap_b32 v160, v162\n"
"v_mfma_f32_16x16x32_bf16 v[144:147], v[28:31], v[232:235], v[144:147]\n"
"v_permlane16_swap_b32 v161, v163\n"
"buffer_store_dwordx4 v[160:163], v201, s[52:55], s77 offen offset:0 sc1\n"
"v_mfma_f32_16x16x32_bf16 v[136:139], v[32:35], v[76:79], v[136:139]\n"
"ds_read_b64_tr_b16 v[28:29], v12 offset:8704\n"
"ds_read_b64_tr_b16 v[30:31], v12 offset:10752\n"
"v_mfma_f32_16x16x32_bf16 v[140:143], v[32:35], v[108:111], v[140:143]\n"
"v_cvt_pk_bf16_f32 v168, v116, v117\n"
"v_cvt_pk_bf16_f32 v169, v118, v119\n"
"v_mfma_f32_16x16x32_bf16 v[144:147], v[32:35], v[236:239], v[144:147]\n"
"v_cvt_pk_bf16_f32 v170, v128, v129\n"
"s_waitcnt lgkmcnt(10)\n"
"v_mfma_f32_16x16x32_bf16 v[136:139], v[36:39], v[80:83], v[136:139]\n"
"ds_read_b64_tr_b16 v[32:33], v12 offset:12800\n"
"ds_read_b64_tr_b16 v[34:35], v12 offset:14848\n"
"v_mfma_f32_16x16x32_bf16 v[140:143], v[36:39], v[208:211], v[140:143]\n"
"v_cvt_pk_bf16_f32 v171, v130, v131\n"
"v_permlane16_swap_b32 v168, v170\n"
"v_mfma_f32_16x16x32_bf16 v[144:147], v[36:39], v[240:243], v[144:147]\n"
"v_permlane16_swap_b32 v169, v171\n"
"buffer_store_dwordx4 v[168:171], v201, s[52:55], s78 offen offset:0 sc1\n"
"v_mfma_f32_16x16x32_bf16 v[136:139], v[40:43], v[84:87], v[136:139]\n"
"ds_read_b64_tr_b16 v[36:37], v12 offset:16896\n"
"ds_read_b64_tr_b16 v[38:39], v12 offset:18944\n"
"v_mfma_f32_16x16x32_bf16 v[140:143], v[40:43], v[212:215], v[140:143]\n"
"v_cvt_pk_bf16_f32 v176, v120, v121\n"
"v_cvt_pk_bf16_f32 v177, v122, v123\n"
"v_mfma_f32_16x16x32_bf16 v[144:147], v[40:43], v[244:247], v[144:147]\n"
"v_cvt_pk_bf16_f32 v178, v132, v133\n"
"s_waitcnt lgkmcnt(10)\n"
"v_mfma_f32_16x16x32_bf16 v[136:139], v[44:47], v[88:91], v[136:139]\n"
"ds_read_b64_tr_b16 v[40:41], v12 offset:20992\n"
"ds_read_b64_tr_b16 v[42:43], v12 offset:23040\n"
"v_mfma_f32_16x16x32_bf16 v[140:143], v[44:47], v[216:219], v[140:143]\n"
"v_cvt_pk_bf16_f32 v179, v134, v135\n"
"v_permlane16_swap_b32 v176, v178\n"
"v_mfma_f32_16x16x32_bf16 v[144:147], v[44:47], v[248:251], v[144:147]\n"
"v_permlane16_swap_b32 v177, v179\n"
"buffer_store_dwordx4 v[176:179], v201, s[52:55], s79 offen offset:0 sc1\n"
"v_mfma_f32_16x16x32_bf16 v[136:139], v[48:51], v[92:95], v[136:139]\n"
"ds_read_b64_tr_b16 v[44:45], v12 offset:25088\n"
"ds_read_b64_tr_b16 v[46:47], v12 offset:27136\n"
"v_mfma_f32_16x16x32_bf16 v[140:143], v[48:51], v[220:223], v[140:143]\n"
"v_mfma_f32_16x16x32_bf16 v[144:147], v[48:51], v[252:255], v[144:147]\n"
"s_waitcnt lgkmcnt(10)\n"
"v_mfma_f32_16x16x32_bf16 v[148:151], v[20:23], v[64:67], 0\n"
"ds_read_b64_tr_b16 v[48:49], v12 offset:29184\n"
"ds_read_b64_tr_b16 v[50:51], v12 offset:31232\n"
"v_mfma_f32_16x16x32_bf16 v[152:155], v[20:23], v[96:99], 0\n"
"v_mfma_f32_16x16x32_bf16 v[156:159], v[20:23], v[224:227], 0\n"
"v_mfma_f32_16x16x32_bf16 v[148:151], v[24:27], v[68:71], v[148:151]\n"
"s_add_u32 s57, s57, 0x8000\n"
"s_cmp_eq_u32 s57, s61\n"
"s_cselect_b32 s57, s60, s57\n"
"v_mfma_f32_16x16x32_bf16 v[152:155], v[24:27], v[100:103], v[152:155]\n"
"v_add_u32 v11, s57, v202\n"
"v_add_u32 v12, s57, v203\n"
"v_mfma_f32_16x16x32_bf16 v[156:159], v[24:27], v[228:231], v[156:159]\n"
"s_waitcnt lgkmcnt(8)\n"
"v_mfma_f32_16x16x32_bf16 v[148:151], v[28:31], v[72:75], v[148:151]\n"
"v_mfma_f32_16x16x32_bf16 v[152:155], v[28:31], v[104:107], v[152:155]\n"
"v_mfma_f32_16x16x32_bf16 v[156:159], v[28:31], v[232:235], v[156:159]\n"
"s_waitcnt vmcnt(29) lgkmcnt(0)\n"
"s_barrier\n"
"v_mfma_f32_16x16x32_bf16 v[148:151], v[32:35], v[76:79], v[148:151]\n"
"ds_read_b64_tr_b16 v[20:21], v11 offset:0\n"
"ds_read_b64_tr_b16 v[22:23], v11 offset:2048\n"
"v_mfma_f32_16x16x32_bf16 v[152:155], v[32:35], v[108:111], v[152:155]\n"
"ds_read_b64_tr_b16 v[24:25], v11 offset:4096\n"
"ds_read_b64_tr_b16 v[26:27], v11 offset:6144\n"
"v_mfma_f32_16x16x32_bf16 v[156:159], v[32:35], v[236:239], v[156:159]\n"
"ds_read_b64_tr_b16 v[28:29], v11 offset:8192\n"
"ds_read_b64_tr_b16 v[30:31], v11 offset:10240\n"
"v_mfma_f32_16x16x32_bf16 v[148:151], v[36:39], v[80:83], v[148:151]\n"
"ds_read_b64_tr_b16 v[32:33], v11 offset:12288\n"
"ds_read_b64_tr_b16 v[34:35], v11 offset:14336\n"
"v_mfma_f32_16x16x32_bf16 v[152:155], v[36:39], v[208:211], v[152:155]\n"
"v_mfma_f32_16x16x32_bf16 v[156:159], v[36:39], v[240:243], v[156:159]\n"
"v_mfma_f32_16x16x32_bf16 v[148:151], v[40:43], v[84:87], v[148:151]\n"
"ds_read_b64_tr_b16 v[36:37], v11 offset:16384\n"
"ds_read_b64_tr_b16 v[38:39], v11 offset:18432\n"
"v_mfma_f32_16x16x32_bf16 v[152:155], v[40:43], v[212:215], v[152:155]\n"
"v_mfma_f32_16x16x32_bf16 v[156:159], v[40:43], v[244:247], v[156:159]\n"
"v_mfma_f32_16x16x32_bf16 v[148:151], v[44:47], v[88:91], v[148:151]\n"
"ds_read_b64_tr_b16 v[40:41], v11 offset:20480\n"
"ds_read_b64_tr_b16 v[42:43], v11 offset:22528\n"
"v_mfma_f32_16x16x32_bf16 v[152:155], v[44:47], v[216:219], v[152:155]\n"
"v_mfma_f32_16x16x32_bf16 v[156:159], v[44:47], v[248:251], v[156:159]\n"
"v_mfma_f32_16x16x32_bf16 v[148:151], v[48:51], v[92:95], v[148:151]\n"
"ds_read_b64_tr_b16 v[44:45], v11 offset:24576\n"
"ds_read_b64_tr_b16 v[46:47], v11 offset:26624\n"
"v_mfma_f32_16x16x32_bf16 v[152:155], v[48:51], v[220:223], v[152:155]\n"
"v_mfma_f32_16x16x32_bf16 v[156:159], v[48:51], v[252:255], v[156:159]\n"
"s_waitcnt lgkmcnt(10)\n"
"v_mfma_f32_16x16x32_bf16 v[112:115], v[20:23], v[64:67], 0\n"
"ds_read_b64_tr_b16 v[48:49], v11 offset:28672\n"
"ds_read_b64_tr_b16 v[50:51], v11 offset:30720\n"
"v_mfma_f32_16x16x32_bf16 v[116:119], v[20:23], v[96:99], 0\n"
"v_mfma_f32_16x16x32_bf16 v[120:123], v[20:23], v[224:227], 0\n"
"v_mfma_f32_16x16x32_bf16 v[112:115], v[24:27], v[68:71], v[112:115]\n"
"ds_read_b64_tr_b16 v[20:21], v12 offset:0\n"
"ds_read_b64_tr_b16 v[22:23], v12 offset:2048\n"
"v_mfma_f32_16x16x32_bf16 v[116:119], v[24:27], v[100:103], v[116:119]\n"
"v_cvt_pk_bf16_f32 v164, v136, v137\n"
"v_cvt_pk_bf16_f32 v165, v138, v139\n"
"v_mfma_f32_16x16x32_bf16 v[120:123], v[24:27], v[228:231], v[120:123]\n"
"v_cvt_pk_bf16_f32 v166, v148, v149\n"
"s_waitcnt lgkmcnt(10)\n"
"v_mfma_f32_16x16x32_bf16 v[112:115], v[28:31], v[72:75], v[112:115]\n"
"ds_read_b64_tr_b16 v[24:25], v12 offset:4096\n"
"ds_read_b64_tr_b16 v[26:27], v12 offset:6144\n"
"v_mfma_f32_16x16x32_bf16 v[116:119], v[28:31], v[104:107], v[116:119]\n"
"v_cvt_pk_bf16_f32 v167, v150, v151\n"
"v_permlane16_swap_b32 v164, v166\n"
"v_mfma_f32_16x16x32_bf16 v[120:123], v[28:31], v[232:235], v[120:123]\n"
"v_permlane16_swap_b32 v165, v167\n"
"buffer_store_dwordx4 v[164:167], v201, s[52:55], s77 offen offset:1024 sc1\n"
"v_mfma_f32_16x16x32_bf16 v[112:115], v[32:35], v[76:79], v[112:115]\n"
"ds_read_b64_tr_b16 v[28:29], v12 offset:8192\n"
"ds_read_b64_tr_b16 v[30:31], v12 offset:10240\n"
"v_mfma_f32_16x16x32_bf16 v[116:119], v[32:35], v[108:111], v[116:119]\n"
"v_cvt_pk_bf16_f32 v172, v140, v141\n"
"v_cvt_pk_bf16_f32 v173, v142, v143\n"
"v_mfma_f32_16x16x32_bf16 v[120:123], v[32:35], v[236:239], v[120:123]\n"
"v_cvt_pk_bf16_f32 v174, v152, v153\n"
"s_waitcnt lgkmcnt(10)\n"
"v_mfma_f32_16x16x32_bf16 v[112:115], v[36:39], v[80:83], v[112:115]\n"
"ds_read_b64_tr_b16 v[32:33], v12 offset:12288\n"
"ds_read_b64_tr_b16 v[34:35], v12 offset:14336\n"
"v_mfma_f32_16x16x32_bf16 v[116:119], v[36:39], v[208:211], v[116:119]\n"
"v_cvt_pk_bf16_f32 v175, v154, v155\n"
"v_permlane16_swap_b32 v172, v174\n"
"v_mfma_f32_16x16x32_bf16 v[120:123], v[36:39], v[240:243], v[120:123]\n"
"v_permlane16_swap_b32 v173, v175\n"
"buffer_store_dwordx4 v[172:175], v201, s[52:55], s78 offen offset:1024 sc1\n"
"v_mfma_f32_16x16x32_bf16 v[112:115], v[40:43], v[84:87], v[112:115]\n"
"ds_read_b64_tr_b16 v[36:37], v12 offset:16384\n"
"ds_read_b64_tr_b16 v[38:39], v12 offset:18432\n"
"v_mfma_f32_16x16x32_bf16 v[116:119], v[40:43], v[212:215], v[116:119]\n"
"v_cvt_pk_bf16_f32 v180, v144, v145\n"
"v_cvt_pk_bf16_f32 v181, v146, v147\n"
"v_mfma_f32_16x16x32_bf16 v[120:123], v[40:43], v[244:247], v[120:123]\n"
"v_cvt_pk_bf16_f32 v182, v156, v157\n"
"s_waitcnt lgkmcnt(10)\n"
"v_mfma_f32_16x16x32_bf16 v[112:115], v[44:47], v[88:91], v[112:115]\n"
"ds_read_b64_tr_b16 v[40:41], v12 offset:20480\n"
"ds_read_b64_tr_b16 v[42:43], v12 offset:22528\n"
"v_mfma_f32_16x16x32_bf16 v[116:119], v[44:47], v[216:219], v[116:119]\n"
"v_cvt_pk_bf16_f32 v183, v158, v159\n"
"v_permlane16_swap_b32 v180, v182\n"
"v_mfma_f32_16x16x32_bf16 v[120:123], v[44:47], v[248:251], v[120:123]\n"
"v_permlane16_swap_b32 v181, v183\n"
"buffer_store_dwordx4 v[180:183], v201, s[52:55], s79 offen offset:1024 sc1\n"
"v_mfma_f32_16x16x32_bf16 v[112:115], v[48:51], v[92:95], v[112:115]\n"
"ds_read_b64_tr_b16 v[44:45], v12 offset:24576\n"
"ds_read_b64_tr_b16 v[46:47], v12 offset:26624\n"
"v_mfma_f32_16x16x32_bf16 v[116:119], v[48:51], v[220:223], v[116:119]\n"
"s_add_u32 s52, s52, 22528\n"
"s_addc_u32 s53, s53, 0\n"
"s_sub_u32 s54, s54, 22528\n"
"v_mfma_f32_16x16x32_bf16 v[120:123], v[48:51], v[252:255], v[120:123]\n"
"s_waitcnt lgkmcnt(10)\n"
"v_mfma_f32_16x16x32_bf16 v[124:127], v[20:23], v[64:67], 0\n"
"ds_read_b64_tr_b16 v[48:49], v12 offset:28672\n"
"ds_read_b64_tr_b16 v[50:51], v12 offset:30720\n"
"v_mfma_f32_16x16x32_bf16 v[128:131], v[20:23], v[96:99], 0\n"
"v_mfma_f32_16x16x32_bf16 v[132:135], v[20:23], v[224:227], 0\n"
"v_mfma_f32_16x16x32_bf16 v[124:127], v[24:27], v[68:71], v[124:127]\n"
"ds_read_b64_tr_b16 v[20:21], v11 offset:512\n"
"ds_read_b64_tr_b16 v[22:23], v11 offset:2560\n"
"v_mfma_f32_16x16x32_bf16 v[128:131], v[24:27], v[100:103], v[128:131]\n"
"v_mfma_f32_16x16x32_bf16 v[132:135], v[24:27], v[228:231], v[132:135]\n"
"s_waitcnt lgkmcnt(10)\n"
"v_mfma_f32_16x16x32_bf16 v[124:127], v[28:31], v[72:75], v[124:127]\n"
"ds_read_b64_tr_b16 v[24:25], v11 offset:4608\n"
"ds_read_b64_tr_b16 v[26:27], v11 offset:6656\n"
"v_mfma_f32_16x16x32_bf16 v[128:131], v[28:31], v[104:107], v[128:131]\n"
"v_mfma_f32_16x16x32_bf16 v[132:135], v[28:31], v[232:235], v[132:135]\n"
"v_mfma_f32_16x16x32_bf16 v[124:127], v[32:35], v[76:79], v[124:127]\n"
"ds_read_b64_tr_b16 v[28:29], v11 offset:8704\n"
"ds_read_b64_tr_b16 v[30:31], v11 offset:10752\n"
"v_mfma_f32_16x16x32_bf16 v[128:131], v[32:35], v[108:111], v[128:131]\n"
"v_mfma_f32_16x16x32_bf16 v[132:135], v[32:35], v[236:239], v[132:135]\n"
"s_waitcnt lgkmcnt(10)\n"
"v_mfma_f32_16x16x32_bf16 v[124:127], v[36:39], v[80:83], v[124:127]\n"
"ds_read_b64_tr_b16 v[32:33], v11 offset:12800\n"
"ds_read_b64_tr_b16 v[34:35], v11 offset:14848\n"
"v_mfma_f32_16x16x32_bf16 v[128:131], v[36:39], v[208:211], v[128:131]\n"
"v_mfma_f32_16x16x32_bf16 v[132:135], v[36:39], v[240:243], v[132:135]\n"
"v_mfma_f32_16x16x32_bf16 v[124:127], v[40:43], v[84:87], v[124:127]\n"
"ds_read_b64_tr_b16 v[36:37], v11 offset:16896\n"
"ds_read_b64_tr_b16 v[38:39], v11 offset:18944\n"
"v_mfma_f32_16x16x32_bf16 v[128:131], v[40:43], v[212:215], v[128:131]\n"
"v_mfma_f32_16x16x32_bf16 v[132:135], v[40:43], v[244:247], v[132:135]\n"
"s_waitcnt lgkmcnt(10)\n"
"v_mfma_f32_16x16x32_bf16 v[124:127], v[44:47], v[88:91], v[124:127]\n"
"ds_read_b64_tr_b16 v[40:41], v11 offset:20992\n"
"ds_read_b64_tr_b16 v[42:43], v11 offset:23040\n"
"v_mfma_f32_16x16x32_bf16 v[128:131], v[44:47], v[216:219], v[128:131]\n"
"v_mfma_f32_16x16x32_bf16 v[132:135], v[44:47], v[248:251], v[132:135]\n"
"v_mfma_f32_16x16x32_bf16 v[124:127], v[48:51], v[92:95], v[124:127]\n"
"ds_read_b64_tr_b16 v[44:45], v11 offset:25088\n"
"ds_read_b64_tr_b16 v[46:47], v11 offset:27136\n"
"v_mfma_f32_16x16x32_bf16 v[128:131], v[48:51], v[220:223], v[128:131]\n"
"v_mfma_f32_16x16x32_bf16 v[132:135], v[48:51], v[252:255], v[132:135]\n"
"s_waitcnt lgkmcnt(10)\n"
"v_mfma_f32_16x16x32_bf16 v[136:139], v[20:23], v[64:67], 0\n"
"ds_read_b64_tr_b16 v[48:49], v11 offset:29184\n"
"ds_read_b64_tr_b16 v[50:51], v11 offset:31232\n"
"v_mfma_f32_16x16x32_bf16 v[140:143], v[20:23], v[96:99], 0\n"
"v_mfma_f32_16x16x32_bf16 v[144:147], v[20:23], v[224:227], 0\n"
"v_mfma_f32_16x16x32_bf16 v[136:139], v[24:27], v[68:71], v[136:139]\n"
"ds_read_b64_tr_b16 v[20:21], v12 offset:512\n"
"ds_read_b64_tr_b16 v[22:23], v12 offset:2560\n"
"v_mfma_f32_16x16x32_bf16 v[140:143], v[24:27], v[100:103], v[140:143]\n"
"v_cvt_pk_bf16_f32 v160, v112, v113\n"
"v_cvt_pk_bf16_f32 v161, v114, v115\n"
"v_mfma_f32_16x16x32_bf16 v[144:147], v[24:27], v[228:231], v[144:147]\n"
"v_cvt_pk_bf16_f32 v162, v124, v125\n"
"s_waitcnt lgkmcnt(10)\n"
"v_mfma_f32_16x16x32_bf16 v[136:139], v[28:31], v[72:75], v[136:139]\n"
"ds_read_b64_tr_b16 v[24:25], v12 offset:4608\n"
"ds_read_b64_tr_b16 v[26:27], v12 offset:6656\n"
"v_mfma_f32_16x16x32_bf16 v[140:143], v[28:31], v[104:107], v[140:143]\n"
"v_cvt_pk_bf16_f32 v163, v126, v127\n"
"v_permlane16_swap_b32 v160, v162\n"
"v_mfma_f32_16x16x32_bf16 v[144:147], v[28:31], v[232:235], v[144:147]\n"
"v_permlane16_swap_b32 v161, v163\n"
"buffer_store_dwordx4 v[160:163], v201, s[52:55], s77 offen offset:0 sc1\n"
"v_mfma_f32_16x16x32_bf16 v[136:139], v[32:35], v[76:79], v[136:139]\n"
"ds_read_b64_tr_b16 v[28:29], v12 offset:8704\n"
"ds_read_b64_tr_b16 v[30:31], v12 offset:10752\n"
"v_mfma_f32_16x16x32_bf16 v[140:143], v[32:35], v[108:111], v[140:143]\n"
"v_cvt_pk_bf16_f32 v168, v116, v117\n"
"v_cvt_pk_bf16_f32 v169, v118, v119\n"
"v_mfma_f32_16x16x32_bf16 v[144:147], v[32:35], v[236:239], v[144:147]\n"
"v_cvt_pk_bf16_f32 v170, v128, v129\n"
"s_waitcnt lgkmcnt(10)\n"
"v_mfma_f32_16x16x32_bf16 v[136:139], v[36:39], v[80:83], v[136:139]\n"
"ds_read_b64_tr_b16 v[32:33], v12 offset:12800\n"
"ds_read_b64_tr_b16 v[34:35], v12 offset:14848\n"
"v_mfma_f32_16x16x32_bf16 v[140:143], v[36:39], v[208:211], v[140:143]\n"
"v_cvt_pk_bf16_f32 v171, v130, v131\n"
"v_permlane16_swap_b32 v168, v170\n"
"v_mfma_f32_16x16x32_bf16 v[144:147], v[36:39], v[240:243], v[144:147]\n"
"v_permlane16_swap_b32 v169, v171\n"
"buffer_store_dwordx4 v[168:171], v201, s[52:55], s78 offen offset:0 sc1\n"
"v_mfma_f32_16x16x32_bf16 v[136:139], v[40:43], v[84:87], v[136:139]\n"
"ds_read_b64_tr_b16 v[36:37], v12 offset:16896\n"
"ds_read_b64_tr_b16 v[38:39], v12 offset:18944\n"
"v_mfma_f32_16x16x32_bf16 v[140:143], v[40:43], v[212:215], v[140:143]\n"
"v_cvt_pk_bf16_f32 v176, v120, v121\n"
"v_cvt_pk_bf16_f32 v177, v122, v123\n"
"v_mfma_f32_16x16x32_bf16 v[144:147], v[40:43], v[244:247], v[144:147]\n"
"v_cvt_pk_bf16_f32 v178, v132, v133\n"
"s_waitcnt lgkmcnt(10)\n"
"v_mfma_f32_16x16x32_bf16 v[136:139], v[44:47], v[88:91], v[136:139]\n"
"ds_read_b64_tr_b16 v[40:41], v12 offset:20992\n"
"ds_read_b64_tr_b16 v[42:43], v12 offset:23040\n"
"v_mfma_f32_16x16x32_bf16 v[140:143], v[44:47], v[216:219], v[140:143]\n"
"v_cvt_pk_bf16_f32 v179, v134, v135\n"
"v_permlane16_swap_b32 v176, v178\n"
"v_mfma_f32_16x16x32_bf16 v[144:147], v[44:47], v[248:251], v[144:147]\n"
"v_permlane16_swap_b32 v177, v179\n"
"buffer_store_dwordx4 v[176:179], v201, s[52:55], s79 offen offset:0 sc1\n"
"v_mfma_f32_16x16x32_bf16 v[136:139], v[48:51], v[92:95], v[136:139]\n"
"ds_read_b64_tr_b16 v[44:45], v12 offset:25088\n"
"ds_read_b64_tr_b16 v[46:47], v12 offset:27136\n"
"v_mfma_f32_16x16x32_bf16 v[140:143], v[48:51], v[220:223], v[140:143]\n"
"v_mfma_f32_16x16x32_bf16 v[144:147], v[48:51], v[252:255], v[144:147]\n"
"s_waitcnt lgkmcnt(10)\n"
"v_mfma_f32_16x16x32_bf16 v[148:151], v[20:23], v[64:67], 0\n"
"ds_read_b64_tr_b16 v[48:49], v12 offset:29184\n"
"ds_read_b64_tr_b16 v[50:51], v12 offset:31232\n"
"v_mfma_f32_16x16x32_bf16 v[152:155], v[20:23], v[96:99], 0\n"
"v_mfma_f32_16x16x32_bf16 v[156:159], v[20:23], v[224:227], 0\n"
"v_mfma_f32_16x16x32_bf16 v[148:151], v[24:27], v[68:71], v[148:151]\n"
"s_add_u32 s57, s57, 0x8000\n"
"s_cmp_eq_u32 s57, s61\n"
"s_cselect_b32 s57, s60, s57\n"
"v_mfma_f32_16x16x32_bf16 v[152:155], v[24:27], v[100:103], v[152:155]\n"
"v_add_u32 v11, s57, v202\n"
"v_add_u32 v12, s57, v203\n"
"v_mfma_f32_16x16x32_bf16 v[156:159], v[24:27], v[228:231], v[156:159]\n"
"s_waitcnt lgkmcnt(8)\n"
"v_mfma_f32_16x16x32_bf16 v[148:151], v[28:31], v[72:75], v[148:151]\n"
"v_mfma_f32_16x16x32_bf16 v[152:155], v[28:31], v[104:107], v[152:155]\n"
"v_mfma_f32_16x16x32_bf16 v[156:159], v[28:31], v[232:235], v[156:159]\n"
"s_waitcnt vmcnt(21) lgkmcnt(0)\n"
"s_barrier\n"
"v_mfma_f32_16x16x32_bf16 v[148:151], v[32:35], v[76:79], v[148:151]\n"
"ds_read_b64_tr_b16 v[20:21], v11 offset:0\n"
"ds_read_b64_tr_b16 v[22:23], v11 offset:2048\n"
"v_mfma_f32_16x16x32_bf16 v[152:155], v[32:35], v[108:111], v[152:155]\n"
"ds_read_b64_tr_b16 v[24:25], v11 offset:4096\n"
"ds_read_b64_tr_b16 v[26:27], v11 offset:6144\n"
"v_mfma_f32_16x16x32_bf16 v[156:159], v[32:35], v[236:239], v[156:159]\n"
"ds_read_b64_tr_b16 v[28:29], v11 offset:8192\n"
"ds_read_b64_tr_b16 v[30:31], v11 offset:10240\n"
"v_mfma_f32_16x16x32_bf16 v[148:151], v[36:39], v[80:83], v[148:151]\n"
"ds_read_b64_tr_b16 v[32:33], v11 offset:12288\n"
"ds_read_b64_tr_b16 v[34:35], v11 offset:14336\n"
"v_mfma_f32_16x16x32_bf16 v[152:155], v[36:39], v[208:211], v[152:155]\n"
"v_mfma_f32_16x16x32_bf16 v[156:159], v[36:39], v[240:243], v[156:159]\n"
"v_mfma_f32_16x16x32_bf16 v[148:151], v[40:43], v[84:87], v[148:151]\n"
"ds_read_b64_tr_b16 v[36:37], v11 offset:16384\n"
"ds_read_b64_tr_b16 v[38:39], v11 offset:18432\n"
"v_mfma_f32_16x16x32_bf16 v[152:155], v[40:43], v[212:215], v[152:155]\n"
"v_mfma_f32_16x16x32_bf16 v[156:159], v[40:43], v[244:247], v[156:159]\n"
"v_mfma_f32_16x16x32_bf16 v[148:151], v[44:47], v[88:91], v[148:151]\n"
"ds_read_b64_tr_b16 v[40:41], v11 offset:20480\n"
"ds_read_b64_tr_b16 v[42:43], v11 offset:22528\n"
"v_mfma_f32_16x16x32_bf16 v[152:155], v[44:47], v[216:219], v[152:155]\n"
"v_mfma_f32_16x16x32_bf16 v[156:159], v[44:47], v[248:251], v[156:159]\n"
"v_mfma_f32_16x16x32_bf16 v[148:151], v[48:51], v[92:95], v[148:151]\n"
"ds_read_b64_tr_b16 v[44:45], v11 offset:24576\n"
"ds_read_b64_tr_b16 v[46:47], v11 offset:26624\n"
"v_mfma_f32_16x16x32_bf16 v[152:155], v[48:51], v[220:223], v[152:155]\n"
"v_mfma_f32_16x16x32_bf16 v[156:159], v[48:51], v[252:255], v[156:159]\n"
"s_waitcnt lgkmcnt(10)\n"
"v_mfma_f32_16x16x32_bf16 v[112:115], v[20:23], v[64:67], 0\n"
"ds_read_b64_tr_b16 v[48:49], v11 offset:28672\n"
"ds_read_b64_tr_b16 v[50:51], v11 offset:30720\n"
"v_mfma_f32_16x16x32_bf16 v[116:119], v[20:23], v[96:99], 0\n"
"v_mfma_f32_16x16x32_bf16 v[120:123], v[20:23], v[224:227], 0\n"
"v_mfma_f32_16x16x32_bf16 v[112:115], v[24:27], v[68:71], v[112:115]\n"
"ds_read_b64_tr_b16 v[20:21], v12 offset:0\n"
"ds_read_b64_tr_b16 v[22:23], v12 offset:2048\n"
"v_mfma_f32_16x16x32_bf16 v[116:119], v[24:27], v[100:103], v[116:119]\n"
"v_cvt_pk_bf16_f32 v164, v136, v137\n"
"v_cvt_pk_bf16_f32 v165, v138, v139\n"
"v_mfma_f32_16x16x32_bf16 v[120:123], v[24:27], v[228:231], v[120:123]\n"
"v_cvt_pk_bf16_f32 v166, v148, v149\n"
"s_waitcnt lgkmcnt(10)\n"
"v_mfma_f32_16x16x32_bf16 v[112:115], v[28:31], v[72:75], v[112:115]\n"
"ds_read_b64_tr_b16 v[24:25], v12 offset:4096\n"
"ds_read_b64_tr_b16 v[26:27], v12 offset:6144\n"
"v_mfma_f32_16x16x32_bf16 v[116:119], v[28:31], v[104:107], v[116:119]\n"
"v_cvt_pk_bf16_f32 v167, v150, v151\n"
"v_permlane16_swap_b32 v164, v166\n"
"v_mfma_f32_16x16x32_bf16 v[120:123], v[28:31], v[232:235], v[120:123]\n"
"v_permlane16_swap_b32 v165, v167\n"
"buffer_store_dwordx4 v[164:167], v201, s[52:55], s77 offen offset:1024 sc1\n"
"v_mfma_f32_16x16x32_bf16 v[112:115], v[32:35], v[76:79], v[112:115]\n"
"ds_read_b64_tr_b16 v[28:29], v12 offset:8192\n"
"ds_read_b64_tr_b16 v[30:31], v12 offset:10240\n"
"v_mfma_f32_16x16x32_bf16 v[116:119], v[32:35], v[108:111], v[116:119]\n"
"v_cvt_pk_bf16_f32 v172, v140, v141\n"
"v_cvt_pk_bf16_f32 v173, v142, v143\n"
"v_mfma_f32_16x16x32_bf16 v[120:123], v[32:35], v[236:239], v[120:123]\n"
"v_cvt_pk_bf16_f32 v174, v152, v153\n"
"s_waitcnt lgkmcnt(10)\n"
"v_mfma_f32_16x16x32_bf16 v[112:115], v[36:39], v[80:83], v[112:115]\n"
"ds_read_b64_tr_b16 v[32:33], v12 offset:12288\n"
"ds_read_b64_tr_b16 v[34:35], v12 offset:14336\n"
"v_mfma_f32_16x16x32_bf16 v[116:119], v[36:39], v[208:211], v[116:119]\n"
"v_cvt_pk_bf16_f32 v175, v154, v155\n"
"v_permlane16_swap_b32 v172, v174\n"
"v_mfma_f32_16x16x32_bf16 v[120:123], v[36:39], v[240:243], v[120:123]\n"
"v_permlane16_swap_b32 v173, v175\n"
"buffer_store_dwordx4 v[172:175], v201, s[52:55], s78 offen offset:1024 sc1\n"
"v_mfma_f32_16x16x32_bf16 v[112:115], v[40:43], v[84:87], v[112:115]\n"
"ds_read_b64_tr_b16 v[36:37], v12 offset:16384\n"
"ds_read_b64_tr_b16 v[38:39], v12 offset:18432\n"
"v_mfma_f32_16x16x32_bf16 v[116:119], v[40:43], v[212:215], v[116:119]\n"
"v_cvt_pk_bf16_f32 v180, v144, v145\n"
"v_cvt_pk_bf16_f32 v181, v146, v147\n"
"v_mfma_f32_16x16x32_bf16 v[120:123], v[40:43], v[244:247], v[120:123]\n"
"v_cvt_pk_bf16_f32 v182, v156, v157\n"
"s_waitcnt lgkmcnt(10)\n"
"v_mfma_f32_16x16x32_bf16 v[112:115], v[44:47], v[88:91], v[112:115]\n"
"ds_read_b64_tr_b16 v[40:41], v12 offset:20480\n"
"ds_read_b64_tr_b16 v[42:43], v12 offset:22528\n"
"v_mfma_f32_16x16x32_bf16 v[116:119], v[44:47], v[216:219], v[116:119]\n"
"v_cvt_pk_bf16_f32 v183, v158, v159\n"
"v_permlane16_swap_b32 v180, v182\n"
"v_mfma_f32_16x16x32_bf16 v[120:123], v[44:47], v[248:251], v[120:123]\n"
"v_permlane16_swap_b32 v181, v183\n"
"buffer_store_dwordx4 v[180:183], v201, s[52:55], s79 offen offset:1024 sc1\n"
"v_mfma_f32_16x16x32_bf16 v[112:115], v[48:51], v[92:95], v[112:115]\n"
"ds_read_b64_tr_b16 v[44:45], v12 offset:24576\n"
"ds_read_b64_tr_b16 v[46:47], v12 offset:26624\n"
"v_mfma_f32_16x16x32_bf16 v[116:119], v[48:51], v[220:223], v[116:119]\n"
"s_add_u32 s52, s52, 22528\n"
"s_addc_u32 s53, s53, 0\n"
"s_sub_u32 s54, s54, 22528\n"
"v_mfma_f32_16x16x32_bf16 v[120:123], v[48:51], v[252:255], v[120:123]\n"
"s_waitcnt lgkmcnt(10)\n"
"v_mfma_f32_16x16x32_bf16 v[124:127], v[20:23], v[64:67], 0\n"
"ds_read_b64_tr_b16 v[48:49], v12 offset:28672\n"
"ds_read_b64_tr_b16 v[50:51], v12 offset:30720\n"
"v_mfma_f32_16x16x32_bf16 v[128:131], v[20:23], v[96:99], 0\n"
"v_mfma_f32_16x16x32_bf16 v[132:135], v[20:23], v[224:227], 0\n"
"v_mfma_f32_16x16x32_bf16 v[124:127], v[24:27], v[68:71], v[124:127]\n"
"ds_read_b64_tr_b16 v[20:21], v11 offset:512\n"
"ds_read_b64_tr_b16 v[22:23], v11 offset:2560\n"
"v_mfma_f32_16x16x32_bf16 v[128:131], v[24:27], v[100:103], v[128:131]\n"
"v_mfma_f32_16x16x32_bf16 v[132:135], v[24:27], v[228:231], v[132:135]\n"
"s_waitcnt lgkmcnt(10)\n"
"v_mfma_f32_16x16x32_bf16 v[124:127], v[28:31], v[72:75], v[124:127]\n"
"ds_read_b64_tr_b16 v[24:25], v11 offset:4608\n"
"ds_read_b64_tr_b16 v[26:27], v11 offset:6656\n"
"v_mfma_f32_16x16x32_bf16 v[128:131], v[28:31], v[104:107], v[128:131]\n"
"v_mfma_f32_16x16x32_bf16 v[132:135], v[28:31], v[232:235], v[132:135]\n"
"v_mfma_f32_16x16x32_bf16 v[124:127], v[32:35], v[76:79], v[124:127]\n"
"ds_read_b64_tr_b16 v[28:29], v11 offset:8704\n"
"ds_read_b64_tr_b16 v[30:31], v11 offset:10752\n"
"v_mfma_f32_16x16x32_bf16 v[128:131], v[32:35], v[108:111], v[128:131]\n"
"v_mfma_f32_16x16x32_bf16 v[132:135], v[32:35], v[236:239], v[132:135]\n"
"s_waitcnt lgkmcnt(10)\n"
"v_mfma_f32_16x16x32_bf16 v[124:127], v[36:39], v[80:83], v[124:127]\n"
"ds_read_b64_tr_b16 v[32:33], v11 offset:12800\n"
"ds_read_b64_tr_b16 v[34:35], v11 offset:14848\n"
"v_mfma_f32_16x16x32_bf16 v[128:131], v[36:39], v[208:211], v[128:131]\n"
"v_mfma_f32_16x16x32_bf16 v[132:135], v[36:39], v[240:243], v[132:135]\n"
"v_mfma_f32_16x16x32_bf16 v[124:127], v[40:43], v[84:87], v[124:127]\n"
"ds_read_b64_tr_b16 v[36:37], v11 offset:16896\n"
"ds_read_b64_tr_b16 v[38:39], v11 offset:18944\n"
"v_mfma_f32_16x16x32_bf16 v[128:131], v[40:43], v[212:215], v[128:131]\n"
"v_mfma_f32_16x16x32_bf16 v[132:135], v[40:43], v[244:247], v[132:135]\n"
"s_waitcnt lgkmcnt(10)\n"
"v_mfma_f32_16x16x32_bf16 v[124:127], v[44:47], v[88:91], v[124:127]\n"
"ds_read_b64_tr_b16 v[40:41], v11 offset:20992\n"
"ds_read_b64_tr_b16 v[42:43], v11 offset:23040\n"
"v_mfma_f32_16x16x32_bf16 v[128:131], v[44:47], v[216:219], v[128:131]\n"
"v_mfma_f32_16x16x32_bf16 v[132:135], v[44:47], v[248:251], v[132:135]\n"
"v_mfma_f32_16x16x32_bf16 v[124:127], v[48:51], v[92:95], v[124:127]\n"
"ds_read_b64_tr_b16 v[44:45], v11 offset:25088\n"
"ds_read_b64_tr_b16 v[46:47], v11 offset:27136\n"
"v_mfma_f32_16x16x32_bf16 v[128:131], v[48:51], v[220:223], v[128:131]\n"
"v_mfma_f32_16x16x32_bf16 v[132:135], v[48:51], v[252:255], v[132:135]\n"
"s_waitcnt lgkmcnt(10)\n"
"v_mfma_f32_16x16x32_bf16 v[136:139], v[20:23], v[64:67], 0\n"
"ds_read_b64_tr_b16 v[48:49], v11 offset:29184\n"
"ds_read_b64_tr_b16 v[50:51], v11 offset:31232\n"
"v_mfma_f32_16x16x32_bf16 v[140:143], v[20:23], v[96:99], 0\n"
"v_mfma_f32_16x16x32_bf16 v[144:147], v[20:23], v[224:227], 0\n"
"v_mfma_f32_16x16x32_bf16 v[136:139], v[24:27], v[68:71], v[136:139]\n"
"ds_read_b64_tr_b16 v[20:21], v12 offset:512\n"
"ds_read_b64_tr_b16 v[22:23], v12 offset:2560\n"
"v_mfma_f32_16x16x32_bf16 v[140:143], v[24:27], v[100:103], v[140:143]\n"
"v_cvt_pk_bf16_f32 v160, v112, v113\n"
"v_cvt_pk_bf16_f32 v161, v114, v115\n"
"v_mfma_f32_16x16x32_bf16 v[144:147], v[24:27], v[228:231], v[144:147]\n"
"v_cvt_pk_bf16_f32 v162, v124, v125\n"
"s_waitcnt lgkmcnt(10)\n"
"v_mfma_f32_16x16x32_bf16 v[136:139], v[28:31], v[72:75], v[136:139]\n"
"ds_read_b64_tr_b16 v[24:25], v12 offset:4608\n"
"ds_read_b64_tr_b16 v[26:27], v12 offset:6656\n"
"v_mfma_f32_16x16x32_bf16 v[140:143], v[28:31], v[104:107], v[140:143]\n"
"v_cvt_pk_bf16_f32 v163, v126, v127\n"
"v_permlane16_swap_b32 v160, v162\n"
"v_mfma_f32_16x16x32_bf16 v[144:147], v[28:31], v[232:235], v[144:147]\n"
"v_permlane16_swap_b32 v161, v163\n"
"buffer_store_dwordx4 v[160:163], v201, s[52:55], s77 offen offset:0 sc1\n"
"v_mfma_f32_16x16x32_bf16 v[136:139], v[32:35], v[76:79], v[136:139]\n"
"ds_read_b64_tr_b16 v[28:29], v12 offset:8704\n"
"ds_read_b64_tr_b16 v[30:31], v12 offset:10752\n"
"v_mfma_f32_16x16x32_bf16 v[140:143], v[32:35], v[108:111], v[140:143]\n"
"v_cvt_pk_bf16_f32 v168, v116, v117\n"
"v_cvt_pk_bf16_f32 v169, v118, v119\n"
"v_mfma_f32_16x16x32_bf16 v[144:147], v[32:35], v[236:239], v[144:147]\n"
"v_cvt_pk_bf16_f32 v170, v128, v129\n"
"s_waitcnt lgkmcnt(10)\n"
"v_mfma_f32_16x16x32_bf16 v[136:139], v[36:39], v[80:83], v[136:139]\n"
"ds_read_b64_tr_b16 v[32:33], v12 offset:12800\n"
"ds_read_b64_tr_b16 v[34:35], v12 offset:14848\n"
"v_mfma_f32_16x16x32_bf16 v[140:143], v[36:39], v[208:211], v[140:143]\n"
"v_cvt_pk_bf16_f32 v171, v130, v131\n"
"v_permlane16_swap_b32 v168, v170\n"
"v_mfma_f32_16x16x32_bf16 v[144:147], v[36:39], v[240:243], v[144:147]\n"
"v_permlane16_swap_b32 v169, v171\n"
"buffer_store_dwordx4 v[168:171], v201, s[52:55], s78 offen offset:0 sc1\n"
"v_mfma_f32_16x16x32_bf16 v[136:139], v[40:43], v[84:87], v[136:139]\n"
"ds_read_b64_tr_b16 v[36:37], v12 offset:16896\n"
"ds_read_b64_tr_b16 v[38:39], v12 offset:18944\n"
"v_mfma_f32_16x16x32_bf16 v[140:143], v[40:43], v[212:215], v[140:143]\n"
"v_cvt_pk_bf16_f32 v176, v120, v121\n"
"v_cvt_pk_bf16_f32 v177, v122, v123\n"
"v_mfma_f32_16x16x32_bf16 v[144:147], v[40:43], v[244:247], v[144:147]\n"
"v_cvt_pk_bf16_f32 v178, v132, v133\n"
"s_waitcnt lgkmcnt(10)\n"
"v_mfma_f32_16x16x32_bf16 v[136:139], v[44:47], v[88:91], v[136:139]\n"
"ds_read_b64_tr_b16 v[40:41], v12 offset:20992\n"
"ds_read_b64_tr_b16 v[42:43], v12 offset:23040\n"
"v_mfma_f32_16x16x32_bf16 v[140:143], v[44:47], v[216:219], v[140:143]\n"
"v_cvt_pk_bf16_f32 v179, v134, v135\n"
"v_permlane16_swap_b32 v176, v178\n"
"v_mfma_f32_16x16x32_bf16 v[144:147], v[44:47], v[248:251], v[144:147]\n"
"v_permlane16_swap_b32 v177, v179\n"
"buffer_store_dwordx4 v[176:179], v201, s[52:55], s79 offen offset:0 sc1\n"
"v_mfma_f32_16x16x32_bf16 v[136:139], v[48:51], v[92:95], v[136:139]\n"
"ds_read_b64_tr_b16 v[44:45], v12 offset:25088\n"
"ds_read_b64_tr_b16 v[46:47], v12 offset:27136\n"
"v_mfma_f32_16x16x32_bf16 v[140:143], v[48:51], v[220:223], v[140:143]\n"
"v_mfma_f32_16x16x32_bf16 v[144:147], v[48:51], v[252:255], v[144:147]\n"
"s_waitcnt lgkmcnt(10)\n"
"v_mfma_f32_16x16x32_bf16 v[148:151], v[20:23], v[64:67], 0\n"
"ds_read_b64_tr_b16 v[48:49], v12 offset:29184\n"
"ds_read_b64_tr_b16 v[50:51], v12 offset:31232\n"
"v_mfma_f32_16x16x32_bf16 v[152:155], v[20:23], v[96:99], 0\n"
"v_mfma_f32_16x16x32_bf16 v[156:159], v[20:23], v[224:227], 0\n"
"v_mfma_f32_16x16x32_bf16 v[148:151], v[24:27], v[68:71], v[148:151]\n"
"v_mfma_f32_16x16x32_bf16 v[152:155], v[24:27], v[100:103], v[152:155]\n"
"v_mfma_f32_16x16x32_bf16 v[156:159], v[24:27], v[228:231], v[156:159]\n"
"s_waitcnt lgkmcnt(8)\n"
"v_mfma_f32_16x16x32_bf16 v[148:151], v[28:31], v[72:75], v[148:151]\n"
"v_mfma_f32_16x16x32_bf16 v[152:155], v[28:31], v[104:107], v[152:155]\n"
"v_mfma_f32_16x16x32_bf16 v[156:159], v[28:31], v[232:235], v[156:159]\n"
"v_mfma_f32_16x16x32_bf16 v[148:151], v[32:35], v[76:79], v[148:151]\n"
"v_mfma_f32_16x16x32_bf16 v[152:155], v[32:35], v[108:111], v[152:155]\n"
"v_mfma_f32_16x16x32_bf16 v[156:159], v[32:35], v[236:239], v[156:159]\n"
"s_waitcnt lgkmcnt(4)\n"
"v_mfma_f32_16x16x32_bf16 v[148:151], v[36:39], v[80:83], v[148:151]\n"
"v_mfma_f32_16x16x32_bf16 v[152:155], v[36:39], v[208:211], v[152:155]\n"
"v_mfma_f32_16x16x32_bf16 v[156:159], v[36:39], v[240:243], v[156:159]\n"
"v_mfma_f32_16x16x32_bf16 v[148:151], v[40:43], v[84:87], v[148:151]\n"
"v_mfma_f32_16x16x32_bf16 v[152:155], v[40:43], v[212:215], v[152:155]\n"
"v_mfma_f32_16x16x32_bf16 v[156:159], v[40:43], v[244:247], v[156:159]\n"
"s_waitcnt lgkmcnt(0)\n"
"v_mfma_f32_16x16x32_bf16 v[148:151], v[44:47], v[88:91], v[148:151]\n"
"v_mfma_f32_16x16x32_bf16 v[152:155], v[44:47], v[216:219], v[152:155]\n"
"v_mfma_f32_16x16x32_bf16 v[156:159], v[44:47], v[248:251], v[156:159]\n"
"v_mfma_f32_16x16x32_bf16 v[148:151], v[48:51], v[92:95], v[148:151]\n"
"v_mfma_f32_16x16x32_bf16 v[152:155], v[48:51], v[220:223], v[152:155]\n"
"v_mfma_f32_16x16x32_bf16 v[156:159], v[48:51], v[252:255], v[156:159]\n"
"s_nop 7\n"
"v_cvt_pk_bf16_f32 v164, v136, v137\n"
"v_cvt_pk_bf16_f32 v165, v138, v139\n"
"v_cvt_pk_bf16_f32 v166, v148, v149\n"
"v_cvt_pk_bf16_f32 v167, v150, v151\n"
"s_nop 0\n"
"v_permlane16_swap_b32 v164, v166\n"
"v_permlane16_swap_b32 v165, v167\n"
"buffer_store_dwordx4 v[164:167], v201, s[52:55], s77 offen offset:1024 sc1\n"
"v_cvt_pk_bf16_f32 v172, v140, v141\n"
"v_cvt_pk_bf16_f32 v173, v142, v143\n"
"v_cvt_pk_bf16_f32 v174, v152, v153\n"
"v_cvt_pk_bf16_f32 v175, v154, v155\n"
"s_nop 0\n"
"v_permlane16_swap_b32 v172, v174\n"
"v_permlane16_swap_b32 v173, v175\n"
"buffer_store_dwordx4 v[172:175], v201, s[52:55], s78 offen offset:1024 sc1\n"
"v_cvt_pk_bf16_f32 v180, v144, v145\n"
"v_cvt_pk_bf16_f32 v181, v146, v147\n"
"v_cvt_pk_bf16_f32 v182, v156, v157\n"
"v_cvt_pk_bf16_f32 v183, v158, v159\n"
"s_nop 0\n"
"v_permlane16_swap_b32 v180, v182\n"
"v_permlane16_swap_b32 v181, v183\n"
"buffer_store_dwordx4 v[180:183], v201, s[52:55], s79 offen offset:1024 sc1\n"
"L_fin_w0:\n"
"s_waitcnt vmcnt(0)\n"
"s_branch L_end\n"
"L_rare_w0f:\n"
"s_nop 7\n"
"v_mov_b32 v187, v13\n"
"s_nop 1\n"
"v_permlane32_swap_b32 v13, v187\n"
"s_nop 1\n"
"v_max_f32 v13, v13, v187\n"
"v_mov_b32 v187, v13\n"
"s_nop 1\n"
"v_permlane16_swap_b32 v13, v187\n"
"s_nop 1\n"
"v_max_f32 v13, v13, v187\n"
"v_max_f32 v13, 0, v13\n"
"v_add_f32 v17, v17, v13\n"
"v_sub_f32 v187, 0, v13\n"
"v_exp_f32 v184, v187\n"
"v_sub_f32 v112, v112, v13\n"
"v_sub_f32 v113, v113, v13\n"
"v_sub_f32 v114, v114, v13\n"
"v_sub_f32 v115, v115, v13\n"
"v_sub_f32 v124, v124, v13\n"
"v_sub_f32 v125, v125, v13\n"
"v_sub_f32 v126, v126, v13\n"
"v_sub_f32 v127, v127, v13\n"
"v_sub_f32 v136, v136, v13\n"
"v_sub_f32 v137, v137, v13\n"
"v_sub_f32 v138, v138, v13\n"
"v_sub_f32 v139, v139, v13\n"
"v_sub_f32 v148, v148, v13\n"
"v_sub_f32 v149, v149, v13\n"
"v_sub_f32 v150, v150, v13\n"
"v_sub_f32 v151, v151, v13\n"
"v_sub_f32 v52, 0, v17\n"
"v_sub_f32 v53, 0, v17\n"
"v_sub_f32 v54, 0, v17\n"
"v_sub_f32 v55, 0, v17\n"
"v_mov_b32 v187, v14\n"
"s_nop 1\n"
"v_permlane32_swap_b32 v14, v187\n"
"s_nop 1\n"
"v_max_f32 v14, v14, v187\n"
"v_mov_b32 v187, v14\n"
"s_nop 1\n"
"v_permlane16_swap_b32 v14, v187\n"
"s_nop 1\n"
"v_max_f32 v14, v14, v187\n"
"v_max_f32 v14, 0, v14\n"
"v_add_f32 v18, v18, v14\n"
"v_sub_f32 v187, 0, v14\n"
"v_exp_f32 v185, v187\n"
"v_sub_f32 v116, v116, v14\n"
"v_sub_f32 v117, v117, v14\n"
"v_sub_f32 v118, v118, v14\n"
"v_sub_f32 v119, v119, v14\n"
"v_sub_f32 v128, v128, v14\n"
"v_sub_f32 v129, v129, v14\n"
"v_sub_f32 v130, v130, v14\n"
"v_sub_f32 v131, v131, v14\n"
"v_sub_f32 v140, v140, v14\n"
"v_sub_f32 v141, v141, v14\n"
"v_sub_f32 v142, v142, v14\n"
"v_sub_f32 v143, v143, v14\n"
"v_sub_f32 v152, v152, v14\n"
"v_sub_f32 v153, v153, v14\n"
"v_sub_f32 v154, v154, v14\n"
"v_sub_f32 v155, v155, v14\n"
"v_sub_f32 v56, 0, v18\n"
"v_sub_f32 v57, 0, v18\n"
"v_sub_f32 v58, 0, v18\n"
"v_sub_f32 v59, 0, v18\n"
"v_mov_b32 v187, v15\n"
"s_nop 1\n"
"v_permlane32_swap_b32 v15, v187\n"
"s_nop 1\n"
"v_max_f32 v15, v15, v187\n"
"v_mov_b32 v187, v15\n"
"s_nop 1\n"
"v_permlane16_swap_b32 v15, v187\n"
"s_nop 1\n"
"v_max_f32 v15, v15, v187\n"
"v_max_f32 v15, 0, v15\n"
"v_add_f32 v19, v19, v15\n"
"v_sub_f32 v187, 0, v15\n"
"v_exp_f32 v186, v187\n"
"v_sub_f32 v120, v120, v15\n"
"v_sub_f32 v121, v121, v15\n"
"v_sub_f32 v122, v122, v15\n"
"v_sub_f32 v123, v123, v15\n"
"v_sub_f32 v132, v132, v15\n"
"v_sub_f32 v133, v133, v15\n"
"v_sub_f32 v134, v134, v15\n"
"v_sub_f32 v135, v135, v15\n"
"v_sub_f32 v144, v144, v15\n"
"v_sub_f32 v145, v145, v15\n"
"v_sub_f32 v146, v146, v15\n"
"v_sub_f32 v147, v147, v15\n"
"v_sub_f32 v156, v156, v15\n"
"v_sub_f32 v157, v157, v15\n"
"v_sub_f32 v158, v158, v15\n"
"v_sub_f32 v159, v159, v15\n"
"v_sub_f32 v60, 0, v19\n"
"v_sub_f32 v61, 0, v19\n"
"v_sub_f32 v62, 0, v19\n"
"v_sub_f32 v63, 0, v19\n"
"s_nop 1\n"
"s_mov_b32 s71, 1\n"
"s_branch L_back_w0f\n"
"L_resc_w0f:\n"
"s_nop 7\n"
"v_accvgpr_read_b32 v187, a0\n"
"v_accvgpr_read_b32 v188, a1\n"
"v_accvgpr_read_b32 v189, a2\n"
"v_accvgpr_read_b32 v190, a3\n"
"v_mul_f32 v187, v184, v187\n"
"v_mul_f32 v188, v184, v188\n"
"v_mul_f32 v189, v184, v189\n"
"v_mul_f32 v190, v184, v190\n"
"v_accvgpr_write_b32 a0, v187\n"
"v_accvgpr_write_b32 a1, v188\n"
"v_accvgpr_write_b32 a2, v189\n"
"v_accvgpr_write_b32 a3, v190\n"
"v_accvgpr_read_b32 v187, a4\n"
"v_accvgpr_read_b32 v188, a5\n"
"v_accvgpr_read_b32 v189, a6\n"
"v_accvgpr_read_b32 v190, a7\n"
"v_mul_f32 v187, v184, v187\n"
"v_mul_f32 v188, v184, v188\n"
"v_mul_f32 v189, v184, v189\n"
"v_mul_f32 v190, v184, v190\n"
"v_accvgpr_write_b32 a4, v187\n"
"v_accvgpr_write_b32 a5, v188\n"
"v_accvgpr_write_b32 a6, v189\n"
"v_accvgpr_write_b32 a7, v190\n"
"v_accvgpr_read_b32 v187, a8\n"
"v_accvgpr_read_b32 v188, a9\n"
"v_accvgpr_read_b32 v189, a10\n"
"v_accvgpr_read_b32 v190, a11\n"
"v_mul_f32 v187, v184, v187\n"
"v_mul_f32 v188, v184, v188\n"
"v_mul_f32 v189, v184, v189\n"
"v_mul_f32 v190, v184, v190\n"
"v_accvgpr_write_b32 a8, v187\n"
"v_accvgpr_write_b32 a9, v188\n"
"v_accvgpr_write_b32 a10, v189\n"
"v_accvgpr_write_b32 a11, v190\n"
"v_accvgpr_read_b32 v187, a12\n"
"v_accvgpr_read_b32 v188, a13\n"
"v_accvgpr_read_b32 v189, a14\n"
"v_accvgpr_read_b32 v190, a15\n"
"v_mul_f32 v187, v184, v187\n"
"v_mul_f32 v188, v184, v188\n"
"v_mul_f32 v189, v184, v189\n"
"v_mul_f32 v190, v184, v190\n"
"v_accvgpr_write_b32 a12, v187\n"
"v_accvgpr_write_b32 a13, v188\n"
"v_accvgpr_write_b32 a14, v189\n"
"v_accvgpr_write_b32 a15, v190\n"
"v_accvgpr_read_b32 v187, a16\n"
"v_accvgpr_read_b32 v188, a17\n"
"v_accvgpr_read_b32 v189, a18\n"
"v_accvgpr_read_b32 v190, a19\n"
"v_mul_f32 v187, v184, v187\n"
"v_mul_f32 v188, v184, v188\n"
"v_mul_f32 v189, v184, v189\n"
"v_mul_f32 v190, v184, v190\n"
"v_accvgpr_write_b32 a16, v187\n"
"v_accvgpr_write_b32 a17, v188\n"
"v_accvgpr_write_b32 a18, v189\n"
"v_accvgpr_write_b32 a19, v190\n"
"v_accvgpr_read_b32 v187, a20\n"
"v_accvgpr_read_b32 v188, a21\n"
"v_accvgpr_read_b32 v189, a22\n"
"v_accvgpr_read_b32 v190, a23\n"
"v_mul_f32 v187, v184, v187\n"
"v_mul_f32 v188, v184, v188\n"
"v_mul_f32 v189, v184, v189\n"
"v_mul_f32 v190, v184, v190\n"
"v_accvgpr_write_b32 a20, v187\n"
"v_accvgpr_write_b32 a21, v188\n"
"v_accvgpr_write_b32 a22, v189\n"
"v_accvgpr_write_b32 a23, v190\n"
"v_accvgpr_read_b32 v187, a24\n"
"v_accvgpr_read_b32 v188, a25\n"
"v_accvgpr_read_b32 v189, a26\n"
"v_accvgpr_read_b32 v190, a27\n"
"v_mul_f32 v187, v184, v187\n"
"v_mul_f32 v188, v184, v188\n"
"v_mul_f32 v189, v184, v189\n"
"v_mul_f32 v190, v184, v190\n"
"v_accvgpr_write_b32 a24, v187\n"
"v_accvgpr_write_b32 a25, v188\n"
"v_accvgpr_write_b32 a26, v189\n"
"v_accvgpr_write_b32 a27, v190\n"
"v_accvgpr_read_b32 v187, a28\n"
"v_accvgpr_read_b32 v188, a29\n"
"v_accvgpr_read_b32 v189, a30\n"
"v_accvgpr_read_b32 v190, a31\n"
"v_mul_f32 v187, v184, v187\n"
"v_mul_f32 v188, v184, v188\n"
"v_mul_f32 v189, v184, v189\n"
"v_mul_f32 v190, v184, v190\n"
"v_accvgpr_write_b32 a28, v187\n"
"v_accvgpr_write_b32 a29, v188\n"
"v_accvgpr_write_b32 a30, v189\n"
"v_accvgpr_write_b32 a31, v190\n"
"v_accvgpr_read_b32 v187, a32\n"
"v_accvgpr_read_b32 v188, a33\n"
"v_accvgpr_read_b32 v189, a34\n"
"v_accvgpr_read_b32 v190, a35\n"
"v_mul_f32 v187, v184, v187\n"
"v_mul_f32 v188, v184, v188\n"
"v_mul_f32 v189, v184, v189\n"
"v_mul_f32 v190, v184, v190\n"
"v_accvgpr_write_b32 a32, v187\n"
"v_accvgpr_write_b32 a33, v188\n"
"v_accvgpr_write_b32 a34, v189\n"
"v_accvgpr_write_b32 a35, v190\n"
"v_accvgpr_read_b32 v187, a36\n"
"v_accvgpr_read_b32 v188, a37\n"
"v_accvgpr_read_b32 v189, a38\n"
"v_accvgpr_read_b32 v190, a39\n"
"v_mul_f32 v187, v184, v187\n"
"v_mul_f32 v188, v184, v188\n"
"v_mul_f32 v189, v184, v189\n"
"v_mul_f32 v190, v184, v190\n"
"v_accvgpr_write_b32 a36, v187\n"
"v_accvgpr_write_b32 a37, v188\n"
"v_accvgpr_write_b32 a38, v189\n"
"v_accvgpr_write_b32 a39, v190\n"
"v_accvgpr_read_b32 v187, a40\n"
"v_accvgpr_read_b32 v188, a41\n"
"v_accvgpr_read_b32 v189, a42\n"
"v_accvgpr_read_b32 v190, a43\n"
"v_mul_f32 v187, v184, v187\n"
"v_mul_f32 v188, v184, v188\n"
"v_mul_f32 v189, v184, v189\n"
"v_mul_f32 v190, v184, v190\n"
"v_accvgpr_write_b32 a40, v187\n"
"v_accvgpr_write_b32 a41, v188\n"
"v_accvgpr_write_b32 a42, v189\n"
"v_accvgpr_write_b32 a43, v190\n"
"v_accvgpr_read_b32 v187, a44\n"
"v_accvgpr_read_b32 v188, a45\n"
"v_accvgpr_read_b32 v189, a46\n"
"v_accvgpr_read_b32 v190, a47\n"
"v_mul_f32 v187, v184, v187\n"
"v_mul_f32 v188, v184, v188\n"
"v_mul_f32 v189, v184, v189\n"
"v_mul_f32 v190, v184, v190\n"
"v_accvgpr_write_b32 a44, v187\n"
"v_accvgpr_write_b32 a45, v188\n"
"v_accvgpr_write_b32 a46, v189\n"
"v_accvgpr_write_b32 a47, v190\n"
"v_accvgpr_read_b32 v187, a48\n"
"v_accvgpr_read_b32 v188, a49\n"
"v_accvgpr_read_b32 v189, a50\n"
"v_accvgpr_read_b32 v190, a51\n"
"v_mul_f32 v187, v184, v187\n"
"v_mul_f32 v188, v184, v188\n"
"v_mul_f32 v189, v184, v189\n"
"v_mul_f32 v190, v184, v190\n"
"v_accvgpr_write_b32 a48, v187\n"
"v_accvgpr_write_b32 a49, v188\n"
"v_accvgpr_write_b32 a50, v189\n"
"v_accvgpr_write_b32 a51, v190\n"
"v_accvgpr_read_b32 v187, a52\n"
"v_accvgpr_read_b32 v188, a53\n"
"v_accvgpr_read_b32 v189, a54\n"
"v_accvgpr_read_b32 v190, a55\n"
"v_mul_f32 v187, v184, v187\n"
"v_mul_f32 v188, v184, v188\n"
"v_mul_f32 v189, v184, v189\n"
"v_mul_f32 v190, v184, v190\n"
"v_accvgpr_write_b32 a52, v187\n"
"v_accvgpr_write_b32 a53, v188\n"
"v_accvgpr_write_b32 a54, v189\n"
"v_accvgpr_write_b32 a55, v190\n"
"v_accvgpr_read_b32 v187, a56\n"
"v_accvgpr_read_b32 v188, a57\n"
"v_accvgpr_read_b32 v189, a58\n"
"v_accvgpr_read_b32 v190, a59\n"
"v_mul_f32 v187, v184, v187\n"
"v_mul_f32 v188, v184, v188\n"
"v_mul_f32 v189, v184, v189\n"
"v_mul_f32 v190, v184, v190\n"
"v_accvgpr_write_b32 a56, v187\n"
"v_accvgpr_write_b32 a57, v188\n"
"v_accvgpr_write_b32 a58, v189\n"
"v_accvgpr_write_b32 a59, v190\n"
"v_accvgpr_read_b32 v187, a60\n"
"v_accvgpr_read_b32 v188, a61\n"
"v_accvgpr_read_b32 v189, a62\n"
"v_accvgpr_read_b32 v190, a63\n"
"v_mul_f32 v187, v184, v187\n"
"v_mul_f32 v188, v184, v188\n"
"v_mul_f32 v189, v184, v189\n"
"v_mul_f32 v190, v184, v190\n"
"v_accvgpr_write_b32 a60, v187\n"
"v_accvgpr_write_b32 a61, v188\n"
"v_accvgpr_write_b32 a62, v189\n"
"v_accvgpr_write_b32 a63, v190\n"
"v_accvgpr_read_b32 v187, a192\n"
"v_accvgpr_read_b32 v188, a193\n"
"v_accvgpr_read_b32 v189, a194\n"
"v_accvgpr_read_b32 v190, a195\n"
"v_mul_f32 v187, v184, v187\n"
"v_mul_f32 v188, v184, v188\n"
"v_mul_f32 v189, v184, v189\n"
"v_mul_f32 v190, v184, v190\n"
"v_accvgpr_write_b32 a192, v187\n"
"v_accvgpr_write_b32 a193, v188\n"
"v_accvgpr_write_b32 a194, v189\n"
"v_accvgpr_write_b32 a195, v190\n"
"v_accvgpr_read_b32 v187, a64\n"
"v_accvgpr_read_b32 v188, a65\n"
"v_accvgpr_read_b32 v189, a66\n"
"v_accvgpr_read_b32 v190, a67\n"
"v_mul_f32 v187, v185, v187\n"
"v_mul_f32 v188, v185, v188\n"
"v_mul_f32 v189, v185, v189\n"
"v_mul_f32 v190, v185, v190\n"
"v_accvgpr_write_b32 a64, v187\n"
"v_accvgpr_write_b32 a65, v188\n"
"v_accvgpr_write_b32 a66, v189\n"
"v_accvgpr_write_b32 a67, v190\n"
"v_accvgpr_read_b32 v187, a68\n"
"v_accvgpr_read_b32 v188, a69\n"
"v_accvgpr_read_b32 v189, a70\n"
"v_accvgpr_read_b32 v190, a71\n"
"v_mul_f32 v187, v185, v187\n"
"v_mul_f32 v188, v185, v188\n"
"v_mul_f32 v189, v185, v189\n"
"v_mul_f32 v190, v185, v190\n"
"v_accvgpr_write_b32 a68, v187\n"
"v_accvgpr_write_b32 a69, v188\n"
"v_accvgpr_write_b32 a70, v189\n"
"v_accvgpr_write_b32 a71, v190\n"
"v_accvgpr_read_b32 v187, a72\n"
"v_accvgpr_read_b32 v188, a73\n"
"v_accvgpr_read_b32 v189, a74\n"
"v_accvgpr_read_b32 v190, a75\n"
"v_mul_f32 v187, v185, v187\n"
"v_mul_f32 v188, v185, v188\n"
"v_mul_f32 v189, v185, v189\n"
"v_mul_f32 v190, v185, v190\n"
"v_accvgpr_write_b32 a72, v187\n"
"v_accvgpr_write_b32 a73, v188\n"
"v_accvgpr_write_b32 a74, v189\n"
"v_accvgpr_write_b32 a75, v190\n"
"v_accvgpr_read_b32 v187, a76\n"
"v_accvgpr_read_b32 v188, a77\n"
"v_accvgpr_read_b32 v189, a78\n"
"v_accvgpr_read_b32 v190, a79\n"
"v_mul_f32 v187, v185, v187\n"
"v_mul_f32 v188, v185, v188\n"
"v_mul_f32 v189, v185, v189\n"
"v_mul_f32 v190, v185, v190\n"
"v_accvgpr_write_b32 a76, v187\n"
"v_accvgpr_write_b32 a77, v188\n"
"v_accvgpr_write_b32 a78, v189\n"
"v_accvgpr_write_b32 a79, v190\n"
"v_accvgpr_read_b32 v187, a80\n"
"v_accvgpr_read_b32 v188, a81\n"
"v_accvgpr_read_b32 v189, a82\n"
"v_accvgpr_read_b32 v190, a83\n"
"v_mul_f32 v187, v185, v187\n"
"v_mul_f32 v188, v185, v188\n"
"v_mul_f32 v189, v185, v189\n"
"v_mul_f32 v190, v185, v190\n"
"v_accvgpr_write_b32 a80, v187\n"
"v_accvgpr_write_b32 a81, v188\n"
"v_accvgpr_write_b32 a82, v189\n"
"v_accvgpr_write_b32 a83, v190\n"
"v_accvgpr_read_b32 v187, a84\n"
"v_accvgpr_read_b32 v188, a85\n"
"v_accvgpr_read_b32 v189, a86\n"
"v_accvgpr_read_b32 v190, a87\n"
"v_mul_f32 v187, v185, v187\n"
"v_mul_f32 v188, v185, v188\n"
"v_mul_f32 v189, v185, v189\n"
"v_mul_f32 v190, v185, v190\n"
"v_accvgpr_write_b32 a84, v187\n"
"v_accvgpr_write_b32 a85, v188\n"
"v_accvgpr_write_b32 a86, v189\n"
"v_accvgpr_write_b32 a87, v190\n"
"v_accvgpr_read_b32 v187, a88\n"
"v_accvgpr_read_b32 v188, a89\n"
"v_accvgpr_read_b32 v189, a90\n"
"v_accvgpr_read_b32 v190, a91\n"
"v_mul_f32 v187, v185, v187\n"
"v_mul_f32 v188, v185, v188\n"
"v_mul_f32 v189, v185, v189\n"
"v_mul_f32 v190, v185, v190\n"
"v_accvgpr_write_b32 a88, v187\n"
"v_accvgpr_write_b32 a89, v188\n"
"v_accvgpr_write_b32 a90, v189\n"
"v_accvgpr_write_b32 a91, v190\n"
"v_accvgpr_read_b32 v187, a92\n"
"v_accvgpr_read_b32 v188, a93\n"
"v_accvgpr_read_b32 v189, a94\n"
"v_accvgpr_read_b32 v190, a95\n"
"v_mul_f32 v187, v185, v187\n"
"v_mul_f32 v188, v185, v188\n"
"v_mul_f32 v189, v185, v189\n"
"v_mul_f32 v190, v185, v190\n"
"v_accvgpr_write_b32 a92, v187\n"
"v_accvgpr_write_b32 a93, v188\n"
"v_accvgpr_write_b32 a94, v189\n"
"v_accvgpr_write_b32 a95, v190\n"
"v_accvgpr_read_b32 v187, a96\n"
"v_accvgpr_read_b32 v188, a97\n"
"v_accvgpr_read_b32 v189, a98\n"
"v_accvgpr_read_b32 v190, a99\n"
"v_mul_f32 v187, v185, v187\n"
"v_mul_f32 v188, v185, v188\n"
"v_mul_f32 v189, v185, v189\n"
"v_mul_f32 v190, v185, v190\n"
"v_accvgpr_write_b32 a96, v187\n"
"v_accvgpr_write_b32 a97, v188\n"
"v_accvgpr_write_b32 a98, v189\n"
"v_accvgpr_write_b32 a99, v190\n"
"v_accvgpr_read_b32 v187, a100\n"
"v_accvgpr_read_b32 v188, a101\n"
"v_accvgpr_read_b32 v189, a102\n"
"v_accvgpr_read_b32 v190, a103\n"
"v_mul_f32 v187, v185, v187\n"
"v_mul_f32 v188, v185, v188\n"
"v_mul_f32 v189, v185, v189\n"
"v_mul_f32 v190, v185, v190\n"
"v_accvgpr_write_b32 a100, v187\n"
"v_accvgpr_write_b32 a101, v188\n"
"v_accvgpr_write_b32 a102, v189\n"
"v_accvgpr_write_b32 a103, v190\n"
"v_accvgpr_read_b32 v187, a104\n"
"v_accvgpr_read_b32 v188, a105\n"
"v_accvgpr_read_b32 v189, a106\n"
"v_accvgpr_read_b32 v190, a107\n"
"v_mul_f32 v187, v185, v187\n"
"v_mul_f32 v188, v185, v188\n"
"v_mul_f32 v189, v185, v189\n"
"v_mul_f32 v190, v185, v190\n"
"v_accvgpr_write_b32 a104, v187\n"
"v_accvgpr_write_b32 a105, v188\n"
"v_accvgpr_write_b32 a106, v189\n"
"v_accvgpr_write_b32 a107, v190\n"
"v_accvgpr_read_b32 v187, a108\n"
"v_accvgpr_read_b32 v188, a109\n"
"v_accvgpr_read_b32 v189, a110\n"
"v_accvgpr_read_b32 v190, a111\n"
"v_mul_f32 v187, v185, v187\n"
"v_mul_f32 v188, v185, v188\n"
"v_mul_f32 v189, v185, v189\n"
"v_mul_f32 v190, v185, v190\n"
"v_accvgpr_write_b32 a108, v187\n"
"v_accvgpr_write_b32 a109, v188\n"
"v_accvgpr_write_b32 a110, v189\n"
"v_accvgpr_write_b32 a111, v190\n"
"v_accvgpr_read_b32 v187, a112\n"
"v_accvgpr_read_b32 v188, a113\n"
"v_accvgpr_read_b32 v189, a114\n"
"v_accvgpr_read_b32 v190, a115\n"
"v_mul_f32 v187, v185, v187\n"
"v_mul_f32 v188, v185, v188\n"
"v_mul_f32 v189, v185, v189\n"
"v_mul_f32 v190, v185, v190\n"
"v_accvgpr_write_b32 a112, v187\n"
"v_accvgpr_write_b32 a113, v188\n"
"v_accvgpr_write_b32 a114, v189\n"
"v_accvgpr_write_b32 a115, v190\n"
"v_accvgpr_read_b32 v187, a116\n"
"v_accvgpr_read_b32 v188, a117\n"
"v_accvgpr_read_b32 v189, a118\n"
"v_accvgpr_read_b32 v190, a119\n"
"v_mul_f32 v187, v185, v187\n"
"v_mul_f32 v188, v185, v188\n"
"v_mul_f32 v189, v185, v189\n"
"v_mul_f32 v190, v185, v190\n"
"v_accvgpr_write_b32 a116, v187\n"
"v_accvgpr_write_b32 a117, v188\n"
"v_accvgpr_write_b32 a118, v189\n"
"v_accvgpr_write_b32 a119, v190\n"
"v_accvgpr_read_b32 v187, a120\n"
"v_accvgpr_read_b32 v188, a121\n"
"v_accvgpr_read_b32 v189, a122\n"
"v_accvgpr_read_b32 v190, a123\n"
"v_mul_f32 v187, v185, v187\n"
"v_mul_f32 v188, v185, v188\n"
"v_mul_f32 v189, v185, v189\n"
"v_mul_f32 v190, v185, v190\n"
"v_accvgpr_write_b32 a120, v187\n"
"v_accvgpr_write_b32 a121, v188\n"
"v_accvgpr_write_b32 a122, v189\n"
"v_accvgpr_write_b32 a123, v190\n"
"v_accvgpr_read_b32 v187, a124\n"
"v_accvgpr_read_b32 v188, a125\n"
"v_accvgpr_read_b32 v189, a126\n"
"v_accvgpr_read_b32 v190, a127\n"
"v_mul_f32 v187, v185, v187\n"
"v_mul_f32 v188, v185, v188\n"
"v_mul_f32 v189, v185, v189\n"
"v_mul_f32 v190, v185, v190\n"
"v_accvgpr_write_b32 a124, v187\n"
"v_accvgpr_write_b32 a125, v188\n"
"v_accvgpr_write_b32 a126, v189\n"
"v_accvgpr_write_b32 a127, v190\n"
"v_accvgpr_read_b32 v187, a196\n"
"v_accvgpr_read_b32 v188, a197\n"
"v_accvgpr_read_b32 v189, a198\n"
"v_accvgpr_read_b32 v190, a199\n"
"v_mul_f32 v187, v185, v187\n"
"v_mul_f32 v188, v185, v188\n"
"v_mul_f32 v189, v185, v189\n"
"v_mul_f32 v190, v185, v190\n"
"v_accvgpr_write_b32 a196, v187\n"
"v_accvgpr_write_b32 a197, v188\n"
"v_accvgpr_write_b32 a198, v189\n"
"v_accvgpr_write_b32 a199, v190\n"
"v_accvgpr_read_b32 v187, a128\n"
"v_accvgpr_read_b32 v188, a129\n"
"v_accvgpr_read_b32 v189, a130\n"
"v_accvgpr_read_b32 v190, a131\n"
"v_mul_f32 v187, v186, v187\n"
"v_mul_f32 v188, v186, v188\n"
"v_mul_f32 v189, v186, v189\n"
"v_mul_f32 v190, v186, v190\n"
"v_accvgpr_write_b32 a128, v187\n"
"v_accvgpr_write_b32 a129, v188\n"
"v_accvgpr_write_b32 a130, v189\n"
"v_accvgpr_write_b32 a131, v190\n"
"v_accvgpr_read_b32 v187, a132\n"
"v_accvgpr_read_b32 v188, a133\n"
"v_accvgpr_read_b32 v189, a134\n"
"v_accvgpr_read_b32 v190, a135\n"
"v_mul_f32 v187, v186, v187\n"
"v_mul_f32 v188, v186, v188\n"
"v_mul_f32 v189, v186, v189\n"
"v_mul_f32 v190, v186, v190\n"
"v_accvgpr_write_b32 a132, v187\n"
"v_accvgpr_write_b32 a133, v188\n"
"v_accvgpr_write_b32 a134, v189\n"
"v_accvgpr_write_b32 a135, v190\n"
"v_accvgpr_read_b32 v187, a136\n"
"v_accvgpr_read_b32 v188, a137\n"
"v_accvgpr_read_b32 v189, a138\n"
"v_accvgpr_read_b32 v190, a139\n"
"v_mul_f32 v187, v186, v187\n"
"v_mul_f32 v188, v186, v188\n"
"v_mul_f32 v189, v186, v189\n"
"v_mul_f32 v190, v186, v190\n"
"v_accvgpr_write_b32 a136, v187\n"
"v_accvgpr_write_b32 a137, v188\n"
"v_accvgpr_write_b32 a138, v189\n"
"v_accvgpr_write_b32 a139, v190\n"
"v_accvgpr_read_b32 v187, a140\n"
"v_accvgpr_read_b32 v188, a141\n"
"v_accvgpr_read_b32 v189, a142\n"
"v_accvgpr_read_b32 v190, a143\n"
"v_mul_f32 v187, v186, v187\n"
"v_mul_f32 v188, v186, v188\n"
"v_mul_f32 v189, v186, v189\n"
"v_mul_f32 v190, v186, v190\n"
"v_accvgpr_write_b32 a140, v187\n"
"v_accvgpr_write_b32 a141, v188\n"
"v_accvgpr_write_b32 a142, v189\n"
"v_accvgpr_write_b32 a143, v190\n"
"v_accvgpr_read_b32 v187, a144\n"
"v_accvgpr_read_b32 v188, a145\n"
"v_accvgpr_read_b32 v189, a146\n"
"v_accvgpr_read_b32 v190, a147\n"
"v_mul_f32 v187, v186, v187\n"
"v_mul_f32 v188, v186, v188\n"
"v_mul_f32 v189, v186, v189\n"
"v_mul_f32 v190, v186, v190\n"
"v_accvgpr_write_b32 a144, v187\n"
"v_accvgpr_write_b32 a145, v188\n"
"v_accvgpr_write_b32 a146, v189\n"
"v_accvgpr_write_b32 a147, v190\n"
"v_accvgpr_read_b32 v187, a148\n"
"v_accvgpr_read_b32 v188, a149\n"
"v_accvgpr_read_b32 v189, a150\n"
"v_accvgpr_read_b32 v190, a151\n"
"v_mul_f32 v187, v186, v187\n"
"v_mul_f32 v188, v186, v188\n"
"v_mul_f32 v189, v186, v189\n"
"v_mul_f32 v190, v186, v190\n"
"v_accvgpr_write_b32 a148, v187\n"
"v_accvgpr_write_b32 a149, v188\n"
"v_accvgpr_write_b32 a150, v189\n"
"v_accvgpr_write_b32 a151, v190\n"
"v_accvgpr_read_b32 v187, a152\n"
"v_accvgpr_read_b32 v188, a153\n"
"v_accvgpr_read_b32 v189, a154\n"
"v_accvgpr_read_b32 v190, a155\n"
"v_mul_f32 v187, v186, v187\n"
"v_mul_f32 v188, v186, v188\n"
"v_mul_f32 v189, v186, v189\n"
"v_mul_f32 v190, v186, v190\n"
"v_accvgpr_write_b32 a152, v187\n"
"v_accvgpr_write_b32 a153, v188\n"
"v_accvgpr_write_b32 a154, v189\n"
"v_accvgpr_write_b32 a155, v190\n"
"v_accvgpr_read_b32 v187, a156\n"
"v_accvgpr_read_b32 v188, a157\n"
"v_accvgpr_read_b32 v189, a158\n"
"v_accvgpr_read_b32 v190, a159\n"
"v_mul_f32 v187, v186, v187\n"
"v_mul_f32 v188, v186, v188\n"
"v_mul_f32 v189, v186, v189\n"
"v_mul_f32 v190, v186, v190\n"
"v_accvgpr_write_b32 a156, v187\n"
"v_accvgpr_write_b32 a157, v188\n"
"v_accvgpr_write_b32 a158, v189\n"
"v_accvgpr_write_b32 a159, v190\n"
"v_accvgpr_read_b32 v187, a160\n"
"v_accvgpr_read_b32 v188, a161\n"
"v_accvgpr_read_b32 v189, a162\n"
"v_accvgpr_read_b32 v190, a163\n"
"v_mul_f32 v187, v186, v187\n"
"v_mul_f32 v188, v186, v188\n"
"v_mul_f32 v189, v186, v189\n"
"v_mul_f32 v190, v186, v190\n"
"v_accvgpr_write_b32 a160, v187\n"
"v_accvgpr_write_b32 a161, v188\n"
"v_accvgpr_write_b32 a162, v189\n"
"v_accvgpr_write_b32 a163, v190\n"
"v_accvgpr_read_b32 v187, a164\n"
"v_accvgpr_read_b32 v188, a165\n"
"v_accvgpr_read_b32 v189, a166\n"
"v_accvgpr_read_b32 v190, a167\n"
"v_mul_f32 v187, v186, v187\n"
"v_mul_f32 v188, v186, v188\n"
"v_mul_f32 v189, v186, v189\n"
"v_mul_f32 v190, v186, v190\n"
"v_accvgpr_write_b32 a164, v187\n"
"v_accvgpr_write_b32 a165, v188\n"
"v_accvgpr_write_b32 a166, v189\n"
"v_accvgpr_write_b32 a167, v190\n"
"v_accvgpr_read_b32 v187, a168\n"
"v_accvgpr_read_b32 v188, a169\n"
"v_accvgpr_read_b32 v189, a170\n"
"v_accvgpr_read_b32 v190, a171\n"
"v_mul_f32 v187, v186, v187\n"
"v_mul_f32 v188, v186, v188\n"
"v_mul_f32 v189, v186, v189\n"
"v_mul_f32 v190, v186, v190\n"
"v_accvgpr_write_b32 a168, v187\n"
"v_accvgpr_write_b32 a169, v188\n"
"v_accvgpr_write_b32 a170, v189\n"
"v_accvgpr_write_b32 a171, v190\n"
"v_accvgpr_read_b32 v187, a172\n"
"v_accvgpr_read_b32 v188, a173\n"
"v_accvgpr_read_b32 v189, a174\n"
"v_accvgpr_read_b32 v190, a175\n"
"v_mul_f32 v187, v186, v187\n"
"v_mul_f32 v188, v186, v188\n"
"v_mul_f32 v189, v186, v189\n"
"v_mul_f32 v190, v186, v190\n"
"v_accvgpr_write_b32 a172, v187\n"
"v_accvgpr_write_b32 a173, v188\n"
"v_accvgpr_write_b32 a174, v189\n"
"v_accvgpr_write_b32 a175, v190\n"
"v_accvgpr_read_b32 v187, a176\n"
"v_accvgpr_read_b32 v188, a177\n"
"v_accvgpr_read_b32 v189, a178\n"
"v_accvgpr_read_b32 v190, a179\n"
"v_mul_f32 v187, v186, v187\n"
"v_mul_f32 v188, v186, v188\n"
"v_mul_f32 v189, v186, v189\n"
"v_mul_f32 v190, v186, v190\n"
"v_accvgpr_write_b32 a176, v187\n"
"v_accvgpr_write_b32 a177, v188\n"
"v_accvgpr_write_b32 a178, v189\n"
"v_accvgpr_write_b32 a179, v190\n"
"v_accvgpr_read_b32 v187, a180\n"
"v_accvgpr_read_b32 v188, a181\n"
"v_accvgpr_read_b32 v189, a182\n"
"v_accvgpr_read_b32 v190, a183\n"
"v_mul_f32 v187, v186, v187\n"
"v_mul_f32 v188, v186, v188\n"
"v_mul_f32 v189, v186, v189\n"
"v_mul_f32 v190, v186, v190\n"
"v_accvgpr_write_b32 a180, v187\n"
"v_accvgpr_write_b32 a181, v188\n"
"v_accvgpr_write_b32 a182, v189\n"
"v_accvgpr_write_b32 a183, v190\n"
"v_accvgpr_read_b32 v187, a184\n"
"v_accvgpr_read_b32 v188, a185\n"
"v_accvgpr_read_b32 v189, a186\n"
"v_accvgpr_read_b32 v190, a187\n"
"v_mul_f32 v187, v186, v187\n"
"v_mul_f32 v188, v186, v188\n"
"v_mul_f32 v189, v186, v189\n"
"v_mul_f32 v190, v186, v190\n"
"v_accvgpr_write_b32 a184, v187\n"
"v_accvgpr_write_b32 a185, v188\n"
"v_accvgpr_write_b32 a186, v189\n"
"v_accvgpr_write_b32 a187, v190\n"
"v_accvgpr_read_b32 v187, a188\n"
"v_accvgpr_read_b32 v188, a189\n"
"v_accvgpr_read_b32 v189, a190\n"
"v_accvgpr_read_b32 v190, a191\n"
"v_mul_f32 v187, v186, v187\n"
"v_mul_f32 v188, v186, v188\n"
"v_mul_f32 v189, v186, v189\n"
"v_mul_f32 v190, v186, v190\n"
"v_accvgpr_write_b32 a188, v187\n"
"v_accvgpr_write_b32 a189, v188\n"
"v_accvgpr_write_b32 a190, v189\n"
"v_accvgpr_write_b32 a191, v190\n"
"v_accvgpr_read_b32 v187, a200\n"
"v_accvgpr_read_b32 v188, a201\n"
"v_accvgpr_read_b32 v189, a202\n"
"v_accvgpr_read_b32 v190, a203\n"
"v_mul_f32 v187, v186, v187\n"
"v_mul_f32 v188, v186, v188\n"
"v_mul_f32 v189, v186, v189\n"
"v_mul_f32 v190, v186, v190\n"
"v_accvgpr_write_b32 a200, v187\n"
"v_accvgpr_write_b32 a201, v188\n"
"v_accvgpr_write_b32 a202, v189\n"
"v_accvgpr_write_b32 a203, v190\n"
"s_nop 1\n"
"s_mov_b32 s71, 0\n"
"s_branch L_rescback_w0f\n"
"L_rare_w0l:\n"
"s_nop 7\n"
"v_mov_b32 v187, v13\n"
"s_nop 1\n"
"v_permlane32_swap_b32 v13, v187\n"
"s_nop 1\n"
"v_max_f32 v13, v13, v187\n"
"v_mov_b32 v187, v13\n"
"s_nop 1\n"
"v_permlane16_swap_b32 v13, v187\n"
"s_nop 1\n"
"v_max_f32 v13, v13, v187\n"
"v_max_f32 v13, 0, v13\n"
"v_add_f32 v17, v17, v13\n"
"v_sub_f32 v187, 0, v13\n"
"v_exp_f32 v184, v187\n"
"v_sub_f32 v112, v112, v13\n"
"v_sub_f32 v113, v113, v13\n"
"v_sub_f32 v114, v114, v13\n"
"v_sub_f32 v115, v115, v13\n"
"v_sub_f32 v124, v124, v13\n"
"v_sub_f32 v125, v125, v13\n"
"v_sub_f32 v126, v126, v13\n"
"v_sub_f32 v127, v127, v13\n"
"v_sub_f32 v136, v136, v13\n"
"v_sub_f32 v137, v137, v13\n"
"v_sub_f32 v138, v138, v13\n"
"v_sub_f32 v139, v139, v13\n"
"v_sub_f32 v148, v148, v13\n"
"v_sub_f32 v149, v149, v13\n"
"v_sub_f32 v150, v150, v13\n"
"v_sub_f32 v151, v151, v13\n"
"v_sub_f32 v52, 0, v17\n"
"v_sub_f32 v53, 0, v17\n"
"v_sub_f32 v54, 0, v17\n"
"v_sub_f32 v55, 0, v17\n"
"v_mov_b32 v187, v14\n"
"s_nop 1\n"
"v_permlane32_swap_b32 v14, v187\n"
"s_nop 1\n"
"v_max_f32 v14, v14, v187\n"
"v_mov_b32 v187, v14\n"
"s_nop 1\n"
"v_permlane16_swap_b32 v14, v187\n"
"s_nop 1\n"
"v_max_f32 v14, v14, v187\n"
"v_max_f32 v14, 0, v14\n"
"v_add_f32 v18, v18, v14\n"
"v_sub_f32 v187, 0, v14\n"
"v_exp_f32 v185, v187\n"
"v_sub_f32 v116, v116, v14\n"
"v_sub_f32 v117, v117, v14\n"
"v_sub_f32 v118, v118, v14\n"
"v_sub_f32 v119, v119, v14\n"
"v_sub_f32 v128, v128, v14\n"
"v_sub_f32 v129, v129, v14\n"
"v_sub_f32 v130, v130, v14\n"
"v_sub_f32 v131, v131, v14\n"
"v_sub_f32 v140, v140, v14\n"
"v_sub_f32 v141, v141, v14\n"
"v_sub_f32 v142, v142, v14\n"
"v_sub_f32 v143, v143, v14\n"
"v_sub_f32 v152, v152, v14\n"
"v_sub_f32 v153, v153, v14\n"
"v_sub_f32 v154, v154, v14\n"
"v_sub_f32 v155, v155, v14\n"
"v_sub_f32 v56, 0, v18\n"
"v_sub_f32 v57, 0, v18\n"
"v_sub_f32 v58, 0, v18\n"
"v_sub_f32 v59, 0, v18\n"
"v_mov_b32 v187, v15\n"
"s_nop 1\n"
"v_permlane32_swap_b32 v15, v187\n"
"s_nop 1\n"
"v_max_f32 v15, v15, v187\n"
"v_mov_b32 v187, v15\n"
"s_nop 1\n"
"v_permlane16_swap_b32 v15, v187\n"
"s_nop 1\n"
"v_max_f32 v15, v15, v187\n"
"v_max_f32 v15, 0, v15\n"
"v_add_f32 v19, v19, v15\n"
"v_sub_f32 v187, 0, v15\n"
"v_exp_f32 v186, v187\n"
"v_sub_f32 v120, v120, v15\n"
"v_sub_f32 v121, v121, v15\n"
"v_sub_f32 v122, v122, v15\n"
"v_sub_f32 v123, v123, v15\n"
"v_sub_f32 v132, v132, v15\n"
"v_sub_f32 v133, v133, v15\n"
"v_sub_f32 v134, v134, v15\n"
"v_sub_f32 v135, v135, v15\n"
"v_sub_f32 v144, v144, v15\n"
"v_sub_f32 v145, v145, v15\n"
"v_sub_f32 v146, v146, v15\n"
"v_sub_f32 v147, v147, v15\n"
"v_sub_f32 v156, v156, v15\n"
"v_sub_f32 v157, v157, v15\n"
"v_sub_f32 v158, v158, v15\n"
"v_sub_f32 v159, v159, v15\n"
"v_sub_f32 v60, 0, v19\n"
"v_sub_f32 v61, 0, v19\n"
"v_sub_f32 v62, 0, v19\n"
"v_sub_f32 v63, 0, v19\n"
"s_nop 1\n"
"s_mov_b32 s71, 1\n"
"s_branch L_back_w0l\n"
"L_resc_w0l:\n"
"s_nop 7\n"
"v_accvgpr_read_b32 v187, a0\n"
"v_accvgpr_read_b32 v188, a1\n"
"v_accvgpr_read_b32 v189, a2\n"
"v_accvgpr_read_b32 v190, a3\n"
"v_mul_f32 v187, v184, v187\n"
"v_mul_f32 v188, v184, v188\n"
"v_mul_f32 v189, v184, v189\n"
"v_mul_f32 v190, v184, v190\n"
"v_accvgpr_write_b32 a0, v187\n"
"v_accvgpr_write_b32 a1, v188\n"
"v_accvgpr_write_b32 a2, v189\n"
"v_accvgpr_write_b32 a3, v190\n"
"v_accvgpr_read_b32 v187, a4\n"
"v_accvgpr_read_b32 v188, a5\n"
"v_accvgpr_read_b32 v189, a6\n"
"v_accvgpr_read_b32 v190, a7\n"
"v_mul_f32 v187, v184, v187\n"
"v_mul_f32 v188, v184, v188\n"
"v_mul_f32 v189, v184, v189\n"
"v_mul_f32 v190, v184, v190\n"
"v_accvgpr_write_b32 a4, v187\n"
"v_accvgpr_write_b32 a5, v188\n"
"v_accvgpr_write_b32 a6, v189\n"
"v_accvgpr_write_b32 a7, v190\n"
"v_accvgpr_read_b32 v187, a8\n"
"v_accvgpr_read_b32 v188, a9\n"
"v_accvgpr_read_b32 v189, a10\n"
"v_accvgpr_read_b32 v190, a11\n"
"v_mul_f32 v187, v184, v187\n"
"v_mul_f32 v188, v184, v188\n"
"v_mul_f32 v189, v184, v189\n"
"v_mul_f32 v190, v184, v190\n"
"v_accvgpr_write_b32 a8, v187\n"
"v_accvgpr_write_b32 a9, v188\n"
"v_accvgpr_write_b32 a10, v189\n"
"v_accvgpr_write_b32 a11, v190\n"
"v_accvgpr_read_b32 v187, a12\n"
"v_accvgpr_read_b32 v188, a13\n"
"v_accvgpr_read_b32 v189, a14\n"
"v_accvgpr_read_b32 v190, a15\n"
"v_mul_f32 v187, v184, v187\n"
"v_mul_f32 v188, v184, v188\n"
"v_mul_f32 v189, v184, v189\n"
"v_mul_f32 v190, v184, v190\n"
"v_accvgpr_write_b32 a12, v187\n"
"v_accvgpr_write_b32 a13, v188\n"
"v_accvgpr_write_b32 a14, v189\n"
"v_accvgpr_write_b32 a15, v190\n"
"v_accvgpr_read_b32 v187, a16\n"
"v_accvgpr_read_b32 v188, a17\n"
"v_accvgpr_read_b32 v189, a18\n"
"v_accvgpr_read_b32 v190, a19\n"
"v_mul_f32 v187, v184, v187\n"
"v_mul_f32 v188, v184, v188\n"
"v_mul_f32 v189, v184, v189\n"
"v_mul_f32 v190, v184, v190\n"
"v_accvgpr_write_b32 a16, v187\n"
"v_accvgpr_write_b32 a17, v188\n"
"v_accvgpr_write_b32 a18, v189\n"
"v_accvgpr_write_b32 a19, v190\n"
"v_accvgpr_read_b32 v187, a20\n"
"v_accvgpr_read_b32 v188, a21\n"
"v_accvgpr_read_b32 v189, a22\n"
"v_accvgpr_read_b32 v190, a23\n"
"v_mul_f32 v187, v184, v187\n"
"v_mul_f32 v188, v184, v188\n"
"v_mul_f32 v189, v184, v189\n"
"v_mul_f32 v190, v184, v190\n"
"v_accvgpr_write_b32 a20, v187\n"
"v_accvgpr_write_b32 a21, v188\n"
"v_accvgpr_write_b32 a22, v189\n"
"v_accvgpr_write_b32 a23, v190\n"
"v_accvgpr_read_b32 v187, a24\n"
"v_accvgpr_read_b32 v188, a25\n"
"v_accvgpr_read_b32 v189, a26\n"
"v_accvgpr_read_b32 v190, a27\n"
"v_mul_f32 v187, v184, v187\n"
"v_mul_f32 v188, v184, v188\n"
"v_mul_f32 v189, v184, v189\n"
"v_mul_f32 v190, v184, v190\n"
"v_accvgpr_write_b32 a24, v187\n"
"v_accvgpr_write_b32 a25, v188\n"
"v_accvgpr_write_b32 a26, v189\n"
"v_accvgpr_write_b32 a27, v190\n"
"v_accvgpr_read_b32 v187, a28\n"
"v_accvgpr_read_b32 v188, a29\n"
"v_accvgpr_read_b32 v189, a30\n"
"v_accvgpr_read_b32 v190, a31\n"
"v_mul_f32 v187, v184, v187\n"
"v_mul_f32 v188, v184, v188\n"
"v_mul_f32 v189, v184, v189\n"
"v_mul_f32 v190, v184, v190\n"
"v_accvgpr_write_b32 a28, v187\n"
"v_accvgpr_write_b32 a29, v188\n"
"v_accvgpr_write_b32 a30, v189\n"
"v_accvgpr_write_b32 a31, v190\n"
"v_accvgpr_read_b32 v187, a32\n"
"v_accvgpr_read_b32 v188, a33\n"
"v_accvgpr_read_b32 v189, a34\n"
"v_accvgpr_read_b32 v190, a35\n"
"v_mul_f32 v187, v184, v187\n"
"v_mul_f32 v188, v184, v188\n"
"v_mul_f32 v189, v184, v189\n"
"v_mul_f32 v190, v184, v190\n"
"v_accvgpr_write_b32 a32, v187\n"
"v_accvgpr_write_b32 a33, v188\n"
"v_accvgpr_write_b32 a34, v189\n"
"v_accvgpr_write_b32 a35, v190\n"
"v_accvgpr_read_b32 v187, a36\n"
"v_accvgpr_read_b32 v188, a37\n"
"v_accvgpr_read_b32 v189, a38\n"
"v_accvgpr_read_b32 v190, a39\n"
"v_mul_f32 v187, v184, v187\n"
"v_mul_f32 v188, v184, v188\n"
"v_mul_f32 v189, v184, v189\n"
"v_mul_f32 v190, v184, v190\n"
"v_accvgpr_write_b32 a36, v187\n"
"v_accvgpr_write_b32 a37, v188\n"
"v_accvgpr_write_b32 a38, v189\n"
"v_accvgpr_write_b32 a39, v190\n"
"v_accvgpr_read_b32 v187, a40\n"
"v_accvgpr_read_b32 v188, a41\n"
"v_accvgpr_read_b32 v189, a42\n"
"v_accvgpr_read_b32 v190, a43\n"
"v_mul_f32 v187, v184, v187\n"
"v_mul_f32 v188, v184, v188\n"
"v_mul_f32 v189, v184, v189\n"
"v_mul_f32 v190, v184, v190\n"
"v_accvgpr_write_b32 a40, v187\n"
"v_accvgpr_write_b32 a41, v188\n"
"v_accvgpr_write_b32 a42, v189\n"
"v_accvgpr_write_b32 a43, v190\n"
"v_accvgpr_read_b32 v187, a44\n"
"v_accvgpr_read_b32 v188, a45\n"
"v_accvgpr_read_b32 v189, a46\n"
"v_accvgpr_read_b32 v190, a47\n"
"v_mul_f32 v187, v184, v187\n"
"v_mul_f32 v188, v184, v188\n"
"v_mul_f32 v189, v184, v189\n"
"v_mul_f32 v190, v184, v190\n"
"v_accvgpr_write_b32 a44, v187\n"
"v_accvgpr_write_b32 a45, v188\n"
"v_accvgpr_write_b32 a46, v189\n"
"v_accvgpr_write_b32 a47, v190\n"
"v_accvgpr_read_b32 v187, a48\n"
"v_accvgpr_read_b32 v188, a49\n"
"v_accvgpr_read_b32 v189, a50\n"
"v_accvgpr_read_b32 v190, a51\n"
"v_mul_f32 v187, v184, v187\n"
"v_mul_f32 v188, v184, v188\n"
"v_mul_f32 v189, v184, v189\n"
"v_mul_f32 v190, v184, v190\n"
"v_accvgpr_write_b32 a48, v187\n"
"v_accvgpr_write_b32 a49, v188\n"
"v_accvgpr_write_b32 a50, v189\n"
"v_accvgpr_write_b32 a51, v190\n"
"v_accvgpr_read_b32 v187, a52\n"
"v_accvgpr_read_b32 v188, a53\n"
"v_accvgpr_read_b32 v189, a54\n"
"v_accvgpr_read_b32 v190, a55\n"
"v_mul_f32 v187, v184, v187\n"
"v_mul_f32 v188, v184, v188\n"
"v_mul_f32 v189, v184, v189\n"
"v_mul_f32 v190, v184, v190\n"
"v_accvgpr_write_b32 a52, v187\n"
"v_accvgpr_write_b32 a53, v188\n"
"v_accvgpr_write_b32 a54, v189\n"
"v_accvgpr_write_b32 a55, v190\n"
"v_accvgpr_read_b32 v187, a56\n"
"v_accvgpr_read_b32 v188, a57\n"
"v_accvgpr_read_b32 v189, a58\n"
"v_accvgpr_read_b32 v190, a59\n"
"v_mul_f32 v187, v184, v187\n"
"v_mul_f32 v188, v184, v188\n"
"v_mul_f32 v189, v184, v189\n"
"v_mul_f32 v190, v184, v190\n"
"v_accvgpr_write_b32 a56, v187\n"
"v_accvgpr_write_b32 a57, v188\n"
"v_accvgpr_write_b32 a58, v189\n"
"v_accvgpr_write_b32 a59, v190\n"
"v_accvgpr_read_b32 v187, a60\n"
"v_accvgpr_read_b32 v188, a61\n"
"v_accvgpr_read_b32 v189, a62\n"
"v_accvgpr_read_b32 v190, a63\n"
"v_mul_f32 v187, v184, v187\n"
"v_mul_f32 v188, v184, v188\n"
"v_mul_f32 v189, v184, v189\n"
"v_mul_f32 v190, v184, v190\n"
"v_accvgpr_write_b32 a60, v187\n"
"v_accvgpr_write_b32 a61, v188\n"
"v_accvgpr_write_b32 a62, v189\n"
"v_accvgpr_write_b32 a63, v190\n"
"v_accvgpr_read_b32 v187, a192\n"
"v_accvgpr_read_b32 v188, a193\n"
"v_accvgpr_read_b32 v189, a194\n"
"v_accvgpr_read_b32 v190, a195\n"
"v_mul_f32 v187, v184, v187\n"
"v_mul_f32 v188, v184, v188\n"
"v_mul_f32 v189, v184, v189\n"
"v_mul_f32 v190, v184, v190\n"
"v_accvgpr_write_b32 a192, v187\n"
"v_accvgpr_write_b32 a193, v188\n"
"v_accvgpr_write_b32 a194, v189\n"
"v_accvgpr_write_b32 a195, v190\n"
"v_accvgpr_read_b32 v187, a64\n"
"v_accvgpr_read_b32 v188, a65\n"
"v_accvgpr_read_b32 v189, a66\n"
"v_accvgpr_read_b32 v190, a67\n"
"v_mul_f32 v187, v185, v187\n"
"v_mul_f32 v188, v185, v188\n"
"v_mul_f32 v189, v185, v189\n"
"v_mul_f32 v190, v185, v190\n"
"v_accvgpr_write_b32 a64, v187\n"
"v_accvgpr_write_b32 a65, v188\n"
"v_accvgpr_write_b32 a66, v189\n"
"v_accvgpr_write_b32 a67, v190\n"
"v_accvgpr_read_b32 v187, a68\n"
"v_accvgpr_read_b32 v188, a69\n"
"v_accvgpr_read_b32 v189, a70\n"
"v_accvgpr_read_b32 v190, a71\n"
"v_mul_f32 v187, v185, v187\n"
"v_mul_f32 v188, v185, v188\n"
"v_mul_f32 v189, v185, v189\n"
"v_mul_f32 v190, v185, v190\n"
"v_accvgpr_write_b32 a68, v187\n"
"v_accvgpr_write_b32 a69, v188\n"
"v_accvgpr_write_b32 a70, v189\n"
"v_accvgpr_write_b32 a71, v190\n"
"v_accvgpr_read_b32 v187, a72\n"
"v_accvgpr_read_b32 v188, a73\n"
"v_accvgpr_read_b32 v189, a74\n"
"v_accvgpr_read_b32 v190, a75\n"
"v_mul_f32 v187, v185, v187\n"
"v_mul_f32 v188, v185, v188\n"
"v_mul_f32 v189, v185, v189\n"
"v_mul_f32 v190, v185, v190\n"
"v_accvgpr_write_b32 a72, v187\n"
"v_accvgpr_write_b32 a73, v188\n"
"v_accvgpr_write_b32 a74, v189\n"
"v_accvgpr_write_b32 a75, v190\n"
"v_accvgpr_read_b32 v187, a76\n"
"v_accvgpr_read_b32 v188, a77\n"
"v_accvgpr_read_b32 v189, a78\n"
"v_accvgpr_read_b32 v190, a79\n"
"v_mul_f32 v187, v185, v187\n"
"v_mul_f32 v188, v185, v188\n"
"v_mul_f32 v189, v185, v189\n"
"v_mul_f32 v190, v185, v190\n"
"v_accvgpr_write_b32 a76, v187\n"
"v_accvgpr_write_b32 a77, v188\n"
"v_accvgpr_write_b32 a78, v189\n"
"v_accvgpr_write_b32 a79, v190\n"
"v_accvgpr_read_b32 v187, a80\n"
"v_accvgpr_read_b32 v188, a81\n"
"v_accvgpr_read_b32 v189, a82\n"
"v_accvgpr_read_b32 v190, a83\n"
"v_mul_f32 v187, v185, v187\n"
"v_mul_f32 v188, v185, v188\n"
"v_mul_f32 v189, v185, v189\n"
"v_mul_f32 v190, v185, v190\n"
"v_accvgpr_write_b32 a80, v187\n"
"v_accvgpr_write_b32 a81, v188\n"
"v_accvgpr_write_b32 a82, v189\n"
"v_accvgpr_write_b32 a83, v190\n"
"v_accvgpr_read_b32 v187, a84\n"
"v_accvgpr_read_b32 v188, a85\n"
"v_accvgpr_read_b32 v189, a86\n"
"v_accvgpr_read_b32 v190, a87\n"
"v_mul_f32 v187, v185, v187\n"
"v_mul_f32 v188, v185, v188\n"
"v_mul_f32 v189, v185, v189\n"
"v_mul_f32 v190, v185, v190\n"
"v_accvgpr_write_b32 a84, v187\n"
"v_accvgpr_write_b32 a85, v188\n"
"v_accvgpr_write_b32 a86, v189\n"
"v_accvgpr_write_b32 a87, v190\n"
"v_accvgpr_read_b32 v187, a88\n"
"v_accvgpr_read_b32 v188, a89\n"
"v_accvgpr_read_b32 v189, a90\n"
"v_accvgpr_read_b32 v190, a91\n"
"v_mul_f32 v187, v185, v187\n"
"v_mul_f32 v188, v185, v188\n"
"v_mul_f32 v189, v185, v189\n"
"v_mul_f32 v190, v185, v190\n"
"v_accvgpr_write_b32 a88, v187\n"
"v_accvgpr_write_b32 a89, v188\n"
"v_accvgpr_write_b32 a90, v189\n"
"v_accvgpr_write_b32 a91, v190\n"
"v_accvgpr_read_b32 v187, a92\n"
"v_accvgpr_read_b32 v188, a93\n"
"v_accvgpr_read_b32 v189, a94\n"
"v_accvgpr_read_b32 v190, a95\n"
"v_mul_f32 v187, v185, v187\n"
"v_mul_f32 v188, v185, v188\n"
"v_mul_f32 v189, v185, v189\n"
"v_mul_f32 v190, v185, v190\n"
"v_accvgpr_write_b32 a92, v187\n"
"v_accvgpr_write_b32 a93, v188\n"
"v_accvgpr_write_b32 a94, v189\n"
"v_accvgpr_write_b32 a95, v190\n"
"v_accvgpr_read_b32 v187, a96\n"
"v_accvgpr_read_b32 v188, a97\n"
"v_accvgpr_read_b32 v189, a98\n"
"v_accvgpr_read_b32 v190, a99\n"
"v_mul_f32 v187, v185, v187\n"
"v_mul_f32 v188, v185, v188\n"
"v_mul_f32 v189, v185, v189\n"
"v_mul_f32 v190, v185, v190\n"
"v_accvgpr_write_b32 a96, v187\n"
"v_accvgpr_write_b32 a97, v188\n"
"v_accvgpr_write_b32 a98, v189\n"
"v_accvgpr_write_b32 a99, v190\n"
"v_accvgpr_read_b32 v187, a100\n"
"v_accvgpr_read_b32 v188, a101\n"
"v_accvgpr_read_b32 v189, a102\n"
"v_accvgpr_read_b32 v190, a103\n"
"v_mul_f32 v187, v185, v187\n"
"v_mul_f32 v188, v185, v188\n"
"v_mul_f32 v189, v185, v189\n"
"v_mul_f32 v190, v185, v190\n"
"v_accvgpr_write_b32 a100, v187\n"
"v_accvgpr_write_b32 a101, v188\n"
"v_accvgpr_write_b32 a102, v189\n"
"v_accvgpr_write_b32 a103, v190\n"
"v_accvgpr_read_b32 v187, a104\n"
"v_accvgpr_read_b32 v188, a105\n"
"v_accvgpr_read_b32 v189, a106\n"
"v_accvgpr_read_b32 v190, a107\n"
"v_mul_f32 v187, v185, v187\n"
"v_mul_f32 v188, v185, v188\n"
"v_mul_f32 v189, v185, v189\n"
"v_mul_f32 v190, v185, v190\n"
"v_accvgpr_write_b32 a104, v187\n"
"v_accvgpr_write_b32 a105, v188\n"
"v_accvgpr_write_b32 a106, v189\n"
"v_accvgpr_write_b32 a107, v190\n"
"v_accvgpr_read_b32 v187, a108\n"
"v_accvgpr_read_b32 v188, a109\n"
"v_accvgpr_read_b32 v189, a110\n"
"v_accvgpr_read_b32 v190, a111\n"
"v_mul_f32 v187, v185, v187\n"
"v_mul_f32 v188, v185, v188\n"
"v_mul_f32 v189, v185, v189\n"
"v_mul_f32 v190, v185, v190\n"
"v_accvgpr_write_b32 a108, v187\n"
"v_accvgpr_write_b32 a109, v188\n"
"v_accvgpr_write_b32 a110, v189\n"
"v_accvgpr_write_b32 a111, v190\n"
"v_accvgpr_read_b32 v187, a112\n"
"v_accvgpr_read_b32 v188, a113\n"
"v_accvgpr_read_b32 v189, a114\n"
"v_accvgpr_read_b32 v190, a115\n"
"v_mul_f32 v187, v185, v187\n"
"v_mul_f32 v188, v185, v188\n"
"v_mul_f32 v189, v185, v189\n"
"v_mul_f32 v190, v185, v190\n"
"v_accvgpr_write_b32 a112, v187\n"
"v_accvgpr_write_b32 a113, v188\n"
"v_accvgpr_write_b32 a114, v189\n"
"v_accvgpr_write_b32 a115, v190\n"
"v_accvgpr_read_b32 v187, a116\n"
"v_accvgpr_read_b32 v188, a117\n"
"v_accvgpr_read_b32 v189, a118\n"
"v_accvgpr_read_b32 v190, a119\n"
"v_mul_f32 v187, v185, v187\n"
"v_mul_f32 v188, v185, v188\n"
"v_mul_f32 v189, v185, v189\n"
"v_mul_f32 v190, v185, v190\n"
"v_accvgpr_write_b32 a116, v187\n"
"v_accvgpr_write_b32 a117, v188\n"
"v_accvgpr_write_b32 a118, v189\n"
"v_accvgpr_write_b32 a119, v190\n"
"v_accvgpr_read_b32 v187, a120\n"
"v_accvgpr_read_b32 v188, a121\n"
"v_accvgpr_read_b32 v189, a122\n"
"v_accvgpr_read_b32 v190, a123\n"
"v_mul_f32 v187, v185, v187\n"
"v_mul_f32 v188, v185, v188\n"
"v_mul_f32 v189, v185, v189\n"
"v_mul_f32 v190, v185, v190\n"
"v_accvgpr_write_b32 a120, v187\n"
"v_accvgpr_write_b32 a121, v188\n"
"v_accvgpr_write_b32 a122, v189\n"
"v_accvgpr_write_b32 a123, v190\n"
"v_accvgpr_read_b32 v187, a124\n"
"v_accvgpr_read_b32 v188, a125\n"
"v_accvgpr_read_b32 v189, a126\n"
"v_accvgpr_read_b32 v190, a127\n"
"v_mul_f32 v187, v185, v187\n"
"v_mul_f32 v188, v185, v188\n"
"v_mul_f32 v189, v185, v189\n"
"v_mul_f32 v190, v185, v190\n"
"v_accvgpr_write_b32 a124, v187\n"
"v_accvgpr_write_b32 a125, v188\n"
"v_accvgpr_write_b32 a126, v189\n"
"v_accvgpr_write_b32 a127, v190\n"
"v_accvgpr_read_b32 v187, a196\n"
"v_accvgpr_read_b32 v188, a197\n"
"v_accvgpr_read_b32 v189, a198\n"
"v_accvgpr_read_b32 v190, a199\n"
"v_mul_f32 v187, v185, v187\n"
"v_mul_f32 v188, v185, v188\n"
"v_mul_f32 v189, v185, v189\n"
"v_mul_f32 v190, v185, v190\n"
"v_accvgpr_write_b32 a196, v187\n"
"v_accvgpr_write_b32 a197, v188\n"
"v_accvgpr_write_b32 a198, v189\n"
"v_accvgpr_write_b32 a199, v190\n"
"v_accvgpr_read_b32 v187, a128\n"
"v_accvgpr_read_b32 v188, a129\n"
"v_accvgpr_read_b32 v189, a130\n"
"v_accvgpr_read_b32 v190, a131\n"
"v_mul_f32 v187, v186, v187\n"
"v_mul_f32 v188, v186, v188\n"
"v_mul_f32 v189, v186, v189\n"
"v_mul_f32 v190, v186, v190\n"
"v_accvgpr_write_b32 a128, v187\n"
"v_accvgpr_write_b32 a129, v188\n"
"v_accvgpr_write_b32 a130, v189\n"
"v_accvgpr_write_b32 a131, v190\n"
"v_accvgpr_read_b32 v187, a132\n"
"v_accvgpr_read_b32 v188, a133\n"
"v_accvgpr_read_b32 v189, a134\n"
"v_accvgpr_read_b32 v190, a135\n"
"v_mul_f32 v187, v186, v187\n"
"v_mul_f32 v188, v186, v188\n"
"v_mul_f32 v189, v186, v189\n"
"v_mul_f32 v190, v186, v190\n"
"v_accvgpr_write_b32 a132, v187\n"
"v_accvgpr_write_b32 a133, v188\n"
"v_accvgpr_write_b32 a134, v189\n"
"v_accvgpr_write_b32 a135, v190\n"
"v_accvgpr_read_b32 v187, a136\n"
"v_accvgpr_read_b32 v188, a137\n"
"v_accvgpr_read_b32 v189, a138\n"
"v_accvgpr_read_b32 v190, a139\n"
"v_mul_f32 v187, v186, v187\n"
"v_mul_f32 v188, v186, v188\n"
"v_mul_f32 v189, v186, v189\n"
"v_mul_f32 v190, v186, v190\n"
"v_accvgpr_write_b32 a136, v187\n"
"v_accvgpr_write_b32 a137, v188\n"
"v_accvgpr_write_b32 a138, v189\n"
"v_accvgpr_write_b32 a139, v190\n"
"v_accvgpr_read_b32 v187, a140\n"
"v_accvgpr_read_b32 v188, a141\n"
"v_accvgpr_read_b32 v189, a142\n"
"v_accvgpr_read_b32 v190, a143\n"
"v_mul_f32 v187, v186, v187\n"
"v_mul_f32 v188, v186, v188\n"
"v_mul_f32 v189, v186, v189\n"
"v_mul_f32 v190, v186, v190\n"
"v_accvgpr_write_b32 a140, v187\n"
"v_accvgpr_write_b32 a141, v188\n"
"v_accvgpr_write_b32 a142, v189\n"
"v_accvgpr_write_b32 a143, v190\n"
"v_accvgpr_read_b32 v187, a144\n"
"v_accvgpr_read_b32 v188, a145\n"
"v_accvgpr_read_b32 v189, a146\n"
"v_accvgpr_read_b32 v190, a147\n"
"v_mul_f32 v187, v186, v187\n"
"v_mul_f32 v188, v186, v188\n"
"v_mul_f32 v189, v186, v189\n"
"v_mul_f32 v190, v186, v190\n"
"v_accvgpr_write_b32 a144, v187\n"
"v_accvgpr_write_b32 a145, v188\n"
"v_accvgpr_write_b32 a146, v189\n"
"v_accvgpr_write_b32 a147, v190\n"
"v_accvgpr_read_b32 v187, a148\n"
"v_accvgpr_read_b32 v188, a149\n"
"v_accvgpr_read_b32 v189, a150\n"
"v_accvgpr_read_b32 v190, a151\n"
"v_mul_f32 v187, v186, v187\n"
"v_mul_f32 v188, v186, v188\n"
"v_mul_f32 v189, v186, v189\n"
"v_mul_f32 v190, v186, v190\n"
"v_accvgpr_write_b32 a148, v187\n"
"v_accvgpr_write_b32 a149, v188\n"
"v_accvgpr_write_b32 a150, v189\n"
"v_accvgpr_write_b32 a151, v190\n"
"v_accvgpr_read_b32 v187, a152\n"
"v_accvgpr_read_b32 v188, a153\n"
"v_accvgpr_read_b32 v189, a154\n"
"v_accvgpr_read_b32 v190, a155\n"
"v_mul_f32 v187, v186, v187\n"
"v_mul_f32 v188, v186, v188\n"
"v_mul_f32 v189, v186, v189\n"
"v_mul_f32 v190, v186, v190\n"
"v_accvgpr_write_b32 a152, v187\n"
"v_accvgpr_write_b32 a153, v188\n"
"v_accvgpr_write_b32 a154, v189\n"
"v_accvgpr_write_b32 a155, v190\n"
"v_accvgpr_read_b32 v187, a156\n"
"v_accvgpr_read_b32 v188, a157\n"
"v_accvgpr_read_b32 v189, a158\n"
"v_accvgpr_read_b32 v190, a159\n"
"v_mul_f32 v187, v186, v187\n"
"v_mul_f32 v188, v186, v188\n"
"v_mul_f32 v189, v186, v189\n"
"v_mul_f32 v190, v186, v190\n"
"v_accvgpr_write_b32 a156, v187\n"
"v_accvgpr_write_b32 a157, v188\n"
"v_accvgpr_write_b32 a158, v189\n"
"v_accvgpr_write_b32 a159, v190\n"
"v_accvgpr_read_b32 v187, a160\n"
"v_accvgpr_read_b32 v188, a161\n"
"v_accvgpr_read_b32 v189, a162\n"
"v_accvgpr_read_b32 v190, a163\n"
"v_mul_f32 v187, v186, v187\n"
"v_mul_f32 v188, v186, v188\n"
"v_mul_f32 v189, v186, v189\n"
"v_mul_f32 v190, v186, v190\n"
"v_accvgpr_write_b32 a160, v187\n"
"v_accvgpr_write_b32 a161, v188\n"
"v_accvgpr_write_b32 a162, v189\n"
"v_accvgpr_write_b32 a163, v190\n"
"v_accvgpr_read_b32 v187, a164\n"
"v_accvgpr_read_b32 v188, a165\n"
"v_accvgpr_read_b32 v189, a166\n"
"v_accvgpr_read_b32 v190, a167\n"
"v_mul_f32 v187, v186, v187\n"
"v_mul_f32 v188, v186, v188\n"
"v_mul_f32 v189, v186, v189\n"
"v_mul_f32 v190, v186, v190\n"
"v_accvgpr_write_b32 a164, v187\n"
"v_accvgpr_write_b32 a165, v188\n"
"v_accvgpr_write_b32 a166, v189\n"
"v_accvgpr_write_b32 a167, v190\n"
"v_accvgpr_read_b32 v187, a168\n"
"v_accvgpr_read_b32 v188, a169\n"
"v_accvgpr_read_b32 v189, a170\n"
"v_accvgpr_read_b32 v190, a171\n"
"v_mul_f32 v187, v186, v187\n"
"v_mul_f32 v188, v186, v188\n"
"v_mul_f32 v189, v186, v189\n"
"v_mul_f32 v190, v186, v190\n"
"v_accvgpr_write_b32 a168, v187\n"
"v_accvgpr_write_b32 a169, v188\n"
"v_accvgpr_write_b32 a170, v189\n"
"v_accvgpr_write_b32 a171, v190\n"
"v_accvgpr_read_b32 v187, a172\n"
"v_accvgpr_read_b32 v188, a173\n"
"v_accvgpr_read_b32 v189, a174\n"
"v_accvgpr_read_b32 v190, a175\n"
"v_mul_f32 v187, v186, v187\n"
"v_mul_f32 v188, v186, v188\n"
"v_mul_f32 v189, v186, v189\n"
"v_mul_f32 v190, v186, v190\n"
"v_accvgpr_write_b32 a172, v187\n"
"v_accvgpr_write_b32 a173, v188\n"
"v_accvgpr_write_b32 a174, v189\n"
"v_accvgpr_write_b32 a175, v190\n"
"v_accvgpr_read_b32 v187, a176\n"
"v_accvgpr_read_b32 v188, a177\n"
"v_accvgpr_read_b32 v189, a178\n"
"v_accvgpr_read_b32 v190, a179\n"
"v_mul_f32 v187, v186, v187\n"
"v_mul_f32 v188, v186, v188\n"
"v_mul_f32 v189, v186, v189\n"
"v_mul_f32 v190, v186, v190\n"
"v_accvgpr_write_b32 a176, v187\n"
"v_accvgpr_write_b32 a177, v188\n"
"v_accvgpr_write_b32 a178, v189\n"
"v_accvgpr_write_b32 a179, v190\n"
"v_accvgpr_read_b32 v187, a180\n"
"v_accvgpr_read_b32 v188, a181\n"
"v_accvgpr_read_b32 v189, a182\n"
"v_accvgpr_read_b32 v190, a183\n"
"v_mul_f32 v187, v186, v187\n"
"v_mul_f32 v188, v186, v188\n"
"v_mul_f32 v189, v186, v189\n"
"v_mul_f32 v190, v186, v190\n"
"v_accvgpr_write_b32 a180, v187\n"
"v_accvgpr_write_b32 a181, v188\n"
"v_accvgpr_write_b32 a182, v189\n"
"v_accvgpr_write_b32 a183, v190\n"
"v_accvgpr_read_b32 v187, a184\n"
"v_accvgpr_read_b32 v188, a185\n"
"v_accvgpr_read_b32 v189, a186\n"
"v_accvgpr_read_b32 v190, a187\n"
"v_mul_f32 v187, v186, v187\n"
"v_mul_f32 v188, v186, v188\n"
"v_mul_f32 v189, v186, v189\n"
"v_mul_f32 v190, v186, v190\n"
"v_accvgpr_write_b32 a184, v187\n"
"v_accvgpr_write_b32 a185, v188\n"
"v_accvgpr_write_b32 a186, v189\n"
"v_accvgpr_write_b32 a187, v190\n"
"v_accvgpr_read_b32 v187, a188\n"
"v_accvgpr_read_b32 v188, a189\n"
"v_accvgpr_read_b32 v189, a190\n"
"v_accvgpr_read_b32 v190, a191\n"
"v_mul_f32 v187, v186, v187\n"
"v_mul_f32 v188, v186, v188\n"
"v_mul_f32 v189, v186, v189\n"
"v_mul_f32 v190, v186, v190\n"
"v_accvgpr_write_b32 a188, v187\n"
"v_accvgpr_write_b32 a189, v188\n"
"v_accvgpr_write_b32 a190, v189\n"
"v_accvgpr_write_b32 a191, v190\n"
"v_accvgpr_read_b32 v187, a200\n"
"v_accvgpr_read_b32 v188, a201\n"
"v_accvgpr_read_b32 v189, a202\n"
"v_accvgpr_read_b32 v190, a203\n"
"v_mul_f32 v187, v186, v187\n"
"v_mul_f32 v188, v186, v188\n"
"v_mul_f32 v189, v186, v189\n"
"v_mul_f32 v190, v186, v190\n"
"v_accvgpr_write_b32 a200, v187\n"
"v_accvgpr_write_b32 a201, v188\n"
"v_accvgpr_write_b32 a202, v189\n"
"v_accvgpr_write_b32 a203, v190\n"
"s_nop 1\n"
"s_mov_b32 s71, 0\n"
"s_branch L_rescback_w0l\n"
"L_rare_w0e:\n"
"s_nop 7\n"
"v_mov_b32 v187, v13\n"
"s_nop 1\n"
"v_permlane32_swap_b32 v13, v187\n"
"s_nop 1\n"
"v_max_f32 v13, v13, v187\n"
"v_mov_b32 v187, v13\n"
"s_nop 1\n"
"v_permlane16_swap_b32 v13, v187\n"
"s_nop 1\n"
"v_max_f32 v13, v13, v187\n"
"v_max_f32 v13, 0, v13\n"
"v_add_f32 v17, v17, v13\n"
"v_sub_f32 v187, 0, v13\n"
"v_exp_f32 v184, v187\n"
"v_sub_f32 v112, v112, v13\n"
"v_sub_f32 v113, v113, v13\n"
"v_sub_f32 v114, v114, v13\n"
"v_sub_f32 v115, v115, v13\n"
"v_sub_f32 v124, v124, v13\n"
"v_sub_f32 v125, v125, v13\n"
"v_sub_f32 v126, v126, v13\n"
"v_sub_f32 v127, v127, v13\n"
"v_sub_f32 v136, v136, v13\n"
"v_sub_f32 v137, v137, v13\n"
"v_sub_f32 v138, v138, v13\n"
"v_sub_f32 v139, v139, v13\n"
"v_sub_f32 v148, v148, v13\n"
"v_sub_f32 v149, v149, v13\n"
"v_sub_f32 v150, v150, v13\n"
"v_sub_f32 v151, v151, v13\n"
"v_sub_f32 v52, 0, v17\n"
"v_sub_f32 v53, 0, v17\n"
"v_sub_f32 v54, 0, v17\n"
"v_sub_f32 v55, 0, v17\n"
"v_mov_b32 v187, v14\n"
"s_nop 1\n"
"v_permlane32_swap_b32 v14, v187\n"
"s_nop 1\n"
"v_max_f32 v14, v14, v187\n"
"v_mov_b32 v187, v14\n"
"s_nop 1\n"
"v_permlane16_swap_b32 v14, v187\n"
"s_nop 1\n"
"v_max_f32 v14, v14, v187\n"
"v_max_f32 v14, 0, v14\n"
"v_add_f32 v18, v18, v14\n"
"v_sub_f32 v187, 0, v14\n"
"v_exp_f32 v185, v187\n"
"v_sub_f32 v116, v116, v14\n"
"v_sub_f32 v117, v117, v14\n"
"v_sub_f32 v118, v118, v14\n"
"v_sub_f32 v119, v119, v14\n"
"v_sub_f32 v128, v128, v14\n"
"v_sub_f32 v129, v129, v14\n"
"v_sub_f32 v130, v130, v14\n"
"v_sub_f32 v131, v131, v14\n"
"v_sub_f32 v140, v140, v14\n"
"v_sub_f32 v141, v141, v14\n"
"v_sub_f32 v142, v142, v14\n"
"v_sub_f32 v143, v143, v14\n"
"v_sub_f32 v152, v152, v14\n"
"v_sub_f32 v153, v153, v14\n"
"v_sub_f32 v154, v154, v14\n"
"v_sub_f32 v155, v155, v14\n"
"v_sub_f32 v56, 0, v18\n"
"v_sub_f32 v57, 0, v18\n"
"v_sub_f32 v58, 0, v18\n"
"v_sub_f32 v59, 0, v18\n"
"v_mov_b32 v187, v15\n"
"s_nop 1\n"
"v_permlane32_swap_b32 v15, v187\n"
"s_nop 1\n"
"v_max_f32 v15, v15, v187\n"
"v_mov_b32 v187, v15\n"
"s_nop 1\n"
"v_permlane16_swap_b32 v15, v187\n"
"s_nop 1\n"
"v_max_f32 v15, v15, v187\n"
"v_max_f32 v15, 0, v15\n"
"v_add_f32 v19, v19, v15\n"
"v_sub_f32 v187, 0, v15\n"
"v_exp_f32 v186, v187\n"
"v_sub_f32 v120, v120, v15\n"
"v_sub_f32 v121, v121, v15\n"
"v_sub_f32 v122, v122, v15\n"
"v_sub_f32 v123, v123, v15\n"
"v_sub_f32 v132, v132, v15\n"
"v_sub_f32 v133, v133, v15\n"
"v_sub_f32 v134, v134, v15\n"
"v_sub_f32 v135, v135, v15\n"
"v_sub_f32 v144, v144, v15\n"
"v_sub_f32 v145, v145, v15\n"
"v_sub_f32 v146, v146, v15\n"
"v_sub_f32 v147, v147, v15\n"
"v_sub_f32 v156, v156, v15\n"
"v_sub_f32 v157, v157, v15\n"
"v_sub_f32 v158, v158, v15\n"
"v_sub_f32 v159, v159, v15\n"
"v_sub_f32 v60, 0, v19\n"
"v_sub_f32 v61, 0, v19\n"
"v_sub_f32 v62, 0, v19\n"
"v_sub_f32 v63, 0, v19\n"
"s_nop 1\n"
"s_mov_b32 s71, 1\n"
"s_branch L_back_w0e\n"
"L_resc_w0e:\n"
"s_nop 7\n"
"v_accvgpr_read_b32 v187, a0\n"
"v_accvgpr_read_b32 v188, a1\n"
"v_accvgpr_read_b32 v189, a2\n"
"v_accvgpr_read_b32 v190, a3\n"
"v_mul_f32 v187, v184, v187\n"
"v_mul_f32 v188, v184, v188\n"
"v_mul_f32 v189, v184, v189\n"
"v_mul_f32 v190, v184, v190\n"
"v_accvgpr_write_b32 a0, v187\n"
"v_accvgpr_write_b32 a1, v188\n"
"v_accvgpr_write_b32 a2, v189\n"
"v_accvgpr_write_b32 a3, v190\n"
"v_accvgpr_read_b32 v187, a4\n"
"v_accvgpr_read_b32 v188, a5\n"
"v_accvgpr_read_b32 v189, a6\n"
"v_accvgpr_read_b32 v190, a7\n"
"v_mul_f32 v187, v184, v187\n"
"v_mul_f32 v188, v184, v188\n"
"v_mul_f32 v189, v184, v189\n"
"v_mul_f32 v190, v184, v190\n"
"v_accvgpr_write_b32 a4, v187\n"
"v_accvgpr_write_b32 a5, v188\n"
"v_accvgpr_write_b32 a6, v189\n"
"v_accvgpr_write_b32 a7, v190\n"
"v_accvgpr_read_b32 v187, a8\n"
"v_accvgpr_read_b32 v188, a9\n"
"v_accvgpr_read_b32 v189, a10\n"
"v_accvgpr_read_b32 v190, a11\n"
"v_mul_f32 v187, v184, v187\n"
"v_mul_f32 v188, v184, v188\n"
"v_mul_f32 v189, v184, v189\n"
"v_mul_f32 v190, v184, v190\n"
"v_accvgpr_write_b32 a8, v187\n"
"v_accvgpr_write_b32 a9, v188\n"
"v_accvgpr_write_b32 a10, v189\n"
"v_accvgpr_write_b32 a11, v190\n"
"v_accvgpr_read_b32 v187, a12\n"
"v_accvgpr_read_b32 v188, a13\n"
"v_accvgpr_read_b32 v189, a14\n"
"v_accvgpr_read_b32 v190, a15\n"
"v_mul_f32 v187, v184, v187\n"
"v_mul_f32 v188, v184, v188\n"
"v_mul_f32 v189, v184, v189\n"
"v_mul_f32 v190, v184, v190\n"
"v_accvgpr_write_b32 a12, v187\n"
"v_accvgpr_write_b32 a13, v188\n"
"v_accvgpr_write_b32 a14, v189\n"
"v_accvgpr_write_b32 a15, v190\n"
"v_accvgpr_read_b32 v187, a16\n"
"v_accvgpr_read_b32 v188, a17\n"
"v_accvgpr_read_b32 v189, a18\n"
"v_accvgpr_read_b32 v190, a19\n"
"v_mul_f32 v187, v184, v187\n"
"v_mul_f32 v188, v184, v188\n"
"v_mul_f32 v189, v184, v189\n"
"v_mul_f32 v190, v184, v190\n"
"v_accvgpr_write_b32 a16, v187\n"
"v_accvgpr_write_b32 a17, v188\n"
"v_accvgpr_write_b32 a18, v189\n"
"v_accvgpr_write_b32 a19, v190\n"
"v_accvgpr_read_b32 v187, a20\n"
"v_accvgpr_read_b32 v188, a21\n"
"v_accvgpr_read_b32 v189, a22\n"
"v_accvgpr_read_b32 v190, a23\n"
"v_mul_f32 v187, v184, v187\n"
"v_mul_f32 v188, v184, v188\n"
"v_mul_f32 v189, v184, v189\n"
"v_mul_f32 v190, v184, v190\n"
"v_accvgpr_write_b32 a20, v187\n"
"v_accvgpr_write_b32 a21, v188\n"
"v_accvgpr_write_b32 a22, v189\n"
"v_accvgpr_write_b32 a23, v190\n"
"v_accvgpr_read_b32 v187, a24\n"
"v_accvgpr_read_b32 v188, a25\n"
"v_accvgpr_read_b32 v189, a26\n"
"v_accvgpr_read_b32 v190, a27\n"
"v_mul_f32 v187, v184, v187\n"
"v_mul_f32 v188, v184, v188\n"
"v_mul_f32 v189, v184, v189\n"
"v_mul_f32 v190, v184, v190\n"
"v_accvgpr_write_b32 a24, v187\n"
"v_accvgpr_write_b32 a25, v188\n"
"v_accvgpr_write_b32 a26, v189\n"
"v_accvgpr_write_b32 a27, v190\n"
"v_accvgpr_read_b32 v187, a28\n"
"v_accvgpr_read_b32 v188, a29\n"
"v_accvgpr_read_b32 v189, a30\n"
"v_accvgpr_read_b32 v190, a31\n"
"v_mul_f32 v187, v184, v187\n"
"v_mul_f32 v188, v184, v188\n"
"v_mul_f32 v189, v184, v189\n"
"v_mul_f32 v190, v184, v190\n"
"v_accvgpr_write_b32 a28, v187\n"
"v_accvgpr_write_b32 a29, v188\n"
"v_accvgpr_write_b32 a30, v189\n"
"v_accvgpr_write_b32 a31, v190\n"
"v_accvgpr_read_b32 v187, a32\n"
"v_accvgpr_read_b32 v188, a33\n"
"v_accvgpr_read_b32 v189, a34\n"
"v_accvgpr_read_b32 v190, a35\n"
"v_mul_f32 v187, v184, v187\n"
"v_mul_f32 v188, v184, v188\n"
"v_mul_f32 v189, v184, v189\n"
"v_mul_f32 v190, v184, v190\n"
"v_accvgpr_write_b32 a32, v187\n"
"v_accvgpr_write_b32 a33, v188\n"
"v_accvgpr_write_b32 a34, v189\n"
"v_accvgpr_write_b32 a35, v190\n"
"v_accvgpr_read_b32 v187, a36\n"
"v_accvgpr_read_b32 v188, a37\n"
"v_accvgpr_read_b32 v189, a38\n"
"v_accvgpr_read_b32 v190, a39\n"
"v_mul_f32 v187, v184, v187\n"
"v_mul_f32 v188, v184, v188\n"
"v_mul_f32 v189, v184, v189\n"
"v_mul_f32 v190, v184, v190\n"
"v_accvgpr_write_b32 a36, v187\n"
"v_accvgpr_write_b32 a37, v188\n"
"v_accvgpr_write_b32 a38, v189\n"
"v_accvgpr_write_b32 a39, v190\n"
"v_accvgpr_read_b32 v187, a40\n"
"v_accvgpr_read_b32 v188, a41\n"
"v_accvgpr_read_b32 v189, a42\n"
"v_accvgpr_read_b32 v190, a43\n"
"v_mul_f32 v187, v184, v187\n"
"v_mul_f32 v188, v184, v188\n"
"v_mul_f32 v189, v184, v189\n"
"v_mul_f32 v190, v184, v190\n"
"v_accvgpr_write_b32 a40, v187\n"
"v_accvgpr_write_b32 a41, v188\n"
"v_accvgpr_write_b32 a42, v189\n"
"v_accvgpr_write_b32 a43, v190\n"
"v_accvgpr_read_b32 v187, a44\n"
"v_accvgpr_read_b32 v188, a45\n"
"v_accvgpr_read_b32 v189, a46\n"
"v_accvgpr_read_b32 v190, a47\n"
"v_mul_f32 v187, v184, v187\n"
"v_mul_f32 v188, v184, v188\n"
"v_mul_f32 v189, v184, v189\n"
"v_mul_f32 v190, v184, v190\n"
"v_accvgpr_write_b32 a44, v187\n"
"v_accvgpr_write_b32 a45, v188\n"
"v_accvgpr_write_b32 a46, v189\n"
"v_accvgpr_write_b32 a47, v190\n"
"v_accvgpr_read_b32 v187, a48\n"
"v_accvgpr_read_b32 v188, a49\n"
"v_accvgpr_read_b32 v189, a50\n"
"v_accvgpr_read_b32 v190, a51\n"
"v_mul_f32 v187, v184, v187\n"
"v_mul_f32 v188, v184, v188\n"
"v_mul_f32 v189, v184, v189\n"
"v_mul_f32 v190, v184, v190\n"
"v_accvgpr_write_b32 a48, v187\n"
"v_accvgpr_write_b32 a49, v188\n"
"v_accvgpr_write_b32 a50, v189\n"
"v_accvgpr_write_b32 a51, v190\n"
"v_accvgpr_read_b32 v187, a52\n"
"v_accvgpr_read_b32 v188, a53\n"
"v_accvgpr_read_b32 v189, a54\n"
"v_accvgpr_read_b32 v190, a55\n"
"v_mul_f32 v187, v184, v187\n"
"v_mul_f32 v188, v184, v188\n"
"v_mul_f32 v189, v184, v189\n"
"v_mul_f32 v190, v184, v190\n"
"v_accvgpr_write_b32 a52, v187\n"
"v_accvgpr_write_b32 a53, v188\n"
"v_accvgpr_write_b32 a54, v189\n"
"v_accvgpr_write_b32 a55, v190\n"
"v_accvgpr_read_b32 v187, a56\n"
"v_accvgpr_read_b32 v188, a57\n"
"v_accvgpr_read_b32 v189, a58\n"
"v_accvgpr_read_b32 v190, a59\n"
"v_mul_f32 v187, v184, v187\n"
"v_mul_f32 v188, v184, v188\n"
"v_mul_f32 v189, v184, v189\n"
"v_mul_f32 v190, v184, v190\n"
"v_accvgpr_write_b32 a56, v187\n"
"v_accvgpr_write_b32 a57, v188\n"
"v_accvgpr_write_b32 a58, v189\n"
"v_accvgpr_write_b32 a59, v190\n"
"v_accvgpr_read_b32 v187, a60\n"
"v_accvgpr_read_b32 v188, a61\n"
"v_accvgpr_read_b32 v189, a62\n"
"v_accvgpr_read_b32 v190, a63\n"
"v_mul_f32 v187, v184, v187\n"
"v_mul_f32 v188, v184, v188\n"
"v_mul_f32 v189, v184, v189\n"
"v_mul_f32 v190, v184, v190\n"
"v_accvgpr_write_b32 a60, v187\n"
"v_accvgpr_write_b32 a61, v188\n"
"v_accvgpr_write_b32 a62, v189\n"
"v_accvgpr_write_b32 a63, v190\n"
"v_accvgpr_read_b32 v187, a192\n"
"v_accvgpr_read_b32 v188, a193\n"
"v_accvgpr_read_b32 v189, a194\n"
"v_accvgpr_read_b32 v190, a195\n"
"v_mul_f32 v187, v184, v187\n"
"v_mul_f32 v188, v184, v188\n"
"v_mul_f32 v189, v184, v189\n"
"v_mul_f32 v190, v184, v190\n"
"v_accvgpr_write_b32 a192, v187\n"
"v_accvgpr_write_b32 a193, v188\n"
"v_accvgpr_write_b32 a194, v189\n"
"v_accvgpr_write_b32 a195, v190\n"
"v_accvgpr_read_b32 v187, a64\n"
"v_accvgpr_read_b32 v188, a65\n"
"v_accvgpr_read_b32 v189, a66\n"
"v_accvgpr_read_b32 v190, a67\n"
"v_mul_f32 v187, v185, v187\n"
"v_mul_f32 v188, v185, v188\n"
"v_mul_f32 v189, v185, v189\n"
"v_mul_f32 v190, v185, v190\n"
"v_accvgpr_write_b32 a64, v187\n"
"v_accvgpr_write_b32 a65, v188\n"
"v_accvgpr_write_b32 a66, v189\n"
"v_accvgpr_write_b32 a67, v190\n"
"v_accvgpr_read_b32 v187, a68\n"
"v_accvgpr_read_b32 v188, a69\n"
"v_accvgpr_read_b32 v189, a70\n"
"v_accvgpr_read_b32 v190, a71\n"
"v_mul_f32 v187, v185, v187\n"
"v_mul_f32 v188, v185, v188\n"
"v_mul_f32 v189, v185, v189\n"
"v_mul_f32 v190, v185, v190\n"
"v_accvgpr_write_b32 a68, v187\n"
"v_accvgpr_write_b32 a69, v188\n"
"v_accvgpr_write_b32 a70, v189\n"
"v_accvgpr_write_b32 a71, v190\n"
"v_accvgpr_read_b32 v187, a72\n"
"v_accvgpr_read_b32 v188, a73\n"
"v_accvgpr_read_b32 v189, a74\n"
"v_accvgpr_read_b32 v190, a75\n"
"v_mul_f32 v187, v185, v187\n"
"v_mul_f32 v188, v185, v188\n"
"v_mul_f32 v189, v185, v189\n"
"v_mul_f32 v190, v185, v190\n"
"v_accvgpr_write_b32 a72, v187\n"
"v_accvgpr_write_b32 a73, v188\n"
"v_accvgpr_write_b32 a74, v189\n"
"v_accvgpr_write_b32 a75, v190\n"
"v_accvgpr_read_b32 v187, a76\n"
"v_accvgpr_read_b32 v188, a77\n"
"v_accvgpr_read_b32 v189, a78\n"
"v_accvgpr_read_b32 v190, a79\n"
"v_mul_f32 v187, v185, v187\n"
"v_mul_f32 v188, v185, v188\n"
"v_mul_f32 v189, v185, v189\n"
"v_mul_f32 v190, v185, v190\n"
"v_accvgpr_write_b32 a76, v187\n"
"v_accvgpr_write_b32 a77, v188\n"
"v_accvgpr_write_b32 a78, v189\n"
"v_accvgpr_write_b32 a79, v190\n"
"v_accvgpr_read_b32 v187, a80\n"
"v_accvgpr_read_b32 v188, a81\n"
"v_accvgpr_read_b32 v189, a82\n"
"v_accvgpr_read_b32 v190, a83\n"
"v_mul_f32 v187, v185, v187\n"
"v_mul_f32 v188, v185, v188\n"
"v_mul_f32 v189, v185, v189\n"
"v_mul_f32 v190, v185, v190\n"
"v_accvgpr_write_b32 a80, v187\n"
"v_accvgpr_write_b32 a81, v188\n"
"v_accvgpr_write_b32 a82, v189\n"
"v_accvgpr_write_b32 a83, v190\n"
"v_accvgpr_read_b32 v187, a84\n"
"v_accvgpr_read_b32 v188, a85\n"
"v_accvgpr_read_b32 v189, a86\n"
"v_accvgpr_read_b32 v190, a87\n"
"v_mul_f32 v187, v185, v187\n"
"v_mul_f32 v188, v185, v188\n"
"v_mul_f32 v189, v185, v189\n"
"v_mul_f32 v190, v185, v190\n"
"v_accvgpr_write_b32 a84, v187\n"
"v_accvgpr_write_b32 a85, v188\n"
"v_accvgpr_write_b32 a86, v189\n"
"v_accvgpr_write_b32 a87, v190\n"
"v_accvgpr_read_b32 v187, a88\n"
"v_accvgpr_read_b32 v188, a89\n"
"v_accvgpr_read_b32 v189, a90\n"
"v_accvgpr_read_b32 v190, a91\n"
"v_mul_f32 v187, v185, v187\n"
"v_mul_f32 v188, v185, v188\n"
"v_mul_f32 v189, v185, v189\n"
"v_mul_f32 v190, v185, v190\n"
"v_accvgpr_write_b32 a88, v187\n"
"v_accvgpr_write_b32 a89, v188\n"
"v_accvgpr_write_b32 a90, v189\n"
"v_accvgpr_write_b32 a91, v190\n"
"v_accvgpr_read_b32 v187, a92\n"
"v_accvgpr_read_b32 v188, a93\n"
"v_accvgpr_read_b32 v189, a94\n"
"v_accvgpr_read_b32 v190, a95\n"
"v_mul_f32 v187, v185, v187\n"
"v_mul_f32 v188, v185, v188\n"
"v_mul_f32 v189, v185, v189\n"
"v_mul_f32 v190, v185, v190\n"
"v_accvgpr_write_b32 a92, v187\n"
"v_accvgpr_write_b32 a93, v188\n"
"v_accvgpr_write_b32 a94, v189\n"
"v_accvgpr_write_b32 a95, v190\n"
"v_accvgpr_read_b32 v187, a96\n"
"v_accvgpr_read_b32 v188, a97\n"
"v_accvgpr_read_b32 v189, a98\n"
"v_accvgpr_read_b32 v190, a99\n"
"v_mul_f32 v187, v185, v187\n"
"v_mul_f32 v188, v185, v188\n"
"v_mul_f32 v189, v185, v189\n"
"v_mul_f32 v190, v185, v190\n"
"v_accvgpr_write_b32 a96, v187\n"
"v_accvgpr_write_b32 a97, v188\n"
"v_accvgpr_write_b32 a98, v189\n"
"v_accvgpr_write_b32 a99, v190\n"
"v_accvgpr_read_b32 v187, a100\n"
"v_accvgpr_read_b32 v188, a101\n"
"v_accvgpr_read_b32 v189, a102\n"
"v_accvgpr_read_b32 v190, a103\n"
"v_mul_f32 v187, v185, v187\n"
"v_mul_f32 v188, v185, v188\n"
"v_mul_f32 v189, v185, v189\n"
"v_mul_f32 v190, v185, v190\n"
"v_accvgpr_write_b32 a100, v187\n"
"v_accvgpr_write_b32 a101, v188\n"
"v_accvgpr_write_b32 a102, v189\n"
"v_accvgpr_write_b32 a103, v190\n"
"v_accvgpr_read_b32 v187, a104\n"
"v_accvgpr_read_b32 v188, a105\n"
"v_accvgpr_read_b32 v189, a106\n"
"v_accvgpr_read_b32 v190, a107\n"
"v_mul_f32 v187, v185, v187\n"
"v_mul_f32 v188, v185, v188\n"
"v_mul_f32 v189, v185, v189\n"
"v_mul_f32 v190, v185, v190\n"
"v_accvgpr_write_b32 a104, v187\n"
"v_accvgpr_write_b32 a105, v188\n"
"v_accvgpr_write_b32 a106, v189\n"
"v_accvgpr_write_b32 a107, v190\n"
"v_accvgpr_read_b32 v187, a108\n"
"v_accvgpr_read_b32 v188, a109\n"
"v_accvgpr_read_b32 v189, a110\n"
"v_accvgpr_read_b32 v190, a111\n"
"v_mul_f32 v187, v185, v187\n"
"v_mul_f32 v188, v185, v188\n"
"v_mul_f32 v189, v185, v189\n"
"v_mul_f32 v190, v185, v190\n"
"v_accvgpr_write_b32 a108, v187\n"
"v_accvgpr_write_b32 a109, v188\n"
"v_accvgpr_write_b32 a110, v189\n"
"v_accvgpr_write_b32 a111, v190\n"
"v_accvgpr_read_b32 v187, a112\n"
"v_accvgpr_read_b32 v188, a113\n"
"v_accvgpr_read_b32 v189, a114\n"
"v_accvgpr_read_b32 v190, a115\n"
"v_mul_f32 v187, v185, v187\n"
"v_mul_f32 v188, v185, v188\n"
"v_mul_f32 v189, v185, v189\n"
"v_mul_f32 v190, v185, v190\n"
"v_accvgpr_write_b32 a112, v187\n"
"v_accvgpr_write_b32 a113, v188\n"
"v_accvgpr_write_b32 a114, v189\n"
"v_accvgpr_write_b32 a115, v190\n"
"v_accvgpr_read_b32 v187, a116\n"
"v_accvgpr_read_b32 v188, a117\n"
"v_accvgpr_read_b32 v189, a118\n"
"v_accvgpr_read_b32 v190, a119\n"
"v_mul_f32 v187, v185, v187\n"
"v_mul_f32 v188, v185, v188\n"
"v_mul_f32 v189, v185, v189\n"
"v_mul_f32 v190, v185, v190\n"
"v_accvgpr_write_b32 a116, v187\n"
"v_accvgpr_write_b32 a117, v188\n"
"v_accvgpr_write_b32 a118, v189\n"
"v_accvgpr_write_b32 a119, v190\n"
"v_accvgpr_read_b32 v187, a120\n"
"v_accvgpr_read_b32 v188, a121\n"
"v_accvgpr_read_b32 v189, a122\n"
"v_accvgpr_read_b32 v190, a123\n"
"v_mul_f32 v187, v185, v187\n"
"v_mul_f32 v188, v185, v188\n"
"v_mul_f32 v189, v185, v189\n"
"v_mul_f32 v190, v185, v190\n"
"v_accvgpr_write_b32 a120, v187\n"
"v_accvgpr_write_b32 a121, v188\n"
"v_accvgpr_write_b32 a122, v189\n"
"v_accvgpr_write_b32 a123, v190\n"
"v_accvgpr_read_b32 v187, a124\n"
"v_accvgpr_read_b32 v188, a125\n"
"v_accvgpr_read_b32 v189, a126\n"
"v_accvgpr_read_b32 v190, a127\n"
"v_mul_f32 v187, v185, v187\n"
"v_mul_f32 v188, v185, v188\n"
"v_mul_f32 v189, v185, v189\n"
"v_mul_f32 v190, v185, v190\n"
"v_accvgpr_write_b32 a124, v187\n"
"v_accvgpr_write_b32 a125, v188\n"
"v_accvgpr_write_b32 a126, v189\n"
"v_accvgpr_write_b32 a127, v190\n"
"v_accvgpr_read_b32 v187, a196\n"
"v_accvgpr_read_b32 v188, a197\n"
"v_accvgpr_read_b32 v189, a198\n"
"v_accvgpr_read_b32 v190, a199\n"
"v_mul_f32 v187, v185, v187\n"
"v_mul_f32 v188, v185, v188\n"
"v_mul_f32 v189, v185, v189\n"
"v_mul_f32 v190, v185, v190\n"
"v_accvgpr_write_b32 a196, v187\n"
"v_accvgpr_write_b32 a197, v188\n"
"v_accvgpr_write_b32 a198, v189\n"
"v_accvgpr_write_b32 a199, v190\n"
"v_accvgpr_read_b32 v187, a128\n"
"v_accvgpr_read_b32 v188, a129\n"
"v_accvgpr_read_b32 v189, a130\n"
"v_accvgpr_read_b32 v190, a131\n"
"v_mul_f32 v187, v186, v187\n"
"v_mul_f32 v188, v186, v188\n"
"v_mul_f32 v189, v186, v189\n"
"v_mul_f32 v190, v186, v190\n"
"v_accvgpr_write_b32 a128, v187\n"
"v_accvgpr_write_b32 a129, v188\n"
"v_accvgpr_write_b32 a130, v189\n"
"v_accvgpr_write_b32 a131, v190\n"
"v_accvgpr_read_b32 v187, a132\n"
"v_accvgpr_read_b32 v188, a133\n"
"v_accvgpr_read_b32 v189, a134\n"
"v_accvgpr_read_b32 v190, a135\n"
"v_mul_f32 v187, v186, v187\n"
"v_mul_f32 v188, v186, v188\n"
"v_mul_f32 v189, v186, v189\n"
"v_mul_f32 v190, v186, v190\n"
"v_accvgpr_write_b32 a132, v187\n"
"v_accvgpr_write_b32 a133, v188\n"
"v_accvgpr_write_b32 a134, v189\n"
"v_accvgpr_write_b32 a135, v190\n"
"v_accvgpr_read_b32 v187, a136\n"
"v_accvgpr_read_b32 v188, a137\n"
"v_accvgpr_read_b32 v189, a138\n"
"v_accvgpr_read_b32 v190, a139\n"
"v_mul_f32 v187, v186, v187\n"
"v_mul_f32 v188, v186, v188\n"
"v_mul_f32 v189, v186, v189\n"
"v_mul_f32 v190, v186, v190\n"
"v_accvgpr_write_b32 a136, v187\n"
"v_accvgpr_write_b32 a137, v188\n"
"v_accvgpr_write_b32 a138, v189\n"
"v_accvgpr_write_b32 a139, v190\n"
"v_accvgpr_read_b32 v187, a140\n"
"v_accvgpr_read_b32 v188, a141\n"
"v_accvgpr_read_b32 v189, a142\n"
"v_accvgpr_read_b32 v190, a143\n"
"v_mul_f32 v187, v186, v187\n"
"v_mul_f32 v188, v186, v188\n"
"v_mul_f32 v189, v186, v189\n"
"v_mul_f32 v190, v186, v190\n"
"v_accvgpr_write_b32 a140, v187\n"
"v_accvgpr_write_b32 a141, v188\n"
"v_accvgpr_write_b32 a142, v189\n"
"v_accvgpr_write_b32 a143, v190\n"
"v_accvgpr_read_b32 v187, a144\n"
"v_accvgpr_read_b32 v188, a145\n"
"v_accvgpr_read_b32 v189, a146\n"
"v_accvgpr_read_b32 v190, a147\n"
"v_mul_f32 v187, v186, v187\n"
"v_mul_f32 v188, v186, v188\n"
"v_mul_f32 v189, v186, v189\n"
"v_mul_f32 v190, v186, v190\n"
"v_accvgpr_write_b32 a144, v187\n"
"v_accvgpr_write_b32 a145, v188\n"
"v_accvgpr_write_b32 a146, v189\n"
"v_accvgpr_write_b32 a147, v190\n"
"v_accvgpr_read_b32 v187, a148\n"
"v_accvgpr_read_b32 v188, a149\n"
"v_accvgpr_read_b32 v189, a150\n"
"v_accvgpr_read_b32 v190, a151\n"
"v_mul_f32 v187, v186, v187\n"
"v_mul_f32 v188, v186, v188\n"
"v_mul_f32 v189, v186, v189\n"
"v_mul_f32 v190, v186, v190\n"
"v_accvgpr_write_b32 a148, v187\n"
"v_accvgpr_write_b32 a149, v188\n"
"v_accvgpr_write_b32 a150, v189\n"
"v_accvgpr_write_b32 a151, v190\n"
"v_accvgpr_read_b32 v187, a152\n"
"v_accvgpr_read_b32 v188, a153\n"
"v_accvgpr_read_b32 v189, a154\n"
"v_accvgpr_read_b32 v190, a155\n"
"v_mul_f32 v187, v186, v187\n"
"v_mul_f32 v188, v186, v188\n"
"v_mul_f32 v189, v186, v189\n"
"v_mul_f32 v190, v186, v190\n"
"v_accvgpr_write_b32 a152, v187\n"
"v_accvgpr_write_b32 a153, v188\n"
"v_accvgpr_write_b32 a154, v189\n"
"v_accvgpr_write_b32 a155, v190\n"
"v_accvgpr_read_b32 v187, a156\n"
"v_accvgpr_read_b32 v188, a157\n"
"v_accvgpr_read_b32 v189, a158\n"
"v_accvgpr_read_b32 v190, a159\n"
"v_mul_f32 v187, v186, v187\n"
"v_mul_f32 v188, v186, v188\n"
"v_mul_f32 v189, v186, v189\n"
"v_mul_f32 v190, v186, v190\n"
"v_accvgpr_write_b32 a156, v187\n"
"v_accvgpr_write_b32 a157, v188\n"
"v_accvgpr_write_b32 a158, v189\n"
"v_accvgpr_write_b32 a159, v190\n"
"v_accvgpr_read_b32 v187, a160\n"
"v_accvgpr_read_b32 v188, a161\n"
"v_accvgpr_read_b32 v189, a162\n"
"v_accvgpr_read_b32 v190, a163\n"
"v_mul_f32 v187, v186, v187\n"
"v_mul_f32 v188, v186, v188\n"
"v_mul_f32 v189, v186, v189\n"
"v_mul_f32 v190, v186, v190\n"
"v_accvgpr_write_b32 a160, v187\n"
"v_accvgpr_write_b32 a161, v188\n"
"v_accvgpr_write_b32 a162, v189\n"
"v_accvgpr_write_b32 a163, v190\n"
"v_accvgpr_read_b32 v187, a164\n"
"v_accvgpr_read_b32 v188, a165\n"
"v_accvgpr_read_b32 v189, a166\n"
"v_accvgpr_read_b32 v190, a167\n"
"v_mul_f32 v187, v186, v187\n"
"v_mul_f32 v188, v186, v188\n"
"v_mul_f32 v189, v186, v189\n"
"v_mul_f32 v190, v186, v190\n"
"v_accvgpr_write_b32 a164, v187\n"
"v_accvgpr_write_b32 a165, v188\n"
"v_accvgpr_write_b32 a166, v189\n"
"v_accvgpr_write_b32 a167, v190\n"
"v_accvgpr_read_b32 v187, a168\n"
"v_accvgpr_read_b32 v188, a169\n"
"v_accvgpr_read_b32 v189, a170\n"
"v_accvgpr_read_b32 v190, a171\n"
"v_mul_f32 v187, v186, v187\n"
"v_mul_f32 v188, v186, v188\n"
"v_mul_f32 v189, v186, v189\n"
"v_mul_f32 v190, v186, v190\n"
"v_accvgpr_write_b32 a168, v187\n"
"v_accvgpr_write_b32 a169, v188\n"
"v_accvgpr_write_b32 a170, v189\n"
"v_accvgpr_write_b32 a171, v190\n"
"v_accvgpr_read_b32 v187, a172\n"
"v_accvgpr_read_b32 v188, a173\n"
"v_accvgpr_read_b32 v189, a174\n"
"v_accvgpr_read_b32 v190, a175\n"
"v_mul_f32 v187, v186, v187\n"
"v_mul_f32 v188, v186, v188\n"
"v_mul_f32 v189, v186, v189\n"
"v_mul_f32 v190, v186, v190\n"
"v_accvgpr_write_b32 a172, v187\n"
"v_accvgpr_write_b32 a173, v188\n"
"v_accvgpr_write_b32 a174, v189\n"
"v_accvgpr_write_b32 a175, v190\n"
"v_accvgpr_read_b32 v187, a176\n"
"v_accvgpr_read_b32 v188, a177\n"
"v_accvgpr_read_b32 v189, a178\n"
"v_accvgpr_read_b32 v190, a179\n"
"v_mul_f32 v187, v186, v187\n"
"v_mul_f32 v188, v186, v188\n"
"v_mul_f32 v189, v186, v189\n"
"v_mul_f32 v190, v186, v190\n"
"v_accvgpr_write_b32 a176, v187\n"
"v_accvgpr_write_b32 a177, v188\n"
"v_accvgpr_write_b32 a178, v189\n"
"v_accvgpr_write_b32 a179, v190\n"
"v_accvgpr_read_b32 v187, a180\n"
"v_accvgpr_read_b32 v188, a181\n"
"v_accvgpr_read_b32 v189, a182\n"
"v_accvgpr_read_b32 v190, a183\n"
"v_mul_f32 v187, v186, v187\n"
"v_mul_f32 v188, v186, v188\n"
"v_mul_f32 v189, v186, v189\n"
"v_mul_f32 v190, v186, v190\n"
"v_accvgpr_write_b32 a180, v187\n"
"v_accvgpr_write_b32 a181, v188\n"
"v_accvgpr_write_b32 a182, v189\n"
"v_accvgpr_write_b32 a183, v190\n"
"v_accvgpr_read_b32 v187, a184\n"
"v_accvgpr_read_b32 v188, a185\n"
"v_accvgpr_read_b32 v189, a186\n"
"v_accvgpr_read_b32 v190, a187\n"
"v_mul_f32 v187, v186, v187\n"
"v_mul_f32 v188, v186, v188\n"
"v_mul_f32 v189, v186, v189\n"
"v_mul_f32 v190, v186, v190\n"
"v_accvgpr_write_b32 a184, v187\n"
"v_accvgpr_write_b32 a185, v188\n"
"v_accvgpr_write_b32 a186, v189\n"
"v_accvgpr_write_b32 a187, v190\n"
"v_accvgpr_read_b32 v187, a188\n"
"v_accvgpr_read_b32 v188, a189\n"
"v_accvgpr_read_b32 v189, a190\n"
"v_accvgpr_read_b32 v190, a191\n"
"v_mul_f32 v187, v186, v187\n"
"v_mul_f32 v188, v186, v188\n"
"v_mul_f32 v189, v186, v189\n"
"v_mul_f32 v190, v186, v190\n"
"v_accvgpr_write_b32 a188, v187\n"
"v_accvgpr_write_b32 a189, v188\n"
"v_accvgpr_write_b32 a190, v189\n"
"v_accvgpr_write_b32 a191, v190\n"
"v_accvgpr_read_b32 v187, a200\n"
"v_accvgpr_read_b32 v188, a201\n"
"v_accvgpr_read_b32 v189, a202\n"
"v_accvgpr_read_b32 v190, a203\n"
"v_mul_f32 v187, v186, v187\n"
"v_mul_f32 v188, v186, v188\n"
"v_mul_f32 v189, v186, v189\n"
"v_mul_f32 v190, v186, v190\n"
"v_accvgpr_write_b32 a200, v187\n"
"v_accvgpr_write_b32 a201, v188\n"
"v_accvgpr_write_b32 a202, v189\n"
"v_accvgpr_write_b32 a203, v190\n"
"s_nop 1\n"
"s_mov_b32 s71, 0\n"
"s_branch L_rescback_w0e\n"
"L_wave3:\n"
"v_mbcnt_lo_u32_b32 v10, -1, 0\n"
"v_mbcnt_hi_u32_b32 v10, -1, v10\n"
"v_lshlrev_b32 v10, 7, v10\n"
"s_add_u32 m0, s85, 1024\n"
"s_nop 0\n"
"buffer_load_dwordx4 %[dmax], s[52:55], s74 offen offset:0 lds\n"
"s_add_u32 m0, s85, 2048\n"
"s_nop 0\n"
"buffer_load_dwordx4 %[dmax], s[52:55], s77 offen offset:0 lds\n"
"s_add_u32 m0, s85, 3072\n"
"s_nop 0\n"
"buffer_load_dwordx4 %[dmax], s[52:55], s75 offen offset:0 lds\n"
"s_add_u32 m0, s85, 4096\n"
"s_nop 0\n"
"buffer_load_dwordx4 %[dmax], s[52:55], s78 offen offset:0 lds\n"
"s_add_u32 m0, s85, 9088\n"
"s_nop 0\n"
"buffer_load_dwordx4 %[dmax], s[52:55], s74 offen offset:128 lds\n"
"s_add_u32 m0, s85, 10112\n"
"s_nop 0\n"
"buffer_load_dwordx4 %[dmax], s[52:55], s77 offen offset:128 lds\n"
"s_add_u32 m0, s85, 11136\n"
"s_nop 0\n"
"buffer_load_dwordx4 %[dmax], s[52:55], s75 offen offset:128 lds\n"
"s_add_u32 m0, s85, 12160\n"
"s_nop 0\n"
"buffer_load_dwordx4 %[dmax], s[52:55], s78 offen offset:128 lds\n"
"s_add_u32 s56, s58, 131072\n"
"s_add_u32 m0, s56, 0\n"
"s_nop 0\n"
"buffer_load_dwordx4 %[dmaw], s[48:51], 0 offen offset:0 lds\n"
"s_add_u32 m0, s56, 896\n"
"s_nop 0\n"
"buffer_load_dwordx4 %[dmaw], s[48:51], 0 offen offset:128 lds\n"
"s_add_u32 m0, s56, 1792\n"
"s_nop 0\n"
"buffer_load_dwordx4 %[dmaw], s[48:51], 0 offen offset:256 lds\n"
"s_add_u32 m0, s56, 2688\n"
"s_nop 0\n"
"buffer_load_dwordx4 %[dmaw], s[48:51], 0 offen offset:384 lds\n"
"s_add_u32 m0, s56, 4096\n"
"s_nop 0\n"
"buffer_load_dwordx4 %[dmaw], s[48:51], s63 offen offset:0 lds\n"
"s_add_u32 m0, s56, 4992\n"
"s_nop 0\n"
"buffer_load_dwordx4 %[dmaw], s[48:51], s63 offen offset:128 lds\n"
"s_add_u32 m0, s56, 5888\n"
"s_nop 0\n"
"buffer_load_dwordx4 %[dmaw], s[48:51], s63 offen offset:256 lds\n"
"s_add_u32 m0, s56, 6784\n"
"s_nop 0\n"
"buffer_load_dwordx4 %[dmaw], s[48:51], s63 offen offset:384 lds\n"
"s_add_u32 s48, s48, 0x80000\n"
"s_addc_u32 s49, s49, 0\n"
"s_sub_u32 s50, s50, 0x80000\n"
"s_cselect_b32 s50, 0, s50\n"
"s_add_u32 s56, s58, 65536\n"
"s_add_u32 m0, s56, 0\n"
"s_nop 0\n"
"buffer_load_dwordx4 %[dmaw], s[48:51], 0 offen offset:0 lds\n"
"s_add_u32 m0, s56, 896\n"
"s_nop 0\n"
"buffer_load_dwordx4 %[dmaw], s[48:51], 0 offen offset:128 lds\n"
"s_add_u32 m0, s56, 1792\n"
"s_nop 0\n"
"buffer_load_dwordx4 %[dmaw], s[48:51], 0 offen offset:256 lds\n"
"s_add_u32 m0, s56, 2688\n"
"s_nop 0\n"
"buffer_load_dwordx4 %[dmaw], s[48:51], 0 offen offset:384 lds\n"
"s_add_u32 m0, s56, 4096\n"
"s_nop 0\n"
"buffer_load_dwordx4 %[dmaw], s[48:51], s63 offen offset:0 lds\n"
"s_add_u32 m0, s56, 4992\n"
"s_nop 0\n"
"buffer_load_dwordx4 %[dmaw], s[48:51], s63 offen offset:128 lds\n"
"s_add_u32 m0, s56, 5888\n"
"s_nop 0\n"
"buffer_load_dwordx4 %[dmaw], s[48:51], s63 offen offset:256 lds\n"
"s_add_u32 m0, s56, 6784\n"
"s_nop 0\n"
"buffer_load_dwordx4 %[dmaw], s[48:51], s63 offen offset:384 lds\n"
"s_add_u32 s48, s48, 0x80000\n"
"s_addc_u32 s49, s49, 0\n"
"s_sub_u32 s50, s50, 0x80000\n"
"s_cselect_b32 s50, 0, s50\n"
"s_add_u32 s56, s58, 98304\n"
"s_add_u32 m0, s56, 0\n"
"s_nop 0\n"
"buffer_load_dwordx4 %[dmaw], s[48:51], 0 offen offset:0 lds\n"
"s_add_u32 m0, s56, 896\n"
"s_nop 0\n"
"buffer_load_dwordx4 %[dmaw], s[48:51], 0 offen offset:128 lds\n"
"s_add_u32 m0, s56, 1792\n"
"s_nop 0\n"
"buffer_load_dwordx4 %[dmaw], s[48:51], 0 offen offset:256 lds\n"
"s_add_u32 m0, s56, 2688\n"
"s_nop 0\n"
"buffer_load_dwordx4 %[dmaw], s[48:51], 0 offen offset:384 lds\n"
"s_add_u32 m0, s56, 4096\n"
"s_nop 0\n"
"buffer_load_dwordx4 %[dmaw], s[48:51], s63 offen offset:0 lds\n"
"s_add_u32 m0, s56, 4992\n"
"s_nop 0\n"
"buffer_load_dwordx4 %[dmaw], s[48:51], s63 offen offset:128 lds\n"
"s_add_u32 m0, s56, 5888\n"
"s_nop 0\n"
"buffer_load_dwordx4 %[dmaw], s[48:51], s63 offen offset:256 lds\n"
"s_add_u32 m0, s56, 6784\n"
"s_nop 0\n"
"buffer_load_dwordx4 %[dmaw], s[48:51], s63 offen offset:384 lds\n"
"s_add_u32 s48, s48, 0x80000\n"
"s_addc_u32 s49, s49, 0\n"
"s_sub_u32 s50, s50, 0x80000\n"
"s_cselect_b32 s50, 0, s50\n"
"s_waitcnt vmcnt(28)\n"
"ds_read_b128 a[0:3], %[xrd0] offset:1024\n"
"ds_read_b128 a[4:7], %[xrd1] offset:1024\n"
"ds_read_b128 a[56:59], %[xrd0] offset:3072\n"
"ds_read_b128 a[60:63], %[xrd1] offset:3072\n"
"s_waitcnt vmcnt(24)\n"
"ds_read_b128 a[8:11], %[xrd0] offset:9216\n"
"ds_read_b128 a[12:15], %[xrd1] offset:9216\n"
"ds_read_b128 a[64:67], %[xrd0] offset:11264\n"
"ds_read_b128 a[68:71], %[xrd1] offset:11264\n"
"s_waitcnt lgkmcnt(0)\n"
"s_add_u32 m0, s85, 768\n"
"s_nop 0\n"
"buffer_load_dwordx4 %[dmax], s[52:55], s74 offen offset:256 lds\n"
"s_add_u32 m0, s85, 1792\n"
"s_nop 0\n"
"buffer_load_dwordx4 %[dmax], s[52:55], s77 offen offset:256 lds\n"
"s_add_u32 m0, s85, 2816\n"
"s_nop 0\n"
"buffer_load_dwordx4 %[dmax], s[52:55], s75 offen offset:256 lds\n"
"s_add_u32 m0, s85, 3840\n"
"s_nop 0\n"
"buffer_load_dwordx4 %[dmax], s[52:55], s78 offen offset:256 lds\n"
"s_add_u32 m0, s85, 8832\n"
"s_nop 0\n"
"buffer_load_dwordx4 %[dmax], s[52:55], s74 offen offset:384 lds\n"
"s_add_u32 m0, s85, 9856\n"
"s_nop 0\n"
"buffer_load_dwordx4 %[dmax], s[52:55], s77 offen offset:384 lds\n"
"s_add_u32 m0, s85, 10880\n"
"s_nop 0\n"
"buffer_load_dwordx4 %[dmax], s[52:55], s75 offen offset:384 lds\n"
"s_add_u32 m0, s85, 11904\n"
"s_nop 0\n"
"buffer_load_dwordx4 %[dmax], s[52:55], s78 offen offset:384 lds\n"
"s_waitcnt vmcnt(24)\n"
"s_barrier\n"
"s_add_u32 s57, s60, 131072\n"
"v_add_u32 v11, s57, %[wl0]\n"
"v_add_u32 v12, s57, %[wl1]\n"
"ds_read_b64_tr_b16 v[20:21], v11 offset:0\n"
"ds_read_b64_tr_b16 v[22:23], v12 offset:0\n"
"ds_read_b64_tr_b16 v[24:25], v11 offset:8\n"
"ds_read_b64_tr_b16 v[26:27], v12 offset:8\n"
"ds_read_b64_tr_b16 v[28:29], v11 offset:512\n"
"ds_read_b64_tr_b16 v[30:31], v12 offset:512\n"
"ds_read_b64_tr_b16 v[32:33], v11 offset:520\n"
"ds_read_b64_tr_b16 v[34:35], v12 offset:520\n"
"ds_read_b64_tr_b16 v[36:37], v11 offset:1024\n"
"ds_read_b64_tr_b16 v[38:39], v12 offset:1024\n"
"ds_read_b64_tr_b16 v[40:41], v11 offset:1032\n"
"ds_read_b64_tr_b16 v[42:43], v12 offset:1032\n"
"ds_read_b64_tr_b16 v[44:45], v11 offset:1536\n"
"ds_read_b64_tr_b16 v[46:47], v12 offset:1536\n"
"s_waitcnt lgkmcnt(10)\n"
"v_mfma_f32_16x16x32_bf16 v[64:67], v[20:23], a[0:3], 0\n"
"ds_read_b64_tr_b16 v[48:49], v11 offset:1544\n"
"ds_read_b64_tr_b16 v[50:51], v12 offset:1544\n"
"v_mfma_f32_16x16x32_bf16 v[128:131], v[20:23], a[56:59], 0\n"
"v_mfma_f32_16x16x32_bf16 v[68:71], v[24:27], a[0:3], 0\n"
"ds_read_b64_tr_b16 v[20:21], v11 offset:2048\n"
"ds_read_b64_tr_b16 v[22:23], v12 offset:2048\n"
"v_mfma_f32_16x16x32_bf16 v[132:135], v[24:27], a[56:59], 0\n"
"s_waitcnt lgkmcnt(10)\n"
"v_mfma_f32_16x16x32_bf16 v[72:75], v[28:31], a[0:3], 0\n"
"ds_read_b64_tr_b16 v[24:25], v11 offset:2056\n"
"ds_read_b64_tr_b16 v[26:27], v12 offset:2056\n"
"v_mfma_f32_16x16x32_bf16 v[136:139], v[28:31], a[56:59], 0\n"
"s_getpc_b64 s[64:65]\n"
"L_pc_L_q1_w3:\n"
"s_add_u32 s64, s64, L_q1_w3-L_pc_L_q1_w3\n"
"s_addc_u32 s65, s65, 0\n"
"v_mfma_f32_16x16x32_bf16 v[76:79], v[32:35], a[0:3], 0\n"
"ds_read_b64_tr_b16 v[28:29], v11 offset:2560\n"
"ds_read_b64_tr_b16 v[30:31], v12 offset:2560\n"
"v_mfma_f32_16x16x32_bf16 v[140:143], v[32:35], a[56:59], 0\n"
"s_waitcnt lgkmcnt(10)\n"
"v_mfma_f32_16x16x32_bf16 v[80:83], v[36:39], a[0:3], 0\n"
"ds_read_b64_tr_b16 v[32:33], v11 offset:2568\n"
"ds_read_b64_tr_b16 v[34:35], v12 offset:2568\n"
"v_mfma_f32_16x16x32_bf16 v[144:147], v[36:39], a[56:59], 0\n"
"s_waitcnt vmcnt(4)\n"
"ds_read_b128 a[16:19], %[xrd0] offset:1024\n"
"v_mfma_f32_16x16x32_bf16 v[84:87], v[40:43], a[0:3], 0\n"
"ds_read_b64_tr_b16 v[36:37], v11 offset:3072\n"
"ds_read_b64_tr_b16 v[38:39], v12 offset:3072\n"
"v_mfma_f32_16x16x32_bf16 v[148:151], v[40:43], a[56:59], 0\n"
"ds_read_b128 a[20:23], %[xrd1] offset:1024\n"
"s_waitcnt lgkmcnt(12)\n"
"v_mfma_f32_16x16x32_bf16 v[88:91], v[44:47], a[0:3], 0\n"
"ds_read_b64_tr_b16 v[40:41], v11 offset:3080\n"
"ds_read_b64_tr_b16 v[42:43], v12 offset:3080\n"
"v_mfma_f32_16x16x32_bf16 v[152:155], v[44:47], a[56:59], 0\n"
"ds_read_b128 a[72:75], %[xrd0] offset:3072\n"
"ds_read_b128 a[76:79], %[xrd1] offset:3072\n"
"v_mfma_f32_16x16x32_bf16 v[92:95], v[48:51], a[0:3], 0\n"
"ds_read_b64_tr_b16 v[44:45], v11 offset:3584\n"
"ds_read_b64_tr_b16 v[46:47], v12 offset:3584\n"
"v_mfma_f32_16x16x32_bf16 v[156:159], v[48:51], a[56:59], 0\n"
"s_waitcnt lgkmcnt(14)\n"
"v_mfma_f32_16x16x32_bf16 v[96:99], v[20:23], a[0:3], 0\n"
"ds_read_b64_tr_b16 v[48:49], v11 offset:3592\n"
"ds_read_b64_tr_b16 v[50:51], v12 offset:3592\n"
"v_mfma_f32_16x16x32_bf16 v[160:163], v[20:23], a[56:59], 0\n"
"s_waitcnt vmcnt(0)\n"
"ds_read_b128 a[24:27], %[xrd0] offset:9216\n"
"v_mfma_f32_16x16x32_bf16 v[100:103], v[24:27], a[0:3], 0\n"
"ds_read_b64_tr_b16 v[20:21], v11 offset:16384\n"
"ds_read_b64_tr_b16 v[22:23], v12 offset:16384\n"
"v_mfma_f32_16x16x32_bf16 v[164:167], v[24:27], a[56:59], 0\n"
"ds_read_b128 a[28:31], %[xrd1] offset:9216\n"
"s_waitcnt lgkmcnt(15)\n"
"v_mfma_f32_16x16x32_bf16 v[104:107], v[28:31], a[0:3], 0\n"
"ds_read_b64_tr_b16 v[24:25], v11 offset:16392\n"
"ds_read_b64_tr_b16 v[26:27], v12 offset:16392\n"
"v_mfma_f32_16x16x32_bf16 v[168:171], v[28:31], a[56:59], 0\n"
"ds_read_b128 a[80:83], %[xrd0] offset:11264\n"
"ds_read_b128 a[84:87], %[xrd1] offset:11264\n"
"v_mfma_f32_16x16x32_bf16 v[108:111], v[32:35], a[0:3], 0\n"
"ds_read_b64_tr_b16 v[28:29], v11 offset:16896\n"
"ds_read_b64_tr_b16 v[30:31], v12 offset:16896\n"
"v_mfma_f32_16x16x32_bf16 v[172:175], v[32:35], a[56:59], 0\n"
"s_waitcnt lgkmcnt(15)\n"
"v_mfma_f32_16x16x32_bf16 v[112:115], v[36:39], a[0:3], 0\n"
"ds_read_b64_tr_b16 v[32:33], v11 offset:16904\n"
"ds_read_b64_tr_b16 v[34:35], v12 offset:16904\n"
"v_mfma_f32_16x16x32_bf16 v[176:179], v[36:39], a[56:59], 0\n"
"v_mbcnt_lo_u32_b32 v13, -1, 0\n"
"v_mbcnt_hi_u32_b32 v13, -1, v13\n"
"v_lshlrev_b32 v13, 7, v13\n"
"v_add_u32 v13, 0, v13\n"
"v_min_u32 v13, L_tail_w3-L_q1_w3-4, v13\n"
"global_load_dword a201, v13, s[64:65]\n"
"v_mfma_f32_16x16x32_bf16 v[116:119], v[40:43], a[0:3], 0\n"
"ds_read_b64_tr_b16 v[36:37], v11 offset:17408\n"
"ds_read_b64_tr_b16 v[38:39], v12 offset:17408\n"
"v_mfma_f32_16x16x32_bf16 v[180:183], v[40:43], a[56:59], 0\n"
"s_waitcnt lgkmcnt(14)\n"
"v_mfma_f32_16x16x32_bf16 v[120:123], v[44:47], a[0:3], 0\n"
"ds_read_b64_tr_b16 v[40:41], v11 offset:17416\n"
"ds_read_b64_tr_b16 v[42:43], v12 offset:17416\n"
"v_mfma_f32_16x16x32_bf16 v[184:187], v[44:47], a[56:59], 0\n"
"s_waitcnt lgkmcnt(15)\n"
"s_add_u32 m0, s85, 512\n"
"v_mfma_f32_16x16x32_bf16 v[124:127], v[48:51], a[0:3], 0\n"
"ds_read_b64_tr_b16 v[44:45], v11 offset:17920\n"
"ds_read_b64_tr_b16 v[46:47], v12 offset:17920\n"
"v_mfma_f32_16x16x32_bf16 v[188:191], v[48:51], a[56:59], 0\n"
"buffer_load_dwordx4 %[dmax], s[52:55], s74 offen offset:512 lds\n"
"s_waitcnt lgkmcnt(12)\n"
"v_mfma_f32_16x16x32_bf16 v[64:67], v[20:23], a[4:7], v[64:67]\n"
"ds_read_b64_tr_b16 v[48:49], v11 offset:17928\n"
"ds_read_b64_tr_b16 v[50:51], v12 offset:17928\n"
"v_mfma_f32_16x16x32_bf16 v[128:131], v[20:23], a[60:63], v[128:131]\n"
"s_add_u32 m0, s85, 1536\n"
"v_mfma_f32_16x16x32_bf16 v[68:71], v[24:27], a[4:7], v[68:71]\n"
"ds_read_b64_tr_b16 v[20:21], v11 offset:18432\n"
"ds_read_b64_tr_b16 v[22:23], v12 offset:18432\n"
"v_mfma_f32_16x16x32_bf16 v[132:135], v[24:27], a[60:63], v[132:135]\n"
"buffer_load_dwordx4 %[dmax], s[52:55], s77 offen offset:512 lds\n"
"s_waitcnt lgkmcnt(10)\n"
"v_mfma_f32_16x16x32_bf16 v[72:75], v[28:31], a[4:7], v[72:75]\n"
"ds_read_b64_tr_b16 v[24:25], v11 offset:18440\n"
"ds_read_b64_tr_b16 v[26:27], v12 offset:18440\n"
"v_mfma_f32_16x16x32_bf16 v[136:139], v[28:31], a[60:63], v[136:139]\n"
"s_add_u32 m0, s85, 2560\n"
"v_mfma_f32_16x16x32_bf16 v[76:79], v[32:35], a[4:7], v[76:79]\n"
"ds_read_b64_tr_b16 v[28:29], v11 offset:18944\n"
"ds_read_b64_tr_b16 v[30:31], v12 offset:18944\n"
"v_mfma_f32_16x16x32_bf16 v[140:143], v[32:35], a[60:63], v[140:143]\n"
"buffer_load_dwordx4 %[dmax], s[52:55], s75 offen offset:512 lds\n"
"s_waitcnt lgkmcnt(10)\n"
"v_mfma_f32_16x16x32_bf16 v[80:83], v[36:39], a[4:7], v[80:83]\n"
"ds_read_b64_tr_b16 v[32:33], v11 offset:18952\n"
"ds_read_b64_tr_b16 v[34:35], v12 offset:18952\n"
"v_mfma_f32_16x16x32_bf16 v[144:147], v[36:39], a[60:63], v[144:147]\n"
"s_add_u32 m0, s85, 3584\n"
"v_mfma_f32_16x16x32_bf16 v[84:87], v[40:43], a[4:7], v[84:87]\n"
"ds_read_b64_tr_b16 v[36:37], v11 offset:19456\n"
"ds_read_b64_tr_b16 v[38:39], v12 offset:19456\n"
"v_mfma_f32_16x16x32_bf16 v[148:151], v[40:43], a[60:63], v[148:151]\n"
"buffer_load_dwordx4 %[dmax], s[52:55], s78 offen offset:512 lds\n"
"s_waitcnt lgkmcnt(10)\n"
"v_mfma_f32_16x16x32_bf16 v[88:91], v[44:47], a[4:7], v[88:91]\n"
"ds_read_b64_tr_b16 v[40:41], v11 offset:19464\n"
"ds_read_b64_tr_b16 v[42:43], v12 offset:19464\n"
"v_mfma_f32_16x16x32_bf16 v[152:155], v[44:47], a[60:63], v[152:155]\n"
"s_waitcnt lgkmcnt(15)\n"
"s_add_u32 m0, s85, 8576\n"
"v_mfma_f32_16x16x32_bf16 v[92:95], v[48:51], a[4:7], v[92:95]\n"
"ds_read_b64_tr_b16 v[44:45], v11 offset:19968\n"
"ds_read_b64_tr_b16 v[46:47], v12 offset:19968\n"
"v_mfma_f32_16x16x32_bf16 v[156:159], v[48:51], a[60:63], v[156:159]\n"
"buffer_load_dwordx4 %[dmax], s[52:55], s74 offen offset:640 lds\n"
"s_waitcnt lgkmcnt(10)\n"
"v_mfma_f32_16x16x32_bf16 v[96:99], v[20:23], a[4:7], v[96:99]\n"
"ds_read_b64_tr_b16 v[48:49], v11 offset:19976\n"
"ds_read_b64_tr_b16 v[50:51], v12 offset:19976\n"
"v_mfma_f32_16x16x32_bf16 v[160:163], v[20:23], a[60:63], v[160:163]\n"
"s_add_u32 m0, s85, 9600\n"
"v_mfma_f32_16x16x32_bf16 v[100:103], v[24:27], a[4:7], v[100:103]\n"
"s_add_u32 s57, s60, 65536\n"
"buffer_load_dwordx4 %[dmax], s[52:55], s77 offen offset:640 lds\n"
"v_mfma_f32_16x16x32_bf16 v[164:167], v[24:27], a[60:63], v[164:167]\n"
"v_add_u32 v11, s57, %[wl0]\n"
"v_add_u32 v12, s57, %[wl1]\n"
"s_add_u32 m0, s85, 10624\n"
"s_nop 0\n"
"buffer_load_dwordx4 %[dmax], s[52:55], s75 offen offset:640 lds\n"
"s_add_u32 m0, s85, 11648\n"
"s_nop 0\n"
"buffer_load_dwordx4 %[dmax], s[52:55], s78 offen offset:640 lds\n"
"s_waitcnt lgkmcnt(8)\n"
"v_mfma_f32_16x16x32_bf16 v[104:107], v[28:31], a[4:7], v[104:107]\n"
"v_mbcnt_lo_u32_b32 v13, -1, 0\n"
"v_mbcnt_hi_u32_b32 v13, -1, v13\n"
"v_lshlrev_b32 v13, 7, v13\n"
"v_add_u32 v13, 8192, v13\n"
"v_min_u32 v13, L_tail_w3-L_q1_w3-4, v13\n"
"global_load_dword a201, v13, s[64:65]\n"
"v_mfma_f32_16x16x32_bf16 v[168:171], v[28:31], a[60:63], v[168:171]\n"
"s_waitcnt vmcnt(26) lgkmcnt(0)\n"
"s_barrier\n"
"v_mfma_f32_16x16x32_bf16 v[108:111], v[32:35], a[4:7], v[108:111]\n"
"ds_read_b64_tr_b16 v[20:21], v11 offset:0\n"
"ds_read_b64_tr_b16 v[22:23], v12 offset:0\n"
"v_mfma_f32_16x16x32_bf16 v[172:175], v[32:35], a[60:63], v[172:175]\n"
"ds_read_b64_tr_b16 v[24:25], v11 offset:8\n"
"ds_read_b64_tr_b16 v[26:27], v12 offset:8\n"
"ds_read_b64_tr_b16 v[28:29], v11 offset:512\n"
"ds_read_b64_tr_b16 v[30:31], v12 offset:512\n"
"v_mfma_f32_16x16x32_bf16 v[112:115], v[36:39], a[4:7], v[112:115]\n"
"ds_read_b64_tr_b16 v[32:33], v11 offset:520\n"
"ds_read_b64_tr_b16 v[34:35], v12 offset:520\n"
"v_mfma_f32_16x16x32_bf16 v[176:179], v[36:39], a[60:63], v[176:179]\n"
"v_mbcnt_lo_u32_b32 v13, -1, 0\n"
"v_mbcnt_hi_u32_b32 v13, -1, v13\n"
"v_lshlrev_b32 v13, 7, v13\n"
"v_add_u32 v13, 16384, v13\n"
"v_min_u32 v13, L_tail_w3-L_q1_w3-4, v13\n"
"global_load_dword a201, v13, s[64:65]\n"
"v_mfma_f32_16x16x32_bf16 v[116:119], v[40:43], a[4:7], v[116:119]\n"
"ds_read_b64_tr_b16 v[36:37], v11 offset:1024\n"
"ds_read_b64_tr_b16 v[38:39], v12 offset:1024\n"
"v_mfma_f32_16x16x32_bf16 v[180:183], v[40:43], a[60:63], v[180:183]\n"
"v_mbcnt_lo_u32_b32 v13, -1, 0\n"
"v_mbcnt_hi_u32_b32 v13, -1, v13\n"
"v_lshlrev_b32 v13, 7, v13\n"
"v_add_u32 v13, 24576, v13\n"
"v_min_u32 v13, L_tail_w3-L_q1_w3-4, v13\n"
"global_load_dword a201, v13, s[64:65]\n"
"v_mfma_f32_16x16x32_bf16 v[120:123], v[44:47], a[4:7], v[120:123]\n"
"ds_read_b64_tr_b16 v[40:41], v11 offset:1032\n"
"ds_read_b64_tr_b16 v[42:43], v12 offset:1032\n"
"v_mfma_f32_16x16x32_bf16 v[184:187], v[44:47], a[60:63], v[184:187]\n"
"v_mfma_f32_16x16x32_bf16 v[124:127], v[48:51], a[4:7], v[124:127]\n"
"ds_read_b64_tr_b16 v[44:45], v11 offset:1536\n"
"ds_read_b64_tr_b16 v[46:47], v12 offset:1536\n"
"v_mfma_f32_16x16x32_bf16 v[188:191], v[48:51], a[60:63], v[188:191]\n"
"L_q1_w3:\n"
"s_waitcnt lgkmcnt(10)\n"
"v_mfma_f32_16x16x32_bf16 v[64:67], v[20:23], a[8:11], v[64:67]\n"
"ds_read_b64_tr_b16 v[48:49], v11 offset:1544\n"
"ds_read_b64_tr_b16 v[50:51], v12 offset:1544\n"
"v_mfma_f32_16x16x32_bf16 v[128:131], v[20:23], a[64:67], v[128:131]\n"
"s_add_u32 s56, s58, 131072\n"
"v_mfma_f32_16x16x32_bf16 v[68:71], v[24:27], a[8:11], v[68:71]\n"
"ds_read_b64_tr_b16 v[20:21], v11 offset:2048\n"
"ds_read_b64_tr_b16 v[22:23], v12 offset:2048\n"
"v_mfma_f32_16x16x32_bf16 v[132:135], v[24:27], a[64:67], v[132:135]\n"
"s_add_u32 m0, s56, 0\n"
"s_waitcnt lgkmcnt(10)\n"
"v_mfma_f32_16x16x32_bf16 v[72:75], v[28:31], a[8:11], v[72:75]\n"
"ds_read_b64_tr_b16 v[24:25], v11 offset:2056\n"
"ds_read_b64_tr_b16 v[26:27], v12 offset:2056\n"
"v_mfma_f32_16x16x32_bf16 v[136:139], v[28:31], a[64:67], v[136:139]\n"
"buffer_load_dwordx4 %[dmaw], s[48:51], 0 offen offset:0 lds\n"
"v_mfma_f32_16x16x32_bf16 v[76:79], v[32:35], a[8:11], v[76:79]\n"
"ds_read_b64_tr_b16 v[28:29], v11 offset:2560\n"
"ds_read_b64_tr_b16 v[30:31], v12 offset:2560\n"
"v_mfma_f32_16x16x32_bf16 v[140:143], v[32:35], a[64:67], v[140:143]\n"
"s_add_u32 m0, s56, 896\n"
"s_waitcnt lgkmcnt(10)\n"
"v_mfma_f32_16x16x32_bf16 v[80:83], v[36:39], a[8:11], v[80:83]\n"
"ds_read_b64_tr_b16 v[32:33], v11 offset:2568\n"
"ds_read_b64_tr_b16 v[34:35], v12 offset:2568\n"
"v_mfma_f32_16x16x32_bf16 v[144:147], v[36:39], a[64:67], v[144:147]\n"
"s_waitcnt vmcnt(8)\n"
"ds_read_b128 a[32:35], %[xrd0] offset:1024\n"
"v_mfma_f32_16x16x32_bf16 v[84:87], v[40:43], a[8:11], v[84:87]\n"
"ds_read_b64_tr_b16 v[36:37], v11 offset:3072\n"
"ds_read_b64_tr_b16 v[38:39], v12 offset:3072\n"
"v_mfma_f32_16x16x32_bf16 v[148:151], v[40:43], a[64:67], v[148:151]\n"
"buffer_load_dwordx4 %[dmaw], s[48:51], 0 offen offset:128 lds\n"
"s_waitcnt lgkmcnt(11)\n"
"v_mfma_f32_16x16x32_bf16 v[88:91], v[44:47], a[8:11], v[88:91]\n"
"ds_read_b64_tr_b16 v[40:41], v11 offset:3080\n"
"ds_read_b64_tr_b16 v[42:43], v12 offset:3080\n"
"v_mfma_f32_16x16x32_bf16 v[152:155], v[44:47], a[64:67], v[152:155]\n"
"s_add_u32 m0, s56, 1792\n"
"ds_read_b128 a[36:39], %[xrd1] offset:1024\n"
"v_mfma_f32_16x16x32_bf16 v[92:95], v[48:51], a[8:11], v[92:95]\n"
"ds_read_b64_tr_b16 v[44:45], v11 offset:3584\n"
"ds_read_b64_tr_b16 v[46:47], v12 offset:3584\n"
"v_mfma_f32_16x16x32_bf16 v[156:159], v[48:51], a[64:67], v[156:159]\n"
"buffer_load_dwordx4 %[dmaw], s[48:51], 0 offen offset:256 lds\n"
"s_waitcnt lgkmcnt(12)\n"
"v_mfma_f32_16x16x32_bf16 v[96:99], v[20:23], a[8:11], v[96:99]\n"
"ds_read_b64_tr_b16 v[48:49], v11 offset:3592\n"
"ds_read_b64_tr_b16 v[50:51], v12 offset:3592\n"
"v_mfma_f32_16x16x32_bf16 v[160:163], v[20:23], a[64:67], v[160:163]\n"
"ds_read_b128 a[88:91], %[xrd0] offset:3072\n"
"ds_read_b128 a[92:95], %[xrd1] offset:3072\n"
"v_mfma_f32_16x16x32_bf16 v[100:103], v[24:27], a[8:11], v[100:103]\n"
"ds_read_b64_tr_b16 v[20:21], v11 offset:16384\n"
"ds_read_b64_tr_b16 v[22:23], v12 offset:16384\n"
"v_mfma_f32_16x16x32_bf16 v[164:167], v[24:27], a[64:67], v[164:167]\n"
"s_add_u32 m0, s56, 2688\n"
"s_waitcnt lgkmcnt(14)\n"
"v_mfma_f32_16x16x32_bf16 v[104:107], v[28:31], a[8:11], v[104:107]\n"
"ds_read_b64_tr_b16 v[24:25], v11 offset:16392\n"
"ds_read_b64_tr_b16 v[26:27], v12 offset:16392\n"
"v_mfma_f32_16x16x32_bf16 v[168:171], v[28:31], a[64:67], v[168:171]\n"
"buffer_load_dwordx4 %[dmaw], s[48:51], 0 offen offset:384 lds\n"
"s_waitcnt vmcnt(7)\n"
"v_mfma_f32_16x16x32_bf16 v[108:111], v[32:35], a[8:11], v[108:111]\n"
"ds_read_b64_tr_b16 v[28:29], v11 offset:16896\n"
"ds_read_b64_tr_b16 v[30:31], v12 offset:16896\n"
"v_mfma_f32_16x16x32_bf16 v[172:175], v[32:35], a[64:67], v[172:175]\n"
"s_add_u32 m0, s56, 4096\n"
"s_waitcnt lgkmcnt(13)\n"
"v_mfma_f32_16x16x32_bf16 v[112:115], v[36:39], a[8:11], v[112:115]\n"
"ds_read_b64_tr_b16 v[32:33], v11 offset:16904\n"
"ds_read_b64_tr_b16 v[34:35], v12 offset:16904\n"
"v_mfma_f32_16x16x32_bf16 v[176:179], v[36:39], a[64:67], v[176:179]\n"
"ds_read_b128 a[40:43], %[xrd0] offset:9216\n"
"ds_read_b128 a[44:47], %[xrd1] offset:9216\n"
"v_mfma_f32_16x16x32_bf16 v[116:119], v[40:43], a[8:11], v[116:119]\n"
"ds_read_b64_tr_b16 v[36:37], v11 offset:17408\n"
"ds_read_b64_tr_b16 v[38:39], v12 offset:17408\n"
"v_mfma_f32_16x16x32_bf16 v[180:183], v[40:43], a[64:67], v[180:183]\n"
"buffer_load_dwordx4 %[dmaw], s[48:51], s63 offen offset:0 lds\n"
"s_waitcnt lgkmcnt(14)\n"
"v_mfma_f32_16x16x32_bf16 v[120:123], v[44:47], a[8:11], v[120:123]\n"
"ds_read_b64_tr_b16 v[40:41], v11 offset:17416\n"
"ds_read_b64_tr_b16 v[42:43], v12 offset:17416\n"
"v_mfma_f32_16x16x32_bf16 v[184:187], v[44:47], a[64:67], v[184:187]\n"
"s_waitcnt lgkmcnt(14)\n"
"s_add_u32 m0, s85, 256\n"
"v_mfma_f32_16x16x32_bf16 v[124:127], v[48:51], a[8:11], v[124:127]\n"
"ds_read_b64_tr_b16 v[44:45], v11 offset:17920\n"
"ds_read_b64_tr_b16 v[46:47], v12 offset:17920\n"
"v_mfma_f32_16x16x32_bf16 v[188:191], v[48:51], a[64:67], v[188:191]\n"
"buffer_load_dwordx4 %[dmax], s[52:55], s74 offen offset:768 lds\n"
"s_waitcnt lgkmcnt(12)\n"
"v_mfma_f32_16x16x32_bf16 v[64:67], v[20:23], a[12:15], v[64:67]\n"
"ds_read_b64_tr_b16 v[48:49], v11 offset:17928\n"
"ds_read_b64_tr_b16 v[50:51], v12 offset:17928\n"
"v_mfma_f32_16x16x32_bf16 v[128:131], v[20:23], a[68:71], v[128:131]\n"
"s_add_u32 m0, s85, 1280\n"
"ds_read_b128 a[96:99], %[xrd0] offset:11264\n"
"v_mfma_f32_16x16x32_bf16 v[68:71], v[24:27], a[12:15], v[68:71]\n"
"ds_read_b64_tr_b16 v[20:21], v11 offset:18432\n"
"ds_read_b64_tr_b16 v[22:23], v12 offset:18432\n"
"v_mfma_f32_16x16x32_bf16 v[132:135], v[24:27], a[68:71], v[132:135]\n"
"buffer_load_dwordx4 %[dmax], s[52:55], s77 offen offset:768 lds\n"
"s_waitcnt lgkmcnt(13)\n"
"v_mfma_f32_16x16x32_bf16 v[72:75], v[28:31], a[12:15], v[72:75]\n"
"ds_read_b64_tr_b16 v[24:25], v11 offset:18440\n"
"ds_read_b64_tr_b16 v[26:27], v12 offset:18440\n"
"v_mfma_f32_16x16x32_bf16 v[136:139], v[28:31], a[68:71], v[136:139]\n"
"s_add_u32 m0, s85, 2304\n"
"ds_read_b128 a[100:103], %[xrd1] offset:11264\n"
"v_mfma_f32_16x16x32_bf16 v[76:79], v[32:35], a[12:15], v[76:79]\n"
"ds_read_b64_tr_b16 v[28:29], v11 offset:18944\n"
"ds_read_b64_tr_b16 v[30:31], v12 offset:18944\n"
"v_mfma_f32_16x16x32_bf16 v[140:143], v[32:35], a[68:71], v[140:143]\n"
"buffer_load_dwordx4 %[dmax], s[52:55], s75 offen offset:768 lds\n"
"s_waitcnt lgkmcnt(12)\n"
"v_mfma_f32_16x16x32_bf16 v[80:83], v[36:39], a[12:15], v[80:83]\n"
"ds_read_b64_tr_b16 v[32:33], v11 offset:18952\n"
"ds_read_b64_tr_b16 v[34:35], v12 offset:18952\n"
"v_mfma_f32_16x16x32_bf16 v[144:147], v[36:39], a[68:71], v[144:147]\n"
"s_add_u32 m0, s85, 3328\n"
"v_mfma_f32_16x16x32_bf16 v[84:87], v[40:43], a[12:15], v[84:87]\n"
"ds_read_b64_tr_b16 v[36:37], v11 offset:19456\n"
"ds_read_b64_tr_b16 v[38:39], v12 offset:19456\n"
"v_mfma_f32_16x16x32_bf16 v[148:151], v[40:43], a[68:71], v[148:151]\n"
"buffer_load_dwordx4 %[dmax], s[52:55], s78 offen offset:768 lds\n"
"s_waitcnt lgkmcnt(12)\n"
"v_mfma_f32_16x16x32_bf16 v[88:91], v[44:47], a[12:15], v[88:91]\n"
"ds_read_b64_tr_b16 v[40:41], v11 offset:19464\n"
"ds_read_b64_tr_b16 v[42:43], v12 offset:19464\n"
"v_mfma_f32_16x16x32_bf16 v[152:155], v[44:47], a[68:71], v[152:155]\n"
"s_add_u32 m0, s56, 4992\n"
"v_mfma_f32_16x16x32_bf16 v[92:95], v[48:51], a[12:15], v[92:95]\n"
"ds_read_b64_tr_b16 v[44:45], v11 offset:19968\n"
"ds_read_b64_tr_b16 v[46:47], v12 offset:19968\n"
"v_mfma_f32_16x16x32_bf16 v[156:159], v[48:51], a[68:71], v[156:159]\n"
"buffer_load_dwordx4 %[dmaw], s[48:51], s63 offen offset:128 lds\n"
"s_waitcnt lgkmcnt(11)\n"
"v_mfma_f32_16x16x32_bf16 v[96:99], v[20:23], a[12:15], v[96:99]\n"
"ds_read_b64_tr_b16 v[48:49], v11 offset:19976\n"
"ds_read_b64_tr_b16 v[50:51], v12 offset:19976\n"
"v_mfma_f32_16x16x32_bf16 v[160:163], v[20:23], a[68:71], v[160:163]\n"
"s_add_u32 m0, s56, 5888\n"
"v_mfma_f32_16x16x32_bf16 v[100:103], v[24:27], a[12:15], v[100:103]\n"
"s_add_u32 s57, s60, 98304\n"
"buffer_load_dwordx4 %[dmaw], s[48:51], s63 offen offset:256 lds\n"
"v_mfma_f32_16x16x32_bf16 v[164:167], v[24:27], a[68:71], v[164:167]\n"
"v_add_u32 v11, s57, %[wl0]\n"
"v_add_u32 v12, s57, %[wl1]\n"
"s_add_u32 m0, s56, 6784\n"
"s_nop 0\n"
"buffer_load_dwordx4 %[dmaw], s[48:51], s63 offen offset:384 lds\n"
"s_add_u32 s48, s48, 0x80000\n"
"s_addc_u32 s49, s49, 0\n"
"s_sub_u32 s50, s50, 0x80000\n"
"s_cselect_b32 s50, 0, s50\n"
"s_waitcnt lgkmcnt(8)\n"
"v_mfma_f32_16x16x32_bf16 v[104:107], v[28:31], a[12:15], v[104:107]\n"
"v_mfma_f32_16x16x32_bf16 v[168:171], v[28:31], a[68:71], v[168:171]\n"
"s_waitcnt vmcnt(32) lgkmcnt(0)\n"
"s_barrier\n"
"v_mfma_f32_16x16x32_bf16 v[108:111], v[32:35], a[12:15], v[108:111]\n"
"ds_read_b64_tr_b16 v[20:21], v11 offset:0\n"
"ds_read_b64_tr_b16 v[22:23], v12 offset:0\n"
"v_mfma_f32_16x16x32_bf16 v[172:175], v[32:35], a[68:71], v[172:175]\n"
"ds_read_b64_tr_b16 v[24:25], v11 offset:8\n"
"ds_read_b64_tr_b16 v[26:27], v12 offset:8\n"
"ds_read_b64_tr_b16 v[28:29], v11 offset:512\n"
"ds_read_b64_tr_b16 v[30:31], v12 offset:512\n"
"v_mfma_f32_16x16x32_bf16 v[112:115], v[36:39], a[12:15], v[112:115]\n"
"ds_read_b64_tr_b16 v[32:33], v11 offset:520\n"
"ds_read_b64_tr_b16 v[34:35], v12 offset:520\n"
"v_mfma_f32_16x16x32_bf16 v[176:179], v[36:39], a[68:71], v[176:179]\n"
"v_mfma_f32_16x16x32_bf16 v[116:119], v[40:43], a[12:15], v[116:119]\n"
"ds_read_b64_tr_b16 v[36:37], v11 offset:1024\n"
"ds_read_b64_tr_b16 v[38:39], v12 offset:1024\n"
"v_mfma_f32_16x16x32_bf16 v[180:183], v[40:43], a[68:71], v[180:183]\n"
"v_mfma_f32_16x16x32_bf16 v[120:123], v[44:47], a[12:15], v[120:123]\n"
"ds_read_b64_tr_b16 v[40:41], v11 offset:1032\n"
"ds_read_b64_tr_b16 v[42:43], v12 offset:1032\n"
"v_mfma_f32_16x16x32_bf16 v[184:187], v[44:47], a[68:71], v[184:187]\n"
"v_mfma_f32_16x16x32_bf16 v[124:127], v[48:51], a[12:15], v[124:127]\n"
"ds_read_b64_tr_b16 v[44:45], v11 offset:1536\n"
"ds_read_b64_tr_b16 v[46:47], v12 offset:1536\n"
"v_mfma_f32_16x16x32_bf16 v[188:191], v[48:51], a[68:71], v[188:191]\n"
"s_waitcnt lgkmcnt(10)\n"
"v_mfma_f32_16x16x32_bf16 v[64:67], v[20:23], a[16:19], v[64:67]\n"
"ds_read_b64_tr_b16 v[48:49], v11 offset:1544\n"
"ds_read_b64_tr_b16 v[50:51], v12 offset:1544\n"
"v_mfma_f32_16x16x32_bf16 v[128:131], v[20:23], a[72:75], v[128:131]\n"
"s_add_u32 s56, s58, 65536\n"
"v_mfma_f32_16x16x32_bf16 v[68:71], v[24:27], a[16:19], v[68:71]\n"
"ds_read_b64_tr_b16 v[20:21], v11 offset:2048\n"
"ds_read_b64_tr_b16 v[22:23], v12 offset:2048\n"
"v_mfma_f32_16x16x32_bf16 v[132:135], v[24:27], a[72:75], v[132:135]\n"
"s_add_u32 m0, s56, 0\n"
"s_waitcnt lgkmcnt(10)\n"
"v_mfma_f32_16x16x32_bf16 v[72:75], v[28:31], a[16:19], v[72:75]\n"
"ds_read_b64_tr_b16 v[24:25], v11 offset:2056\n"
"ds_read_b64_tr_b16 v[26:27], v12 offset:2056\n"
"v_mfma_f32_16x16x32_bf16 v[136:139], v[28:31], a[72:75], v[136:139]\n"
"buffer_load_dwordx4 %[dmaw], s[48:51], 0 offen offset:0 lds\n"
"v_mfma_f32_16x16x32_bf16 v[76:79], v[32:35], a[16:19], v[76:79]\n"
"ds_read_b64_tr_b16 v[28:29], v11 offset:2560\n"
"ds_read_b64_tr_b16 v[30:31], v12 offset:2560\n"
"v_mfma_f32_16x16x32_bf16 v[140:143], v[32:35], a[72:75], v[140:143]\n"
"s_add_u32 m0, s56, 896\n"
"s_waitcnt lgkmcnt(10)\n"
"v_mfma_f32_16x16x32_bf16 v[80:83], v[36:39], a[16:19], v[80:83]\n"
"ds_read_b64_tr_b16 v[32:33], v11 offset:2568\n"
"ds_read_b64_tr_b16 v[34:35], v12 offset:2568\n"
"v_mfma_f32_16x16x32_bf16 v[144:147], v[36:39], a[72:75], v[144:147]\n"
"s_waitcnt vmcnt(4)\n"
"ds_read_b128 a[48:51], %[xrd0] offset:1024\n"
"v_mfma_f32_16x16x32_bf16 v[84:87], v[40:43], a[16:19], v[84:87]\n"
"ds_read_b64_tr_b16 v[36:37], v11 offset:3072\n"
"ds_read_b64_tr_b16 v[38:39], v12 offset:3072\n"
"v_mfma_f32_16x16x32_bf16 v[148:151], v[40:43], a[72:75], v[148:151]\n"
"buffer_load_dwordx4 %[dmaw], s[48:51], 0 offen offset:128 lds\n"
"s_waitcnt lgkmcnt(11)\n"
"v_mfma_f32_16x16x32_bf16 v[88:91], v[44:47], a[16:19], v[88:91]\n"
"ds_read_b64_tr_b16 v[40:41], v11 offset:3080\n"
"ds_read_b64_tr_b16 v[42:43], v12 offset:3080\n"
"v_mfma_f32_16x16x32_bf16 v[152:155], v[44:47], a[72:75], v[152:155]\n"
"s_add_u32 m0, s56, 1792\n"
"ds_read_b128 a[52:55], %[xrd1] offset:1024\n"
"v_mfma_f32_16x16x32_bf16 v[92:95], v[48:51], a[16:19], v[92:95]\n"
"ds_read_b64_tr_b16 v[44:45], v11 offset:3584\n"
"ds_read_b64_tr_b16 v[46:47], v12 offset:3584\n"
"v_mfma_f32_16x16x32_bf16 v[156:159], v[48:51], a[72:75], v[156:159]\n"
"buffer_load_dwordx4 %[dmaw], s[48:51], 0 offen offset:256 lds\n"
"s_waitcnt lgkmcnt(12)\n"
"v_mfma_f32_16x16x32_bf16 v[96:99], v[20:23], a[16:19], v[96:99]\n"
"ds_read_b64_tr_b16 v[48:49], v11 offset:3592\n"
"ds_read_b64_tr_b16 v[50:51], v12 offset:3592\n"
"v_mfma_f32_16x16x32_bf16 v[160:163], v[20:23], a[72:75], v[160:163]\n"
"ds_read_b128 a[104:107], %[xrd0] offset:3072\n"
"ds_read_b128 a[108:111], %[xrd1] offset:3072\n"
"v_mfma_f32_16x16x32_bf16 v[100:103], v[24:27], a[16:19], v[100:103]\n"
"ds_read_b64_tr_b16 v[20:21], v11 offset:16384\n"
"ds_read_b64_tr_b16 v[22:23], v12 offset:16384\n"
"v_mfma_f32_16x16x32_bf16 v[164:167], v[24:27], a[72:75], v[164:167]\n"
"s_add_u32 m0, s56, 2688\n"
"s_waitcnt lgkmcnt(14)\n"
"v_mfma_f32_16x16x32_bf16 v[104:107], v[28:31], a[16:19], v[104:107]\n"
"ds_read_b64_tr_b16 v[24:25], v11 offset:16392\n"
"ds_read_b64_tr_b16 v[26:27], v12 offset:16392\n"
"v_mfma_f32_16x16x32_bf16 v[168:171], v[28:31], a[72:75], v[168:171]\n"
"buffer_load_dwordx4 %[dmaw], s[48:51], 0 offen offset:384 lds\n"
"v_mfma_f32_16x16x32_bf16 v[108:111], v[32:35], a[16:19], v[108:111]\n"
"ds_read_b64_tr_b16 v[28:29], v11 offset:16896\n"
"ds_read_b64_tr_b16 v[30:31], v12 offset:16896\n"
"v_mfma_f32_16x16x32_bf16 v[172:175], v[32:35], a[72:75], v[172:175]\n"
"s_add_u32 m0, s56, 4096\n"
"s_waitcnt lgkmcnt(13)\n"
"v_mfma_f32_16x16x32_bf16 v[112:115], v[36:39], a[16:19], v[112:115]\n"
"ds_read_b64_tr_b16 v[32:33], v11 offset:16904\n"
"ds_read_b64_tr_b16 v[34:35], v12 offset:16904\n"
"v_mfma_f32_16x16x32_bf16 v[176:179], v[36:39], a[72:75], v[176:179]\n"
"v_mfma_f32_16x16x32_bf16 v[116:119], v[40:43], a[16:19], v[116:119]\n"
"ds_read_b64_tr_b16 v[36:37], v11 offset:17408\n"
"ds_read_b64_tr_b16 v[38:39], v12 offset:17408\n"
"v_mfma_f32_16x16x32_bf16 v[180:183], v[40:43], a[72:75], v[180:183]\n"
"buffer_load_dwordx4 %[dmaw], s[48:51], s63 offen offset:0 lds\n"
"s_waitcnt lgkmcnt(12)\n"
"v_mfma_f32_16x16x32_bf16 v[120:123], v[44:47], a[16:19], v[120:123]\n"
"ds_read_b64_tr_b16 v[40:41], v11 offset:17416\n"
"ds_read_b64_tr_b16 v[42:43], v12 offset:17416\n"
"v_mfma_f32_16x16x32_bf16 v[184:187], v[44:47], a[72:75], v[184:187]\n"
"s_add_u32 m0, s56, 4992\n"
"v_mfma_f32_16x16x32_bf16 v[124:127], v[48:51], a[16:19], v[124:127]\n"
"ds_read_b64_tr_b16 v[44:45], v11 offset:17920\n"
"ds_read_b64_tr_b16 v[46:47], v12 offset:17920\n"
"v_mfma_f32_16x16x32_bf16 v[188:191], v[48:51], a[72:75], v[188:191]\n"
"buffer_load_dwordx4 %[dmaw], s[48:51], s63 offen offset:128 lds\n"
"s_waitcnt lgkmcnt(10)\n"
"v_mfma_f32_16x16x32_bf16 v[64:67], v[20:23], a[20:23], v[64:67]\n"
"ds_read_b64_tr_b16 v[48:49], v11 offset:17928\n"
"ds_read_b64_tr_b16 v[50:51], v12 offset:17928\n"
"v_mfma_f32_16x16x32_bf16 v[128:131], v[20:23], a[76:79], v[128:131]\n"
"v_mfma_f32_16x16x32_bf16 v[68:71], v[24:27], a[20:23], v[68:71]\n"
"ds_read_b64_tr_b16 v[20:21], v11 offset:18432\n"
"ds_read_b64_tr_b16 v[22:23], v12 offset:18432\n"
"v_mfma_f32_16x16x32_bf16 v[132:135], v[24:27], a[76:79], v[132:135]\n"
"s_add_u32 m0, s56, 5888\n"
"s_waitcnt lgkmcnt(10)\n"
"v_mfma_f32_16x16x32_bf16 v[72:75], v[28:31], a[20:23], v[72:75]\n"
"ds_read_b64_tr_b16 v[24:25], v11 offset:18440\n"
"ds_read_b64_tr_b16 v[26:27], v12 offset:18440\n"
"v_mfma_f32_16x16x32_bf16 v[136:139], v[28:31], a[76:79], v[136:139]\n"
"buffer_load_dwordx4 %[dmaw], s[48:51], s63 offen offset:256 lds\n"
"v_mfma_f32_16x16x32_bf16 v[76:79], v[32:35], a[20:23], v[76:79]\n"
"ds_read_b64_tr_b16 v[28:29], v11 offset:18944\n"
"ds_read_b64_tr_b16 v[30:31], v12 offset:18944\n"
"v_mfma_f32_16x16x32_bf16 v[140:143], v[32:35], a[76:79], v[140:143]\n"
"s_add_u32 m0, s56, 6784\n"
"s_waitcnt lgkmcnt(10)\n"
"v_mfma_f32_16x16x32_bf16 v[80:83], v[36:39], a[20:23], v[80:83]\n"
"ds_read_b64_tr_b16 v[32:33], v11 offset:18952\n"
"ds_read_b64_tr_b16 v[34:35], v12 offset:18952\n"
"v_mfma_f32_16x16x32_bf16 v[144:147], v[36:39], a[76:79], v[144:147]\n"
"v_mfma_f32_16x16x32_bf16 v[84:87], v[40:43], a[20:23], v[84:87]\n"
"ds_read_b64_tr_b16 v[36:37], v11 offset:19456\n"
"ds_read_b64_tr_b16 v[38:39], v12 offset:19456\n"
"v_mfma_f32_16x16x32_bf16 v[148:151], v[40:43], a[76:79], v[148:151]\n"
"buffer_load_dwordx4 %[dmaw], s[48:51], s63 offen offset:384 lds\n"
"s_waitcnt lgkmcnt(10)\n"
"v_mfma_f32_16x16x32_bf16 v[88:91], v[44:47], a[20:23], v[88:91]\n"
"ds_read_b64_tr_b16 v[40:41], v11 offset:19464\n"
"ds_read_b64_tr_b16 v[42:43], v12 offset:19464\n"
"v_mfma_f32_16x16x32_bf16 v[152:155], v[44:47], a[76:79], v[152:155]\n"
"s_add_u32 s48, s48, 0x80000\n"
"s_addc_u32 s49, s49, 0\n"
"v_mfma_f32_16x16x32_bf16 v[92:95], v[48:51], a[20:23], v[92:95]\n"
"ds_read_b64_tr_b16 v[44:45], v11 offset:19968\n"
"ds_read_b64_tr_b16 v[46:47], v12 offset:19968\n"
"v_mfma_f32_16x16x32_bf16 v[156:159], v[48:51], a[76:79], v[156:159]\n"
"s_waitcnt lgkmcnt(10)\n"
"v_mfma_f32_16x16x32_bf16 v[96:99], v[20:23], a[20:23], v[96:99]\n"
"ds_read_b64_tr_b16 v[48:49], v11 offset:19976\n"
"ds_read_b64_tr_b16 v[50:51], v12 offset:19976\n"
"v_mfma_f32_16x16x32_bf16 v[160:163], v[20:23], a[76:79], v[160:163]\n"
"s_sub_u32 s50, s50, 0x80000\n"
"s_cselect_b32 s50, 0, s50\n"
"v_mfma_f32_16x16x32_bf16 v[100:103], v[24:27], a[20:23], v[100:103]\n"
"s_add_u32 s57, s60, 131072\n"
"v_mfma_f32_16x16x32_bf16 v[164:167], v[24:27], a[76:79], v[164:167]\n"
"v_add_u32 v11, s57, %[wl0]\n"
"v_add_u32 v12, s57, %[wl1]\n"
"s_waitcnt lgkmcnt(8)\n"
"v_mfma_f32_16x16x32_bf16 v[104:107], v[28:31], a[20:23], v[104:107]\n"
"v_mfma_f32_16x16x32_bf16 v[168:171], v[28:31], a[76:79], v[168:171]\n"
"s_waitcnt vmcnt(8) lgkmcnt(0)\n"
"s_barrier\n"
"v_mfma_f32_16x16x32_bf16 v[108:111], v[32:35], a[20:23], v[108:111]\n"
"ds_read_b64_tr_b16 v[20:21], v11 offset:0\n"
"ds_read_b64_tr_b16 v[22:23], v12 offset:0\n"
"v_mfma_f32_16x16x32_bf16 v[172:175], v[32:35], a[76:79], v[172:175]\n"
"ds_read_b64_tr_b16 v[24:25], v11 offset:8\n"
"ds_read_b64_tr_b16 v[26:27], v12 offset:8\n"
"ds_read_b64_tr_b16 v[28:29], v11 offset:512\n"
"ds_read_b64_tr_b16 v[30:31], v12 offset:512\n"
"v_mfma_f32_16x16x32_bf16 v[112:115], v[36:39], a[20:23], v[112:115]\n"
"ds_read_b64_tr_b16 v[32:33], v11 offset:520\n"
"ds_read_b64_tr_b16 v[34:35], v12 offset:520\n"
"v_mfma_f32_16x16x32_bf16 v[176:179], v[36:39], a[76:79], v[176:179]\n"
"v_mfma_f32_16x16x32_bf16 v[116:119], v[40:43], a[20:23], v[116:119]\n"
"ds_read_b64_tr_b16 v[36:37], v11 offset:1024\n"
"ds_read_b64_tr_b16 v[38:39], v12 offset:1024\n"
"v_mfma_f32_16x16x32_bf16 v[180:183], v[40:43], a[76:79], v[180:183]\n"
"v_mfma_f32_16x16x32_bf16 v[120:123], v[44:47], a[20:23], v[120:123]\n"
"ds_read_b64_tr_b16 v[40:41], v11 offset:1032\n"
"ds_read_b64_tr_b16 v[42:43], v12 offset:1032\n"
"v_mfma_f32_16x16x32_bf16 v[184:187], v[44:47], a[76:79], v[184:187]\n"
"v_mfma_f32_16x16x32_bf16 v[124:127], v[48:51], a[20:23], v[124:127]\n"
"ds_read_b64_tr_b16 v[44:45], v11 offset:1536\n"
"ds_read_b64_tr_b16 v[46:47], v12 offset:1536\n"
"v_mfma_f32_16x16x32_bf16 v[188:191], v[48:51], a[76:79], v[188:191]\n"
"s_waitcnt lgkmcnt(10)\n"
"v_mfma_f32_16x16x32_bf16 v[64:67], v[20:23], a[24:27], v[64:67]\n"
"ds_read_b64_tr_b16 v[48:49], v11 offset:1544\n"
"ds_read_b64_tr_b16 v[50:51], v12 offset:1544\n"
"v_mfma_f32_16x16x32_bf16 v[128:131], v[20:23], a[80:83], v[128:131]\n"
"s_add_u32 s56, s58, 98304\n"
"s_add_u32 m0, s56, 0\n"
"v_mfma_f32_16x16x32_bf16 v[68:71], v[24:27], a[24:27], v[68:71]\n"
"ds_read_b64_tr_b16 v[20:21], v11 offset:2048\n"
"ds_read_b64_tr_b16 v[22:23], v12 offset:2048\n"
"v_mfma_f32_16x16x32_bf16 v[132:135], v[24:27], a[80:83], v[132:135]\n"
"buffer_load_dwordx4 %[dmaw], s[48:51], 0 offen offset:0 lds\n"
"s_waitcnt lgkmcnt(10)\n"
"v_mfma_f32_16x16x32_bf16 v[72:75], v[28:31], a[24:27], v[72:75]\n"
"ds_read_b64_tr_b16 v[24:25], v11 offset:2056\n"
"ds_read_b64_tr_b16 v[26:27], v12 offset:2056\n"
"v_mfma_f32_16x16x32_bf16 v[136:139], v[28:31], a[80:83], v[136:139]\n"
"s_add_u32 m0, s56, 896\n"
"v_mfma_f32_16x16x32_bf16 v[76:79], v[32:35], a[24:27], v[76:79]\n"
"ds_read_b64_tr_b16 v[28:29], v11 offset:2560\n"
"ds_read_b64_tr_b16 v[30:31], v12 offset:2560\n"
"v_mfma_f32_16x16x32_bf16 v[140:143], v[32:35], a[80:83], v[140:143]\n"
"buffer_load_dwordx4 %[dmaw], s[48:51], 0 offen offset:128 lds\n"
"s_waitcnt lgkmcnt(10)\n"
"v_mfma_f32_16x16x32_bf16 v[80:83], v[36:39], a[24:27], v[80:83]\n"
"ds_read_b64_tr_b16 v[32:33], v11 offset:2568\n"
"ds_read_b64_tr_b16 v[34:35], v12 offset:2568\n"
"v_mfma_f32_16x16x32_bf16 v[144:147], v[36:39], a[80:83], v[144:147]\n"
"s_add_u32 m0, s56, 1792\n"
"v_mfma_f32_16x16x32_bf16 v[84:87], v[40:43], a[24:27], v[84:87]\n"
"ds_read_b64_tr_b16 v[36:37], v11 offset:3072\n"
"ds_read_b64_tr_b16 v[38:39], v12 offset:3072\n"
"v_mfma_f32_16x16x32_bf16 v[148:151], v[40:43], a[80:83], v[148:151]\n"
"buffer_load_dwordx4 %[dmaw], s[48:51], 0 offen offset:256 lds\n"
"s_waitcnt lgkmcnt(10)\n"
"v_mfma_f32_16x16x32_bf16 v[88:91], v[44:47], a[24:27], v[88:91]\n"
"ds_read_b64_tr_b16 v[40:41], v11 offset:3080\n"
"ds_read_b64_tr_b16 v[42:43], v12 offset:3080\n"
"v_mfma_f32_16x16x32_bf16 v[152:155], v[44:47], a[80:83], v[152:155]\n"
"s_add_u32 m0, s56, 2688\n"
"v_mfma_f32_16x16x32_bf16 v[92:95], v[48:51], a[24:27], v[92:95]\n"
"ds_read_b64_tr_b16 v[44:45], v11 offset:3584\n"
"ds_read_b64_tr_b16 v[46:47], v12 offset:3584\n"
"v_mfma_f32_16x16x32_bf16 v[156:159], v[48:51], a[80:83], v[156:159]\n"
"buffer_load_dwordx4 %[dmaw], s[48:51], 0 offen offset:384 lds\n"
"s_waitcnt lgkmcnt(10)\n"
"v_mfma_f32_16x16x32_bf16 v[96:99], v[20:23], a[24:27], v[96:99]\n"
"ds_read_b64_tr_b16 v[48:49], v11 offset:3592\n"
"ds_read_b64_tr_b16 v[50:51], v12 offset:3592\n"
"v_mfma_f32_16x16x32_bf16 v[160:163], v[20:23], a[80:83], v[160:163]\n"
"s_add_u32 m0, s56, 4096\n"
"v_mfma_f32_16x16x32_bf16 v[100:103], v[24:27], a[24:27], v[100:103]\n"
"ds_read_b64_tr_b16 v[20:21], v11 offset:16384\n"
"ds_read_b64_tr_b16 v[22:23], v12 offset:16384\n"
"v_mfma_f32_16x16x32_bf16 v[164:167], v[24:27], a[80:83], v[164:167]\n"
"buffer_load_dwordx4 %[dmaw], s[48:51], s63 offen offset:0 lds\n"
"s_waitcnt lgkmcnt(10)\n"
"v_mfma_f32_16x16x32_bf16 v[104:107], v[28:31], a[24:27], v[104:107]\n"
"ds_read_b64_tr_b16 v[24:25], v11 offset:16392\n"
"ds_read_b64_tr_b16 v[26:27], v12 offset:16392\n"
"v_mfma_f32_16x16x32_bf16 v[168:171], v[28:31], a[80:83], v[168:171]\n"
"s_add_u32 m0, s56, 4992\n"
"v_mfma_f32_16x16x32_bf16 v[108:111], v[32:35], a[24:27], v[108:111]\n"
"ds_read_b64_tr_b16 v[28:29], v11 offset:16896\n"
"ds_read_b64_tr_b16 v[30:31], v12 offset:16896\n"
"v_mfma_f32_16x16x32_bf16 v[172:175], v[32:35], a[80:83], v[172:175]\n"
"buffer_load_dwordx4 %[dmaw], s[48:51], s63 offen offset:128 lds\n"
"s_waitcnt lgkmcnt(10)\n"
"v_mfma_f32_16x16x32_bf16 v[112:115], v[36:39], a[24:27], v[112:115]\n"
"ds_read_b64_tr_b16 v[32:33], v11 offset:16904\n"
"ds_read_b64_tr_b16 v[34:35], v12 offset:16904\n"
"v_mfma_f32_16x16x32_bf16 v[176:179], v[36:39], a[80:83], v[176:179]\n"
"s_add_u32 m0, s56, 5888\n"
"v_mfma_f32_16x16x32_bf16 v[116:119], v[40:43], a[24:27], v[116:119]\n"
"ds_read_b64_tr_b16 v[36:37], v11 offset:17408\n"
"ds_read_b64_tr_b16 v[38:39], v12 offset:17408\n"
"v_mfma_f32_16x16x32_bf16 v[180:183], v[40:43], a[80:83], v[180:183]\n"
"buffer_load_dwordx4 %[dmaw], s[48:51], s63 offen offset:256 lds\n"
"s_waitcnt lgkmcnt(10)\n"
"v_mfma_f32_16x16x32_bf16 v[120:123], v[44:47], a[24:27], v[120:123]\n"
"ds_read_b64_tr_b16 v[40:41], v11 offset:17416\n"
"ds_read_b64_tr_b16 v[42:43], v12 offset:17416\n"
"v_mfma_f32_16x16x32_bf16 v[184:187], v[44:47], a[80:83], v[184:187]\n"
"s_add_u32 m0, s56, 6784\n"
"v_mfma_f32_16x16x32_bf16 v[124:127], v[48:51], a[24:27], v[124:127]\n"
"ds_read_b64_tr_b16 v[44:45], v11 offset:17920\n"
"ds_read_b64_tr_b16 v[46:47], v12 offset:17920\n"
"v_mfma_f32_16x16x32_bf16 v[188:191], v[48:51], a[80:83], v[188:191]\n"
"buffer_load_dwordx4 %[dmaw], s[48:51], s63 offen offset:384 lds\n"
"s_waitcnt lgkmcnt(10)\n"
"v_mfma_f32_16x16x32_bf16 v[64:67], v[20:23], a[28:31], v[64:67]\n"
"ds_read_b64_tr_b16 v[48:49], v11 offset:17928\n"
"ds_read_b64_tr_b16 v[50:51], v12 offset:17928\n"
"v_mfma_f32_16x16x32_bf16 v[128:131], v[20:23], a[84:87], v[128:131]\n"
"s_add_u32 s48, s48, 0x80000\n"
"s_addc_u32 s49, s49, 0\n"
"v_mfma_f32_16x16x32_bf16 v[68:71], v[24:27], a[28:31], v[68:71]\n"
"ds_read_b64_tr_b16 v[20:21], v11 offset:18432\n"
"ds_read_b64_tr_b16 v[22:23], v12 offset:18432\n"
"v_mfma_f32_16x16x32_bf16 v[132:135], v[24:27], a[84:87], v[132:135]\n"
"s_waitcnt lgkmcnt(10)\n"
"v_mfma_f32_16x16x32_bf16 v[72:75], v[28:31], a[28:31], v[72:75]\n"
"ds_read_b64_tr_b16 v[24:25], v11 offset:18440\n"
"ds_read_b64_tr_b16 v[26:27], v12 offset:18440\n"
"v_mfma_f32_16x16x32_bf16 v[136:139], v[28:31], a[84:87], v[136:139]\n"
"s_sub_u32 s50, s50, 0x80000\n"
"s_cselect_b32 s50, 0, s50\n"
"v_mfma_f32_16x16x32_bf16 v[76:79], v[32:35], a[28:31], v[76:79]\n"
"ds_read_b64_tr_b16 v[28:29], v11 offset:18944\n"
"ds_read_b64_tr_b16 v[30:31], v12 offset:18944\n"
"v_mfma_f32_16x16x32_bf16 v[140:143], v[32:35], a[84:87], v[140:143]\n"
"s_add_u32 s56, s58, 0\n"
"s_waitcnt lgkmcnt(10)\n"
"v_mfma_f32_16x16x32_bf16 v[80:83], v[36:39], a[28:31], v[80:83]\n"
"ds_read_b64_tr_b16 v[32:33], v11 offset:18952\n"
"ds_read_b64_tr_b16 v[34:35], v12 offset:18952\n"
"v_mfma_f32_16x16x32_bf16 v[144:147], v[36:39], a[84:87], v[144:147]\n"
"s_add_u32 m0, s56, 0\n"
"v_mfma_f32_16x16x32_bf16 v[84:87], v[40:43], a[28:31], v[84:87]\n"
"ds_read_b64_tr_b16 v[36:37], v11 offset:19456\n"
"ds_read_b64_tr_b16 v[38:39], v12 offset:19456\n"
"v_mfma_f32_16x16x32_bf16 v[148:151], v[40:43], a[84:87], v[148:151]\n"
"buffer_load_dwordx4 %[dmakv], s[40:43], 0 offen offset:0 nt lds\n"
"s_waitcnt lgkmcnt(10)\n"
"v_mfma_f32_16x16x32_bf16 v[88:91], v[44:47], a[28:31], v[88:91]\n"
"ds_read_b64_tr_b16 v[40:41], v11 offset:19464\n"
"ds_read_b64_tr_b16 v[42:43], v12 offset:19464\n"
"v_mfma_f32_16x16x32_bf16 v[152:155], v[44:47], a[84:87], v[152:155]\n"
"s_add_u32 m0, s56, 896\n"
"v_mfma_f32_16x16x32_bf16 v[92:95], v[48:51], a[28:31], v[92:95]\n"
"ds_read_b64_tr_b16 v[44:45], v11 offset:19968\n"
"ds_read_b64_tr_b16 v[46:47], v12 offset:19968\n"
"v_mfma_f32_16x16x32_bf16 v[156:159], v[48:51], a[84:87], v[156:159]\n"
"buffer_load_dwordx4 %[dmakv], s[40:43], 0 offen offset:128 nt lds\n"
"s_waitcnt lgkmcnt(10)\n"
"v_mfma_f32_16x16x32_bf16 v[96:99], v[20:23], a[28:31], v[96:99]\n"
"ds_read_b64_tr_b16 v[48:49], v11 offset:19976\n"
"ds_read_b64_tr_b16 v[50:51], v12 offset:19976\n"
"v_mfma_f32_16x16x32_bf16 v[160:163], v[20:23], a[84:87], v[160:163]\n"
"s_add_u32 m0, s56, 1792\n"
"v_mfma_f32_16x16x32_bf16 v[100:103], v[24:27], a[28:31], v[100:103]\n"
"s_add_u32 s57, s60, 65536\n"
"buffer_load_dwordx4 %[dmakv], s[40:43], 0 offen offset:256 nt lds\n"
"v_mfma_f32_16x16x32_bf16 v[164:167], v[24:27], a[84:87], v[164:167]\n"
"v_add_u32 v11, s57, %[wl0]\n"
"v_add_u32 v12, s57, %[wl1]\n"
"s_add_u32 m0, s56, 2688\n"
"s_nop 0\n"
"buffer_load_dwordx4 %[dmakv], s[40:43], 0 offen offset:384 nt lds\n"
"s_add_u32 m0, s56, 4096\n"
"s_nop 0\n"
"buffer_load_dwordx4 %[dmakv], s[40:43], s62 offen offset:0 nt lds\n"
"s_add_u32 m0, s56, 4992\n"
"s_nop 0\n"
"buffer_load_dwordx4 %[dmakv], s[40:43], s62 offen offset:128 nt lds\n"
"s_add_u32 m0, s56, 5888\n"
"s_nop 0\n"
"buffer_load_dwordx4 %[dmakv], s[40:43], s62 offen offset:256 nt lds\n"
"s_add_u32 m0, s56, 6784\n"
"s_nop 0\n"
"buffer_load_dwordx4 %[dmakv], s[40:43], s62 offen offset:384 nt lds\n"
"s_add_u32 s40, s40, 0x8000\n"
"s_addc_u32 s41, s41, 0\n"
"s_sub_u32 s42, s42, 0x8000\n"
"s_cselect_b32 s42, 0, s42\n"
"s_waitcnt lgkmcnt(8)\n"
"v_mfma_f32_16x16x32_bf16 v[104:107], v[28:31], a[28:31], v[104:107]\n"
"v_mfma_f32_16x16x32_bf16 v[168:171], v[28:31], a[84:87], v[168:171]\n"
"s_waitcnt vmcnt(16) lgkmcnt(0)\n"
"s_barrier\n"
"v_mfma_f32_16x16x32_bf16 v[108:111], v[32:35], a[28:31], v[108:111]\n"
"ds_read_b64_tr_b16 v[20:21], v11 offset:0\n"
"ds_read_b64_tr_b16 v[22:23], v12 offset:0\n"
"v_mfma_f32_16x16x32_bf16 v[172:175], v[32:35], a[84:87], v[172:175]\n"
"ds_read_b64_tr_b16 v[24:25], v11 offset:8\n"
"ds_read_b64_tr_b16 v[26:27], v12 offset:8\n"
"ds_read_b64_tr_b16 v[28:29], v11 offset:512\n"
"ds_read_b64_tr_b16 v[30:31], v12 offset:512\n"
"v_mfma_f32_16x16x32_bf16 v[112:115], v[36:39], a[28:31], v[112:115]\n"
"ds_read_b64_tr_b16 v[32:33], v11 offset:520\n"
"ds_read_b64_tr_b16 v[34:35], v12 offset:520\n"
"v_mfma_f32_16x16x32_bf16 v[176:179], v[36:39], a[84:87], v[176:179]\n"
"v_mfma_f32_16x16x32_bf16 v[116:119], v[40:43], a[28:31], v[116:119]\n"
"ds_read_b64_tr_b16 v[36:37], v11 offset:1024\n"
"ds_read_b64_tr_b16 v[38:39], v12 offset:1024\n"
"v_mfma_f32_16x16x32_bf16 v[180:183], v[40:43], a[84:87], v[180:183]\n"
"v_mfma_f32_16x16x32_bf16 v[120:123], v[44:47], a[28:31], v[120:123]\n"
"ds_read_b64_tr_b16 v[40:41], v11 offset:1032\n"
"ds_read_b64_tr_b16 v[42:43], v12 offset:1032\n"
"v_mfma_f32_16x16x32_bf16 v[184:187], v[44:47], a[84:87], v[184:187]\n"
"v_mfma_f32_16x16x32_bf16 v[124:127], v[48:51], a[28:31], v[124:127]\n"
"ds_read_b64_tr_b16 v[44:45], v11 offset:1536\n"
"ds_read_b64_tr_b16 v[46:47], v12 offset:1536\n"
"v_mfma_f32_16x16x32_bf16 v[188:191], v[48:51], a[84:87], v[188:191]\n"
"s_waitcnt lgkmcnt(10)\n"
"v_mfma_f32_16x16x32_bf16 v[64:67], v[20:23], a[32:35], v[64:67]\n"
"ds_read_b64_tr_b16 v[48:49], v11 offset:1544\n"
"ds_read_b64_tr_b16 v[50:51], v12 offset:1544\n"
"v_mfma_f32_16x16x32_bf16 v[128:131], v[20:23], a[88:91], v[128:131]\n"
"s_add_u32 s56, s58, 131072\n"
"s_add_u32 m0, s56, 0\n"
"v_mfma_f32_16x16x32_bf16 v[68:71], v[24:27], a[32:35], v[68:71]\n"
"ds_read_b64_tr_b16 v[20:21], v11 offset:2048\n"
"ds_read_b64_tr_b16 v[22:23], v12 offset:2048\n"
"v_mfma_f32_16x16x32_bf16 v[132:135], v[24:27], a[88:91], v[132:135]\n"
"buffer_load_dwordx4 %[dmaw], s[48:51], 0 offen offset:0 lds\n"
"s_waitcnt lgkmcnt(10)\n"
"v_mfma_f32_16x16x32_bf16 v[72:75], v[28:31], a[32:35], v[72:75]\n"
"ds_read_b64_tr_b16 v[24:25], v11 offset:2056\n"
"ds_read_b64_tr_b16 v[26:27], v12 offset:2056\n"
"v_mfma_f32_16x16x32_bf16 v[136:139], v[28:31], a[88:91], v[136:139]\n"
"s_add_u32 m0, s56, 896\n"
"v_mfma_f32_16x16x32_bf16 v[76:79], v[32:35], a[32:35], v[76:79]\n"
"ds_read_b64_tr_b16 v[28:29], v11 offset:2560\n"
"ds_read_b64_tr_b16 v[30:31], v12 offset:2560\n"
"v_mfma_f32_16x16x32_bf16 v[140:143], v[32:35], a[88:91], v[140:143]\n"
"buffer_load_dwordx4 %[dmaw], s[48:51], 0 offen offset:128 lds\n"
"s_waitcnt lgkmcnt(10)\n"
"v_mfma_f32_16x16x32_bf16 v[80:83], v[36:39], a[32:35], v[80:83]\n"
"ds_read_b64_tr_b16 v[32:33], v11 offset:2568\n"
"ds_read_b64_tr_b16 v[34:35], v12 offset:2568\n"
"v_mfma_f32_16x16x32_bf16 v[144:147], v[36:39], a[88:91], v[144:147]\n"
"s_add_u32 m0, s56, 1792\n"
"v_mfma_f32_16x16x32_bf16 v[84:87], v[40:43], a[32:35], v[84:87]\n"
"ds_read_b64_tr_b16 v[36:37], v11 offset:3072\n"
"ds_read_b64_tr_b16 v[38:39], v12 offset:3072\n"
"v_mfma_f32_16x16x32_bf16 v[148:151], v[40:43], a[88:91], v[148:151]\n"
"buffer_load_dwordx4 %[dmaw], s[48:51], 0 offen offset:256 lds\n"
"s_waitcnt lgkmcnt(10)\n"
"v_mfma_f32_16x16x32_bf16 v[88:91], v[44:47], a[32:35], v[88:91]\n"
"ds_read_b64_tr_b16 v[40:41], v11 offset:3080\n"
"ds_read_b64_tr_b16 v[42:43], v12 offset:3080\n"
"v_mfma_f32_16x16x32_bf16 v[152:155], v[44:47], a[88:91], v[152:155]\n"
"s_add_u32 m0, s56, 2688\n"
"v_mfma_f32_16x16x32_bf16 v[92:95], v[48:51], a[32:35], v[92:95]\n"
"ds_read_b64_tr_b16 v[44:45], v11 offset:3584\n"
"ds_read_b64_tr_b16 v[46:47], v12 offset:3584\n"
"v_mfma_f32_16x16x32_bf16 v[156:159], v[48:51], a[88:91], v[156:159]\n"
"buffer_load_dwordx4 %[dmaw], s[48:51], 0 offen offset:384 lds\n"
"s_waitcnt lgkmcnt(10)\n"
"v_mfma_f32_16x16x32_bf16 v[96:99], v[20:23], a[32:35], v[96:99]\n"
"ds_read_b64_tr_b16 v[48:49], v11 offset:3592\n"
"ds_read_b64_tr_b16 v[50:51], v12 offset:3592\n"
"v_mfma_f32_16x16x32_bf16 v[160:163], v[20:23], a[88:91], v[160:163]\n"
"s_add_u32 m0, s56, 4096\n"
"v_mfma_f32_16x16x32_bf16 v[100:103], v[24:27], a[32:35], v[100:103]\n"
"ds_read_b64_tr_b16 v[20:21], v11 offset:16384\n"
"ds_read_b64_tr_b16 v[22:23], v12 offset:16384\n"
"v_mfma_f32_16x16x32_bf16 v[164:167], v[24:27], a[88:91], v[164:167]\n"
"buffer_load_dwordx4 %[dmaw], s[48:51], s63 offen offset:0 lds\n"
"s_waitcnt lgkmcnt(10)\n"
"v_mfma_f32_16x16x32_bf16 v[104:107], v[28:31], a[32:35], v[104:107]\n"
"ds_read_b64_tr_b16 v[24:25], v11 offset:16392\n"
"ds_read_b64_tr_b16 v[26:27], v12 offset:16392\n"
"v_mfma_f32_16x16x32_bf16 v[168:171], v[28:31], a[88:91], v[168:171]\n"
"s_add_u32 m0, s56, 4992\n"
"v_mfma_f32_16x16x32_bf16 v[108:111], v[32:35], a[32:35], v[108:111]\n"
"ds_read_b64_tr_b16 v[28:29], v11 offset:16896\n"
"ds_read_b64_tr_b16 v[30:31], v12 offset:16896\n"
"v_mfma_f32_16x16x32_bf16 v[172:175], v[32:35], a[88:91], v[172:175]\n"
"buffer_load_dwordx4 %[dmaw], s[48:51], s63 offen offset:128 lds\n"
"s_waitcnt lgkmcnt(10)\n"
"v_mfma_f32_16x16x32_bf16 v[112:115], v[36:39], a[32:35], v[112:115]\n"
"ds_read_b64_tr_b16 v[32:33], v11 offset:16904\n"
"ds_read_b64_tr_b16 v[34:35], v12 offset:16904\n"
"v_mfma_f32_16x16x32_bf16 v[176:179], v[36:39], a[88:91], v[176:179]\n"
"s_add_u32 m0, s56, 5888\n"
"v_mfma_f32_16x16x32_bf16 v[116:119], v[40:43], a[32:35], v[116:119]\n"
"ds_read_b64_tr_b16 v[36:37], v11 offset:17408\n"
"ds_read_b64_tr_b16 v[38:39], v12 offset:17408\n"
"v_mfma_f32_16x16x32_bf16 v[180:183], v[40:43], a[88:91], v[180:183]\n"
"buffer_load_dwordx4 %[dmaw], s[48:51], s63 offen offset:256 lds\n"
"s_waitcnt lgkmcnt(10)\n"
"v_mfma_f32_16x16x32_bf16 v[120:123], v[44:47], a[32:35], v[120:123]\n"
"ds_read_b64_tr_b16 v[40:41], v11 offset:17416\n"
"ds_read_b64_tr_b16 v[42:43], v12 offset:17416\n"
"v_mfma_f32_16x16x32_bf16 v[184:187], v[44:47], a[88:91], v[184:187]\n"
"s_add_u32 m0, s56, 6784\n"
"v_mfma_f32_16x16x32_bf16 v[124:127], v[48:51], a[32:35], v[124:127]\n"
"ds_read_b64_tr_b16 v[44:45], v11 offset:17920\n"
"ds_read_b64_tr_b16 v[46:47], v12 offset:17920\n"
"v_mfma_f32_16x16x32_bf16 v[188:191], v[48:51], a[88:91], v[188:191]\n"
"buffer_load_dwordx4 %[dmaw], s[48:51], s63 offen offset:384 lds\n"
"s_waitcnt lgkmcnt(10)\n"
"v_mfma_f32_16x16x32_bf16 v[64:67], v[20:23], a[36:39], v[64:67]\n"
"ds_read_b64_tr_b16 v[48:49], v11 offset:17928\n"
"ds_read_b64_tr_b16 v[50:51], v12 offset:17928\n"
"v_mfma_f32_16x16x32_bf16 v[128:131], v[20:23], a[92:95], v[128:131]\n"
"s_add_u32 s48, s48, 0x80000\n"
"s_addc_u32 s49, s49, 0\n"
"v_mfma_f32_16x16x32_bf16 v[68:71], v[24:27], a[36:39], v[68:71]\n"
"ds_read_b64_tr_b16 v[20:21], v11 offset:18432\n"
"ds_read_b64_tr_b16 v[22:23], v12 offset:18432\n"
"v_mfma_f32_16x16x32_bf16 v[132:135], v[24:27], a[92:95], v[132:135]\n"
"s_waitcnt lgkmcnt(10)\n"
"v_mfma_f32_16x16x32_bf16 v[72:75], v[28:31], a[36:39], v[72:75]\n"
"ds_read_b64_tr_b16 v[24:25], v11 offset:18440\n"
"ds_read_b64_tr_b16 v[26:27], v12 offset:18440\n"
"v_mfma_f32_16x16x32_bf16 v[136:139], v[28:31], a[92:95], v[136:139]\n"
"s_sub_u32 s50, s50, 0x80000\n"
"s_cselect_b32 s50, 0, s50\n"
"v_mfma_f32_16x16x32_bf16 v[76:79], v[32:35], a[36:39], v[76:79]\n"
"ds_read_b64_tr_b16 v[28:29], v11 offset:18944\n"
"ds_read_b64_tr_b16 v[30:31], v12 offset:18944\n"
"v_mfma_f32_16x16x32_bf16 v[140:143], v[32:35], a[92:95], v[140:143]\n"
"s_add_u32 s56, s58, 32768\n"
"s_waitcnt lgkmcnt(10)\n"
"v_mfma_f32_16x16x32_bf16 v[80:83], v[36:39], a[36:39], v[80:83]\n"
"ds_read_b64_tr_b16 v[32:33], v11 offset:18952\n"
"ds_read_b64_tr_b16 v[34:35], v12 offset:18952\n"
"v_mfma_f32_16x16x32_bf16 v[144:147], v[36:39], a[92:95], v[144:147]\n"
"s_add_u32 m0, s56, 0\n"
"v_mfma_f32_16x16x32_bf16 v[84:87], v[40:43], a[36:39], v[84:87]\n"
"ds_read_b64_tr_b16 v[36:37], v11 offset:19456\n"
"ds_read_b64_tr_b16 v[38:39], v12 offset:19456\n"
"v_mfma_f32_16x16x32_bf16 v[148:151], v[40:43], a[92:95], v[148:151]\n"
"buffer_load_dwordx4 %[dmakv], s[40:43], 0 offen offset:0 nt lds\n"
"s_waitcnt lgkmcnt(10)\n"
"v_mfma_f32_16x16x32_bf16 v[88:91], v[44:47], a[36:39], v[88:91]\n"
"ds_read_b64_tr_b16 v[40:41], v11 offset:19464\n"
"ds_read_b64_tr_b16 v[42:43], v12 offset:19464\n"
"v_mfma_f32_16x16x32_bf16 v[152:155], v[44:47], a[92:95], v[152:155]\n"
"s_add_u32 m0, s56, 896\n"
"v_mfma_f32_16x16x32_bf16 v[92:95], v[48:51], a[36:39], v[92:95]\n"
"ds_read_b64_tr_b16 v[44:45], v11 offset:19968\n"
"ds_read_b64_tr_b16 v[46:47], v12 offset:19968\n"
"v_mfma_f32_16x16x32_bf16 v[156:159], v[48:51], a[92:95], v[156:159]\n"
"buffer_load_dwordx4 %[dmakv], s[40:43], 0 offen offset:128 nt lds\n"
"s_waitcnt lgkmcnt(10)\n"
"v_mfma_f32_16x16x32_bf16 v[96:99], v[20:23], a[36:39], v[96:99]\n"
"ds_read_b64_tr_b16 v[48:49], v11 offset:19976\n"
"ds_read_b64_tr_b16 v[50:51], v12 offset:19976\n"
"v_mfma_f32_16x16x32_bf16 v[160:163], v[20:23], a[92:95], v[160:163]\n"
"s_add_u32 m0, s56, 1792\n"
"v_mfma_f32_16x16x32_bf16 v[100:103], v[24:27], a[36:39], v[100:103]\n"
"s_add_u32 s57, s60, 98304\n"
"buffer_load_dwordx4 %[dmakv], s[40:43], 0 offen offset:256 nt lds\n"
"v_mfma_f32_16x16x32_bf16 v[164:167], v[24:27], a[92:95], v[164:167]\n"
"v_add_u32 v11, s57, %[wl0]\n"
"v_add_u32 v12, s57, %[wl1]\n"
"s_add_u32 m0, s56, 2688\n"
"s_nop 0\n"
"buffer_load_dwordx4 %[dmakv], s[40:43], 0 offen offset:384 nt lds\n"
"s_add_u32 m0, s56, 4096\n"
"s_nop 0\n"
"buffer_load_dwordx4 %[dmakv], s[40:43], s62 offen offset:0 nt lds\n"
"s_add_u32 m0, s56, 4992\n"
"s_nop 0\n"
"buffer_load_dwordx4 %[dmakv], s[40:43], s62 offen offset:128 nt lds\n"
"s_add_u32 m0, s56, 5888\n"
"s_nop 0\n"
"buffer_load_dwordx4 %[dmakv], s[40:43], s62 offen offset:256 nt lds\n"
"s_add_u32 m0, s56, 6784\n"
"s_nop 0\n"
"buffer_load_dwordx4 %[dmakv], s[40:43], s62 offen offset:384 nt lds\n"
"s_add_u32 s40, s40, 0x8000\n"
"s_addc_u32 s41, s41, 0\n"
"s_sub_u32 s42, s42, 0x8000\n"
"s_cselect_b32 s42, 0, s42\n"
"s_waitcnt lgkmcnt(8)\n"
"v_mfma_f32_16x16x32_bf16 v[104:107], v[28:31], a[36:39], v[104:107]\n"
"v_mfma_f32_16x16x32_bf16 v[168:171], v[28:31], a[92:95], v[168:171]\n"
"s_waitcnt vmcnt(24) lgkmcnt(0)\n"
"s_barrier\n"
"v_mfma_f32_16x16x32_bf16 v[108:111], v[32:35], a[36:39], v[108:111]\n"
"ds_read_b64_tr_b16 v[20:21], v11 offset:0\n"
"ds_read_b64_tr_b16 v[22:23], v12 offset:0\n"
"v_mfma_f32_16x16x32_bf16 v[172:175], v[32:35], a[92:95], v[172:175]\n"
"ds_read_b64_tr_b16 v[24:25], v11 offset:8\n"
"ds_read_b64_tr_b16 v[26:27], v12 offset:8\n"
"ds_read_b64_tr_b16 v[28:29], v11 offset:512\n"
"ds_read_b64_tr_b16 v[30:31], v12 offset:512\n"
"v_mfma_f32_16x16x32_bf16 v[112:115], v[36:39], a[36:39], v[112:115]\n"
"ds_read_b64_tr_b16 v[32:33], v11 offset:520\n"
"ds_read_b64_tr_b16 v[34:35], v12 offset:520\n"
"v_mfma_f32_16x16x32_bf16 v[176:179], v[36:39], a[92:95], v[176:179]\n"
"v_mfma_f32_16x16x32_bf16 v[116:119], v[40:43], a[36:39], v[116:119]\n"
"ds_read_b64_tr_b16 v[36:37], v11 offset:1024\n"
"ds_read_b64_tr_b16 v[38:39], v12 offset:1024\n"
"v_mfma_f32_16x16x32_bf16 v[180:183], v[40:43], a[92:95], v[180:183]\n"
"v_mfma_f32_16x16x32_bf16 v[120:123], v[44:47], a[36:39], v[120:123]\n"
"ds_read_b64_tr_b16 v[40:41], v11 offset:1032\n"
"ds_read_b64_tr_b16 v[42:43], v12 offset:1032\n"
"v_mfma_f32_16x16x32_bf16 v[184:187], v[44:47], a[92:95], v[184:187]\n"
"v_mfma_f32_16x16x32_bf16 v[124:127], v[48:51], a[36:39], v[124:127]\n"
"ds_read_b64_tr_b16 v[44:45], v11 offset:1536\n"
"ds_read_b64_tr_b16 v[46:47], v12 offset:1536\n"
"v_mfma_f32_16x16x32_bf16 v[188:191], v[48:51], a[92:95], v[188:191]\n"
"s_waitcnt lgkmcnt(10)\n"
"v_mfma_f32_16x16x32_bf16 v[64:67], v[20:23], a[40:43], v[64:67]\n"
"ds_read_b64_tr_b16 v[48:49], v11 offset:1544\n"
"ds_read_b64_tr_b16 v[50:51], v12 offset:1544\n"
"v_mfma_f32_16x16x32_bf16 v[128:131], v[20:23], a[96:99], v[128:131]\n"
"s_add_u32 s56, s58, 65536\n"
"v_mfma_f32_16x16x32_bf16 v[68:71], v[24:27], a[40:43], v[68:71]\n"
"ds_read_b64_tr_b16 v[20:21], v11 offset:2048\n"
"ds_read_b64_tr_b16 v[22:23], v12 offset:2048\n"
"v_mfma_f32_16x16x32_bf16 v[132:135], v[24:27], a[96:99], v[132:135]\n"
"s_add_u32 m0, s56, 0\n"
"s_waitcnt lgkmcnt(10)\n"
"v_mfma_f32_16x16x32_bf16 v[72:75], v[28:31], a[40:43], v[72:75]\n"
"ds_read_b64_tr_b16 v[24:25], v11 offset:2056\n"
"ds_read_b64_tr_b16 v[26:27], v12 offset:2056\n"
"v_mfma_f32_16x16x32_bf16 v[136:139], v[28:31], a[96:99], v[136:139]\n"
"buffer_load_dwordx4 %[dmakv], s[44:47], 0 offen offset:0 nt lds\n"
"v_mfma_f32_16x16x32_bf16 v[76:79], v[32:35], a[40:43], v[76:79]\n"
"ds_read_b64_tr_b16 v[28:29], v11 offset:2560\n"
"ds_read_b64_tr_b16 v[30:31], v12 offset:2560\n"
"v_mfma_f32_16x16x32_bf16 v[140:143], v[32:35], a[96:99], v[140:143]\n"
"s_add_u32 m0, s56, 896\n"
"s_waitcnt lgkmcnt(10)\n"
"v_mfma_f32_16x16x32_bf16 v[80:83], v[36:39], a[40:43], v[80:83]\n"
"ds_read_b64_tr_b16 v[32:33], v11 offset:2568\n"
"ds_read_b64_tr_b16 v[34:35], v12 offset:2568\n"
"v_mfma_f32_16x16x32_bf16 v[144:147], v[36:39], a[96:99], v[144:147]\n"
"v_mfma_f32_16x16x32_bf16 v[84:87], v[40:43], a[40:43], v[84:87]\n"
"ds_read_b64_tr_b16 v[36:37], v11 offset:3072\n"
"ds_read_b64_tr_b16 v[38:39], v12 offset:3072\n"
"v_mfma_f32_16x16x32_bf16 v[148:151], v[40:43], a[96:99], v[148:151]\n"
"buffer_load_dwordx4 %[dmakv], s[44:47], 0 offen offset:128 nt lds\n"
"s_waitcnt lgkmcnt(10)\n"
"v_mfma_f32_16x16x32_bf16 v[88:91], v[44:47], a[40:43], v[88:91]\n"
"ds_read_b64_tr_b16 v[40:41], v11 offset:3080\n"
"ds_read_b64_tr_b16 v[42:43], v12 offset:3080\n"
"v_mfma_f32_16x16x32_bf16 v[152:155], v[44:47], a[96:99], v[152:155]\n"
"s_add_u32 m0, s56, 1792\n"
"v_mfma_f32_16x16x32_bf16 v[92:95], v[48:51], a[40:43], v[92:95]\n"
"ds_read_b64_tr_b16 v[44:45], v11 offset:3584\n"
"ds_read_b64_tr_b16 v[46:47], v12 offset:3584\n"
"v_mfma_f32_16x16x32_bf16 v[156:159], v[48:51], a[96:99], v[156:159]\n"
"buffer_load_dwordx4 %[dmakv], s[44:47], 0 offen offset:256 nt lds\n"
"s_waitcnt lgkmcnt(10)\n"
"v_mfma_f32_16x16x32_bf16 v[96:99], v[20:23], a[40:43], v[96:99]\n"
"ds_read_b64_tr_b16 v[48:49], v11 offset:3592\n"
"ds_read_b64_tr_b16 v[50:51], v12 offset:3592\n"
"v_mfma_f32_16x16x32_bf16 v[160:163], v[20:23], a[96:99], v[160:163]\n"
"v_mfma_f32_16x16x32_bf16 v[100:103], v[24:27], a[40:43], v[100:103]\n"
"ds_read_b64_tr_b16 v[20:21], v11 offset:16384\n"
"ds_read_b64_tr_b16 v[22:23], v12 offset:16384\n"
"v_mfma_f32_16x16x32_bf16 v[164:167], v[24:27], a[96:99], v[164:167]\n"
"s_add_u32 m0, s56, 2688\n"
"s_waitcnt lgkmcnt(10)\n"
"v_mfma_f32_16x16x32_bf16 v[104:107], v[28:31], a[40:43], v[104:107]\n"
"ds_read_b64_tr_b16 v[24:25], v11 offset:16392\n"
"ds_read_b64_tr_b16 v[26:27], v12 offset:16392\n"
"v_mfma_f32_16x16x32_bf16 v[168:171], v[28:31], a[96:99], v[168:171]\n"
"buffer_load_dwordx4 %[dmakv], s[44:47], 0 offen offset:384 nt lds\n"
"v_mfma_f32_16x16x32_bf16 v[108:111], v[32:35], a[40:43], v[108:111]\n"
"ds_read_b64_tr_b16 v[28:29], v11 offset:16896\n"
"ds_read_b64_tr_b16 v[30:31], v12 offset:16896\n"
"v_mfma_f32_16x16x32_bf16 v[172:175], v[32:35], a[96:99], v[172:175]\n"
"s_add_u32 m0, s56, 4096\n"
"s_waitcnt lgkmcnt(10)\n"
"v_mfma_f32_16x16x32_bf16 v[112:115], v[36:39], a[40:43], v[112:115]\n"
"ds_read_b64_tr_b16 v[32:33], v11 offset:16904\n"
"ds_read_b64_tr_b16 v[34:35], v12 offset:16904\n"
"v_mfma_f32_16x16x32_bf16 v[176:179], v[36:39], a[96:99], v[176:179]\n"
"v_mfma_f32_16x16x32_bf16 v[116:119], v[40:43], a[40:43], v[116:119]\n"
"ds_read_b64_tr_b16 v[36:37], v11 offset:17408\n"
"ds_read_b64_tr_b16 v[38:39], v12 offset:17408\n"
"v_mfma_f32_16x16x32_bf16 v[180:183], v[40:43], a[96:99], v[180:183]\n"
"buffer_load_dwordx4 %[dmakv], s[44:47], s62 offen offset:0 nt lds\n"
"s_waitcnt lgkmcnt(10)\n"
"v_mfma_f32_16x16x32_bf16 v[120:123], v[44:47], a[40:43], v[120:123]\n"
"ds_read_b64_tr_b16 v[40:41], v11 offset:17416\n"
"ds_read_b64_tr_b16 v[42:43], v12 offset:17416\n"
"v_mfma_f32_16x16x32_bf16 v[184:187], v[44:47], a[96:99], v[184:187]\n"
"s_add_u32 m0, s56, 4992\n"
"v_mfma_f32_16x16x32_bf16 v[124:127], v[48:51], a[40:43], v[124:127]\n"
"ds_read_b64_tr_b16 v[44:45], v11 offset:17920\n"
"ds_read_b64_tr_b16 v[46:47], v12 offset:17920\n"
"v_mfma_f32_16x16x32_bf16 v[188:191], v[48:51], a[96:99], v[188:191]\n"
"buffer_load_dwordx4 %[dmakv], s[44:47], s62 offen offset:128 nt lds\n"
"s_waitcnt lgkmcnt(10)\n"
"v_mfma_f32_16x16x32_bf16 v[64:67], v[20:23], a[44:47], v[64:67]\n"
"ds_read_b64_tr_b16 v[48:49], v11 offset:17928\n"
"ds_read_b64_tr_b16 v[50:51], v12 offset:17928\n"
"v_mfma_f32_16x16x32_bf16 v[128:131], v[20:23], a[100:103], v[128:131]\n"
"v_mfma_f32_16x16x32_bf16 v[68:71], v[24:27], a[44:47], v[68:71]\n"
"ds_read_b64_tr_b16 v[20:21], v11 offset:18432\n"
"ds_read_b64_tr_b16 v[22:23], v12 offset:18432\n"
"v_mfma_f32_16x16x32_bf16 v[132:135], v[24:27], a[100:103], v[132:135]\n"
"s_add_u32 m0, s56, 5888\n"
"s_waitcnt lgkmcnt(10)\n"
"v_mfma_f32_16x16x32_bf16 v[72:75], v[28:31], a[44:47], v[72:75]\n"
"ds_read_b64_tr_b16 v[24:25], v11 offset:18440\n"
"ds_read_b64_tr_b16 v[26:27], v12 offset:18440\n"
"v_mfma_f32_16x16x32_bf16 v[136:139], v[28:31], a[100:103], v[136:139]\n"
"buffer_load_dwordx4 %[dmakv], s[44:47], s62 offen offset:256 nt lds\n"
"v_mfma_f32_16x16x32_bf16 v[76:79], v[32:35], a[44:47], v[76:79]\n"
"ds_read_b64_tr_b16 v[28:29], v11 offset:18944\n"
"ds_read_b64_tr_b16 v[30:31], v12 offset:18944\n"
"v_mfma_f32_16x16x32_bf16 v[140:143], v[32:35], a[100:103], v[140:143]\n"
"s_add_u32 m0, s56, 6784\n"
"s_waitcnt lgkmcnt(10)\n"
"v_mfma_f32_16x16x32_bf16 v[80:83], v[36:39], a[44:47], v[80:83]\n"
"ds_read_b64_tr_b16 v[32:33], v11 offset:18952\n"
"ds_read_b64_tr_b16 v[34:35], v12 offset:18952\n"
"v_mfma_f32_16x16x32_bf16 v[144:147], v[36:39], a[100:103], v[144:147]\n"
"v_mfma_f32_16x16x32_bf16 v[84:87], v[40:43], a[44:47], v[84:87]\n"
"ds_read_b64_tr_b16 v[36:37], v11 offset:19456\n"
"ds_read_b64_tr_b16 v[38:39], v12 offset:19456\n"
"v_mfma_f32_16x16x32_bf16 v[148:151], v[40:43], a[100:103], v[148:151]\n"
"buffer_load_dwordx4 %[dmakv], s[44:47], s62 offen offset:384 nt lds\n"
"s_waitcnt lgkmcnt(10)\n"
"v_mfma_f32_16x16x32_bf16 v[88:91], v[44:47], a[44:47], v[88:91]\n"
"ds_read_b64_tr_b16 v[40:41], v11 offset:19464\n"
"ds_read_b64_tr_b16 v[42:43], v12 offset:19464\n"
"v_mfma_f32_16x16x32_bf16 v[152:155], v[44:47], a[100:103], v[152:155]\n"
"s_add_u32 s44, s44, 0x8000\n"
"s_addc_u32 s45, s45, 0\n"
"v_mfma_f32_16x16x32_bf16 v[92:95], v[48:51], a[44:47], v[92:95]\n"
"ds_read_b64_tr_b16 v[44:45], v11 offset:19968\n"
"ds_read_b64_tr_b16 v[46:47], v12 offset:19968\n"
"v_mfma_f32_16x16x32_bf16 v[156:159], v[48:51], a[100:103], v[156:159]\n"
"s_waitcnt lgkmcnt(10)\n"
"v_mfma_f32_16x16x32_bf16 v[96:99], v[20:23], a[44:47], v[96:99]\n"
"ds_read_b64_tr_b16 v[48:49], v11 offset:19976\n"
"ds_read_b64_tr_b16 v[50:51], v12 offset:19976\n"
"v_mfma_f32_16x16x32_bf16 v[160:163], v[20:23], a[100:103], v[160:163]\n"
"s_sub_u32 s46, s46, 0x8000\n"
"s_cselect_b32 s46, 0, s46\n"
"v_mfma_f32_16x16x32_bf16 v[100:103], v[24:27], a[44:47], v[100:103]\n"
"s_add_u32 s57, s60, 131072\n"
"v_mfma_f32_16x16x32_bf16 v[164:167], v[24:27], a[100:103], v[164:167]\n"
"v_add_u32 v11, s57, %[wl0]\n"
"v_add_u32 v12, s57, %[wl1]\n"
"s_waitcnt lgkmcnt(8)\n"
"v_mfma_f32_16x16x32_bf16 v[104:107], v[28:31], a[44:47], v[104:107]\n"
"v_mfma_f32_16x16x32_bf16 v[168:171], v[28:31], a[100:103], v[168:171]\n"
"s_waitcnt vmcnt(16) lgkmcnt(0)\n"
"s_barrier\n"
"v_mfma_f32_16x16x32_bf16 v[108:111], v[32:35], a[44:47], v[108:111]\n"
"ds_read_b64_tr_b16 v[20:21], v11 offset:0\n"
"ds_read_b64_tr_b16 v[22:23], v12 offset:0\n"
"v_mfma_f32_16x16x32_bf16 v[172:175], v[32:35], a[100:103], v[172:175]\n"
"ds_read_b64_tr_b16 v[24:25], v11 offset:8\n"
"ds_read_b64_tr_b16 v[26:27], v12 offset:8\n"
"ds_read_b64_tr_b16 v[28:29], v11 offset:512\n"
"ds_read_b64_tr_b16 v[30:31], v12 offset:512\n"
"v_mfma_f32_16x16x32_bf16 v[112:115], v[36:39], a[44:47], v[112:115]\n"
"ds_read_b64_tr_b16 v[32:33], v11 offset:520\n"
"ds_read_b64_tr_b16 v[34:35], v12 offset:520\n"
"v_mfma_f32_16x16x32_bf16 v[176:179], v[36:39], a[100:103], v[176:179]\n"
"v_mfma_f32_16x16x32_bf16 v[116:119], v[40:43], a[44:47], v[116:119]\n"
"ds_read_b64_tr_b16 v[36:37], v11 offset:1024\n"
"ds_read_b64_tr_b16 v[38:39], v12 offset:1024\n"
"v_mfma_f32_16x16x32_bf16 v[180:183], v[40:43], a[100:103], v[180:183]\n"
"v_mfma_f32_16x16x32_bf16 v[120:123], v[44:47], a[44:47], v[120:123]\n"
"ds_read_b64_tr_b16 v[40:41], v11 offset:1032\n"
"ds_read_b64_tr_b16 v[42:43], v12 offset:1032\n"
"v_mfma_f32_16x16x32_bf16 v[184:187], v[44:47], a[100:103], v[184:187]\n"
"v_mfma_f32_16x16x32_bf16 v[124:127], v[48:51], a[44:47], v[124:127]\n"
"ds_read_b64_tr_b16 v[44:45], v11 offset:1536\n"
"ds_read_b64_tr_b16 v[46:47], v12 offset:1536\n"
"v_mfma_f32_16x16x32_bf16 v[188:191], v[48:51], a[100:103], v[188:191]\n"
"s_waitcnt lgkmcnt(10)\n"
"v_mfma_f32_16x16x32_bf16 v[64:67], v[20:23], a[48:51], v[64:67]\n"
"ds_read_b64_tr_b16 v[48:49], v11 offset:1544\n"
"ds_read_b64_tr_b16 v[50:51], v12 offset:1544\n"
"v_mfma_f32_16x16x32_bf16 v[128:131], v[20:23], a[104:107], v[128:131]\n"
"s_add_u32 s56, s58, 98304\n"
"v_mfma_f32_16x16x32_bf16 v[68:71], v[24:27], a[48:51], v[68:71]\n"
"ds_read_b64_tr_b16 v[20:21], v11 offset:2048\n"
"ds_read_b64_tr_b16 v[22:23], v12 offset:2048\n"
"v_mfma_f32_16x16x32_bf16 v[132:135], v[24:27], a[104:107], v[132:135]\n"
"s_add_u32 m0, s56, 0\n"
"s_waitcnt lgkmcnt(10)\n"
"v_mfma_f32_16x16x32_bf16 v[72:75], v[28:31], a[48:51], v[72:75]\n"
"ds_read_b64_tr_b16 v[24:25], v11 offset:2056\n"
"ds_read_b64_tr_b16 v[26:27], v12 offset:2056\n"
"v_mfma_f32_16x16x32_bf16 v[136:139], v[28:31], a[104:107], v[136:139]\n"
"buffer_load_dwordx4 %[dmakv], s[40:43], 0 offen offset:0 nt lds\n"
"v_mfma_f32_16x16x32_bf16 v[76:79], v[32:35], a[48:51], v[76:79]\n"
"ds_read_b64_tr_b16 v[28:29], v11 offset:2560\n"
"ds_read_b64_tr_b16 v[30:31], v12 offset:2560\n"
"v_mfma_f32_16x16x32_bf16 v[140:143], v[32:35], a[104:107], v[140:143]\n"
"s_add_u32 m0, s56, 896\n"
"s_waitcnt lgkmcnt(10)\n"
"v_mfma_f32_16x16x32_bf16 v[80:83], v[36:39], a[48:51], v[80:83]\n"
"ds_read_b64_tr_b16 v[32:33], v11 offset:2568\n"
"ds_read_b64_tr_b16 v[34:35], v12 offset:2568\n"
"v_mfma_f32_16x16x32_bf16 v[144:147], v[36:39], a[104:107], v[144:147]\n"
"v_mfma_f32_16x16x32_bf16 v[84:87], v[40:43], a[48:51], v[84:87]\n"
"ds_read_b64_tr_b16 v[36:37], v11 offset:3072\n"
"ds_read_b64_tr_b16 v[38:39], v12 offset:3072\n"
"v_mfma_f32_16x16x32_bf16 v[148:151], v[40:43], a[104:107], v[148:151]\n"
"buffer_load_dwordx4 %[dmakv], s[40:43], 0 offen offset:128 nt lds\n"
"s_waitcnt lgkmcnt(10)\n"
"v_mfma_f32_16x16x32_bf16 v[88:91], v[44:47], a[48:51], v[88:91]\n"
"ds_read_b64_tr_b16 v[40:41], v11 offset:3080\n"
"ds_read_b64_tr_b16 v[42:43], v12 offset:3080\n"
"v_mfma_f32_16x16x32_bf16 v[152:155], v[44:47], a[104:107], v[152:155]\n"
"s_add_u32 m0, s56, 1792\n"
"v_mfma_f32_16x16x32_bf16 v[92:95], v[48:51], a[48:51], v[92:95]\n"
"ds_read_b64_tr_b16 v[44:45], v11 offset:3584\n"
"ds_read_b64_tr_b16 v[46:47], v12 offset:3584\n"
"v_mfma_f32_16x16x32_bf16 v[156:159], v[48:51], a[104:107], v[156:159]\n"
"buffer_load_dwordx4 %[dmakv], s[40:43], 0 offen offset:256 nt lds\n"
"s_waitcnt lgkmcnt(10)\n"
"v_mfma_f32_16x16x32_bf16 v[96:99], v[20:23], a[48:51], v[96:99]\n"
"ds_read_b64_tr_b16 v[48:49], v11 offset:3592\n"
"ds_read_b64_tr_b16 v[50:51], v12 offset:3592\n"
"v_mfma_f32_16x16x32_bf16 v[160:163], v[20:23], a[104:107], v[160:163]\n"
"s_add_u32 m0, s56, 2688\n"
"v_mfma_f32_16x16x32_bf16 v[100:103], v[24:27], a[48:51], v[100:103]\n"
"ds_read_b64_tr_b16 v[20:21], v11 offset:16384\n"
"ds_read_b64_tr_b16 v[22:23], v12 offset:16384\n"
"v_mfma_f32_16x16x32_bf16 v[164:167], v[24:27], a[104:107], v[164:167]\n"
"s_waitcnt lgkmcnt(10)\n"
"v_mfma_f32_16x16x32_bf16 v[104:107], v[28:31], a[48:51], v[104:107]\n"
"ds_read_b64_tr_b16 v[24:25], v11 offset:16392\n"
"ds_read_b64_tr_b16 v[26:27], v12 offset:16392\n"
"v_mfma_f32_16x16x32_bf16 v[168:171], v[28:31], a[104:107], v[168:171]\n"
"buffer_load_dwordx4 %[dmakv], s[40:43], 0 offen offset:384 nt lds\n"
"v_mfma_f32_16x16x32_bf16 v[108:111], v[32:35], a[48:51], v[108:111]\n"
"ds_read_b64_tr_b16 v[28:29], v11 offset:16896\n"
"ds_read_b64_tr_b16 v[30:31], v12 offset:16896\n"
"v_mfma_f32_16x16x32_bf16 v[172:175], v[32:35], a[104:107], v[172:175]\n"
"s_add_u32 m0, s56, 4096\n"
"s_waitcnt lgkmcnt(10)\n"
"v_mfma_f32_16x16x32_bf16 v[112:115], v[36:39], a[48:51], v[112:115]\n"
"ds_read_b64_tr_b16 v[32:33], v11 offset:16904\n"
"ds_read_b64_tr_b16 v[34:35], v12 offset:16904\n"
"v_mfma_f32_16x16x32_bf16 v[176:179], v[36:39], a[104:107], v[176:179]\n"
"buffer_load_dwordx4 %[dmakv], s[40:43], s62 offen offset:0 nt lds\n"
"v_mfma_f32_16x16x32_bf16 v[116:119], v[40:43], a[48:51], v[116:119]\n"
"ds_read_b64_tr_b16 v[36:37], v11 offset:17408\n"
"ds_read_b64_tr_b16 v[38:39], v12 offset:17408\n"
"v_mfma_f32_16x16x32_bf16 v[180:183], v[40:43], a[104:107], v[180:183]\n"
"s_add_u32 m0, s56, 4992\n"
"s_waitcnt lgkmcnt(10)\n"
"v_mfma_f32_16x16x32_bf16 v[120:123], v[44:47], a[48:51], v[120:123]\n"
"ds_read_b64_tr_b16 v[40:41], v11 offset:17416\n"
"ds_read_b64_tr_b16 v[42:43], v12 offset:17416\n"
"v_mfma_f32_16x16x32_bf16 v[184:187], v[44:47], a[104:107], v[184:187]\n"
"v_mfma_f32_16x16x32_bf16 v[124:127], v[48:51], a[48:51], v[124:127]\n"
"ds_read_b64_tr_b16 v[44:45], v11 offset:17920\n"
"ds_read_b64_tr_b16 v[46:47], v12 offset:17920\n"
"v_mfma_f32_16x16x32_bf16 v[188:191], v[48:51], a[104:107], v[188:191]\n"
"buffer_load_dwordx4 %[dmakv], s[40:43], s62 offen offset:128 nt lds\n"
"s_waitcnt lgkmcnt(10)\n"
"v_mfma_f32_16x16x32_bf16 v[64:67], v[20:23], a[52:55], v[64:67]\n"
"ds_read_b64_tr_b16 v[48:49], v11 offset:17928\n"
"ds_read_b64_tr_b16 v[50:51], v12 offset:17928\n"
"v_mfma_f32_16x16x32_bf16 v[128:131], v[20:23], a[108:111], v[128:131]\n"
"s_add_u32 m0, s56, 5888\n"
"v_mfma_f32_16x16x32_bf16 v[68:71], v[24:27], a[52:55], v[68:71]\n"
"ds_read_b64_tr_b16 v[20:21], v11 offset:18432\n"
"ds_read_b64_tr_b16 v[22:23], v12 offset:18432\n"
"v_mfma_f32_16x16x32_bf16 v[132:135], v[24:27], a[108:111], v[132:135]\n"
"buffer_load_dwordx4 %[dmakv], s[40:43], s62 offen offset:256 nt lds\n"
"s_waitcnt lgkmcnt(10)\n"
"v_mfma_f32_16x16x32_bf16 v[72:75], v[28:31], a[52:55], v[72:75]\n"
"ds_read_b64_tr_b16 v[24:25], v11 offset:18440\n"
"ds_read_b64_tr_b16 v[26:27], v12 offset:18440\n"
"v_mfma_f32_16x16x32_bf16 v[136:139], v[28:31], a[108:111], v[136:139]\n"
"s_add_u32 m0, s56, 6784\n"
"v_mfma_f32_16x16x32_bf16 v[76:79], v[32:35], a[52:55], v[76:79]\n"
"ds_read_b64_tr_b16 v[28:29], v11 offset:18944\n"
"ds_read_b64_tr_b16 v[30:31], v12 offset:18944\n"
"v_mfma_f32_16x16x32_bf16 v[140:143], v[32:35], a[108:111], v[140:143]\n"
"s_waitcnt lgkmcnt(10)\n"
"v_mfma_f32_16x16x32_bf16 v[80:83], v[36:39], a[52:55], v[80:83]\n"
"ds_read_b64_tr_b16 v[32:33], v11 offset:18952\n"
"ds_read_b64_tr_b16 v[34:35], v12 offset:18952\n"
"v_mfma_f32_16x16x32_bf16 v[144:147], v[36:39], a[108:111], v[144:147]\n"
"buffer_load_dwordx4 %[dmakv], s[40:43], s62 offen offset:384 nt lds\n"
"v_mfma_f32_16x16x32_bf16 v[84:87], v[40:43], a[52:55], v[84:87]\n"
"ds_read_b64_tr_b16 v[36:37], v11 offset:19456\n"
"ds_read_b64_tr_b16 v[38:39], v12 offset:19456\n"
"v_mfma_f32_16x16x32_bf16 v[148:151], v[40:43], a[108:111], v[148:151]\n"
"s_waitcnt lgkmcnt(10)\n"
"v_mfma_f32_16x16x32_bf16 v[88:91], v[44:47], a[52:55], v[88:91]\n"
"ds_read_b64_tr_b16 v[40:41], v11 offset:19464\n"
"ds_read_b64_tr_b16 v[42:43], v12 offset:19464\n"
"v_mfma_f32_16x16x32_bf16 v[152:155], v[44:47], a[108:111], v[152:155]\n"
"s_add_u32 s40, s40, 0x8000\n"
"s_addc_u32 s41, s41, 0\n"
"v_mfma_f32_16x16x32_bf16 v[92:95], v[48:51], a[52:55], v[92:95]\n"
"ds_read_b64_tr_b16 v[44:45], v11 offset:19968\n"
"ds_read_b64_tr_b16 v[46:47], v12 offset:19968\n"
"v_mfma_f32_16x16x32_bf16 v[156:159], v[48:51], a[108:111], v[156:159]\n"
"s_waitcnt lgkmcnt(10)\n"
"v_mfma_f32_16x16x32_bf16 v[96:99], v[20:23], a[52:55], v[96:99]\n"
"ds_read_b64_tr_b16 v[48:49], v11 offset:19976\n"
"ds_read_b64_tr_b16 v[50:51], v12 offset:19976\n"
"v_mfma_f32_16x16x32_bf16 v[160:163], v[20:23], a[108:111], v[160:163]\n"
"s_sub_u32 s42, s42, 0x8000\n"
"s_cselect_b32 s42, 0, s42\n"
"v_mfma_f32_16x16x32_bf16 v[100:103], v[24:27], a[52:55], v[100:103]\n"
"s_add_u32 s57, s60, 0\n"
"s_add_u32 s56, s58, 131072\n"
"v_mfma_f32_16x16x32_bf16 v[164:167], v[24:27], a[108:111], v[164:167]\n"
"v_add_u32 v10, s57, %[klane]\n"
"s_waitcnt lgkmcnt(8)\n"
"v_mfma_f32_16x16x32_bf16 v[104:107], v[28:31], a[52:55], v[104:107]\n"
"v_mfma_f32_16x16x32_bf16 v[168:171], v[28:31], a[108:111], v[168:171]\n"
"s_waitcnt vmcnt(32) lgkmcnt(0)\n"
"s_barrier\n"
"v_mfma_f32_16x16x32_bf16 v[108:111], v[32:35], a[52:55], v[108:111]\n"
"ds_read_b128 v[20:23], v10 offset:0\n"
"ds_read_b128 v[24:27], v10 offset:512\n"
"v_mfma_f32_16x16x32_bf16 v[172:175], v[32:35], a[108:111], v[172:175]\n"
"ds_read_b128 v[28:31], v10 offset:1024\n"
"v_mfma_f32_16x16x32_bf16 v[112:115], v[36:39], a[52:55], v[112:115]\n"
"ds_read_b128 v[32:35], v10 offset:1536\n"
"v_mfma_f32_16x16x32_bf16 v[176:179], v[36:39], a[108:111], v[176:179]\n"
"v_mfma_f32_16x16x32_bf16 v[116:119], v[40:43], a[52:55], v[116:119]\n"
"ds_read_b128 v[36:39], v10 offset:2048\n"
"v_mfma_f32_16x16x32_bf16 v[180:183], v[40:43], a[108:111], v[180:183]\n"
"v_mfma_f32_16x16x32_bf16 v[120:123], v[44:47], a[52:55], v[120:123]\n"
"ds_read_b128 v[40:43], v10 offset:2560\n"
"v_mfma_f32_16x16x32_bf16 v[184:187], v[44:47], a[108:111], v[184:187]\n"
"v_mfma_f32_16x16x32_bf16 v[124:127], v[48:51], a[52:55], v[124:127]\n"
"ds_read_b128 v[44:47], v10 offset:3072\n"
"v_mfma_f32_16x16x32_bf16 v[188:191], v[48:51], a[108:111], v[188:191]\n"
"s_nop 7\n"
"v_pk_mul_f32 v[64:65], v[64:65], s[86:87]\n"
"v_pk_mul_f32 v[66:67], v[66:67], s[86:87]\n"
"v_pk_mul_f32 v[68:69], v[68:69], s[86:87]\n"
"v_pk_mul_f32 v[70:71], v[70:71], s[86:87]\n"
"v_cvt_pk_bf16_f32 v52, v64, v65\n"
"v_cvt_pk_bf16_f32 v53, v66, v67\n"
"v_cvt_pk_bf16_f32 v54, v68, v69\n"
"v_cvt_pk_bf16_f32 v55, v70, v71\n"
"v_accvgpr_write_b32 a208, v52\n"
"v_accvgpr_write_b32 a209, v53\n"
"v_accvgpr_write_b32 a210, v54\n"
"v_accvgpr_write_b32 a211, v55\n"
"v_pk_mul_f32 v[72:73], v[72:73], s[86:87]\n"
"v_pk_mul_f32 v[74:75], v[74:75], s[86:87]\n"
"v_pk_mul_f32 v[76:77], v[76:77], s[86:87]\n"
"v_pk_mul_f32 v[78:79], v[78:79], s[86:87]\n"
"v_cvt_pk_bf16_f32 v52, v72, v73\n"
"v_cvt_pk_bf16_f32 v53, v74, v75\n"
"v_cvt_pk_bf16_f32 v54, v76, v77\n"
"v_cvt_pk_bf16_f32 v55, v78, v79\n"
"v_accvgpr_write_b32 a212, v52\n"
"v_accvgpr_write_b32 a213, v53\n"
"v_accvgpr_write_b32 a214, v54\n"
"v_accvgpr_write_b32 a215, v55\n"
"v_pk_mul_f32 v[80:81], v[80:81], s[86:87]\n"
"v_pk_mul_f32 v[82:83], v[82:83], s[86:87]\n"
"v_pk_mul_f32 v[84:85], v[84:85], s[86:87]\n"
"v_pk_mul_f32 v[86:87], v[86:87], s[86:87]\n"
"v_cvt_pk_bf16_f32 v52, v80, v81\n"
"v_cvt_pk_bf16_f32 v53, v82, v83\n"
"v_cvt_pk_bf16_f32 v54, v84, v85\n"
"v_cvt_pk_bf16_f32 v55, v86, v87\n"
"v_accvgpr_write_b32 a216, v52\n"
"v_accvgpr_write_b32 a217, v53\n"
"v_accvgpr_write_b32 a218, v54\n"
"v_accvgpr_write_b32 a219, v55\n"
"v_pk_mul_f32 v[88:89], v[88:89], s[86:87]\n"
"v_pk_mul_f32 v[90:91], v[90:91], s[86:87]\n"
"v_pk_mul_f32 v[92:93], v[92:93], s[86:87]\n"
"v_pk_mul_f32 v[94:95], v[94:95], s[86:87]\n"
"v_cvt_pk_bf16_f32 v52, v88, v89\n"
"v_cvt_pk_bf16_f32 v53, v90, v91\n"
"v_cvt_pk_bf16_f32 v54, v92, v93\n"
"v_cvt_pk_bf16_f32 v55, v94, v95\n"
"v_accvgpr_write_b32 a220, v52\n"
"v_accvgpr_write_b32 a221, v53\n"
"v_accvgpr_write_b32 a222, v54\n"
"v_accvgpr_write_b32 a223, v55\n"
"v_pk_mul_f32 v[96:97], v[96:97], s[86:87]\n"
"v_pk_mul_f32 v[98:99], v[98:99], s[86:87]\n"
"v_pk_mul_f32 v[100:101], v[100:101], s[86:87]\n"
"v_pk_mul_f32 v[102:103], v[102:103], s[86:87]\n"
"v_cvt_pk_bf16_f32 v52, v96, v97\n"
"v_cvt_pk_bf16_f32 v53, v98, v99\n"
"v_cvt_pk_bf16_f32 v54, v100, v101\n"
"v_cvt_pk_bf16_f32 v55, v102, v103\n"
"v_accvgpr_write_b32 a224, v52\n"
"v_accvgpr_write_b32 a225, v53\n"
"v_accvgpr_write_b32 a226, v54\n"
"v_accvgpr_write_b32 a227, v55\n"
"v_pk_mul_f32 v[104:105], v[104:105], s[86:87]\n"
"v_pk_mul_f32 v[106:107], v[106:107], s[86:87]\n"
"v_pk_mul_f32 v[108:109], v[108:109], s[86:87]\n"
"v_pk_mul_f32 v[110:111], v[110:111], s[86:87]\n"
"v_cvt_pk_bf16_f32 v52, v104, v105\n"
"v_cvt_pk_bf16_f32 v53, v106, v107\n"
"v_cvt_pk_bf16_f32 v54, v108, v109\n"
"v_cvt_pk_bf16_f32 v55, v110, v111\n"
"v_accvgpr_write_b32 a228, v52\n"
"v_accvgpr_write_b32 a229, v53\n"
"v_accvgpr_write_b32 a230, v54\n"
"v_accvgpr_write_b32 a231, v55\n"
"v_pk_mul_f32 v[112:113], v[112:113], s[86:87]\n"
"v_pk_mul_f32 v[114:115], v[114:115], s[86:87]\n"
"v_pk_mul_f32 v[116:117], v[116:117], s[86:87]\n"
"v_pk_mul_f32 v[118:119], v[118:119], s[86:87]\n"
"v_cvt_pk_bf16_f32 v52, v112, v113\n"
"v_cvt_pk_bf16_f32 v53, v114, v115\n"
"v_cvt_pk_bf16_f32 v54, v116, v117\n"
"v_cvt_pk_bf16_f32 v55, v118, v119\n"
"v_accvgpr_write_b32 a232, v52\n"
"v_accvgpr_write_b32 a233, v53\n"
"v_accvgpr_write_b32 a234, v54\n"
"v_accvgpr_write_b32 a235, v55\n"
"v_pk_mul_f32 v[120:121], v[120:121], s[86:87]\n"
"v_pk_mul_f32 v[122:123], v[122:123], s[86:87]\n"
"v_pk_mul_f32 v[124:125], v[124:125], s[86:87]\n"
"v_pk_mul_f32 v[126:127], v[126:127], s[86:87]\n"
"v_cvt_pk_bf16_f32 v52, v120, v121\n"
"v_cvt_pk_bf16_f32 v53, v122, v123\n"
"v_cvt_pk_bf16_f32 v54, v124, v125\n"
"v_cvt_pk_bf16_f32 v55, v126, v127\n"
"v_accvgpr_write_b32 a236, v52\n"
"v_accvgpr_write_b32 a237, v53\n"
"v_accvgpr_write_b32 a238, v54\n"
"v_accvgpr_write_b32 a239, v55\n"
"v_pk_mul_f32 v[128:129], v[128:129], s[86:87]\n"
"v_pk_mul_f32 v[130:131], v[130:131], s[86:87]\n"
"v_pk_mul_f32 v[132:133], v[132:133], s[86:87]\n"
"v_pk_mul_f32 v[134:135], v[134:135], s[86:87]\n"
"v_cvt_pk_bf16_f32 v52, v128, v129\n"
"v_cvt_pk_bf16_f32 v53, v130, v131\n"
"v_cvt_pk_bf16_f32 v54, v132, v133\n"
"v_cvt_pk_bf16_f32 v55, v134, v135\n"
"v_accvgpr_write_b32 a240, v52\n"
"v_accvgpr_write_b32 a241, v53\n"
"v_accvgpr_write_b32 a242, v54\n"
"v_accvgpr_write_b32 a243, v55\n"
"v_pk_mul_f32 v[136:137], v[136:137], s[86:87]\n"
"v_pk_mul_f32 v[138:139], v[138:139], s[86:87]\n"
"v_pk_mul_f32 v[140:141], v[140:141], s[86:87]\n"
"v_pk_mul_f32 v[142:143], v[142:143], s[86:87]\n"
"v_cvt_pk_bf16_f32 v52, v136, v137\n"
"v_cvt_pk_bf16_f32 v53, v138, v139\n"
"v_cvt_pk_bf16_f32 v54, v140, v141\n"
"v_cvt_pk_bf16_f32 v55, v142, v143\n"
"v_accvgpr_write_b32 a244, v52\n"
"v_accvgpr_write_b32 a245, v53\n"
"v_accvgpr_write_b32 a246, v54\n"
"v_accvgpr_write_b32 a247, v55\n"
"v_pk_mul_f32 v[144:145], v[144:145], s[86:87]\n"
"v_pk_mul_f32 v[146:147], v[146:147], s[86:87]\n"
"v_pk_mul_f32 v[148:149], v[148:149], s[86:87]\n"
"v_pk_mul_f32 v[150:151], v[150:151], s[86:87]\n"
"v_cvt_pk_bf16_f32 v52, v144, v145\n"
"v_cvt_pk_bf16_f32 v53, v146, v147\n"
"v_cvt_pk_bf16_f32 v54, v148, v149\n"
"v_cvt_pk_bf16_f32 v55, v150, v151\n"
"v_accvgpr_write_b32 a248, v52\n"
"v_accvgpr_write_b32 a249, v53\n"
"v_accvgpr_write_b32 a250, v54\n"
"v_accvgpr_write_b32 a251, v55\n"
"v_pk_mul_f32 v[152:153], v[152:153], s[86:87]\n"
"v_pk_mul_f32 v[154:155], v[154:155], s[86:87]\n"
"v_pk_mul_f32 v[156:157], v[156:157], s[86:87]\n"
"v_pk_mul_f32 v[158:159], v[158:159], s[86:87]\n"
"v_cvt_pk_bf16_f32 v52, v152, v153\n"
"v_cvt_pk_bf16_f32 v53, v154, v155\n"
"v_cvt_pk_bf16_f32 v54, v156, v157\n"
"v_cvt_pk_bf16_f32 v55, v158, v159\n"
"v_accvgpr_write_b32 a252, v52\n"
"v_accvgpr_write_b32 a253, v53\n"
"v_accvgpr_write_b32 a254, v54\n"
"v_accvgpr_write_b32 a255, v55\n"
"v_pk_mul_f32 v[160:161], v[160:161], s[86:87]\n"
"v_pk_mul_f32 v[162:163], v[162:163], s[86:87]\n"
"v_pk_mul_f32 v[164:165], v[164:165], s[86:87]\n"
"v_pk_mul_f32 v[166:167], v[166:167], s[86:87]\n"
"v_cvt_pk_bf16_f32 v64, v160, v161\n"
"v_cvt_pk_bf16_f32 v65, v162, v163\n"
"v_cvt_pk_bf16_f32 v66, v164, v165\n"
"v_cvt_pk_bf16_f32 v67, v166, v167\n"
"v_pk_mul_f32 v[168:169], v[168:169], s[86:87]\n"
"v_pk_mul_f32 v[170:171], v[170:171], s[86:87]\n"
"v_pk_mul_f32 v[172:173], v[172:173], s[86:87]\n"
"v_pk_mul_f32 v[174:175], v[174:175], s[86:87]\n"
"v_cvt_pk_bf16_f32 v68, v168, v169\n"
"v_cvt_pk_bf16_f32 v69, v170, v171\n"
"v_cvt_pk_bf16_f32 v70, v172, v173\n"
"v_cvt_pk_bf16_f32 v71, v174, v175\n"
"v_pk_mul_f32 v[176:177], v[176:177], s[86:87]\n"
"v_pk_mul_f32 v[178:179], v[178:179], s[86:87]\n"
"v_pk_mul_f32 v[180:181], v[180:181], s[86:87]\n"
"v_pk_mul_f32 v[182:183], v[182:183], s[86:87]\n"
"v_cvt_pk_bf16_f32 v72, v176, v177\n"
"v_cvt_pk_bf16_f32 v73, v178, v179\n"
"v_cvt_pk_bf16_f32 v74, v180, v181\n"
"v_cvt_pk_bf16_f32 v75, v182, v183\n"
"v_pk_mul_f32 v[184:185], v[184:185], s[86:87]\n"
"v_pk_mul_f32 v[186:187], v[186:187], s[86:87]\n"
"v_pk_mul_f32 v[188:189], v[188:189], s[86:87]\n"
"v_pk_mul_f32 v[190:191], v[190:191], s[86:87]\n"
"v_cvt_pk_bf16_f32 v76, v184, v185\n"
"v_cvt_pk_bf16_f32 v77, v186, v187\n"
"v_cvt_pk_bf16_f32 v78, v188, v189\n"
"v_cvt_pk_bf16_f32 v79, v190, v191\n"
"v_mov_b32 v187, 0x3f803f80\n"
"v_accvgpr_write_b32 a204, v187\n"
"v_accvgpr_write_b32 a205, v187\n"
"v_accvgpr_write_b32 a206, v187\n"
"v_accvgpr_write_b32 a207, v187\n"
"v_mbcnt_lo_u32_b32 v200, -1, 0\n"
"v_mbcnt_hi_u32_b32 v200, -1, v200\n"
"v_lshlrev_b32 v201, 4, v200\n"
"v_lshrrev_b32 v188, 5, v200\n"
"v_mul_u32_u24 v188, 3072, v188\n"
"v_sub_u32 v202, %[vl0], v188\n"
"v_sub_u32 v203, %[vl1], v188\n"
"v_bfe_u32 v188, v200, 2, 3\n"
"v_mul_u32_u24 v188, 384, v188\n"
"v_add_u32 v204, %[dmakv], v188\n"
"s_mov_b64 s[48:49], %[wo]\n"
"s_mul_i32 s72, s84, 7168\n"
"s_add_u32 s48, s48, s72\n"
"s_addc_u32 s49, s49, 0\n"
"s_sub_u32 s50, 229376, s72\n"
"s_mov_b32 s83, 0\n"
"s_mov_b64 s[52:53], %[part]\n"
"s_mov_b32 s54, 157696\n"
"s_mul_i32 s77, %[wave], 6144\n"
"s_add_u32 s78, s77, 2048\n"
"s_add_u32 s79, s77, 4096\n"
"s_add_u32 s88, s77, 22528\n"
"s_add_u32 s89, s78, 22528\n"
"s_add_u32 s69, s79, 22528\n"
"s_nop 1\n"
"s_waitcnt lgkmcnt(5)\n"
"v_mfma_f32_16x16x32_bf16 v[112:115], v[20:23], a[208:211], 0\n"
"ds_read_b128 v[48:51], v10 offset:3584\n"
"s_add_u32 m0, s56, 0\n"
"v_mfma_f32_16x16x32_bf16 v[116:119], v[20:23], a[240:243], 0\n"
"v_mfma_f32_16x16x32_bf16 v[112:115], v[24:27], a[212:215], v[112:115]\n"
"ds_read_b128 v[20:23], v10 offset:8192\n"
"buffer_load_dwordx4 %[dmakv], s[44:47], 0 offen offset:0 nt lds\n"
"v_mfma_f32_16x16x32_bf16 v[116:119], v[24:27], a[244:247], v[116:119]\n"
"s_waitcnt lgkmcnt(5)\n"
"v_mfma_f32_16x16x32_bf16 v[112:115], v[28:31], a[216:219], v[112:115]\n"
"ds_read_b128 v[24:27], v10 offset:8704\n"
"v_mfma_f32_16x16x32_bf16 v[116:119], v[28:31], a[248:251], v[116:119]\n"
"s_add_u32 m0, s56, 896\n"
"v_mfma_f32_16x16x32_bf16 v[112:115], v[32:35], a[220:223], v[112:115]\n"
"ds_read_b128 v[28:31], v10 offset:9216\n"
"v_mfma_f32_16x16x32_bf16 v[116:119], v[32:35], a[252:255], v[116:119]\n"
"buffer_load_dwordx4 %[dmakv], s[44:47], 0 offen offset:128 nt lds\n"
"s_waitcnt lgkmcnt(5)\n"
"v_mfma_f32_16x16x32_bf16 v[112:115], v[36:39], a[224:227], v[112:115]\n"
"ds_read_b128 v[32:35], v10 offset:9728\n"
"v_mfma_f32_16x16x32_bf16 v[116:119], v[36:39], v[64:67], v[116:119]\n"
"v_mfma_f32_16x16x32_bf16 v[112:115], v[40:43], a[228:231], v[112:115]\n"
"ds_read_b128 v[36:39], v10 offset:10240\n"
"s_add_u32 m0, s56, 1792\n"
"v_mfma_f32_16x16x32_bf16 v[116:119], v[40:43], v[68:71], v[116:119]\n"
"s_waitcnt lgkmcnt(5)\n"
"v_mfma_f32_16x16x32_bf16 v[112:115], v[44:47], a[232:235], v[112:115]\n"
"ds_read_b128 v[40:43], v10 offset:10752\n"
"v_mfma_f32_16x16x32_bf16 v[116:119], v[44:47], v[72:75], v[116:119]\n"
"buffer_load_dwordx4 %[dmakv], s[44:47], 0 offen offset:256 nt lds\n"
"v_mfma_f32_16x16x32_bf16 v[112:115], v[48:51], a[236:239], v[112:115]\n"
"ds_read_b128 v[44:47], v10 offset:11264\n"
"v_mfma_f32_16x16x32_bf16 v[116:119], v[48:51], v[76:79], v[116:119]\n"
"s_add_u32 m0, s56, 2688\n"
"s_waitcnt lgkmcnt(5)\n"
"v_mfma_f32_16x16x32_bf16 v[124:127], v[20:23], a[208:211], 0\n"
"ds_read_b128 v[48:51], v10 offset:11776\n"
"v_mfma_f32_16x16x32_bf16 v[128:131], v[20:23], a[240:243], 0\n"
"v_mfma_f32_16x16x32_bf16 v[124:127], v[24:27], a[212:215], v[124:127]\n"
"ds_read_b128 v[20:23], v10 offset:16384\n"
"buffer_load_dwordx4 %[dmakv], s[44:47], 0 offen offset:384 nt lds\n"
"v_mfma_f32_16x16x32_bf16 v[128:131], v[24:27], a[244:247], v[128:131]\n"
"v_max3_f32 v13, v112, v113, v114\n"
"s_waitcnt lgkmcnt(5)\n"
"v_mfma_f32_16x16x32_bf16 v[124:127], v[28:31], a[216:219], v[124:127]\n"
"ds_read_b128 v[24:27], v10 offset:16896\n"
"v_max3_f32 v14, v116, v117, v118\n"
"v_mfma_f32_16x16x32_bf16 v[128:131], v[28:31], a[248:251], v[128:131]\n"
"s_add_u32 m0, s56, 4096\n"
"v_max_f32 v13, v13, v115\n"
"v_mfma_f32_16x16x32_bf16 v[124:127], v[32:35], a[220:223], v[124:127]\n"
"ds_read_b128 v[28:31], v10 offset:17408\n"
"v_max_f32 v14, v14, v119\n"
"v_mfma_f32_16x16x32_bf16 v[128:131], v[32:35], a[252:255], v[128:131]\n"
"buffer_load_dwordx4 %[dmakv], s[44:47], s62 offen offset:0 nt lds\n"
"s_waitcnt lgkmcnt(5)\n"
"v_mfma_f32_16x16x32_bf16 v[124:127], v[36:39], a[224:227], v[124:127]\n"
"ds_read_b128 v[32:35], v10 offset:17920\n"
"v_mfma_f32_16x16x32_bf16 v[128:131], v[36:39], v[64:67], v[128:131]\n"
"v_mfma_f32_16x16x32_bf16 v[124:127], v[40:43], a[228:231], v[124:127]\n"
"ds_read_b128 v[36:39], v10 offset:18432\n"
"s_add_u32 m0, s56, 4992\n"
"v_mfma_f32_16x16x32_bf16 v[128:131], v[40:43], v[68:71], v[128:131]\n"
"s_waitcnt lgkmcnt(5)\n"
"v_mfma_f32_16x16x32_bf16 v[124:127], v[44:47], a[232:235], v[124:127]\n"
"ds_read_b128 v[40:43], v10 offset:18944\n"
"buffer_load_dwordx4 %[dmakv], s[44:47], s62 offen offset:128 nt lds\n"
"v_mfma_f32_16x16x32_bf16 v[128:131], v[44:47], v[72:75], v[128:131]\n"
"v_mfma_f32_16x16x32_bf16 v[124:127], v[48:51], a[236:239], v[124:127]\n"
"ds_read_b128 v[44:47], v10 offset:19456\n"
"v_mfma_f32_16x16x32_bf16 v[128:131], v[48:51], v[76:79], v[128:131]\n"
"s_add_u32 m0, s56, 5888\n"
"s_waitcnt lgkmcnt(5)\n"
"v_mfma_f32_16x16x32_bf16 v[136:139], v[20:23], a[208:211], 0\n"
"ds_read_b128 v[48:51], v10 offset:19968\n"
"v_mfma_f32_16x16x32_bf16 v[140:143], v[20:23], a[240:243], 0\n"
"v_mfma_f32_16x16x32_bf16 v[136:139], v[24:27], a[212:215], v[136:139]\n"
"ds_read_b128 v[20:23], v10 offset:24576\n"
"buffer_load_dwordx4 %[dmakv], s[44:47], s62 offen offset:256 nt lds\n"
"v_mfma_f32_16x16x32_bf16 v[140:143], v[24:27], a[244:247], v[140:143]\n"
"v_max3_f32 v13, v13, v124, v125\n"
"s_waitcnt lgkmcnt(5)\n"
"v_mfma_f32_16x16x32_bf16 v[136:139], v[28:31], a[216:219], v[136:139]\n"
"ds_read_b128 v[24:27], v10 offset:25088\n"
"s_add_u32 m0, s56, 6784\n"
"v_mfma_f32_16x16x32_bf16 v[140:143], v[28:31], a[248:251], v[140:143]\n"
"v_max3_f32 v14, v14, v128, v129\n"
"v_max3_f32 v13, v13, v126, v127\n"
"v_mfma_f32_16x16x32_bf16 v[136:139], v[32:35], a[220:223], v[136:139]\n"
"ds_read_b128 v[28:31], v10 offset:25600\n"
"v_max3_f32 v14, v14, v130, v131\n"
"v_mfma_f32_16x16x32_bf16 v[140:143], v[32:35], a[252:255], v[140:143]\n"
"buffer_load_dwordx4 %[dmakv], s[44:47], s62 offen offset:384 nt lds\n"
"s_waitcnt lgkmcnt(5)\n"
"v_mfma_f32_16x16x32_bf16 v[136:139], v[36:39], a[224:227], v[136:139]\n"
"ds_read_b128 v[32:35], v10 offset:26112\n"
"v_mfma_f32_16x16x32_bf16 v[140:143], v[36:39], v[64:67], v[140:143]\n"
"v_mfma_f32_16x16x32_bf16 v[136:139], v[40:43], a[228:231], v[136:139]\n"
"ds_read_b128 v[36:39], v10 offset:26624\n"
"v_mfma_f32_16x16x32_bf16 v[140:143], v[40:43], v[68:71], v[140:143]\n"
"s_waitcnt lgkmcnt(5)\n"
"v_mfma_f32_16x16x32_bf16 v[136:139], v[44:47], a[232:235], v[136:139]\n"
"ds_read_b128 v[40:43], v10 offset:27136\n"
"v_mfma_f32_16x16x32_bf16 v[140:143], v[44:47], v[72:75], v[140:143]\n"
"s_add_u32 s44, s44, 0x8000\n"
"s_addc_u32 s45, s45, 0\n"
"v_mfma_f32_16x16x32_bf16 v[136:139], v[48:51], a[236:239], v[136:139]\n"
"ds_read_b128 v[44:47], v10 offset:27648\n"
"v_mfma_f32_16x16x32_bf16 v[140:143], v[48:51], v[76:79], v[140:143]\n"
"s_waitcnt lgkmcnt(5)\n"
"v_mfma_f32_16x16x32_bf16 v[148:151], v[20:23], a[208:211], 0\n"
"ds_read_b128 v[48:51], v10 offset:28160\n"
"v_mfma_f32_16x16x32_bf16 v[152:155], v[20:23], a[240:243], 0\n"
"s_sub_u32 s46, s46, 0x8000\n"
"s_cselect_b32 s46, 0, s46\n"
"v_mfma_f32_16x16x32_bf16 v[148:151], v[24:27], a[212:215], v[148:151]\n"
"s_add_u32 s57, s57, 0x8000\n"
"s_cmp_eq_u32 s57, s61\n"
"s_cselect_b32 s57, s60, s57\n"
"v_mfma_f32_16x16x32_bf16 v[152:155], v[24:27], a[244:247], v[152:155]\n"
"v_add_u32 v10, s57, %[klane]\n"
"s_add_u32 s56, s56, 0x8000\n"
"s_cmp_eq_u32 s56, s59\n"
"s_cselect_b32 s56, s58, s56\n"
"s_waitcnt lgkmcnt(4)\n"
"v_mfma_f32_16x16x32_bf16 v[148:151], v[28:31], a[216:219], v[148:151]\n"
"v_max3_f32 v13, v13, v136, v137\n"
"v_max3_f32 v14, v14, v140, v141\n"
"v_mfma_f32_16x16x32_bf16 v[152:155], v[28:31], a[248:251], v[152:155]\n"
"s_waitcnt vmcnt(24) lgkmcnt(0)\n"
"s_barrier\n"
"v_mfma_f32_16x16x32_bf16 v[148:151], v[32:35], a[220:223], v[148:151]\n"
"ds_read_b128 v[20:23], v10 offset:0\n"
"ds_read_b128 v[24:27], v10 offset:512\n"
"v_mfma_f32_16x16x32_bf16 v[152:155], v[32:35], a[252:255], v[152:155]\n"
"ds_read_b128 v[28:31], v10 offset:1024\n"
"v_max3_f32 v13, v13, v138, v139\n"
"v_mfma_f32_16x16x32_bf16 v[148:151], v[36:39], a[224:227], v[148:151]\n"
"ds_read_b128 v[32:35], v10 offset:1536\n"
"v_max3_f32 v14, v14, v142, v143\n"
"v_mfma_f32_16x16x32_bf16 v[152:155], v[36:39], v[64:67], v[152:155]\n"
"v_mfma_f32_16x16x32_bf16 v[148:151], v[40:43], a[228:231], v[148:151]\n"
"ds_read_b128 v[36:39], v10 offset:2048\n"
"v_mfma_f32_16x16x32_bf16 v[152:155], v[40:43], v[68:71], v[152:155]\n"
"v_mfma_f32_16x16x32_bf16 v[148:151], v[44:47], a[232:235], v[148:151]\n"
"ds_read_b128 v[40:43], v10 offset:2560\n"
"v_mfma_f32_16x16x32_bf16 v[152:155], v[44:47], v[72:75], v[152:155]\n"
"v_mfma_f32_16x16x32_bf16 v[148:151], v[48:51], a[236:239], v[148:151]\n"
"ds_read_b128 v[44:47], v10 offset:3072\n"
"v_mfma_f32_16x16x32_bf16 v[152:155], v[48:51], v[76:79], v[152:155]\n"
"s_nop 7\n"
"v_max3_f32 v13, v13, v148, v149\n"
"v_max3_f32 v14, v14, v152, v153\n"
"v_max3_f32 v13, v13, v150, v151\n"
"v_max3_f32 v14, v14, v154, v155\n"
"s_nop 7\n"
"v_mov_b32 v187, v13\n"
"s_nop 1\n"
"v_permlane32_swap_b32 v13, v187\n"
"s_nop 1\n"
"v_max_f32 v13, v13, v187\n"
"v_mov_b32 v187, v13\n"
"s_nop 1\n"
"v_permlane16_swap_b32 v13, v187\n"
"s_nop 1\n"
"v_max_f32 v13, v13, v187\n"
"v_mov_b32 v17, v13\n"
"v_sub_f32 v112, v112, v13\n"
"v_sub_f32 v113, v113, v13\n"
"v_sub_f32 v114, v114, v13\n"
"v_sub_f32 v115, v115, v13\n"
"v_sub_f32 v124, v124, v13\n"
"v_sub_f32 v125, v125, v13\n"
"v_sub_f32 v126, v126, v13\n"
"v_sub_f32 v127, v127, v13\n"
"v_sub_f32 v136, v136, v13\n"
"v_sub_f32 v137, v137, v13\n"
"v_sub_f32 v138, v138, v13\n"
"v_sub_f32 v139, v139, v13\n"
"v_sub_f32 v148, v148, v13\n"
"v_sub_f32 v149, v149, v13\n"
"v_sub_f32 v150, v150, v13\n"
"v_sub_f32 v151, v151, v13\n"
"v_sub_f32 v52, 0, v17\n"
"v_sub_f32 v53, 0, v17\n"
"v_sub_f32 v54, 0, v17\n"
"v_sub_f32 v55, 0, v17\n"
"v_mov_b32 v187, v14\n"
"s_nop 1\n"
"v_permlane32_swap_b32 v14, v187\n"
"s_nop 1\n"
"v_max_f32 v14, v14, v187\n"
"v_mov_b32 v187, v14\n"
"s_nop 1\n"
"v_permlane16_swap_b32 v14, v187\n"
"s_nop 1\n"
"v_max_f32 v14, v14, v187\n"
"v_mov_b32 v18, v14\n"
"v_sub_f32 v116, v116, v14\n"
"v_sub_f32 v117, v117, v14\n"
"v_sub_f32 v118, v118, v14\n"
"v_sub_f32 v119, v119, v14\n"
"v_sub_f32 v128, v128, v14\n"
"v_sub_f32 v129, v129, v14\n"
"v_sub_f32 v130, v130, v14\n"
"v_sub_f32 v131, v131, v14\n"
"v_sub_f32 v140, v140, v14\n"
"v_sub_f32 v141, v141, v14\n"
"v_sub_f32 v142, v142, v14\n"
"v_sub_f32 v143, v143, v14\n"
"v_sub_f32 v152, v152, v14\n"
"v_sub_f32 v153, v153, v14\n"
"v_sub_f32 v154, v154, v14\n"
"v_sub_f32 v155, v155, v14\n"
"v_sub_f32 v56, 0, v18\n"
"v_sub_f32 v57, 0, v18\n"
"v_sub_f32 v58, 0, v18\n"
"v_sub_f32 v59, 0, v18\n"
"s_nop 1\n"
"v_exp_f32 v112, v112\n"
"v_exp_f32 v113, v113\n"
"v_exp_f32 v114, v114\n"
"v_exp_f32 v115, v115\n"
"v_exp_f32 v116, v116\n"
"v_exp_f32 v117, v117\n"
"v_exp_f32 v118, v118\n"
"v_exp_f32 v119, v119\n"
"s_nop 0\n"
"v_cvt_pk_bf16_f32 v160, v112, v113\n"
"v_cvt_pk_bf16_f32 v161, v114, v115\n"
"v_cvt_pk_bf16_f32 v168, v116, v117\n"
"v_cvt_pk_bf16_f32 v169, v118, v119\n"
"s_waitcnt lgkmcnt(5)\n"
"v_mfma_f32_16x16x32_bf16 v[112:115], v[20:23], a[208:211], v[52:55]\n"
"ds_read_b128 v[48:51], v10 offset:3584\n"
"s_add_u32 m0, s56, 0\n"
"v_mfma_f32_16x16x32_bf16 v[116:119], v[20:23], a[240:243], v[56:59]\n"
"v_exp_f32 v124, v124\n"
"v_mfma_f32_16x16x32_bf16 v[112:115], v[24:27], a[212:215], v[112:115]\n"
"ds_read_b128 v[20:23], v10 offset:8192\n"
"buffer_load_dwordx4 %[dmakv], s[40:43], 0 offen offset:0 nt lds\n"
"v_mfma_f32_16x16x32_bf16 v[116:119], v[24:27], a[244:247], v[116:119]\n"
"s_waitcnt lgkmcnt(5)\n"
"v_mfma_f32_16x16x32_bf16 v[112:115], v[28:31], a[216:219], v[112:115]\n"
"ds_read_b128 v[24:27], v10 offset:8704\n"
"v_mfma_f32_16x16x32_bf16 v[116:119], v[28:31], a[248:251], v[116:119]\n"
"v_exp_f32 v125, v125\n"
"v_mfma_f32_16x16x32_bf16 v[112:115], v[32:35], a[220:223], v[112:115]\n"
"ds_read_b128 v[28:31], v10 offset:9216\n"
"v_cvt_pk_bf16_f32 v162, v124, v125\n"
"v_mfma_f32_16x16x32_bf16 v[116:119], v[32:35], a[252:255], v[116:119]\n"
"s_add_u32 m0, s56, 896\n"
"s_waitcnt lgkmcnt(5)\n"
"v_mfma_f32_16x16x32_bf16 v[112:115], v[36:39], a[224:227], v[112:115]\n"
"ds_read_b128 v[32:35], v10 offset:9728\n"
"buffer_load_dwordx4 %[dmakv], s[40:43], 0 offen offset:128 nt lds\n"
"v_mfma_f32_16x16x32_bf16 v[116:119], v[36:39], v[64:67], v[116:119]\n"
"v_exp_f32 v126, v126\n"
"v_mfma_f32_16x16x32_bf16 v[112:115], v[40:43], a[228:231], v[112:115]\n"
"ds_read_b128 v[36:39], v10 offset:10240\n"
"s_add_u32 m0, s56, 1792\n"
"v_mfma_f32_16x16x32_bf16 v[116:119], v[40:43], v[68:71], v[116:119]\n"
"s_waitcnt lgkmcnt(5)\n"
"v_mfma_f32_16x16x32_bf16 v[112:115], v[44:47], a[232:235], v[112:115]\n"
"ds_read_b128 v[40:43], v10 offset:10752\n"
"v_mfma_f32_16x16x32_bf16 v[116:119], v[44:47], v[72:75], v[116:119]\n"
"v_exp_f32 v127, v127\n"
"v_mfma_f32_16x16x32_bf16 v[112:115], v[48:51], a[236:239], v[112:115]\n"
"ds_read_b128 v[44:47], v10 offset:11264\n"
"v_exp_f32 v128, v128\n"
"v_exp_f32 v129, v129\n"
"v_exp_f32 v130, v130\n"
"v_exp_f32 v131, v131\n"
"v_mfma_f32_16x16x32_bf16 v[116:119], v[48:51], v[76:79], v[116:119]\n"
"v_cvt_pk_bf16_f32 v163, v126, v127\n"
"v_cvt_pk_bf16_f32 v170, v128, v129\n"
"v_cvt_pk_bf16_f32 v171, v130, v131\n"
"s_waitcnt lgkmcnt(5)\n"
"v_mfma_f32_16x16x32_bf16 v[124:127], v[20:23], a[208:211], v[52:55]\n"
"ds_read_b128 v[48:51], v10 offset:11776\n"
"buffer_load_dwordx4 %[dmakv], s[40:43], 0 offen offset:256 nt lds\n"
"v_mfma_f32_16x16x32_bf16 v[128:131], v[20:23], a[240:243], v[56:59]\n"
"v_exp_f32 v136, v136\n"
"v_mfma_f32_16x16x32_bf16 v[124:127], v[24:27], a[212:215], v[124:127]\n"
"ds_read_b128 v[20:23], v10 offset:16384\n"
"s_add_u32 m0, s56, 2688\n"
"v_mfma_f32_16x16x32_bf16 v[128:131], v[24:27], a[244:247], v[128:131]\n"
"buffer_load_dwordx4 %[dmakv], s[40:43], 0 offen offset:384 nt lds\n"
"s_waitcnt lgkmcnt(5)\n"
"v_mfma_f32_16x16x32_bf16 v[124:127], v[28:31], a[216:219], v[124:127]\n"
"ds_read_b128 v[24:27], v10 offset:16896\n"
"v_max3_f32 v13, v112, v113, v114\n"
"v_mfma_f32_16x16x32_bf16 v[128:131], v[28:31], a[248:251], v[128:131]\n"
"v_exp_f32 v137, v137\n"
"v_mfma_f32_16x16x32_bf16 v[124:127], v[32:35], a[220:223], v[124:127]\n"
"ds_read_b128 v[28:31], v10 offset:17408\n"
"v_cvt_pk_bf16_f32 v164, v136, v137\n"
"v_mfma_f32_16x16x32_bf16 v[128:131], v[32:35], a[252:255], v[128:131]\n"
"s_add_u32 m0, s56, 4096\n"
"s_waitcnt lgkmcnt(5)\n"
"v_mfma_f32_16x16x32_bf16 v[124:127], v[36:39], a[224:227], v[124:127]\n"
"ds_read_b128 v[32:35], v10 offset:17920\n"
"buffer_load_dwordx4 %[dmakv], s[40:43], s62 offen offset:0 nt lds\n"
"v_mfma_f32_16x16x32_bf16 v[128:131], v[36:39], v[64:67], v[128:131]\n"
"v_exp_f32 v138, v138\n"
"v_mfma_f32_16x16x32_bf16 v[124:127], v[40:43], a[228:231], v[124:127]\n"
"ds_read_b128 v[36:39], v10 offset:18432\n"
"s_add_u32 m0, s56, 4992\n"
"v_mfma_f32_16x16x32_bf16 v[128:131], v[40:43], v[68:71], v[128:131]\n"
"v_max3_f32 v14, v116, v117, v118\n"
"s_waitcnt lgkmcnt(5)\n"
"v_mfma_f32_16x16x32_bf16 v[124:127], v[44:47], a[232:235], v[124:127]\n"
"ds_read_b128 v[40:43], v10 offset:18944\n"
"buffer_load_dwordx4 %[dmakv], s[40:43], s62 offen offset:128 nt lds\n"
"v_mfma_f32_16x16x32_bf16 v[128:131], v[44:47], v[72:75], v[128:131]\n"
"v_exp_f32 v139, v139\n"
"v_mfma_f32_16x16x32_bf16 v[124:127], v[48:51], a[236:239], v[124:127]\n"
"ds_read_b128 v[44:47], v10 offset:19456\n"
"v_exp_f32 v140, v140\n"
"v_exp_f32 v141, v141\n"
"v_exp_f32 v142, v142\n"
"v_exp_f32 v143, v143\n"
"v_mfma_f32_16x16x32_bf16 v[128:131], v[48:51], v[76:79], v[128:131]\n"
"v_cvt_pk_bf16_f32 v165, v138, v139\n"
"v_cvt_pk_bf16_f32 v172, v140, v141\n"
"v_cvt_pk_bf16_f32 v173, v142, v143\n"
"s_waitcnt lgkmcnt(5)\n"
"v_mfma_f32_16x16x32_bf16 v[136:139], v[20:23], a[208:211], v[52:55]\n"
"ds_read_b128 v[48:51], v10 offset:19968\n"
"s_add_u32 m0, s56, 5888\n"
"v_mfma_f32_16x16x32_bf16 v[140:143], v[20:23], a[240:243], v[56:59]\n"
"v_exp_f32 v148, v148\n"
"v_mfma_f32_16x16x32_bf16 v[136:139], v[24:27], a[212:215], v[136:139]\n"
"ds_read_b128 v[20:23], v10 offset:24576\n"
"buffer_load_dwordx4 %[dmakv], s[40:43], s62 offen offset:256 nt lds\n"
"v_mfma_f32_16x16x32_bf16 v[140:143], v[24:27], a[244:247], v[140:143]\n"
"v_max_f32 v13, v13, v115\n"
"s_waitcnt lgkmcnt(5)\n"
"v_mfma_f32_16x16x32_bf16 v[136:139], v[28:31], a[216:219], v[136:139]\n"
"ds_read_b128 v[24:27], v10 offset:25088\n"
"s_add_u32 m0, s56, 6784\n"
"v_mfma_f32_16x16x32_bf16 v[140:143], v[28:31], a[248:251], v[140:143]\n"
"v_exp_f32 v149, v149\n"
"v_mfma_f32_16x16x32_bf16 v[136:139], v[32:35], a[220:223], v[136:139]\n"
"ds_read_b128 v[28:31], v10 offset:25600\n"
"v_cvt_pk_bf16_f32 v166, v148, v149\n"
"v_mfma_f32_16x16x32_bf16 v[140:143], v[32:35], a[252:255], v[140:143]\n"
"buffer_load_dwordx4 %[dmakv], s[40:43], s62 offen offset:384 nt lds\n"
"s_waitcnt lgkmcnt(5)\n"
"v_mfma_f32_16x16x32_bf16 v[136:139], v[36:39], a[224:227], v[136:139]\n"
"ds_read_b128 v[32:35], v10 offset:26112\n"
"v_max_f32 v14, v14, v119\n"
"v_mfma_f32_16x16x32_bf16 v[140:143], v[36:39], v[64:67], v[140:143]\n"
"v_exp_f32 v150, v150\n"
"v_mfma_f32_16x16x32_bf16 v[136:139], v[40:43], a[228:231], v[136:139]\n"
"ds_read_b128 v[36:39], v10 offset:26624\n"
"v_max3_f32 v13, v13, v124, v125\n"
"v_mfma_f32_16x16x32_bf16 v[140:143], v[40:43], v[68:71], v[140:143]\n"
"v_max3_f32 v14, v14, v128, v129\n"
"s_waitcnt lgkmcnt(5)\n"
"v_mfma_f32_16x16x32_bf16 v[136:139], v[44:47], a[232:235], v[136:139]\n"
"ds_read_b128 v[40:43], v10 offset:27136\n"
"v_max3_f32 v13, v13, v126, v127\n"
"v_mfma_f32_16x16x32_bf16 v[140:143], v[44:47], v[72:75], v[140:143]\n"
"v_exp_f32 v151, v151\n"
"v_mfma_f32_16x16x32_bf16 v[136:139], v[48:51], a[236:239], v[136:139]\n"
"ds_read_b128 v[44:47], v10 offset:27648\n"
"v_exp_f32 v152, v152\n"
"v_exp_f32 v153, v153\n"
"v_exp_f32 v154, v154\n"
"v_exp_f32 v155, v155\n"
"v_mfma_f32_16x16x32_bf16 v[140:143], v[48:51], v[76:79], v[140:143]\n"
"v_cvt_pk_bf16_f32 v167, v150, v151\n"
"v_cvt_pk_bf16_f32 v174, v152, v153\n"
"v_cvt_pk_bf16_f32 v175, v154, v155\n"
"s_waitcnt lgkmcnt(5)\n"
"v_mfma_f32_16x16x32_bf16 v[148:151], v[20:23], a[208:211], v[52:55]\n"
"ds_read_b128 v[48:51], v10 offset:28160\n"
"v_max3_f32 v14, v14, v130, v131\n"
"v_mfma_f32_16x16x32_bf16 v[152:155], v[20:23], a[240:243], v[56:59]\n"
"s_add_u32 s40, s40, 0x8000\n"
"s_addc_u32 s41, s41, 0\n"
"v_mfma_f32_16x16x32_bf16 v[148:151], v[24:27], a[212:215], v[148:151]\n"
"s_add_u32 s57, s57, 0x8000\n"
"s_cmp_eq_u32 s57, s61\n"
"s_cselect_b32 s57, s60, s57\n"
"v_mfma_f32_16x16x32_bf16 v[152:155], v[24:27], a[244:247], v[152:155]\n"
"v_add_u32 v11, s57, %[vl0]\n"
"v_add_u32 v12, s57, %[vl1]\n"
"s_sub_u32 s42, s42, 0x8000\n"
"s_cselect_b32 s42, 0, s42\n"
"s_add_u32 s56, s56, 0x8000\n"
"s_cmp_eq_u32 s56, s59\n"
"s_cselect_b32 s56, s58, s56\n"
"s_waitcnt lgkmcnt(4)\n"
"v_mfma_f32_16x16x32_bf16 v[148:151], v[28:31], a[216:219], v[148:151]\n"
"v_max3_f32 v13, v13, v136, v137\n"
"v_max3_f32 v14, v14, v140, v141\n"
"v_mfma_f32_16x16x32_bf16 v[152:155], v[28:31], a[248:251], v[152:155]\n"
"s_waitcnt vmcnt(24) lgkmcnt(0)\n"
"s_barrier\n"
"v_mfma_f32_16x16x32_bf16 v[148:151], v[32:35], a[220:223], v[148:151]\n"
"ds_read_b64_tr_b16 v[20:21], v11 offset:0\n"
"ds_read_b64_tr_b16 v[22:23], v11 offset:8192\n"
"v_mfma_f32_16x16x32_bf16 v[152:155], v[32:35], a[252:255], v[152:155]\n"
"ds_read_b64_tr_b16 v[24:25], v12 offset:0\n"
"ds_read_b64_tr_b16 v[26:27], v12 offset:8192\n"
"ds_read_b64_tr_b16 v[28:29], v11 offset:512\n"
"ds_read_b64_tr_b16 v[30:31], v11 offset:8704\n"
"v_mfma_f32_16x16x32_bf16 v[148:151], v[36:39], a[224:227], v[148:151]\n"
"ds_read_b64_tr_b16 v[32:33], v12 offset:512\n"
"ds_read_b64_tr_b16 v[34:35], v12 offset:8704\n"
"v_mfma_f32_16x16x32_bf16 v[152:155], v[36:39], v[64:67], v[152:155]\n"
"v_max3_f32 v13, v13, v138, v139\n"
"v_max3_f32 v14, v14, v142, v143\n"
"v_mfma_f32_16x16x32_bf16 v[148:151], v[40:43], a[228:231], v[148:151]\n"
"ds_read_b64_tr_b16 v[36:37], v11 offset:1024\n"
"ds_read_b64_tr_b16 v[38:39], v11 offset:9216\n"
"v_mfma_f32_16x16x32_bf16 v[152:155], v[40:43], v[68:71], v[152:155]\n"
"v_mfma_f32_16x16x32_bf16 v[148:151], v[44:47], a[232:235], v[148:151]\n"
"ds_read_b64_tr_b16 v[40:41], v12 offset:1024\n"
"ds_read_b64_tr_b16 v[42:43], v12 offset:9216\n"
"v_mfma_f32_16x16x32_bf16 v[152:155], v[44:47], v[72:75], v[152:155]\n"
"v_mfma_f32_16x16x32_bf16 v[148:151], v[48:51], a[236:239], v[148:151]\n"
"ds_read_b64_tr_b16 v[44:45], v11 offset:1536\n"
"ds_read_b64_tr_b16 v[46:47], v11 offset:9728\n"
"v_mfma_f32_16x16x32_bf16 v[152:155], v[48:51], v[76:79], v[152:155]\n"
"s_waitcnt lgkmcnt(10)\n"
"v_mfma_f32_16x16x32_bf16 a[0:3], v[20:23], v[160:163], 0\n"
"ds_read_b64_tr_b16 v[48:49], v12 offset:1536\n"
"ds_read_b64_tr_b16 v[50:51], v12 offset:9728\n"
"v_mfma_f32_16x16x32_bf16 a[64:67], v[20:23], v[168:171], 0\n"
"s_add_u32 m0, s56, 0\n"
"v_mfma_f32_16x16x32_bf16 a[4:7], v[24:27], v[160:163], 0\n"
"ds_read_b64_tr_b16 v[20:21], v11 offset:2048\n"
"ds_read_b64_tr_b16 v[22:23], v11 offset:10240\n"
"v_mfma_f32_16x16x32_bf16 a[68:71], v[24:27], v[168:171], 0\n"
"buffer_load_dwordx4 %[dmakv], s[44:47], 0 offen offset:0 nt lds\n"
"v_mfma_f32_16x16x32_bf16 a[192:195], a[204:207], v[160:163], 0\n"
"v_max3_f32 v13, v13, v148, v149\n"
"s_waitcnt lgkmcnt(10)\n"
"v_mfma_f32_16x16x32_bf16 a[8:11], v[28:31], v[160:163], 0\n"
"ds_read_b64_tr_b16 v[24:25], v12 offset:2048\n"
"ds_read_b64_tr_b16 v[26:27], v12 offset:10240\n"
"v_mfma_f32_16x16x32_bf16 a[72:75], v[28:31], v[168:171], 0\n"
"s_add_u32 m0, s56, 896\n"
"v_max3_f32 v14, v14, v152, v153\n"
"v_mfma_f32_16x16x32_bf16 a[196:199], a[204:207], v[168:171], 0\n"
"v_max3_f32 v13, v13, v150, v151\n"
"v_max3_f32 v14, v14, v154, v155\n"
"v_mfma_f32_16x16x32_bf16 a[12:15], v[32:35], v[160:163], 0\n"
"ds_read_b64_tr_b16 v[28:29], v11 offset:2560\n"
"ds_read_b64_tr_b16 v[30:31], v11 offset:10752\n"
"v_mfma_f32_16x16x32_bf16 a[76:79], v[32:35], v[168:171], 0\n"
"buffer_load_dwordx4 %[dmakv], s[44:47], 0 offen offset:128 nt lds\n"
"s_waitcnt lgkmcnt(10)\n"
"v_mfma_f32_16x16x32_bf16 a[16:19], v[36:39], v[160:163], 0\n"
"ds_read_b64_tr_b16 v[32:33], v12 offset:2560\n"
"ds_read_b64_tr_b16 v[34:35], v12 offset:10752\n"
"v_mfma_f32_16x16x32_bf16 a[80:83], v[36:39], v[168:171], 0\n"
"s_add_u32 m0, s56, 1792\n"
"v_max_f32 v16, v13, v14\n"
"v_mfma_f32_16x16x32_bf16 a[20:23], v[40:43], v[160:163], 0\n"
"ds_read_b64_tr_b16 v[36:37], v11 offset:3072\n"
"ds_read_b64_tr_b16 v[38:39], v11 offset:11264\n"
"v_mfma_f32_16x16x32_bf16 a[84:87], v[40:43], v[168:171], 0\n"
"s_waitcnt lgkmcnt(10)\n"
"v_mfma_f32_16x16x32_bf16 a[24:27], v[44:47], v[160:163], 0\n"
"ds_read_b64_tr_b16 v[40:41], v12 offset:3072\n"
"ds_read_b64_tr_b16 v[42:43], v12 offset:11264\n"
"v_mfma_f32_16x16x32_bf16 a[88:91], v[44:47], v[168:171], 0\n"
"v_cmp_lt_f32 vcc, s73, v16\n"
"s_cbranch_vccnz L_rare_w3f\n"
"L_back_w3f:\n"
"v_mfma_f32_16x16x32_bf16 a[28:31], v[48:51], v[160:163], 0\n"
"ds_read_b64_tr_b16 v[44:45], v11 offset:3584\n"
"ds_read_b64_tr_b16 v[46:47], v11 offset:11776\n"
"v_mfma_f32_16x16x32_bf16 a[92:95], v[48:51], v[168:171], 0\n"
"buffer_load_dwordx4 %[dmakv], s[44:47], 0 offen offset:256 nt lds\n"
"s_waitcnt lgkmcnt(10)\n"
"v_mfma_f32_16x16x32_bf16 a[32:35], v[20:23], v[160:163], 0\n"
"ds_read_b64_tr_b16 v[48:49], v12 offset:3584\n"
"ds_read_b64_tr_b16 v[50:51], v12 offset:11776\n"
"v_mfma_f32_16x16x32_bf16 a[96:99], v[20:23], v[168:171], 0\n"
"v_exp_f32 v112, v112\n"
"v_mfma_f32_16x16x32_bf16 a[36:39], v[24:27], v[160:163], 0\n"
"ds_read_b64_tr_b16 v[20:21], v11 offset:16384\n"
"ds_read_b64_tr_b16 v[22:23], v11 offset:24576\n"
"v_mfma_f32_16x16x32_bf16 a[100:103], v[24:27], v[168:171], 0\n"
"s_add_u32 m0, s56, 2688\n"
"s_waitcnt lgkmcnt(10)\n"
"v_mfma_f32_16x16x32_bf16 a[40:43], v[28:31], v[160:163], 0\n"
"ds_read_b64_tr_b16 v[24:25], v12 offset:16384\n"
"ds_read_b64_tr_b16 v[26:27], v12 offset:24576\n"
"v_mfma_f32_16x16x32_bf16 a[104:107], v[28:31], v[168:171], 0\n"
"v_exp_f32 v113, v113\n"
"v_mfma_f32_16x16x32_bf16 a[44:47], v[32:35], v[160:163], 0\n"
"ds_read_b64_tr_b16 v[28:29], v11 offset:16896\n"
"ds_read_b64_tr_b16 v[30:31], v11 offset:25088\n"
"v_mfma_f32_16x16x32_bf16 a[108:111], v[32:35], v[168:171], 0\n"
"buffer_load_dwordx4 %[dmakv], s[44:47], 0 offen offset:384 nt lds\n"
"s_waitcnt lgkmcnt(10)\n"
"v_mfma_f32_16x16x32_bf16 a[48:51], v[36:39], v[160:163], 0\n"
"ds_read_b64_tr_b16 v[32:33], v12 offset:16896\n"
"ds_read_b64_tr_b16 v[34:35], v12 offset:25088\n"
"v_mfma_f32_16x16x32_bf16 a[112:115], v[36:39], v[168:171], 0\n"
"v_exp_f32 v114, v114\n"
"v_mfma_f32_16x16x32_bf16 a[52:55], v[40:43], v[160:163], 0\n"
"ds_read_b64_tr_b16 v[36:37], v11 offset:17408\n"
"ds_read_b64_tr_b16 v[38:39], v11 offset:25600\n"
"v_mfma_f32_16x16x32_bf16 a[116:119], v[40:43], v[168:171], 0\n"
"s_add_u32 m0, s56, 4096\n"
"s_waitcnt lgkmcnt(10)\n"
"v_mfma_f32_16x16x32_bf16 a[56:59], v[44:47], v[160:163], 0\n"
"ds_read_b64_tr_b16 v[40:41], v12 offset:17408\n"
"ds_read_b64_tr_b16 v[42:43], v12 offset:25600\n"
"v_mfma_f32_16x16x32_bf16 a[120:123], v[44:47], v[168:171], 0\n"
"v_exp_f32 v115, v115\n"
"v_mfma_f32_16x16x32_bf16 a[60:63], v[48:51], v[160:163], 0\n"
"ds_read_b64_tr_b16 v[44:45], v11 offset:17920\n"
"ds_read_b64_tr_b16 v[46:47], v11 offset:26112\n"
"v_mfma_f32_16x16x32_bf16 a[124:127], v[48:51], v[168:171], 0\n"
"buffer_load_dwordx4 %[dmakv], s[44:47], s62 offen offset:0 nt lds\n"
"s_waitcnt lgkmcnt(10)\n"
"v_mfma_f32_16x16x32_bf16 a[0:3], v[20:23], v[164:167], a[0:3]\n"
"ds_read_b64_tr_b16 v[48:49], v12 offset:17920\n"
"ds_read_b64_tr_b16 v[50:51], v12 offset:26112\n"
"v_mfma_f32_16x16x32_bf16 a[64:67], v[20:23], v[172:175], a[64:67]\n"
"v_exp_f32 v116, v116\n"
"v_mfma_f32_16x16x32_bf16 a[4:7], v[24:27], v[164:167], a[4:7]\n"
"ds_read_b64_tr_b16 v[20:21], v11 offset:18432\n"
"ds_read_b64_tr_b16 v[22:23], v11 offset:26624\n"
"v_mfma_f32_16x16x32_bf16 a[68:71], v[24:27], v[172:175], a[68:71]\n"
"v_exp_f32 v117, v117\n"
"v_mfma_f32_16x16x32_bf16 a[192:195], a[204:207], v[164:167], a[192:195]\n"
"s_add_u32 m0, s56, 4992\n"
"s_waitcnt lgkmcnt(10)\n"
"v_mfma_f32_16x16x32_bf16 a[8:11], v[28:31], v[164:167], a[8:11]\n"
"ds_read_b64_tr_b16 v[24:25], v12 offset:18432\n"
"ds_read_b64_tr_b16 v[26:27], v12 offset:26624\n"
"v_mfma_f32_16x16x32_bf16 a[72:75], v[28:31], v[172:175], a[72:75]\n"
"v_exp_f32 v118, v118\n"
"v_mfma_f32_16x16x32_bf16 a[196:199], a[204:207], v[172:175], a[196:199]\n"
"v_exp_f32 v119, v119\n"
"v_mfma_f32_16x16x32_bf16 a[12:15], v[32:35], v[164:167], a[12:15]\n"
"ds_read_b64_tr_b16 v[28:29], v11 offset:18944\n"
"ds_read_b64_tr_b16 v[30:31], v11 offset:27136\n"
"v_mfma_f32_16x16x32_bf16 a[76:79], v[32:35], v[172:175], a[76:79]\n"
"buffer_load_dwordx4 %[dmakv], s[44:47], s62 offen offset:128 nt lds\n"
"s_waitcnt lgkmcnt(10)\n"
"v_mfma_f32_16x16x32_bf16 a[16:19], v[36:39], v[164:167], a[16:19]\n"
"ds_read_b64_tr_b16 v[32:33], v12 offset:18944\n"
"ds_read_b64_tr_b16 v[34:35], v12 offset:27136\n"
"v_mfma_f32_16x16x32_bf16 a[80:83], v[36:39], v[172:175], a[80:83]\n"
"s_add_u32 m0, s56, 5888\n"
"v_cvt_pk_bf16_f32 v160, v112, v113\n"
"v_mfma_f32_16x16x32_bf16 a[20:23], v[40:43], v[164:167], a[20:23]\n"
"ds_read_b64_tr_b16 v[36:37], v11 offset:19456\n"
"ds_read_b64_tr_b16 v[38:39], v11 offset:27648\n"
"v_mfma_f32_16x16x32_bf16 a[84:87], v[40:43], v[172:175], a[84:87]\n"
"buffer_load_dwordx4 %[dmakv], s[44:47], s62 offen offset:256 nt lds\n"
"s_waitcnt lgkmcnt(10)\n"
"v_mfma_f32_16x16x32_bf16 a[24:27], v[44:47], v[164:167], a[24:27]\n"
"ds_read_b64_tr_b16 v[40:41], v12 offset:19456\n"
"ds_read_b64_tr_b16 v[42:43], v12 offset:27648\n"
"v_mfma_f32_16x16x32_bf16 a[88:91], v[44:47], v[172:175], a[88:91]\n"
"s_add_u32 m0, s56, 6784\n"
"v_cvt_pk_bf16_f32 v161, v114, v115\n"
"v_mfma_f32_16x16x32_bf16 a[28:31], v[48:51], v[164:167], a[28:31]\n"
"ds_read_b64_tr_b16 v[44:45], v11 offset:19968\n"
"ds_read_b64_tr_b16 v[46:47], v11 offset:28160\n"
"v_mfma_f32_16x16x32_bf16 a[92:95], v[48:51], v[172:175], a[92:95]\n"
"buffer_load_dwordx4 %[dmakv], s[44:47], s62 offen offset:384 nt lds\n"
"s_waitcnt lgkmcnt(10)\n"
"v_mfma_f32_16x16x32_bf16 a[32:35], v[20:23], v[164:167], a[32:35]\n"
"ds_read_b64_tr_b16 v[48:49], v12 offset:19968\n"
"ds_read_b64_tr_b16 v[50:51], v12 offset:28160\n"
"v_mfma_f32_16x16x32_bf16 a[96:99], v[20:23], v[172:175], a[96:99]\n"
"s_add_u32 s44, s44, 0x8000\n"
"s_addc_u32 s45, s45, 0\n"
"v_mfma_f32_16x16x32_bf16 a[36:39], v[24:27], v[164:167], a[36:39]\n"
"s_add_u32 s57, s57, 0x8000\n"
"s_cmp_eq_u32 s57, s61\n"
"s_cselect_b32 s57, s60, s57\n"
"v_mfma_f32_16x16x32_bf16 a[100:103], v[24:27], v[172:175], a[100:103]\n"
"v_add_u32 v10, s57, %[klane]\n"
"s_sub_u32 s46, s46, 0x8000\n"
"s_cselect_b32 s46, 0, s46\n"
"s_add_u32 s56, s56, 0x8000\n"
"s_cmp_eq_u32 s56, s59\n"
"s_cselect_b32 s56, s58, s56\n"
"s_waitcnt lgkmcnt(8)\n"
"v_mfma_f32_16x16x32_bf16 a[40:43], v[28:31], v[164:167], a[40:43]\n"
"v_cvt_pk_bf16_f32 v168, v116, v117\n"
"v_cvt_pk_bf16_f32 v169, v118, v119\n"
"v_mfma_f32_16x16x32_bf16 a[104:107], v[28:31], v[172:175], a[104:107]\n"
"s_waitcnt vmcnt(24) lgkmcnt(0)\n"
"s_barrier\n"
"v_mfma_f32_16x16x32_bf16 a[44:47], v[32:35], v[164:167], a[44:47]\n"
"ds_read_b128 v[20:23], v10 offset:0\n"
"ds_read_b128 v[24:27], v10 offset:512\n"
"v_mfma_f32_16x16x32_bf16 a[108:111], v[32:35], v[172:175], a[108:111]\n"
"ds_read_b128 v[28:31], v10 offset:1024\n"
"v_mfma_f32_16x16x32_bf16 a[48:51], v[36:39], v[164:167], a[48:51]\n"
"ds_read_b128 v[32:35], v10 offset:1536\n"
"v_mfma_f32_16x16x32_bf16 a[112:115], v[36:39], v[172:175], a[112:115]\n"
"v_mfma_f32_16x16x32_bf16 a[52:55], v[40:43], v[164:167], a[52:55]\n"
"ds_read_b128 v[36:39], v10 offset:2048\n"
"v_mfma_f32_16x16x32_bf16 a[116:119], v[40:43], v[172:175], a[116:119]\n"
"v_mfma_f32_16x16x32_bf16 a[56:59], v[44:47], v[164:167], a[56:59]\n"
"ds_read_b128 v[40:43], v10 offset:2560\n"
"v_mfma_f32_16x16x32_bf16 a[120:123], v[44:47], v[172:175], a[120:123]\n"
"v_mfma_f32_16x16x32_bf16 a[60:63], v[48:51], v[164:167], a[60:63]\n"
"ds_read_b128 v[44:47], v10 offset:3072\n"
"v_mfma_f32_16x16x32_bf16 a[124:127], v[48:51], v[172:175], a[124:127]\n"
"s_cmp_lg_u32 s71, 0\n"
"s_cbranch_scc1 L_resc_w3f\n"
"L_rescback_w3f:\n"
"s_mov_b32 s70, 29\n"
"L_loop_w3:\n"
"s_waitcnt lgkmcnt(5)\n"
"v_mfma_f32_16x16x32_bf16 v[112:115], v[20:23], a[208:211], v[52:55]\n"
"ds_read_b128 v[48:51], v10 offset:3584\n"
"s_add_u32 m0, s56, 0\n"
"v_mfma_f32_16x16x32_bf16 v[116:119], v[20:23], a[240:243], v[56:59]\n"
"v_exp_f32 v124, v124\n"
"v_mfma_f32_16x16x32_bf16 v[112:115], v[24:27], a[212:215], v[112:115]\n"
"ds_read_b128 v[20:23], v10 offset:8192\n"
"buffer_load_dwordx4 %[dmakv], s[40:43], 0 offen offset:0 nt lds\n"
"v_mfma_f32_16x16x32_bf16 v[116:119], v[24:27], a[244:247], v[116:119]\n"
"s_waitcnt lgkmcnt(5)\n"
"v_mfma_f32_16x16x32_bf16 v[112:115], v[28:31], a[216:219], v[112:115]\n"
"ds_read_b128 v[24:27], v10 offset:8704\n"
"v_mfma_f32_16x16x32_bf16 v[116:119], v[28:31], a[248:251], v[116:119]\n"
"v_exp_f32 v125, v125\n"
"v_mfma_f32_16x16x32_bf16 v[112:115], v[32:35], a[220:223], v[112:115]\n"
"ds_read_b128 v[28:31], v10 offset:9216\n"
"v_cvt_pk_bf16_f32 v162, v124, v125\n"
"v_mfma_f32_16x16x32_bf16 v[116:119], v[32:35], a[252:255], v[116:119]\n"
"s_add_u32 m0, s56, 896\n"
"s_waitcnt lgkmcnt(5)\n"
"v_mfma_f32_16x16x32_bf16 v[112:115], v[36:39], a[224:227], v[112:115]\n"
"ds_read_b128 v[32:35], v10 offset:9728\n"
"buffer_load_dwordx4 %[dmakv], s[40:43], 0 offen offset:128 nt lds\n"
"v_mfma_f32_16x16x32_bf16 v[116:119], v[36:39], v[64:67], v[116:119]\n"
"v_exp_f32 v126, v126\n"
"v_mfma_f32_16x16x32_bf16 v[112:115], v[40:43], a[228:231], v[112:115]\n"
"ds_read_b128 v[36:39], v10 offset:10240\n"
"s_add_u32 m0, s56, 1792\n"
"v_mfma_f32_16x16x32_bf16 v[116:119], v[40:43], v[68:71], v[116:119]\n"
"s_waitcnt lgkmcnt(5)\n"
"v_mfma_f32_16x16x32_bf16 v[112:115], v[44:47], a[232:235], v[112:115]\n"
"ds_read_b128 v[40:43], v10 offset:10752\n"
"v_mfma_f32_16x16x32_bf16 v[116:119], v[44:47], v[72:75], v[116:119]\n"
"v_exp_f32 v127, v127\n"
"v_mfma_f32_16x16x32_bf16 v[112:115], v[48:51], a[236:239], v[112:115]\n"
"ds_read_b128 v[44:47], v10 offset:11264\n"
"v_exp_f32 v128, v128\n"
"v_exp_f32 v129, v129\n"
"v_exp_f32 v130, v130\n"
"v_exp_f32 v131, v131\n"
"v_mfma_f32_16x16x32_bf16 v[116:119], v[48:51], v[76:79], v[116:119]\n"
"v_cvt_pk_bf16_f32 v163, v126, v127\n"
"v_cvt_pk_bf16_f32 v170, v128, v129\n"
"v_cvt_pk_bf16_f32 v171, v130, v131\n"
"s_waitcnt lgkmcnt(5)\n"
"v_mfma_f32_16x16x32_bf16 v[124:127], v[20:23], a[208:211], v[52:55]\n"
"ds_read_b128 v[48:51], v10 offset:11776\n"
"buffer_load_dwordx4 %[dmakv], s[40:43], 0 offen offset:256 nt lds\n"
"v_mfma_f32_16x16x32_bf16 v[128:131], v[20:23], a[240:243], v[56:59]\n"
"v_exp_f32 v136, v136\n"
"v_mfma_f32_16x16x32_bf16 v[124:127], v[24:27], a[212:215], v[124:127]\n"
"ds_read_b128 v[20:23], v10 offset:16384\n"
"s_add_u32 m0, s56, 2688\n"
"v_mfma_f32_16x16x32_bf16 v[128:131], v[24:27], a[244:247], v[128:131]\n"
"buffer_load_dwordx4 %[dmakv], s[40:43], 0 offen offset:384 nt lds\n"
"s_waitcnt lgkmcnt(5)\n"
"v_mfma_f32_16x16x32_bf16 v[124:127], v[28:31], a[216:219], v[124:127]\n"
"ds_read_b128 v[24:27], v10 offset:16896\n"
"v_max3_f32 v13, v112, v113, v114\n"
"v_mfma_f32_16x16x32_bf16 v[128:131], v[28:31], a[248:251], v[128:131]\n"
"v_exp_f32 v137, v137\n"
"v_mfma_f32_16x16x32_bf16 v[124:127], v[32:35], a[220:223], v[124:127]\n"
"ds_read_b128 v[28:31], v10 offset:17408\n"
"v_cvt_pk_bf16_f32 v164, v136, v137\n"
"v_mfma_f32_16x16x32_bf16 v[128:131], v[32:35], a[252:255], v[128:131]\n"
"s_add_u32 m0, s56, 4096\n"
"s_waitcnt lgkmcnt(5)\n"
"v_mfma_f32_16x16x32_bf16 v[124:127], v[36:39], a[224:227], v[124:127]\n"
"ds_read_b128 v[32:35], v10 offset:17920\n"
"buffer_load_dwordx4 %[dmakv], s[40:43], s62 offen offset:0 nt lds\n"
"v_mfma_f32_16x16x32_bf16 v[128:131], v[36:39], v[64:67], v[128:131]\n"
"v_exp_f32 v138, v138\n"
"v_mfma_f32_16x16x32_bf16 v[124:127], v[40:43], a[228:231], v[124:127]\n"
"ds_read_b128 v[36:39], v10 offset:18432\n"
"s_add_u32 m0, s56, 4992\n"
"v_mfma_f32_16x16x32_bf16 v[128:131], v[40:43], v[68:71], v[128:131]\n"
"v_max3_f32 v14, v116, v117, v118\n"
"s_waitcnt lgkmcnt(5)\n"
"v_mfma_f32_16x16x32_bf16 v[124:127], v[44:47], a[232:235], v[124:127]\n"
"ds_read_b128 v[40:43], v10 offset:18944\n"
"buffer_load_dwordx4 %[dmakv], s[40:43], s62 offen offset:128 nt lds\n"
"v_mfma_f32_16x16x32_bf16 v[128:131], v[44:47], v[72:75], v[128:131]\n"
"v_exp_f32 v139, v139\n"
"v_mfma_f32_16x16x32_bf16 v[124:127], v[48:51], a[236:239], v[124:127]\n"
"ds_read_b128 v[44:47], v10 offset:19456\n"
"v_exp_f32 v140, v140\n"
"v_exp_f32 v141, v141\n"
"v_exp_f32 v142, v142\n"
"v_exp_f32 v143, v143\n"
"v_mfma_f32_16x16x32_bf16 v[128:131], v[48:51], v[76:79], v[128:131]\n"
"v_cvt_pk_bf16_f32 v165, v138, v139\n"
"v_cvt_pk_bf16_f32 v172, v140, v141\n"
"v_cvt_pk_bf16_f32 v173, v142, v143\n"
"s_waitcnt lgkmcnt(5)\n"
"v_mfma_f32_16x16x32_bf16 v[136:139], v[20:23], a[208:211], v[52:55]\n"
"ds_read_b128 v[48:51], v10 offset:19968\n"
"s_add_u32 m0, s56, 5888\n"
"v_mfma_f32_16x16x32_bf16 v[140:143], v[20:23], a[240:243], v[56:59]\n"
"v_exp_f32 v148, v148\n"
"v_mfma_f32_16x16x32_bf16 v[136:139], v[24:27], a[212:215], v[136:139]\n"
"ds_read_b128 v[20:23], v10 offset:24576\n"
"buffer_load_dwordx4 %[dmakv], s[40:43], s62 offen offset:256 nt lds\n"
"v_mfma_f32_16x16x32_bf16 v[140:143], v[24:27], a[244:247], v[140:143]\n"
"v_max_f32 v13, v13, v115\n"
"s_waitcnt lgkmcnt(5)\n"
"v_mfma_f32_16x16x32_bf16 v[136:139], v[28:31], a[216:219], v[136:139]\n"
"ds_read_b128 v[24:27], v10 offset:25088\n"
"s_add_u32 m0, s56, 6784\n"
"v_mfma_f32_16x16x32_bf16 v[140:143], v[28:31], a[248:251], v[140:143]\n"
"v_exp_f32 v149, v149\n"
"v_mfma_f32_16x16x32_bf16 v[136:139], v[32:35], a[220:223], v[136:139]\n"
"ds_read_b128 v[28:31], v10 offset:25600\n"
"v_cvt_pk_bf16_f32 v166, v148, v149\n"
"v_mfma_f32_16x16x32_bf16 v[140:143], v[32:35], a[252:255], v[140:143]\n"
"buffer_load_dwordx4 %[dmakv], s[40:43], s62 offen offset:384 nt lds\n"
"s_waitcnt lgkmcnt(5)\n"
"v_mfma_f32_16x16x32_bf16 v[136:139], v[36:39], a[224:227], v[136:139]\n"
"ds_read_b128 v[32:35], v10 offset:26112\n"
"v_max_f32 v14, v14, v119\n"
"v_mfma_f32_16x16x32_bf16 v[140:143], v[36:39], v[64:67], v[140:143]\n"
"v_exp_f32 v150, v150\n"
"v_mfma_f32_16x16x32_bf16 v[136:139], v[40:43], a[228:231], v[136:139]\n"
"ds_read_b128 v[36:39], v10 offset:26624\n"
"v_max3_f32 v13, v13, v124, v125\n"
"v_mfma_f32_16x16x32_bf16 v[140:143], v[40:43], v[68:71], v[140:143]\n"
"v_max3_f32 v14, v14, v128, v129\n"
"s_waitcnt lgkmcnt(5)\n"
"v_mfma_f32_16x16x32_bf16 v[136:139], v[44:47], a[232:235], v[136:139]\n"
"ds_read_b128 v[40:43], v10 offset:27136\n"
"v_max3_f32 v13, v13, v126, v127\n"
"v_mfma_f32_16x16x32_bf16 v[140:143], v[44:47], v[72:75], v[140:143]\n"
"v_exp_f32 v151, v151\n"
"v_mfma_f32_16x16x32_bf16 v[136:139], v[48:51], a[236:239], v[136:139]\n"
"ds_read_b128 v[44:47], v10 offset:27648\n"
"v_exp_f32 v152, v152\n"
"v_exp_f32 v153, v153\n"
"v_exp_f32 v154, v154\n"
"v_exp_f32 v155, v155\n"
"v_mfma_f32_16x16x32_bf16 v[140:143], v[48:51], v[76:79], v[140:143]\n"
"v_cvt_pk_bf16_f32 v167, v150, v151\n"
"v_cvt_pk_bf16_f32 v174, v152, v153\n"
"v_cvt_pk_bf16_f32 v175, v154, v155\n"
"s_waitcnt lgkmcnt(5)\n"
"v_mfma_f32_16x16x32_bf16 v[148:151], v[20:23], a[208:211], v[52:55]\n"
"ds_read_b128 v[48:51], v10 offset:28160\n"
"v_max3_f32 v14, v14, v130, v131\n"
"v_mfma_f32_16x16x32_bf16 v[152:155], v[20:23], a[240:243], v[56:59]\n"
"s_add_u32 s40, s40, 0x8000\n"
"s_addc_u32 s41, s41, 0\n"
"v_mfma_f32_16x16x32_bf16 v[148:151], v[24:27], a[212:215], v[148:151]\n"
"s_add_u32 s57, s57, 0x8000\n"
"s_cmp_eq_u32 s57, s61\n"
"s_cselect_b32 s57, s60, s57\n"
"v_mfma_f32_16x16x32_bf16 v[152:155], v[24:27], a[244:247], v[152:155]\n"
"v_add_u32 v11, s57, %[vl0]\n"
"v_add_u32 v12, s57, %[vl1]\n"
"s_sub_u32 s42, s42, 0x8000\n"
"s_cselect_b32 s42, 0, s42\n"
"s_add_u32 s56, s56, 0x8000\n"
"s_cmp_eq_u32 s56, s59\n"
"s_cselect_b32 s56, s58, s56\n"
"s_waitcnt lgkmcnt(4)\n"
"v_mfma_f32_16x16x32_bf16 v[148:151], v[28:31], a[216:219], v[148:151]\n"
"v_max3_f32 v13, v13, v136, v137\n"
"v_max3_f32 v14, v14, v140, v141\n"
"v_mfma_f32_16x16x32_bf16 v[152:155], v[28:31], a[248:251], v[152:155]\n"
"s_waitcnt vmcnt(24) lgkmcnt(0)\n"
"s_barrier\n"
"v_mfma_f32_16x16x32_bf16 v[148:151], v[32:35], a[220:223], v[148:151]\n"
"ds_read_b64_tr_b16 v[20:21], v11 offset:0\n"
"ds_read_b64_tr_b16 v[22:23], v11 offset:8192\n"
"v_mfma_f32_16x16x32_bf16 v[152:155], v[32:35], a[252:255], v[152:155]\n"
"ds_read_b64_tr_b16 v[24:25], v12 offset:0\n"
"ds_read_b64_tr_b16 v[26:27], v12 offset:8192\n"
"ds_read_b64_tr_b16 v[28:29], v11 offset:512\n"
"ds_read_b64_tr_b16 v[30:31], v11 offset:8704\n"
"v_mfma_f32_16x16x32_bf16 v[148:151], v[36:39], a[224:227], v[148:151]\n"
"ds_read_b64_tr_b16 v[32:33], v12 offset:512\n"
"ds_read_b64_tr_b16 v[34:35], v12 offset:8704\n"
"v_mfma_f32_16x16x32_bf16 v[152:155], v[36:39], v[64:67], v[152:155]\n"
"v_max3_f32 v13, v13, v138, v139\n"
"v_max3_f32 v14, v14, v142, v143\n"
"v_mfma_f32_16x16x32_bf16 v[148:151], v[40:43], a[228:231], v[148:151]\n"
"ds_read_b64_tr_b16 v[36:37], v11 offset:1024\n"
"ds_read_b64_tr_b16 v[38:39], v11 offset:9216\n"
"v_mfma_f32_16x16x32_bf16 v[152:155], v[40:43], v[68:71], v[152:155]\n"
"v_mfma_f32_16x16x32_bf16 v[148:151], v[44:47], a[232:235], v[148:151]\n"
"ds_read_b64_tr_b16 v[40:41], v12 offset:1024\n"
"ds_read_b64_tr_b16 v[42:43], v12 offset:9216\n"
"v_mfma_f32_16x16x32_bf16 v[152:155], v[44:47], v[72:75], v[152:155]\n"
"v_mfma_f32_16x16x32_bf16 v[148:151], v[48:51], a[236:239], v[148:151]\n"
"ds_read_b64_tr_b16 v[44:45], v11 offset:1536\n"
"ds_read_b64_tr_b16 v[46:47], v11 offset:9728\n"
"v_mfma_f32_16x16x32_bf16 v[152:155], v[48:51], v[76:79], v[152:155]\n"
"s_waitcnt lgkmcnt(10)\n"
"v_mfma_f32_16x16x32_bf16 a[0:3], v[20:23], v[160:163], a[0:3]\n"
"ds_read_b64_tr_b16 v[48:49], v12 offset:1536\n"
"ds_read_b64_tr_b16 v[50:51], v12 offset:9728\n"
"v_mfma_f32_16x16x32_bf16 a[64:67], v[20:23], v[168:171], a[64:67]\n"
"s_add_u32 m0, s56, 0\n"
"v_mfma_f32_16x16x32_bf16 a[4:7], v[24:27], v[160:163], a[4:7]\n"
"ds_read_b64_tr_b16 v[20:21], v11 offset:2048\n"
"ds_read_b64_tr_b16 v[22:23], v11 offset:10240\n"
"v_mfma_f32_16x16x32_bf16 a[68:71], v[24:27], v[168:171], a[68:71]\n"
"buffer_load_dwordx4 %[dmakv], s[44:47], 0 offen offset:0 nt lds\n"
"v_mfma_f32_16x16x32_bf16 a[192:195], a[204:207], v[160:163], a[192:195]\n"
"v_max3_f32 v13, v13, v148, v149\n"
"s_waitcnt lgkmcnt(10)\n"
"v_mfma_f32_16x16x32_bf16 a[8:11], v[28:31], v[160:163], a[8:11]\n"
"ds_read_b64_tr_b16 v[24:25], v12 offset:2048\n"
"ds_read_b64_tr_b16 v[26:27], v12 offset:10240\n"
"v_mfma_f32_16x16x32_bf16 a[72:75], v[28:31], v[168:171], a[72:75]\n"
"s_add_u32 m0, s56, 896\n"
"v_max3_f32 v14, v14, v152, v153\n"
"v_mfma_f32_16x16x32_bf16 a[196:199], a[204:207], v[168:171], a[196:199]\n"
"v_max3_f32 v13, v13, v150, v151\n"
"v_max3_f32 v14, v14, v154, v155\n"
"v_mfma_f32_16x16x32_bf16 a[12:15], v[32:35], v[160:163], a[12:15]\n"
"ds_read_b64_tr_b16 v[28:29], v11 offset:2560\n"
"ds_read_b64_tr_b16 v[30:31], v11 offset:10752\n"
"v_mfma_f32_16x16x32_bf16 a[76:79], v[32:35], v[168:171], a[76:79]\n"
"buffer_load_dwordx4 %[dmakv], s[44:47], 0 offen offset:128 nt lds\n"
"s_waitcnt lgkmcnt(10)\n"
"v_mfma_f32_16x16x32_bf16 a[16:19], v[36:39], v[160:163], a[16:19]\n"
"ds_read_b64_tr_b16 v[32:33], v12 offset:2560\n"
"ds_read_b64_tr_b16 v[34:35], v12 offset:10752\n"
"v_mfma_f32_16x16x32_bf16 a[80:83], v[36:39], v[168:171], a[80:83]\n"
"s_add_u32 m0, s56, 1792\n"
"v_max_f32 v16, v13, v14\n"
"v_mfma_f32_16x16x32_bf16 a[20:23], v[40:43], v[160:163], a[20:23]\n"
"ds_read_b64_tr_b16 v[36:37], v11 offset:3072\n"
"ds_read_b64_tr_b16 v[38:39], v11 offset:11264\n"
"v_mfma_f32_16x16x32_bf16 a[84:87], v[40:43], v[168:171], a[84:87]\n"
"s_waitcnt lgkmcnt(10)\n"
"v_mfma_f32_16x16x32_bf16 a[24:27], v[44:47], v[160:163], a[24:27]\n"
"ds_read_b64_tr_b16 v[40:41], v12 offset:3072\n"
"ds_read_b64_tr_b16 v[42:43], v12 offset:11264\n"
"v_mfma_f32_16x16x32_bf16 a[88:91], v[44:47], v[168:171], a[88:91]\n"
"v_cmp_lt_f32 vcc, s73, v16\n"
"s_cbranch_vccnz L_rare_w3l\n"
"L_back_w3l:\n"
"v_mfma_f32_16x16x32_bf16 a[28:31], v[48:51], v[160:163], a[28:31]\n"
"ds_read_b64_tr_b16 v[44:45], v11 offset:3584\n"
"ds_read_b64_tr_b16 v[46:47], v11 offset:11776\n"
"v_mfma_f32_16x16x32_bf16 a[92:95], v[48:51], v[168:171], a[92:95]\n"
"buffer_load_dwordx4 %[dmakv], s[44:47], 0 offen offset:256 nt lds\n"
"s_waitcnt lgkmcnt(10)\n"
"v_mfma_f32_16x16x32_bf16 a[32:35], v[20:23], v[160:163], a[32:35]\n"
"ds_read_b64_tr_b16 v[48:49], v12 offset:3584\n"
"ds_read_b64_tr_b16 v[50:51], v12 offset:11776\n"
"v_mfma_f32_16x16x32_bf16 a[96:99], v[20:23], v[168:171], a[96:99]\n"
"v_exp_f32 v112, v112\n"
"v_mfma_f32_16x16x32_bf16 a[36:39], v[24:27], v[160:163], a[36:39]\n"
"ds_read_b64_tr_b16 v[20:21], v11 offset:16384\n"
"ds_read_b64_tr_b16 v[22:23], v11 offset:24576\n"
"v_mfma_f32_16x16x32_bf16 a[100:103], v[24:27], v[168:171], a[100:103]\n"
"s_add_u32 m0, s56, 2688\n"
"s_waitcnt lgkmcnt(10)\n"
"v_mfma_f32_16x16x32_bf16 a[40:43], v[28:31], v[160:163], a[40:43]\n"
"ds_read_b64_tr_b16 v[24:25], v12 offset:16384\n"
"ds_read_b64_tr_b16 v[26:27], v12 offset:24576\n"
"v_mfma_f32_16x16x32_bf16 a[104:107], v[28:31], v[168:171], a[104:107]\n"
"v_exp_f32 v113, v113\n"
"v_mfma_f32_16x16x32_bf16 a[44:47], v[32:35], v[160:163], a[44:47]\n"
"ds_read_b64_tr_b16 v[28:29], v11 offset:16896\n"
"ds_read_b64_tr_b16 v[30:31], v11 offset:25088\n"
"v_mfma_f32_16x16x32_bf16 a[108:111], v[32:35], v[168:171], a[108:111]\n"
"buffer_load_dwordx4 %[dmakv], s[44:47], 0 offen offset:384 nt lds\n"
"s_waitcnt lgkmcnt(10)\n"
"v_mfma_f32_16x16x32_bf16 a[48:51], v[36:39], v[160:163], a[48:51]\n"
"ds_read_b64_tr_b16 v[32:33], v12 offset:16896\n"
"ds_read_b64_tr_b16 v[34:35], v12 offset:25088\n"
"v_mfma_f32_16x16x32_bf16 a[112:115], v[36:39], v[168:171], a[112:115]\n"
"v_exp_f32 v114, v114\n"
"v_mfma_f32_16x16x32_bf16 a[52:55], v[40:43], v[160:163], a[52:55]\n"
"ds_read_b64_tr_b16 v[36:37], v11 offset:17408\n"
"ds_read_b64_tr_b16 v[38:39], v11 offset:25600\n"
"v_mfma_f32_16x16x32_bf16 a[116:119], v[40:43], v[168:171], a[116:119]\n"
"s_add_u32 m0, s56, 4096\n"
"s_waitcnt lgkmcnt(10)\n"
"v_mfma_f32_16x16x32_bf16 a[56:59], v[44:47], v[160:163], a[56:59]\n"
"ds_read_b64_tr_b16 v[40:41], v12 offset:17408\n"
"ds_read_b64_tr_b16 v[42:43], v12 offset:25600\n"
"v_mfma_f32_16x16x32_bf16 a[120:123], v[44:47], v[168:171], a[120:123]\n"
"v_exp_f32 v115, v115\n"
"v_mfma_f32_16x16x32_bf16 a[60:63], v[48:51], v[160:163], a[60:63]\n"
"ds_read_b64_tr_b16 v[44:45], v11 offset:17920\n"
"ds_read_b64_tr_b16 v[46:47], v11 offset:26112\n"
"v_mfma_f32_16x16x32_bf16 a[124:127], v[48:51], v[168:171], a[124:127]\n"
"buffer_load_dwordx4 %[dmakv], s[44:47], s62 offen offset:0 nt lds\n"
"s_waitcnt lgkmcnt(10)\n"
"v_mfma_f32_16x16x32_bf16 a[0:3], v[20:23], v[164:167], a[0:3]\n"
"ds_read_b64_tr_b16 v[48:49], v12 offset:17920\n"
"ds_read_b64_tr_b16 v[50:51], v12 offset:26112\n"
"v_mfma_f32_16x16x32_bf16 a[64:67], v[20:23], v[172:175], a[64:67]\n"
"v_exp_f32 v116, v116\n"
"v_mfma_f32_16x16x32_bf16 a[4:7], v[24:27], v[164:167], a[4:7]\n"
"ds_read_b64_tr_b16 v[20:21], v11 offset:18432\n"
"ds_read_b64_tr_b16 v[22:23], v11 offset:26624\n"
"v_mfma_f32_16x16x32_bf16 a[68:71], v[24:27], v[172:175], a[68:71]\n"
"v_exp_f32 v117, v117\n"
"v_mfma_f32_16x16x32_bf16 a[192:195], a[204:207], v[164:167], a[192:195]\n"
"s_add_u32 m0, s56, 4992\n"
"s_waitcnt lgkmcnt(10)\n"
"v_mfma_f32_16x16x32_bf16 a[8:11], v[28:31], v[164:167], a[8:11]\n"
"ds_read_b64_tr_b16 v[24:25], v12 offset:18432\n"
"ds_read_b64_tr_b16 v[26:27], v12 offset:26624\n"
"v_mfma_f32_16x16x32_bf16 a[72:75], v[28:31], v[172:175], a[72:75]\n"
"v_exp_f32 v118, v118\n"
"v_mfma_f32_16x16x32_bf16 a[196:199], a[204:207], v[172:175], a[196:199]\n"
"v_exp_f32 v119, v119\n"
"v_mfma_f32_16x16x32_bf16 a[12:15], v[32:35], v[164:167], a[12:15]\n"
"ds_read_b64_tr_b16 v[28:29], v11 offset:18944\n"
"ds_read_b64_tr_b16 v[30:31], v11 offset:27136\n"
"v_mfma_f32_16x16x32_bf16 a[76:79], v[32:35], v[172:175], a[76:79]\n"
"buffer_load_dwordx4 %[dmakv], s[44:47], s62 offen offset:128 nt lds\n"
"s_waitcnt lgkmcnt(10)\n"
"v_mfma_f32_16x16x32_bf16 a[16:19], v[36:39], v[164:167], a[16:19]\n"
"ds_read_b64_tr_b16 v[32:33], v12 offset:18944\n"
"ds_read_b64_tr_b16 v[34:35], v12 offset:27136\n"
"v_mfma_f32_16x16x32_bf16 a[80:83], v[36:39], v[172:175], a[80:83]\n"
"s_add_u32 m0, s56, 5888\n"
"v_cvt_pk_bf16_f32 v160, v112, v113\n"
"v_mfma_f32_16x16x32_bf16 a[20:23], v[40:43], v[164:167], a[20:23]\n"
"ds_read_b64_tr_b16 v[36:37], v11 offset:19456\n"
"ds_read_b64_tr_b16 v[38:39], v11 offset:27648\n"
"v_mfma_f32_16x16x32_bf16 a[84:87], v[40:43], v[172:175], a[84:87]\n"
"buffer_load_dwordx4 %[dmakv], s[44:47], s62 offen offset:256 nt lds\n"
"s_waitcnt lgkmcnt(10)\n"
"v_mfma_f32_16x16x32_bf16 a[24:27], v[44:47], v[164:167], a[24:27]\n"
"ds_read_b64_tr_b16 v[40:41], v12 offset:19456\n"
"ds_read_b64_tr_b16 v[42:43], v12 offset:27648\n"
"v_mfma_f32_16x16x32_bf16 a[88:91], v[44:47], v[172:175], a[88:91]\n"
"s_add_u32 m0, s56, 6784\n"
"v_cvt_pk_bf16_f32 v161, v114, v115\n"
"v_mfma_f32_16x16x32_bf16 a[28:31], v[48:51], v[164:167], a[28:31]\n"
"ds_read_b64_tr_b16 v[44:45], v11 offset:19968\n"
"ds_read_b64_tr_b16 v[46:47], v11 offset:28160\n"
"v_mfma_f32_16x16x32_bf16 a[92:95], v[48:51], v[172:175], a[92:95]\n"
"buffer_load_dwordx4 %[dmakv], s[44:47], s62 offen offset:384 nt lds\n"
"s_waitcnt lgkmcnt(10)\n"
"v_mfma_f32_16x16x32_bf16 a[32:35], v[20:23], v[164:167], a[32:35]\n"
"ds_read_b64_tr_b16 v[48:49], v12 offset:19968\n"
"ds_read_b64_tr_b16 v[50:51], v12 offset:28160\n"
"v_mfma_f32_16x16x32_bf16 a[96:99], v[20:23], v[172:175], a[96:99]\n"
"s_add_u32 s44, s44, 0x8000\n"
"s_addc_u32 s45, s45, 0\n"
"v_mfma_f32_16x16x32_bf16 a[36:39], v[24:27], v[164:167], a[36:39]\n"
"s_add_u32 s57, s57, 0x8000\n"
"s_cmp_eq_u32 s57, s61\n"
"s_cselect_b32 s57, s60, s57\n"
"v_mfma_f32_16x16x32_bf16 a[100:103], v[24:27], v[172:175], a[100:103]\n"
"v_add_u32 v10, s57, %[klane]\n"
"s_sub_u32 s46, s46, 0x8000\n"
"s_cselect_b32 s46, 0, s46\n"
"s_add_u32 s56, s56, 0x8000\n"
"s_cmp_eq_u32 s56, s59\n"
"s_cselect_b32 s56, s58, s56\n"
"s_waitcnt lgkmcnt(8)\n"
"v_mfma_f32_16x16x32_bf16 a[40:43], v[28:31], v[164:167], a[40:43]\n"
"v_cvt_pk_bf16_f32 v168, v116, v117\n"
"v_cvt_pk_bf16_f32 v169, v118, v119\n"
"v_mfma_f32_16x16x32_bf16 a[104:107], v[28:31], v[172:175], a[104:107]\n"
"s_waitcnt vmcnt(24) lgkmcnt(0)\n"
"s_barrier\n"
"v_mfma_f32_16x16x32_bf16 a[44:47], v[32:35], v[164:167], a[44:47]\n"
"ds_read_b128 v[20:23], v10 offset:0\n"
"ds_read_b128 v[24:27], v10 offset:512\n"
"v_mfma_f32_16x16x32_bf16 a[108:111], v[32:35], v[172:175], a[108:111]\n"
"ds_read_b128 v[28:31], v10 offset:1024\n"
"v_mfma_f32_16x16x32_bf16 a[48:51], v[36:39], v[164:167], a[48:51]\n"
"ds_read_b128 v[32:35], v10 offset:1536\n"
"v_mfma_f32_16x16x32_bf16 a[112:115], v[36:39], v[172:175], a[112:115]\n"
"v_mfma_f32_16x16x32_bf16 a[52:55], v[40:43], v[164:167], a[52:55]\n"
"ds_read_b128 v[36:39], v10 offset:2048\n"
"v_mfma_f32_16x16x32_bf16 a[116:119], v[40:43], v[172:175], a[116:119]\n"
"v_mfma_f32_16x16x32_bf16 a[56:59], v[44:47], v[164:167], a[56:59]\n"
"ds_read_b128 v[40:43], v10 offset:2560\n"
"v_mfma_f32_16x16x32_bf16 a[120:123], v[44:47], v[172:175], a[120:123]\n"
"v_mfma_f32_16x16x32_bf16 a[60:63], v[48:51], v[164:167], a[60:63]\n"
"ds_read_b128 v[44:47], v10 offset:3072\n"
"v_mfma_f32_16x16x32_bf16 a[124:127], v[48:51], v[172:175], a[124:127]\n"
"s_cmp_lg_u32 s71, 0\n"
"s_cbranch_scc1 L_resc_w3l\n"
"L_rescback_w3l:\n"
"s_sub_u32 s70, s70, 1\n"
"s_cmp_lg_u32 s70, 0\n"
"s_cbranch_scc1 L_loop_w3\n"
"s_getpc_b64 s[64:65]\n"
"L_pc_L_tail_w3:\n"
"s_add_u32 s64, s64, L_tail_w3-L_pc_L_tail_w3\n"
"s_addc_u32 s65, s65, 0\n"
"v_mbcnt_lo_u32_b32 v206, -1, 0\n"
"v_mbcnt_hi_u32_b32 v206, -1, v206\n"
"v_lshlrev_b32 v206, 7, v206\n"
"v_add_u32 v206, 0, v206\n"
"v_min_u32 v206, L_fin_w3-L_tail_w3-4, v206\n"
"global_load_dword v205, v206, s[64:65]\n"
"v_mbcnt_lo_u32_b32 v206, -1, 0\n"
"v_mbcnt_hi_u32_b32 v206, -1, v206\n"
"v_lshlrev_b32 v206, 7, v206\n"
"v_add_u32 v206, 8192, v206\n"
"v_min_u32 v206, L_fin_w3-L_tail_w3-4, v206\n"
"global_load_dword v205, v206, s[64:65]\n"
"v_mbcnt_lo_u32_b32 v206, -1, 0\n"
"v_mbcnt_hi_u32_b32 v206, -1, v206\n"
"v_lshlrev_b32 v206, 7, v206\n"
"v_add_u32 v206, 16384, v206\n"
"v_min_u32 v206, L_fin_w3-L_tail_w3-4, v206\n"
"global_load_dword v205, v206, s[64:65]\n"
"v_mbcnt_lo_u32_b32 v206, -1, 0\n"
"v_mbcnt_hi_u32_b32 v206, -1, v206\n"
"v_lshlrev_b32 v206, 7, v206\n"
"v_add_u32 v206, 24576, v206\n"
"v_min_u32 v206, L_fin_w3-L_tail_w3-4, v206\n"
"global_load_dword v205, v206, s[64:65]\n"
"L_tail_w3:\n"
"s_waitcnt lgkmcnt(5)\n"
"v_mfma_f32_16x16x32_bf16 v[112:115], v[20:23], a[208:211], v[52:55]\n"
"ds_read_b128 v[48:51], v10 offset:3584\n"
"s_add_u32 m0, s56, 0\n"
"v_mfma_f32_16x16x32_bf16 v[116:119], v[20:23], a[240:243], v[56:59]\n"
"v_exp_f32 v124, v124\n"
"v_mfma_f32_16x16x32_bf16 v[112:115], v[24:27], a[212:215], v[112:115]\n"
"ds_read_b128 v[20:23], v10 offset:8192\n"
"buffer_load_dwordx4 v204, s[48:51], s83 offen lds\n"
"v_mfma_f32_16x16x32_bf16 v[116:119], v[24:27], a[244:247], v[116:119]\n"
"s_add_u32 s83, s83, 7168\n"
"s_waitcnt lgkmcnt(5)\n"
"v_mfma_f32_16x16x32_bf16 v[112:115], v[28:31], a[216:219], v[112:115]\n"
"ds_read_b128 v[24:27], v10 offset:8704\n"
"v_mfma_f32_16x16x32_bf16 v[116:119], v[28:31], a[248:251], v[116:119]\n"
"v_exp_f32 v125, v125\n"
"v_mfma_f32_16x16x32_bf16 v[112:115], v[32:35], a[220:223], v[112:115]\n"
"ds_read_b128 v[28:31], v10 offset:9216\n"
"v_cvt_pk_bf16_f32 v162, v124, v125\n"
"v_mfma_f32_16x16x32_bf16 v[116:119], v[32:35], a[252:255], v[116:119]\n"
"s_add_u32 m0, s56, 1024\n"
"s_waitcnt lgkmcnt(5)\n"
"v_mfma_f32_16x16x32_bf16 v[112:115], v[36:39], a[224:227], v[112:115]\n"
"ds_read_b128 v[32:35], v10 offset:9728\n"
"buffer_load_dwordx4 v204, s[48:51], s83 offen lds\n"
"v_mfma_f32_16x16x32_bf16 v[116:119], v[36:39], v[64:67], v[116:119]\n"
"v_exp_f32 v126, v126\n"
"v_mfma_f32_16x16x32_bf16 v[112:115], v[40:43], a[228:231], v[112:115]\n"
"ds_read_b128 v[36:39], v10 offset:10240\n"
"s_add_u32 s83, s83, 7168\n"
"v_mfma_f32_16x16x32_bf16 v[116:119], v[40:43], v[68:71], v[116:119]\n"
"s_add_u32 m0, s56, 2048\n"
"s_waitcnt lgkmcnt(5)\n"
"v_mfma_f32_16x16x32_bf16 v[112:115], v[44:47], a[232:235], v[112:115]\n"
"ds_read_b128 v[40:43], v10 offset:10752\n"
"buffer_load_dwordx4 v204, s[48:51], s83 offen lds\n"
"v_mfma_f32_16x16x32_bf16 v[116:119], v[44:47], v[72:75], v[116:119]\n"
"v_exp_f32 v127, v127\n"
"v_mfma_f32_16x16x32_bf16 v[112:115], v[48:51], a[236:239], v[112:115]\n"
"ds_read_b128 v[44:47], v10 offset:11264\n"
"v_exp_f32 v128, v128\n"
"v_exp_f32 v129, v129\n"
"v_exp_f32 v130, v130\n"
"v_exp_f32 v131, v131\n"
"v_mfma_f32_16x16x32_bf16 v[116:119], v[48:51], v[76:79], v[116:119]\n"
"v_cvt_pk_bf16_f32 v163, v126, v127\n"
"v_cvt_pk_bf16_f32 v170, v128, v129\n"
"v_cvt_pk_bf16_f32 v171, v130, v131\n"
"s_waitcnt lgkmcnt(5)\n"
"v_mfma_f32_16x16x32_bf16 v[124:127], v[20:23], a[208:211], v[52:55]\n"
"ds_read_b128 v[48:51], v10 offset:11776\n"
"s_add_u32 s83, s83, 7168\n"
"v_mfma_f32_16x16x32_bf16 v[128:131], v[20:23], a[240:243], v[56:59]\n"
"v_exp_f32 v136, v136\n"
"v_mfma_f32_16x16x32_bf16 v[124:127], v[24:27], a[212:215], v[124:127]\n"
"ds_read_b128 v[20:23], v10 offset:16384\n"
"s_add_u32 m0, s56, 3072\n"
"v_mfma_f32_16x16x32_bf16 v[128:131], v[24:27], a[244:247], v[128:131]\n"
"buffer_load_dwordx4 v204, s[48:51], s83 offen lds\n"
"s_waitcnt lgkmcnt(5)\n"
"v_mfma_f32_16x16x32_bf16 v[124:127], v[28:31], a[216:219], v[124:127]\n"
"ds_read_b128 v[24:27], v10 offset:16896\n"
"s_add_u32 s83, s83, 7168\n"
"v_mfma_f32_16x16x32_bf16 v[128:131], v[28:31], a[248:251], v[128:131]\n"
"v_exp_f32 v137, v137\n"
"v_mfma_f32_16x16x32_bf16 v[124:127], v[32:35], a[220:223], v[124:127]\n"
"ds_read_b128 v[28:31], v10 offset:17408\n"
"v_cvt_pk_bf16_f32 v164, v136, v137\n"
"v_mfma_f32_16x16x32_bf16 v[128:131], v[32:35], a[252:255], v[128:131]\n"
"s_add_u32 m0, s56, 4096\n"
"s_waitcnt lgkmcnt(5)\n"
"v_mfma_f32_16x16x32_bf16 v[124:127], v[36:39], a[224:227], v[124:127]\n"
"ds_read_b128 v[32:35], v10 offset:17920\n"
"buffer_load_dwordx4 v204, s[48:51], s83 offen lds\n"
"v_mfma_f32_16x16x32_bf16 v[128:131], v[36:39], v[64:67], v[128:131]\n"
"v_exp_f32 v138, v138\n"
"v_mfma_f32_16x16x32_bf16 v[124:127], v[40:43], a[228:231], v[124:127]\n"
"ds_read_b128 v[36:39], v10 offset:18432\n"
"s_add_u32 s83, s83, 7168\n"
"v_mfma_f32_16x16x32_bf16 v[128:131], v[40:43], v[68:71], v[128:131]\n"
"s_add_u32 m0, s56, 5120\n"
"s_waitcnt lgkmcnt(5)\n"
"v_mfma_f32_16x16x32_bf16 v[124:127], v[44:47], a[232:235], v[124:127]\n"
"ds_read_b128 v[40:43], v10 offset:18944\n"
"v_max3_f32 v13, v112, v113, v114\n"
"v_mfma_f32_16x16x32_bf16 v[128:131], v[44:47], v[72:75], v[128:131]\n"
"v_exp_f32 v139, v139\n"
"v_mfma_f32_16x16x32_bf16 v[124:127], v[48:51], a[236:239], v[124:127]\n"
"ds_read_b128 v[44:47], v10 offset:19456\n"
"v_exp_f32 v140, v140\n"
"v_exp_f32 v141, v141\n"
"v_exp_f32 v142, v142\n"
"v_exp_f32 v143, v143\n"
"v_mfma_f32_16x16x32_bf16 v[128:131], v[48:51], v[76:79], v[128:131]\n"
"v_cvt_pk_bf16_f32 v165, v138, v139\n"
"v_cvt_pk_bf16_f32 v172, v140, v141\n"
"v_cvt_pk_bf16_f32 v173, v142, v143\n"
"s_waitcnt lgkmcnt(5)\n"
"v_mfma_f32_16x16x32_bf16 v[136:139], v[20:23], a[208:211], v[52:55]\n"
"ds_read_b128 v[48:51], v10 offset:19968\n"
"buffer_load_dwordx4 v204, s[48:51], s83 offen lds\n"
"v_mfma_f32_16x16x32_bf16 v[140:143], v[20:23], a[240:243], v[56:59]\n"
"v_exp_f32 v148, v148\n"
"v_mfma_f32_16x16x32_bf16 v[136:139], v[24:27], a[212:215], v[136:139]\n"
"ds_read_b128 v[20:23], v10 offset:24576\n"
"s_add_u32 s83, s83, 7168\n"
"v_mfma_f32_16x16x32_bf16 v[140:143], v[24:27], a[244:247], v[140:143]\n"
"s_add_u32 m0, s56, 6144\n"
"s_waitcnt lgkmcnt(5)\n"
"v_mfma_f32_16x16x32_bf16 v[136:139], v[28:31], a[216:219], v[136:139]\n"
"ds_read_b128 v[24:27], v10 offset:25088\n"
"buffer_load_dwordx4 v204, s[48:51], s83 offen lds\n"
"v_mfma_f32_16x16x32_bf16 v[140:143], v[28:31], a[248:251], v[140:143]\n"
"v_exp_f32 v149, v149\n"
"v_mfma_f32_16x16x32_bf16 v[136:139], v[32:35], a[220:223], v[136:139]\n"
"ds_read_b128 v[28:31], v10 offset:25600\n"
"v_cvt_pk_bf16_f32 v166, v148, v149\n"
"v_mfma_f32_16x16x32_bf16 v[140:143], v[32:35], a[252:255], v[140:143]\n"
"s_add_u32 s83, s83, 7168\n"
"s_waitcnt lgkmcnt(5)\n"
"v_mfma_f32_16x16x32_bf16 v[136:139], v[36:39], a[224:227], v[136:139]\n"
"ds_read_b128 v[32:35], v10 offset:26112\n"
"s_add_u32 m0, s56, 7168\n"
"v_mfma_f32_16x16x32_bf16 v[140:143], v[36:39], v[64:67], v[140:143]\n"
"v_exp_f32 v150, v150\n"
"v_mfma_f32_16x16x32_bf16 v[136:139], v[40:43], a[228:231], v[136:139]\n"
"ds_read_b128 v[36:39], v10 offset:26624\n"
"buffer_load_dwordx4 v204, s[48:51], s83 offen lds\n"
"v_mfma_f32_16x16x32_bf16 v[140:143], v[40:43], v[68:71], v[140:143]\n"
"s_add_u32 s83, s83, 7168\n"
"s_waitcnt lgkmcnt(5)\n"
"v_mfma_f32_16x16x32_bf16 v[136:139], v[44:47], a[232:235], v[136:139]\n"
"ds_read_b128 v[40:43], v10 offset:27136\n"
"v_max3_f32 v14, v116, v117, v118\n"
"v_mfma_f32_16x16x32_bf16 v[140:143], v[44:47], v[72:75], v[140:143]\n"
"v_exp_f32 v151, v151\n"
"v_mfma_f32_16x16x32_bf16 v[136:139], v[48:51], a[236:239], v[136:139]\n"
"ds_read_b128 v[44:47], v10 offset:27648\n"
"v_exp_f32 v152, v152\n"
"v_exp_f32 v153, v153\n"
"v_exp_f32 v154, v154\n"
"v_exp_f32 v155, v155\n"
"v_mfma_f32_16x16x32_bf16 v[140:143], v[48:51], v[76:79], v[140:143]\n"
"v_cvt_pk_bf16_f32 v167, v150, v151\n"
"v_cvt_pk_bf16_f32 v174, v152, v153\n"
"v_cvt_pk_bf16_f32 v175, v154, v155\n"
"s_waitcnt lgkmcnt(5)\n"
"v_mfma_f32_16x16x32_bf16 v[148:151], v[20:23], a[208:211], v[52:55]\n"
"ds_read_b128 v[48:51], v10 offset:28160\n"
"v_max_f32 v13, v13, v115\n"
"v_mfma_f32_16x16x32_bf16 v[152:155], v[20:23], a[240:243], v[56:59]\n"
"s_mov_b32 s83, 0\n"
"s_add_u32 s48, s48, 128\n"
"s_addc_u32 s49, s49, 0\n"
"v_mfma_f32_16x16x32_bf16 v[148:151], v[24:27], a[212:215], v[148:151]\n"
"s_add_u32 s57, s57, 0x8000\n"
"s_cmp_eq_u32 s57, s61\n"
"s_cselect_b32 s57, s60, s57\n"
"v_mfma_f32_16x16x32_bf16 v[152:155], v[24:27], a[244:247], v[152:155]\n"
"v_add_u32 v11, s57, %[vl0]\n"
"v_add_u32 v12, s57, %[vl1]\n"
"s_sub_u32 s50, s50, 128\n"
"s_add_u32 s56, s56, 0x8000\n"
"s_cmp_eq_u32 s56, s59\n"
"s_cselect_b32 s56, s58, s56\n"
"s_waitcnt lgkmcnt(4)\n"
"v_mfma_f32_16x16x32_bf16 v[148:151], v[28:31], a[216:219], v[148:151]\n"
"v_max_f32 v14, v14, v119\n"
"v_max3_f32 v13, v13, v124, v125\n"
"v_mfma_f32_16x16x32_bf16 v[152:155], v[28:31], a[248:251], v[152:155]\n"
"s_waitcnt vmcnt(28) lgkmcnt(0)\n"
"s_barrier\n"
"v_mfma_f32_16x16x32_bf16 v[148:151], v[32:35], a[220:223], v[148:151]\n"
"ds_read_b64_tr_b16 v[20:21], v11 offset:0\n"
"ds_read_b64_tr_b16 v[22:23], v11 offset:8192\n"
"v_mfma_f32_16x16x32_bf16 v[152:155], v[32:35], a[252:255], v[152:155]\n"
"ds_read_b64_tr_b16 v[24:25], v12 offset:0\n"
"ds_read_b64_tr_b16 v[26:27], v12 offset:8192\n"
"ds_read_b64_tr_b16 v[28:29], v11 offset:512\n"
"ds_read_b64_tr_b16 v[30:31], v11 offset:8704\n"
"v_mfma_f32_16x16x32_bf16 v[148:151], v[36:39], a[224:227], v[148:151]\n"
"ds_read_b64_tr_b16 v[32:33], v12 offset:512\n"
"ds_read_b64_tr_b16 v[34:35], v12 offset:8704\n"
"v_mfma_f32_16x16x32_bf16 v[152:155], v[36:39], v[64:67], v[152:155]\n"
"v_max3_f32 v14, v14, v128, v129\n"
"v_max3_f32 v13, v13, v126, v127\n"
"v_mfma_f32_16x16x32_bf16 v[148:151], v[40:43], a[228:231], v[148:151]\n"
"ds_read_b64_tr_b16 v[36:37], v11 offset:1024\n"
"ds_read_b64_tr_b16 v[38:39], v11 offset:9216\n"
"v_mfma_f32_16x16x32_bf16 v[152:155], v[40:43], v[68:71], v[152:155]\n"
"v_max3_f32 v14, v14, v130, v131\n"
"v_max3_f32 v13, v13, v136, v137\n"
"v_mfma_f32_16x16x32_bf16 v[148:151], v[44:47], a[232:235], v[148:151]\n"
"ds_read_b64_tr_b16 v[40:41], v12 offset:1024\n"
"ds_read_b64_tr_b16 v[42:43], v12 offset:9216\n"
"v_mfma_f32_16x16x32_bf16 v[152:155], v[44:47], v[72:75], v[152:155]\n"
"v_max3_f32 v14, v14, v140, v141\n"
"v_max3_f32 v13, v13, v138, v139\n"
"v_mfma_f32_16x16x32_bf16 v[148:151], v[48:51], a[236:239], v[148:151]\n"
"ds_read_b64_tr_b16 v[44:45], v11 offset:1536\n"
"ds_read_b64_tr_b16 v[46:47], v11 offset:9728\n"
"v_mfma_f32_16x16x32_bf16 v[152:155], v[48:51], v[76:79], v[152:155]\n"
"v_max3_f32 v14, v14, v142, v143\n"
"s_waitcnt lgkmcnt(10)\n"
"v_mfma_f32_16x16x32_bf16 a[0:3], v[20:23], v[160:163], a[0:3]\n"
"ds_read_b64_tr_b16 v[48:49], v12 offset:1536\n"
"ds_read_b64_tr_b16 v[50:51], v12 offset:9728\n"
"v_mfma_f32_16x16x32_bf16 a[64:67], v[20:23], v[168:171], a[64:67]\n"
"s_add_u32 m0, s56, 0\n"
"v_mfma_f32_16x16x32_bf16 a[4:7], v[24:27], v[160:163], a[4:7]\n"
"ds_read_b64_tr_b16 v[20:21], v11 offset:2048\n"
"ds_read_b64_tr_b16 v[22:23], v11 offset:10240\n"
"v_mfma_f32_16x16x32_bf16 a[68:71], v[24:27], v[168:171], a[68:71]\n"
"buffer_load_dwordx4 v204, s[48:51], s83 offen lds\n"
"v_mfma_f32_16x16x32_bf16 a[192:195], a[204:207], v[160:163], a[192:195]\n"
"s_add_u32 s83, s83, 7168\n"
"s_waitcnt lgkmcnt(10)\n"
"v_mfma_f32_16x16x32_bf16 a[8:11], v[28:31], v[160:163], a[8:11]\n"
"ds_read_b64_tr_b16 v[24:25], v12 offset:2048\n"
"ds_read_b64_tr_b16 v[26:27], v12 offset:10240\n"
"v_mfma_f32_16x16x32_bf16 a[72:75], v[28:31], v[168:171], a[72:75]\n"
"s_add_u32 m0, s56, 1024\n"
"v_max3_f32 v13, v13, v148, v149\n"
"v_mfma_f32_16x16x32_bf16 a[196:199], a[204:207], v[168:171], a[196:199]\n"
"v_max3_f32 v14, v14, v152, v153\n"
"v_max3_f32 v13, v13, v150, v151\n"
"v_mfma_f32_16x16x32_bf16 a[12:15], v[32:35], v[160:163], a[12:15]\n"
"ds_read_b64_tr_b16 v[28:29], v11 offset:2560\n"
"ds_read_b64_tr_b16 v[30:31], v11 offset:10752\n"
"v_mfma_f32_16x16x32_bf16 a[76:79], v[32:35], v[168:171], a[76:79]\n"
"buffer_load_dwordx4 v204, s[48:51], s83 offen lds\n"
"s_waitcnt lgkmcnt(10)\n"
"v_mfma_f32_16x16x32_bf16 a[16:19], v[36:39], v[160:163], a[16:19]\n"
"ds_read_b64_tr_b16 v[32:33], v12 offset:2560\n"
"ds_read_b64_tr_b16 v[34:35], v12 offset:10752\n"
"v_mfma_f32_16x16x32_bf16 a[80:83], v[36:39], v[168:171], a[80:83]\n"
"s_add_u32 s83, s83, 7168\n"
"v_max3_f32 v14, v14, v154, v155\n"
"v_mfma_f32_16x16x32_bf16 a[20:23], v[40:43], v[160:163], a[20:23]\n"
"ds_read_b64_tr_b16 v[36:37], v11 offset:3072\n"
"ds_read_b64_tr_b16 v[38:39], v11 offset:11264\n"
"v_mfma_f32_16x16x32_bf16 a[84:87], v[40:43], v[168:171], a[84:87]\n"
"s_add_u32 m0, s56, 2048\n"
"s_waitcnt lgkmcnt(10)\n"
"v_mfma_f32_16x16x32_bf16 a[24:27], v[44:47], v[160:163], a[24:27]\n"
"ds_read_b64_tr_b16 v[40:41], v12 offset:3072\n"
"ds_read_b64_tr_b16 v[42:43], v12 offset:11264\n"
"v_mfma_f32_16x16x32_bf16 a[88:91], v[44:47], v[168:171], a[88:91]\n"
"buffer_load_dwordx4 v204, s[48:51], s83 offen lds\n"
"v_max_f32 v16, v13, v14\n"
"v_mfma_f32_16x16x32_bf16 a[28:31], v[48:51], v[160:163], a[28:31]\n"
"ds_read_b64_tr_b16 v[44:45], v11 offset:3584\n"
"ds_read_b64_tr_b16 v[46:47], v11 offset:11776\n"
"v_mfma_f32_16x16x32_bf16 a[92:95], v[48:51], v[168:171], a[92:95]\n"
"s_add_u32 s83, s83, 7168\n"
"s_waitcnt lgkmcnt(10)\n"
"v_mfma_f32_16x16x32_bf16 a[32:35], v[20:23], v[160:163], a[32:35]\n"
"ds_read_b64_tr_b16 v[48:49], v12 offset:3584\n"
"ds_read_b64_tr_b16 v[50:51], v12 offset:11776\n"
"v_mfma_f32_16x16x32_bf16 a[96:99], v[20:23], v[168:171], a[96:99]\n"
"v_cmp_lt_f32 vcc, s73, v16\n"
"s_cbranch_vccnz L_rare_w3e\n"
"L_back_w3e:\n"
"v_mfma_f32_16x16x32_bf16 a[36:39], v[24:27], v[160:163], a[36:39]\n"
"ds_read_b64_tr_b16 v[20:21], v11 offset:16384\n"
"ds_read_b64_tr_b16 v[22:23], v11 offset:24576\n"
"v_mfma_f32_16x16x32_bf16 a[100:103], v[24:27], v[168:171], a[100:103]\n"
"s_add_u32 m0, s56, 3072\n"
"s_waitcnt lgkmcnt(10)\n"
"v_mfma_f32_16x16x32_bf16 a[40:43], v[28:31], v[160:163], a[40:43]\n"
"ds_read_b64_tr_b16 v[24:25], v12 offset:16384\n"
"ds_read_b64_tr_b16 v[26:27], v12 offset:24576\n"
"v_mfma_f32_16x16x32_bf16 a[104:107], v[28:31], v[168:171], a[104:107]\n"
"v_exp_f32 v112, v112\n"
"v_mfma_f32_16x16x32_bf16 a[44:47], v[32:35], v[160:163], a[44:47]\n"
"ds_read_b64_tr_b16 v[28:29], v11 offset:16896\n"
"ds_read_b64_tr_b16 v[30:31], v11 offset:25088\n"
"v_mfma_f32_16x16x32_bf16 a[108:111], v[32:35], v[168:171], a[108:111]\n"
"buffer_load_dwordx4 v204, s[48:51], s83 offen lds\n"
"s_waitcnt lgkmcnt(10)\n"
"v_mfma_f32_16x16x32_bf16 a[48:51], v[36:39], v[160:163], a[48:51]\n"
"ds_read_b64_tr_b16 v[32:33], v12 offset:16896\n"
"ds_read_b64_tr_b16 v[34:35], v12 offset:25088\n"
"v_mfma_f32_16x16x32_bf16 a[112:115], v[36:39], v[168:171], a[112:115]\n"
"v_exp_f32 v113, v113\n"
"v_mfma_f32_16x16x32_bf16 a[52:55], v[40:43], v[160:163], a[52:55]\n"
"ds_read_b64_tr_b16 v[36:37], v11 offset:17408\n"
"ds_read_b64_tr_b16 v[38:39], v11 offset:25600\n"
"v_mfma_f32_16x16x32_bf16 a[116:119], v[40:43], v[168:171], a[116:119]\n"
"s_add_u32 s83, s83, 7168\n"
"s_waitcnt lgkmcnt(10)\n"
"v_mfma_f32_16x16x32_bf16 a[56:59], v[44:47], v[160:163], a[56:59]\n"
"ds_read_b64_tr_b16 v[40:41], v12 offset:17408\n"
"ds_read_b64_tr_b16 v[42:43], v12 offset:25600\n"
"v_mfma_f32_16x16x32_bf16 a[120:123], v[44:47], v[168:171], a[120:123]\n"
"v_exp_f32 v114, v114\n"
"v_mfma_f32_16x16x32_bf16 a[60:63], v[48:51], v[160:163], a[60:63]\n"
"ds_read_b64_tr_b16 v[44:45], v11 offset:17920\n"
"ds_read_b64_tr_b16 v[46:47], v11 offset:26112\n"
"v_mfma_f32_16x16x32_bf16 a[124:127], v[48:51], v[168:171], a[124:127]\n"
"s_add_u32 m0, s56, 4096\n"
"s_waitcnt lgkmcnt(10)\n"
"v_mfma_f32_16x16x32_bf16 a[0:3], v[20:23], v[164:167], a[0:3]\n"
"ds_read_b64_tr_b16 v[48:49], v12 offset:17920\n"
"ds_read_b64_tr_b16 v[50:51], v12 offset:26112\n"
"v_mfma_f32_16x16x32_bf16 a[64:67], v[20:23], v[172:175], a[64:67]\n"
"v_exp_f32 v115, v115\n"
"v_mfma_f32_16x16x32_bf16 a[4:7], v[24:27], v[164:167], a[4:7]\n"
"ds_read_b64_tr_b16 v[20:21], v11 offset:18432\n"
"ds_read_b64_tr_b16 v[22:23], v11 offset:26624\n"
"v_mfma_f32_16x16x32_bf16 a[68:71], v[24:27], v[172:175], a[68:71]\n"
"v_exp_f32 v116, v116\n"
"v_mfma_f32_16x16x32_bf16 a[192:195], a[204:207], v[164:167], a[192:195]\n"
"buffer_load_dwordx4 v204, s[48:51], s83 offen lds\n"
"s_waitcnt lgkmcnt(10)\n"
"v_mfma_f32_16x16x32_bf16 a[8:11], v[28:31], v[164:167], a[8:11]\n"
"ds_read_b64_tr_b16 v[24:25], v12 offset:18432\n"
"ds_read_b64_tr_b16 v[26:27], v12 offset:26624\n"
"v_mfma_f32_16x16x32_bf16 a[72:75], v[28:31], v[172:175], a[72:75]\n"
"v_exp_f32 v117, v117\n"
"v_mfma_f32_16x16x32_bf16 a[196:199], a[204:207], v[172:175], a[196:199]\n"
"v_exp_f32 v118, v118\n"
"v_mfma_f32_16x16x32_bf16 a[12:15], v[32:35], v[164:167], a[12:15]\n"
"ds_read_b64_tr_b16 v[28:29], v11 offset:18944\n"
"ds_read_b64_tr_b16 v[30:31], v11 offset:27136\n"
"v_mfma_f32_16x16x32_bf16 a[76:79], v[32:35], v[172:175], a[76:79]\n"
"s_add_u32 s83, s83, 7168\n"
"s_waitcnt lgkmcnt(10)\n"
"v_mfma_f32_16x16x32_bf16 a[16:19], v[36:39], v[164:167], a[16:19]\n"
"ds_read_b64_tr_b16 v[32:33], v12 offset:18944\n"
"ds_read_b64_tr_b16 v[34:35], v12 offset:27136\n"
"v_mfma_f32_16x16x32_bf16 a[80:83], v[36:39], v[172:175], a[80:83]\n"
"v_exp_f32 v119, v119\n"
"v_mfma_f32_16x16x32_bf16 a[20:23], v[40:43], v[164:167], a[20:23]\n"
"ds_read_b64_tr_b16 v[36:37], v11 offset:19456\n"
"ds_read_b64_tr_b16 v[38:39], v11 offset:27648\n"
"v_mfma_f32_16x16x32_bf16 a[84:87], v[40:43], v[172:175], a[84:87]\n"
"s_add_u32 m0, s56, 5120\n"
"s_waitcnt lgkmcnt(10)\n"
"v_mfma_f32_16x16x32_bf16 a[24:27], v[44:47], v[164:167], a[24:27]\n"
"ds_read_b64_tr_b16 v[40:41], v12 offset:19456\n"
"ds_read_b64_tr_b16 v[42:43], v12 offset:27648\n"
"v_mfma_f32_16x16x32_bf16 a[88:91], v[44:47], v[172:175], a[88:91]\n"
"buffer_load_dwordx4 v204, s[48:51], s83 offen lds\n"
"s_add_u32 s83, s83, 7168\n"
"v_mfma_f32_16x16x32_bf16 a[28:31], v[48:51], v[164:167], a[28:31]\n"
"ds_read_b64_tr_b16 v[44:45], v11 offset:19968\n"
"ds_read_b64_tr_b16 v[46:47], v11 offset:28160\n"
"v_mfma_f32_16x16x32_bf16 a[92:95], v[48:51], v[172:175], a[92:95]\n"
"s_add_u32 m0, s56, 6144\n"
"s_waitcnt lgkmcnt(10)\n"
"v_mfma_f32_16x16x32_bf16 a[32:35], v[20:23], v[164:167], a[32:35]\n"
"ds_read_b64_tr_b16 v[48:49], v12 offset:19968\n"
"ds_read_b64_tr_b16 v[50:51], v12 offset:28160\n"
"v_mfma_f32_16x16x32_bf16 a[96:99], v[20:23], v[172:175], a[96:99]\n"
"buffer_load_dwordx4 v204, s[48:51], s83 offen lds\n"
"s_add_u32 s83, s83, 7168\n"
"v_mfma_f32_16x16x32_bf16 a[36:39], v[24:27], v[164:167], a[36:39]\n"
"s_add_u32 s57, s57, 0x8000\n"
"s_cmp_eq_u32 s57, s61\n"
"s_cselect_b32 s57, s60, s57\n"
"v_mfma_f32_16x16x32_bf16 a[100:103], v[24:27], v[172:175], a[100:103]\n"
"v_add_u32 v10, s57, %[klane]\n"
"s_add_u32 m0, s56, 7168\n"
"s_nop 0\n"
"buffer_load_dwordx4 v204, s[48:51], s83 offen lds\n"
"s_add_u32 s83, s83, 7168\n"
"s_mov_b32 s83, 0\n"
"s_add_u32 s48, s48, 128\n"
"s_addc_u32 s49, s49, 0\n"
"s_sub_u32 s50, s50, 128\n"
"s_add_u32 s56, s56, 0x8000\n"
"s_cmp_eq_u32 s56, s59\n"
"s_cselect_b32 s56, s58, s56\n"
"s_waitcnt lgkmcnt(8)\n"
"v_mfma_f32_16x16x32_bf16 a[40:43], v[28:31], v[164:167], a[40:43]\n"
"v_cvt_pk_bf16_f32 v160, v112, v113\n"
"v_cvt_pk_bf16_f32 v161, v114, v115\n"
"v_mfma_f32_16x16x32_bf16 a[104:107], v[28:31], v[172:175], a[104:107]\n"
"s_waitcnt vmcnt(28) lgkmcnt(0)\n"
"s_barrier\n"
"v_mfma_f32_16x16x32_bf16 a[44:47], v[32:35], v[164:167], a[44:47]\n"
"ds_read_b128 v[20:23], v10 offset:0\n"
"ds_read_b128 v[24:27], v10 offset:512\n"
"v_mfma_f32_16x16x32_bf16 a[108:111], v[32:35], v[172:175], a[108:111]\n"
"ds_read_b128 v[28:31], v10 offset:1024\n"
"v_cvt_pk_bf16_f32 v168, v116, v117\n"
"v_mfma_f32_16x16x32_bf16 a[48:51], v[36:39], v[164:167], a[48:51]\n"
"ds_read_b128 v[32:35], v10 offset:1536\n"
"v_cvt_pk_bf16_f32 v169, v118, v119\n"
"v_mfma_f32_16x16x32_bf16 a[112:115], v[36:39], v[172:175], a[112:115]\n"
"v_mfma_f32_16x16x32_bf16 a[52:55], v[40:43], v[164:167], a[52:55]\n"
"ds_read_b128 v[36:39], v10 offset:2048\n"
"v_mfma_f32_16x16x32_bf16 a[116:119], v[40:43], v[172:175], a[116:119]\n"
"v_mfma_f32_16x16x32_bf16 a[56:59], v[44:47], v[164:167], a[56:59]\n"
"ds_read_b128 v[40:43], v10 offset:2560\n"
"v_mfma_f32_16x16x32_bf16 a[120:123], v[44:47], v[172:175], a[120:123]\n"
"v_mfma_f32_16x16x32_bf16 a[60:63], v[48:51], v[164:167], a[60:63]\n"
"ds_read_b128 v[44:47], v10 offset:3072\n"
"v_mfma_f32_16x16x32_bf16 a[124:127], v[48:51], v[172:175], a[124:127]\n"
"s_cmp_lg_u32 s71, 0\n"
"s_cbranch_scc1 L_resc_w3e\n"
"L_rescback_w3e:\n"
"s_add_u32 m0, s56, 0\n"
"s_nop 0\n"
"buffer_load_dwordx4 v204, s[48:51], s83 offen lds\n"
"s_add_u32 s83, s83, 7168\n"
"s_add_u32 m0, s56, 1024\n"
"s_nop 0\n"
"buffer_load_dwordx4 v204, s[48:51], s83 offen lds\n"
"s_add_u32 s83, s83, 7168\n"
"s_add_u32 m0, s56, 2048\n"
"s_nop 0\n"
"buffer_load_dwordx4 v204, s[48:51], s83 offen lds\n"
"s_add_u32 s83, s83, 7168\n"
"s_add_u32 m0, s56, 3072\n"
"s_nop 0\n"
"buffer_load_dwordx4 v204, s[48:51], s83 offen lds\n"
"s_add_u32 s83, s83, 7168\n"
"s_add_u32 m0, s56, 4096\n"
"s_nop 0\n"
"buffer_load_dwordx4 v204, s[48:51], s83 offen lds\n"
"s_add_u32 s83, s83, 7168\n"
"s_add_u32 m0, s56, 5120\n"
"s_nop 0\n"
"buffer_load_dwordx4 v204, s[48:51], s83 offen lds\n"
"s_add_u32 s83, s83, 7168\n"
"s_add_u32 m0, s56, 6144\n"
"s_nop 0\n"
"buffer_load_dwordx4 v204, s[48:51], s83 offen lds\n"
"s_add_u32 s83, s83, 7168\n"
"s_add_u32 m0, s56, 7168\n"
"s_nop 0\n"
"buffer_load_dwordx4 v204, s[48:51], s83 offen lds\n"
"s_add_u32 s83, s83, 7168\n"
"s_mov_b32 s83, 0\n"
"s_add_u32 s48, s48, 128\n"
"s_addc_u32 s49, s49, 0\n"
"s_sub_u32 s50, s50, 128\n"
"s_add_u32 s56, s56, 0x8000\n"
"s_cmp_eq_u32 s56, s59\n"
"s_cselect_b32 s56, s58, s56\n"
"v_exp_f32 v124, v124\n"
"v_exp_f32 v125, v125\n"
"v_exp_f32 v126, v126\n"
"v_exp_f32 v127, v127\n"
"v_exp_f32 v128, v128\n"
"v_exp_f32 v129, v129\n"
"v_exp_f32 v130, v130\n"
"v_exp_f32 v131, v131\n"
"v_exp_f32 v136, v136\n"
"v_exp_f32 v137, v137\n"
"v_exp_f32 v138, v138\n"
"v_exp_f32 v139, v139\n"
"v_exp_f32 v140, v140\n"
"v_exp_f32 v141, v141\n"
"v_exp_f32 v142, v142\n"
"v_exp_f32 v143, v143\n"
"v_exp_f32 v148, v148\n"
"v_exp_f32 v149, v149\n"
"v_exp_f32 v150, v150\n"
"v_exp_f32 v151, v151\n"
"v_exp_f32 v152, v152\n"
"v_exp_f32 v153, v153\n"
"v_exp_f32 v154, v154\n"
"v_exp_f32 v155, v155\n"
"s_nop 0\n"
"v_cvt_pk_bf16_f32 v162, v124, v125\n"
"v_cvt_pk_bf16_f32 v163, v126, v127\n"
"v_cvt_pk_bf16_f32 v170, v128, v129\n"
"v_cvt_pk_bf16_f32 v171, v130, v131\n"
"v_cvt_pk_bf16_f32 v164, v136, v137\n"
"v_cvt_pk_bf16_f32 v165, v138, v139\n"
"v_cvt_pk_bf16_f32 v172, v140, v141\n"
"v_cvt_pk_bf16_f32 v173, v142, v143\n"
"v_cvt_pk_bf16_f32 v166, v148, v149\n"
"v_cvt_pk_bf16_f32 v167, v150, v151\n"
"v_cvt_pk_bf16_f32 v174, v152, v153\n"
"v_cvt_pk_bf16_f32 v175, v154, v155\n"
"s_waitcnt vmcnt(28) lgkmcnt(0)\n"
"s_barrier\n"
"s_add_u32 s57, s57, 0x8000\n"
"s_cmp_eq_u32 s57, s61\n"
"s_cselect_b32 s57, s60, s57\n"
"v_add_u32 v11, s57, %[vl0]\n"
"v_add_u32 v12, s57, %[vl1]\n"
"ds_read_b64_tr_b16 v[20:21], v11 offset:0\n"
"ds_read_b64_tr_b16 v[22:23], v11 offset:8192\n"
"ds_read_b64_tr_b16 v[24:25], v12 offset:0\n"
"ds_read_b64_tr_b16 v[26:27], v12 offset:8192\n"
"ds_read_b64_tr_b16 v[28:29], v11 offset:512\n"
"ds_read_b64_tr_b16 v[30:31], v11 offset:8704\n"
"ds_read_b64_tr_b16 v[32:33], v12 offset:512\n"
"ds_read_b64_tr_b16 v[34:35], v12 offset:8704\n"
"ds_read_b64_tr_b16 v[36:37], v11 offset:1024\n"
"ds_read_b64_tr_b16 v[38:39], v11 offset:9216\n"
"ds_read_b64_tr_b16 v[40:41], v12 offset:1024\n"
"ds_read_b64_tr_b16 v[42:43], v12 offset:9216\n"
"ds_read_b64_tr_b16 v[44:45], v11 offset:1536\n"
"ds_read_b64_tr_b16 v[46:47], v11 offset:9728\n"
"s_waitcnt lgkmcnt(10)\n"
"v_mfma_f32_16x16x32_bf16 a[0:3], v[20:23], v[160:163], a[0:3]\n"
"ds_read_b64_tr_b16 v[48:49], v12 offset:1536\n"
"ds_read_b64_tr_b16 v[50:51], v12 offset:9728\n"
"v_mfma_f32_16x16x32_bf16 a[64:67], v[20:23], v[168:171], a[64:67]\n"
"s_add_u32 m0, s56, 0\n"
"v_mfma_f32_16x16x32_bf16 a[4:7], v[24:27], v[160:163], a[4:7]\n"
"ds_read_b64_tr_b16 v[20:21], v11 offset:2048\n"
"ds_read_b64_tr_b16 v[22:23], v11 offset:10240\n"
"v_mfma_f32_16x16x32_bf16 a[68:71], v[24:27], v[168:171], a[68:71]\n"
"buffer_load_dwordx4 v204, s[48:51], s83 offen lds\n"
"v_mfma_f32_16x16x32_bf16 a[192:195], a[204:207], v[160:163], a[192:195]\n"
"s_add_u32 s83, s83, 7168\n"
"s_waitcnt lgkmcnt(10)\n"
"v_mfma_f32_16x16x32_bf16 a[8:11], v[28:31], v[160:163], a[8:11]\n"
"ds_read_b64_tr_b16 v[24:25], v12 offset:2048\n"
"ds_read_b64_tr_b16 v[26:27], v12 offset:10240\n"
"v_mfma_f32_16x16x32_bf16 a[72:75], v[28:31], v[168:171], a[72:75]\n"
"s_add_u32 m0, s56, 1024\n"
"v_mfma_f32_16x16x32_bf16 a[196:199], a[204:207], v[168:171], a[196:199]\n"
"v_mfma_f32_16x16x32_bf16 a[12:15], v[32:35], v[160:163], a[12:15]\n"
"ds_read_b64_tr_b16 v[28:29], v11 offset:2560\n"
"ds_read_b64_tr_b16 v[30:31], v11 offset:10752\n"
"v_mfma_f32_16x16x32_bf16 a[76:79], v[32:35], v[168:171], a[76:79]\n"
"buffer_load_dwordx4 v204, s[48:51], s83 offen lds\n"
"s_waitcnt lgkmcnt(10)\n"
"v_mfma_f32_16x16x32_bf16 a[16:19], v[36:39], v[160:163], a[16:19]\n"
"ds_read_b64_tr_b16 v[32:33], v12 offset:2560\n"
"ds_read_b64_tr_b16 v[34:35], v12 offset:10752\n"
"v_mfma_f32_16x16x32_bf16 a[80:83], v[36:39], v[168:171], a[80:83]\n"
"s_add_u32 s83, s83, 7168\n"
"v_mfma_f32_16x16x32_bf16 a[20:23], v[40:43], v[160:163], a[20:23]\n"
"ds_read_b64_tr_b16 v[36:37], v11 offset:3072\n"
"ds_read_b64_tr_b16 v[38:39], v11 offset:11264\n"
"v_mfma_f32_16x16x32_bf16 a[84:87], v[40:43], v[168:171], a[84:87]\n"
"s_add_u32 m0, s56, 2048\n"
"s_waitcnt lgkmcnt(10)\n"
"v_mfma_f32_16x16x32_bf16 a[24:27], v[44:47], v[160:163], a[24:27]\n"
"ds_read_b64_tr_b16 v[40:41], v12 offset:3072\n"
"ds_read_b64_tr_b16 v[42:43], v12 offset:11264\n"
"v_mfma_f32_16x16x32_bf16 a[88:91], v[44:47], v[168:171], a[88:91]\n"
"buffer_load_dwordx4 v204, s[48:51], s83 offen lds\n"
"v_mfma_f32_16x16x32_bf16 a[28:31], v[48:51], v[160:163], a[28:31]\n"
"ds_read_b64_tr_b16 v[44:45], v11 offset:3584\n"
"ds_read_b64_tr_b16 v[46:47], v11 offset:11776\n"
"v_mfma_f32_16x16x32_bf16 a[92:95], v[48:51], v[168:171], a[92:95]\n"
"s_add_u32 s83, s83, 7168\n"
"s_waitcnt lgkmcnt(10)\n"
"v_mfma_f32_16x16x32_bf16 a[32:35], v[20:23], v[160:163], a[32:35]\n"
"ds_read_b64_tr_b16 v[48:49], v12 offset:3584\n"
"ds_read_b64_tr_b16 v[50:51], v12 offset:11776\n"
"v_mfma_f32_16x16x32_bf16 a[96:99], v[20:23], v[168:171], a[96:99]\n"
"s_add_u32 m0, s56, 3072\n"
"v_mfma_f32_16x16x32_bf16 a[36:39], v[24:27], v[160:163], a[36:39]\n"
"ds_read_b64_tr_b16 v[20:21], v11 offset:16384\n"
"ds_read_b64_tr_b16 v[22:23], v11 offset:24576\n"
"v_mfma_f32_16x16x32_bf16 a[100:103], v[24:27], v[168:171], a[100:103]\n"
"buffer_load_dwordx4 v204, s[48:51], s83 offen lds\n"
"s_waitcnt lgkmcnt(10)\n"
"v_mfma_f32_16x16x32_bf16 a[40:43], v[28:31], v[160:163], a[40:43]\n"
"ds_read_b64_tr_b16 v[24:25], v12 offset:16384\n"
"ds_read_b64_tr_b16 v[26:27], v12 offset:24576\n"
"v_mfma_f32_16x16x32_bf16 a[104:107], v[28:31], v[168:171], a[104:107]\n"
"s_add_u32 s83, s83, 7168\n"
"v_mfma_f32_16x16x32_bf16 a[44:47], v[32:35], v[160:163], a[44:47]\n"
"ds_read_b64_tr_b16 v[28:29], v11 offset:16896\n"
"ds_read_b64_tr_b16 v[30:31], v11 offset:25088\n"
"v_mfma_f32_16x16x32_bf16 a[108:111], v[32:35], v[168:171], a[108:111]\n"
"s_add_u32 m0, s56, 4096\n"
"s_waitcnt lgkmcnt(10)\n"
"v_mfma_f32_16x16x32_bf16 a[48:51], v[36:39], v[160:163], a[48:51]\n"
"ds_read_b64_tr_b16 v[32:33], v12 offset:16896\n"
"ds_read_b64_tr_b16 v[34:35], v12 offset:25088\n"
"v_mfma_f32_16x16x32_bf16 a[112:115], v[36:39], v[168:171], a[112:115]\n"
"buffer_load_dwordx4 v204, s[48:51], s83 offen lds\n"
"v_mfma_f32_16x16x32_bf16 a[52:55], v[40:43], v[160:163], a[52:55]\n"
"ds_read_b64_tr_b16 v[36:37], v11 offset:17408\n"
"ds_read_b64_tr_b16 v[38:39], v11 offset:25600\n"
"v_mfma_f32_16x16x32_bf16 a[116:119], v[40:43], v[168:171], a[116:119]\n"
"s_add_u32 s83, s83, 7168\n"
"s_waitcnt lgkmcnt(10)\n"
"v_mfma_f32_16x16x32_bf16 a[56:59], v[44:47], v[160:163], a[56:59]\n"
"ds_read_b64_tr_b16 v[40:41], v12 offset:17408\n"
"ds_read_b64_tr_b16 v[42:43], v12 offset:25600\n"
"v_mfma_f32_16x16x32_bf16 a[120:123], v[44:47], v[168:171], a[120:123]\n"
"s_add_u32 m0, s56, 5120\n"
"v_mfma_f32_16x16x32_bf16 a[60:63], v[48:51], v[160:163], a[60:63]\n"
"ds_read_b64_tr_b16 v[44:45], v11 offset:17920\n"
"ds_read_b64_tr_b16 v[46:47], v11 offset:26112\n"
"v_mfma_f32_16x16x32_bf16 a[124:127], v[48:51], v[168:171], a[124:127]\n"
"buffer_load_dwordx4 v204, s[48:51], s83 offen lds\n"
"s_waitcnt lgkmcnt(10)\n"
"v_mfma_f32_16x16x32_bf16 a[0:3], v[20:23], v[164:167], a[0:3]\n"
"ds_read_b64_tr_b16 v[48:49], v12 offset:17920\n"
"ds_read_b64_tr_b16 v[50:51], v12 offset:26112\n"
"v_mfma_f32_16x16x32_bf16 a[64:67], v[20:23], v[172:175], a[64:67]\n"
"s_add_u32 s83, s83, 7168\n"
"v_mfma_f32_16x16x32_bf16 a[4:7], v[24:27], v[164:167], a[4:7]\n"
"ds_read_b64_tr_b16 v[20:21], v11 offset:18432\n"
"ds_read_b64_tr_b16 v[22:23], v11 offset:26624\n"
"v_mfma_f32_16x16x32_bf16 a[68:71], v[24:27], v[172:175], a[68:71]\n"
"s_add_u32 m0, s56, 6144\n"
"v_mfma_f32_16x16x32_bf16 a[192:195], a[204:207], v[164:167], a[192:195]\n"
"buffer_load_dwordx4 v204, s[48:51], s83 offen lds\n"
"s_waitcnt lgkmcnt(10)\n"
"v_mfma_f32_16x16x32_bf16 a[8:11], v[28:31], v[164:167], a[8:11]\n"
"ds_read_b64_tr_b16 v[24:25], v12 offset:18432\n"
"ds_read_b64_tr_b16 v[26:27], v12 offset:26624\n"
"v_mfma_f32_16x16x32_bf16 a[72:75], v[28:31], v[172:175], a[72:75]\n"
"s_add_u32 s83, s83, 7168\n"
"v_mfma_f32_16x16x32_bf16 a[196:199], a[204:207], v[172:175], a[196:199]\n"
"v_mfma_f32_16x16x32_bf16 a[12:15], v[32:35], v[164:167], a[12:15]\n"
"ds_read_b64_tr_b16 v[28:29], v11 offset:18944\n"
"ds_read_b64_tr_b16 v[30:31], v11 offset:27136\n"
"v_mfma_f32_16x16x32_bf16 a[76:79], v[32:35], v[172:175], a[76:79]\n"
"s_add_u32 m0, s56, 7168\n"
"s_waitcnt lgkmcnt(10)\n"
"v_mfma_f32_16x16x32_bf16 a[16:19], v[36:39], v[164:167], a[16:19]\n"
"ds_read_b64_tr_b16 v[32:33], v12 offset:18944\n"
"ds_read_b64_tr_b16 v[34:35], v12 offset:27136\n"
"v_mfma_f32_16x16x32_bf16 a[80:83], v[36:39], v[172:175], a[80:83]\n"
"buffer_load_dwordx4 v204, s[48:51], s83 offen lds\n"
"v_mfma_f32_16x16x32_bf16 a[20:23], v[40:43], v[164:167], a[20:23]\n"
"ds_read_b64_tr_b16 v[36:37], v11 offset:19456\n"
"ds_read_b64_tr_b16 v[38:39], v11 offset:27648\n"
"v_mfma_f32_16x16x32_bf16 a[84:87], v[40:43], v[172:175], a[84:87]\n"
"s_add_u32 s83, s83, 7168\n"
"s_waitcnt lgkmcnt(10)\n"
"v_mfma_f32_16x16x32_bf16 a[24:27], v[44:47], v[164:167], a[24:27]\n"
"ds_read_b64_tr_b16 v[40:41], v12 offset:19456\n"
"ds_read_b64_tr_b16 v[42:43], v12 offset:27648\n"
"v_mfma_f32_16x16x32_bf16 a[88:91], v[44:47], v[172:175], a[88:91]\n"
"s_mov_b32 s83, 0\n"
"s_add_u32 s48, s48, 128\n"
"s_addc_u32 s49, s49, 0\n"
"v_mfma_f32_16x16x32_bf16 a[28:31], v[48:51], v[164:167], a[28:31]\n"
"ds_read_b64_tr_b16 v[44:45], v11 offset:19968\n"
"ds_read_b64_tr_b16 v[46:47], v11 offset:28160\n"
"v_mfma_f32_16x16x32_bf16 a[92:95], v[48:51], v[172:175], a[92:95]\n"
"s_sub_u32 s50, s50, 128\n"
"s_waitcnt lgkmcnt(10)\n"
"v_mfma_f32_16x16x32_bf16 a[32:35], v[20:23], v[164:167], a[32:35]\n"
"ds_read_b64_tr_b16 v[48:49], v12 offset:19968\n"
"ds_read_b64_tr_b16 v[50:51], v12 offset:28160\n"
"v_mfma_f32_16x16x32_bf16 a[96:99], v[20:23], v[172:175], a[96:99]\n"
"s_add_u32 s56, s56, 0x8000\n"
"s_cmp_eq_u32 s56, s59\n"
"s_cselect_b32 s56, s58, s56\n"
"v_mfma_f32_16x16x32_bf16 a[36:39], v[24:27], v[164:167], a[36:39]\n"
"s_add_u32 s57, s57, 0x8000\n"
"s_cmp_eq_u32 s57, s61\n"
"s_cselect_b32 s57, s60, s57\n"
"v_mfma_f32_16x16x32_bf16 a[100:103], v[24:27], v[172:175], a[100:103]\n"
"v_add_u32 v11, s57, v202\n"
"v_add_u32 v12, s57, v203\n"
"s_waitcnt lgkmcnt(8)\n"
"v_mfma_f32_16x16x32_bf16 a[40:43], v[28:31], v[164:167], a[40:43]\n"
"v_mfma_f32_16x16x32_bf16 a[104:107], v[28:31], v[172:175], a[104:107]\n"
"s_waitcnt vmcnt(24) lgkmcnt(0)\n"
"s_barrier\n"
"v_mfma_f32_16x16x32_bf16 a[44:47], v[32:35], v[164:167], a[44:47]\n"
"ds_read_b64_tr_b16 v[20:21], v11 offset:0\n"
"ds_read_b64_tr_b16 v[22:23], v11 offset:2048\n"
"v_mfma_f32_16x16x32_bf16 a[108:111], v[32:35], v[172:175], a[108:111]\n"
"ds_read_b64_tr_b16 v[24:25], v11 offset:4096\n"
"ds_read_b64_tr_b16 v[26:27], v11 offset:6144\n"
"ds_read_b64_tr_b16 v[28:29], v11 offset:8192\n"
"ds_read_b64_tr_b16 v[30:31], v11 offset:10240\n"
"v_mfma_f32_16x16x32_bf16 a[48:51], v[36:39], v[164:167], a[48:51]\n"
"ds_read_b64_tr_b16 v[32:33], v11 offset:12288\n"
"ds_read_b64_tr_b16 v[34:35], v11 offset:14336\n"
"v_mfma_f32_16x16x32_bf16 a[112:115], v[36:39], v[172:175], a[112:115]\n"
"v_mfma_f32_16x16x32_bf16 a[52:55], v[40:43], v[164:167], a[52:55]\n"
"ds_read_b64_tr_b16 v[36:37], v11 offset:16384\n"
"ds_read_b64_tr_b16 v[38:39], v11 offset:18432\n"
"v_mfma_f32_16x16x32_bf16 a[116:119], v[40:43], v[172:175], a[116:119]\n"
"v_mfma_f32_16x16x32_bf16 a[56:59], v[44:47], v[164:167], a[56:59]\n"
"ds_read_b64_tr_b16 v[40:41], v11 offset:20480\n"
"ds_read_b64_tr_b16 v[42:43], v11 offset:22528\n"
"v_mfma_f32_16x16x32_bf16 a[120:123], v[44:47], v[172:175], a[120:123]\n"
"v_mfma_f32_16x16x32_bf16 a[60:63], v[48:51], v[164:167], a[60:63]\n"
"ds_read_b64_tr_b16 v[44:45], v11 offset:24576\n"
"ds_read_b64_tr_b16 v[46:47], v11 offset:26624\n"
"v_mfma_f32_16x16x32_bf16 a[124:127], v[48:51], v[172:175], a[124:127]\n"
"s_nop 7\n"
"v_accvgpr_read_b32 v184, a192\n"
"v_accvgpr_read_b32 v185, a196\n"
"v_rcp_f32 v184, v184\n"
"v_rcp_f32 v185, v185\n"
"s_mov_b64 s[64:65], %[linv]\n"
"s_mov_b32 s66, 704\n"
"s_mov_b32 s67, 0x00020000\n"
"s_mul_i32 s68, %[wave], 192\n"
"v_lshlrev_b32 v187, 2, v200\n"
"s_mov_b64 exec, 0xffff\n"
"buffer_store_dword v184, v187, s[64:67], s68 offen offset:0 sc1\n"
"buffer_store_dword v185, v187, s[64:67], s68 offen offset:64 sc1\n"
"s_mov_b64 exec, -1\n"
"v_accvgpr_read_b32 v160, a0\n"
"v_accvgpr_read_b32 v161, a1\n"
"v_accvgpr_read_b32 v162, a2\n"
"v_accvgpr_read_b32 v163, a3\n"
"v_accvgpr_read_b32 v164, a4\n"
"v_accvgpr_read_b32 v165, a5\n"
"v_accvgpr_read_b32 v166, a6\n"
"v_accvgpr_read_b32 v167, a7\n"
"v_cvt_pk_bf16_f32 v64, v160, v161\n"
"v_cvt_pk_bf16_f32 v65, v162, v163\n"
"v_cvt_pk_bf16_f32 v66, v164, v165\n"
"v_cvt_pk_bf16_f32 v67, v166, v167\n"
"v_accvgpr_read_b32 v168, a64\n"
"v_accvgpr_read_b32 v169, a65\n"
"v_accvgpr_read_b32 v170, a66\n"
"v_accvgpr_read_b32 v171, a67\n"
"v_accvgpr_read_b32 v172, a68\n"
"v_accvgpr_read_b32 v173, a69\n"
"v_accvgpr_read_b32 v174, a70\n"
"v_accvgpr_read_b32 v175, a71\n"
"v_cvt_pk_bf16_f32 v96, v168, v169\n"
"v_cvt_pk_bf16_f32 v97, v170, v171\n"
"v_cvt_pk_bf16_f32 v98, v172, v173\n"
"v_cvt_pk_bf16_f32 v99, v174, v175\n"
"v_accvgpr_read_b32 v176, a8\n"
"v_accvgpr_read_b32 v177, a9\n"
"v_accvgpr_read_b32 v178, a10\n"
"v_accvgpr_read_b32 v179, a11\n"
"v_accvgpr_read_b32 v180, a12\n"
"v_accvgpr_read_b32 v181, a13\n"
"v_accvgpr_read_b32 v182, a14\n"
"v_accvgpr_read_b32 v183, a15\n"
"v_cvt_pk_bf16_f32 v68, v176, v177\n"
"v_cvt_pk_bf16_f32 v69, v178, v179\n"
"v_cvt_pk_bf16_f32 v70, v180, v181\n"
"v_cvt_pk_bf16_f32 v71, v182, v183\n"
"v_accvgpr_read_b32 v160, a72\n"
"v_accvgpr_read_b32 v161, a73\n"
"v_accvgpr_read_b32 v162, a74\n"
"v_accvgpr_read_b32 v163, a75\n"
"v_accvgpr_read_b32 v164, a76\n"
"v_accvgpr_read_b32 v165, a77\n"
"v_accvgpr_read_b32 v166, a78\n"
"v_accvgpr_read_b32 v167, a79\n"
"v_cvt_pk_bf16_f32 v100, v160, v161\n"
"v_cvt_pk_bf16_f32 v101, v162, v163\n"
"v_cvt_pk_bf16_f32 v102, v164, v165\n"
"v_cvt_pk_bf16_f32 v103, v166, v167\n"
"v_accvgpr_read_b32 v168, a16\n"
"v_accvgpr_read_b32 v169, a17\n"
"v_accvgpr_read_b32 v170, a18\n"
"v_accvgpr_read_b32 v171, a19\n"
"v_accvgpr_read_b32 v172, a20\n"
"v_accvgpr_read_b32 v173, a21\n"
"v_accvgpr_read_b32 v174, a22\n"
"v_accvgpr_read_b32 v175, a23\n"
"v_cvt_pk_bf16_f32 v72, v168, v169\n"
"v_cvt_pk_bf16_f32 v73, v170, v171\n"
"v_cvt_pk_bf16_f32 v74, v172, v173\n"
"v_cvt_pk_bf16_f32 v75, v174, v175\n"
"v_accvgpr_read_b32 v176, a80\n"
"v_accvgpr_read_b32 v177, a81\n"
"v_accvgpr_read_b32 v178, a82\n"
"v_accvgpr_read_b32 v179, a83\n"
"v_accvgpr_read_b32 v180, a84\n"
"v_accvgpr_read_b32 v181, a85\n"
"v_accvgpr_read_b32 v182, a86\n"
"v_accvgpr_read_b32 v183, a87\n"
"v_cvt_pk_bf16_f32 v104, v176, v177\n"
"v_cvt_pk_bf16_f32 v105, v178, v179\n"
"v_cvt_pk_bf16_f32 v106, v180, v181\n"
"v_cvt_pk_bf16_f32 v107, v182, v183\n"
"v_accvgpr_read_b32 v160, a24\n"
"v_accvgpr_read_b32 v161, a25\n"
"v_accvgpr_read_b32 v162, a26\n"
"v_accvgpr_read_b32 v163, a27\n"
"v_accvgpr_read_b32 v164, a28\n"
"v_accvgpr_read_b32 v165, a29\n"
"v_accvgpr_read_b32 v166, a30\n"
"v_accvgpr_read_b32 v167, a31\n"
"v_cvt_pk_bf16_f32 v76, v160, v161\n"
"v_cvt_pk_bf16_f32 v77, v162, v163\n"
"v_cvt_pk_bf16_f32 v78, v164, v165\n"
"v_cvt_pk_bf16_f32 v79, v166, v167\n"
"v_accvgpr_read_b32 v168, a88\n"
"v_accvgpr_read_b32 v169, a89\n"
"v_accvgpr_read_b32 v170, a90\n"
"v_accvgpr_read_b32 v171, a91\n"
"v_accvgpr_read_b32 v172, a92\n"
"v_accvgpr_read_b32 v173, a93\n"
"v_accvgpr_read_b32 v174, a94\n"
"v_accvgpr_read_b32 v175, a95\n"
"v_cvt_pk_bf16_f32 v108, v168, v169\n"
"v_cvt_pk_bf16_f32 v109, v170, v171\n"
"v_cvt_pk_bf16_f32 v110, v172, v173\n"
"v_cvt_pk_bf16_f32 v111, v174, v175\n"
"v_accvgpr_read_b32 v176, a32\n"
"v_accvgpr_read_b32 v177, a33\n"
"v_accvgpr_read_b32 v178, a34\n"
"v_accvgpr_read_b32 v179, a35\n"
"v_accvgpr_read_b32 v180, a36\n"
"v_accvgpr_read_b32 v181, a37\n"
"v_accvgpr_read_b32 v182, a38\n"
"v_accvgpr_read_b32 v183, a39\n"
"v_cvt_pk_bf16_f32 v80, v176, v177\n"
"v_cvt_pk_bf16_f32 v81, v178, v179\n"
"v_cvt_pk_bf16_f32 v82, v180, v181\n"
"v_cvt_pk_bf16_f32 v83, v182, v183\n"
"v_accvgpr_read_b32 v160, a96\n"
"v_accvgpr_read_b32 v161, a97\n"
"v_accvgpr_read_b32 v162, a98\n"
"v_accvgpr_read_b32 v163, a99\n"
"v_accvgpr_read_b32 v164, a100\n"
"v_accvgpr_read_b32 v165, a101\n"
"v_accvgpr_read_b32 v166, a102\n"
"v_accvgpr_read_b32 v167, a103\n"
"v_cvt_pk_bf16_f32 v208, v160, v161\n"
"v_cvt_pk_bf16_f32 v209, v162, v163\n"
"v_cvt_pk_bf16_f32 v210, v164, v165\n"
"v_cvt_pk_bf16_f32 v211, v166, v167\n"
"v_accvgpr_read_b32 v168, a40\n"
"v_accvgpr_read_b32 v169, a41\n"
"v_accvgpr_read_b32 v170, a42\n"
"v_accvgpr_read_b32 v171, a43\n"
"v_accvgpr_read_b32 v172, a44\n"
"v_accvgpr_read_b32 v173, a45\n"
"v_accvgpr_read_b32 v174, a46\n"
"v_accvgpr_read_b32 v175, a47\n"
"v_cvt_pk_bf16_f32 v84, v168, v169\n"
"v_cvt_pk_bf16_f32 v85, v170, v171\n"
"v_cvt_pk_bf16_f32 v86, v172, v173\n"
"v_cvt_pk_bf16_f32 v87, v174, v175\n"
"v_accvgpr_read_b32 v176, a104\n"
"v_accvgpr_read_b32 v177, a105\n"
"v_accvgpr_read_b32 v178, a106\n"
"v_accvgpr_read_b32 v179, a107\n"
"v_accvgpr_read_b32 v180, a108\n"
"v_accvgpr_read_b32 v181, a109\n"
"v_accvgpr_read_b32 v182, a110\n"
"v_accvgpr_read_b32 v183, a111\n"
"v_cvt_pk_bf16_f32 v212, v176, v177\n"
"v_cvt_pk_bf16_f32 v213, v178, v179\n"
"v_cvt_pk_bf16_f32 v214, v180, v181\n"
"v_cvt_pk_bf16_f32 v215, v182, v183\n"
"v_accvgpr_read_b32 v160, a48\n"
"v_accvgpr_read_b32 v161, a49\n"
"v_accvgpr_read_b32 v162, a50\n"
"v_accvgpr_read_b32 v163, a51\n"
"v_accvgpr_read_b32 v164, a52\n"
"v_accvgpr_read_b32 v165, a53\n"
"v_accvgpr_read_b32 v166, a54\n"
"v_accvgpr_read_b32 v167, a55\n"
"v_cvt_pk_bf16_f32 v88, v160, v161\n"
"v_cvt_pk_bf16_f32 v89, v162, v163\n"
"v_cvt_pk_bf16_f32 v90, v164, v165\n"
"v_cvt_pk_bf16_f32 v91, v166, v167\n"
"v_accvgpr_read_b32 v168, a112\n"
"v_accvgpr_read_b32 v169, a113\n"
"v_accvgpr_read_b32 v170, a114\n"
"v_accvgpr_read_b32 v171, a115\n"
"v_accvgpr_read_b32 v172, a116\n"
"v_accvgpr_read_b32 v173, a117\n"
"v_accvgpr_read_b32 v174, a118\n"
"v_accvgpr_read_b32 v175, a119\n"
"v_cvt_pk_bf16_f32 v216, v168, v169\n"
"v_cvt_pk_bf16_f32 v217, v170, v171\n"
"v_cvt_pk_bf16_f32 v218, v172, v173\n"
"v_cvt_pk_bf16_f32 v219, v174, v175\n"
"v_accvgpr_read_b32 v176, a56\n"
"v_accvgpr_read_b32 v177, a57\n"
"v_accvgpr_read_b32 v178, a58\n"
"v_accvgpr_read_b32 v179, a59\n"
"v_accvgpr_read_b32 v180, a60\n"
"v_accvgpr_read_b32 v181, a61\n"
"v_accvgpr_read_b32 v182, a62\n"
"v_accvgpr_read_b32 v183, a63\n"
"v_cvt_pk_bf16_f32 v92, v176, v177\n"
"v_cvt_pk_bf16_f32 v93, v178, v179\n"
"v_cvt_pk_bf16_f32 v94, v180, v181\n"
"v_cvt_pk_bf16_f32 v95, v182, v183\n"
"v_accvgpr_read_b32 v160, a120\n"
"v_accvgpr_read_b32 v161, a121\n"
"v_accvgpr_read_b32 v162, a122\n"
"v_accvgpr_read_b32 v163, a123\n"
"v_accvgpr_read_b32 v164, a124\n"
"v_accvgpr_read_b32 v165, a125\n"
"v_accvgpr_read_b32 v166, a126\n"
"v_accvgpr_read_b32 v167, a127\n"
"v_cvt_pk_bf16_f32 v220, v160, v161\n"
"v_cvt_pk_bf16_f32 v221, v162, v163\n"
"v_cvt_pk_bf16_f32 v222, v164, v165\n"
"v_cvt_pk_bf16_f32 v223, v166, v167\n"
"s_nop 1\n"
"s_waitcnt lgkmcnt(10)\n"
"v_mfma_f32_16x16x32_bf16 v[112:115], v[20:23], v[64:67], 0\n"
"ds_read_b64_tr_b16 v[48:49], v11 offset:28672\n"
"ds_read_b64_tr_b16 v[50:51], v11 offset:30720\n"
"v_mfma_f32_16x16x32_bf16 v[116:119], v[20:23], v[96:99], 0\n"
"s_add_u32 m0, s56, 0\n"
"v_mfma_f32_16x16x32_bf16 v[112:115], v[24:27], v[68:71], v[112:115]\n"
"ds_read_b64_tr_b16 v[20:21], v12 offset:0\n"
"ds_read_b64_tr_b16 v[22:23], v12 offset:2048\n"
"v_mfma_f32_16x16x32_bf16 v[116:119], v[24:27], v[100:103], v[116:119]\n"
"buffer_load_dwordx4 v204, s[48:51], s83 offen lds\n"
"s_waitcnt lgkmcnt(10)\n"
"v_mfma_f32_16x16x32_bf16 v[112:115], v[28:31], v[72:75], v[112:115]\n"
"ds_read_b64_tr_b16 v[24:25], v12 offset:4096\n"
"ds_read_b64_tr_b16 v[26:27], v12 offset:6144\n"
"v_mfma_f32_16x16x32_bf16 v[116:119], v[28:31], v[104:107], v[116:119]\n"
"s_add_u32 s83, s83, 7168\n"
"s_add_u32 m0, s56, 1024\n"
"v_mfma_f32_16x16x32_bf16 v[112:115], v[32:35], v[76:79], v[112:115]\n"
"ds_read_b64_tr_b16 v[28:29], v12 offset:8192\n"
"ds_read_b64_tr_b16 v[30:31], v12 offset:10240\n"
"v_mfma_f32_16x16x32_bf16 v[116:119], v[32:35], v[108:111], v[116:119]\n"
"buffer_load_dwordx4 v204, s[48:51], s83 offen lds\n"
"s_waitcnt lgkmcnt(10)\n"
"v_mfma_f32_16x16x32_bf16 v[112:115], v[36:39], v[80:83], v[112:115]\n"
"ds_read_b64_tr_b16 v[32:33], v12 offset:12288\n"
"ds_read_b64_tr_b16 v[34:35], v12 offset:14336\n"
"v_mfma_f32_16x16x32_bf16 v[116:119], v[36:39], v[208:211], v[116:119]\n"
"s_add_u32 s83, s83, 7168\n"
"s_add_u32 m0, s56, 2048\n"
"v_mfma_f32_16x16x32_bf16 v[112:115], v[40:43], v[84:87], v[112:115]\n"
"ds_read_b64_tr_b16 v[36:37], v12 offset:16384\n"
"ds_read_b64_tr_b16 v[38:39], v12 offset:18432\n"
"v_mfma_f32_16x16x32_bf16 v[116:119], v[40:43], v[212:215], v[116:119]\n"
"buffer_load_dwordx4 v204, s[48:51], s83 offen lds\n"
"s_waitcnt lgkmcnt(10)\n"
"v_mfma_f32_16x16x32_bf16 v[112:115], v[44:47], v[88:91], v[112:115]\n"
"ds_read_b64_tr_b16 v[40:41], v12 offset:20480\n"
"ds_read_b64_tr_b16 v[42:43], v12 offset:22528\n"
"v_mfma_f32_16x16x32_bf16 v[116:119], v[44:47], v[216:219], v[116:119]\n"
"s_add_u32 s83, s83, 7168\n"
"s_add_u32 m0, s56, 3072\n"
"v_mfma_f32_16x16x32_bf16 v[112:115], v[48:51], v[92:95], v[112:115]\n"
"ds_read_b64_tr_b16 v[44:45], v12 offset:24576\n"
"ds_read_b64_tr_b16 v[46:47], v12 offset:26624\n"
"v_mfma_f32_16x16x32_bf16 v[116:119], v[48:51], v[220:223], v[116:119]\n"
"buffer_load_dwordx4 v204, s[48:51], s83 offen lds\n"
"s_waitcnt lgkmcnt(10)\n"
"v_mfma_f32_16x16x32_bf16 v[124:127], v[20:23], v[64:67], 0\n"
"ds_read_b64_tr_b16 v[48:49], v12 offset:28672\n"
"ds_read_b64_tr_b16 v[50:51], v12 offset:30720\n"
"v_mfma_f32_16x16x32_bf16 v[128:131], v[20:23], v[96:99], 0\n"
"s_add_u32 s83, s83, 7168\n"
"s_add_u32 m0, s56, 4096\n"
"v_mfma_f32_16x16x32_bf16 v[124:127], v[24:27], v[68:71], v[124:127]\n"
"ds_read_b64_tr_b16 v[20:21], v11 offset:512\n"
"ds_read_b64_tr_b16 v[22:23], v11 offset:2560\n"
"v_mfma_f32_16x16x32_bf16 v[128:131], v[24:27], v[100:103], v[128:131]\n"
"buffer_load_dwordx4 v204, s[48:51], s83 offen lds\n"
"s_waitcnt lgkmcnt(10)\n"
"v_mfma_f32_16x16x32_bf16 v[124:127], v[28:31], v[72:75], v[124:127]\n"
"ds_read_b64_tr_b16 v[24:25], v11 offset:4608\n"
"ds_read_b64_tr_b16 v[26:27], v11 offset:6656\n"
"v_mfma_f32_16x16x32_bf16 v[128:131], v[28:31], v[104:107], v[128:131]\n"
"s_add_u32 s83, s83, 7168\n"
"s_add_u32 m0, s56, 5120\n"
"v_mfma_f32_16x16x32_bf16 v[124:127], v[32:35], v[76:79], v[124:127]\n"
"ds_read_b64_tr_b16 v[28:29], v11 offset:8704\n"
"ds_read_b64_tr_b16 v[30:31], v11 offset:10752\n"
"v_mfma_f32_16x16x32_bf16 v[128:131], v[32:35], v[108:111], v[128:131]\n"
"buffer_load_dwordx4 v204, s[48:51], s83 offen lds\n"
"s_waitcnt lgkmcnt(10)\n"
"v_mfma_f32_16x16x32_bf16 v[124:127], v[36:39], v[80:83], v[124:127]\n"
"ds_read_b64_tr_b16 v[32:33], v11 offset:12800\n"
"ds_read_b64_tr_b16 v[34:35], v11 offset:14848\n"
"v_mfma_f32_16x16x32_bf16 v[128:131], v[36:39], v[208:211], v[128:131]\n"
"s_add_u32 s83, s83, 7168\n"
"s_add_u32 m0, s56, 6144\n"
"v_mfma_f32_16x16x32_bf16 v[124:127], v[40:43], v[84:87], v[124:127]\n"
"ds_read_b64_tr_b16 v[36:37], v11 offset:16896\n"
"ds_read_b64_tr_b16 v[38:39], v11 offset:18944\n"
"v_mfma_f32_16x16x32_bf16 v[128:131], v[40:43], v[212:215], v[128:131]\n"
"buffer_load_dwordx4 v204, s[48:51], s83 offen lds\n"
"s_waitcnt lgkmcnt(10)\n"
"v_mfma_f32_16x16x32_bf16 v[124:127], v[44:47], v[88:91], v[124:127]\n"
"ds_read_b64_tr_b16 v[40:41], v11 offset:20992\n"
"ds_read_b64_tr_b16 v[42:43], v11 offset:23040\n"
"v_mfma_f32_16x16x32_bf16 v[128:131], v[44:47], v[216:219], v[128:131]\n"
"s_add_u32 s83, s83, 7168\n"
"s_add_u32 m0, s56, 7168\n"
"v_mfma_f32_16x16x32_bf16 v[124:127], v[48:51], v[92:95], v[124:127]\n"
"ds_read_b64_tr_b16 v[44:45], v11 offset:25088\n"
"ds_read_b64_tr_b16 v[46:47], v11 offset:27136\n"
"v_mfma_f32_16x16x32_bf16 v[128:131], v[48:51], v[220:223], v[128:131]\n"
"buffer_load_dwordx4 v204, s[48:51], s83 offen lds\n"
"s_waitcnt lgkmcnt(10)\n"
"v_mfma_f32_16x16x32_bf16 v[136:139], v[20:23], v[64:67], 0\n"
"ds_read_b64_tr_b16 v[48:49], v11 offset:29184\n"
"ds_read_b64_tr_b16 v[50:51], v11 offset:31232\n"
"v_mfma_f32_16x16x32_bf16 v[140:143], v[20:23], v[96:99], 0\n"
"s_add_u32 s83, s83, 7168\n"
"v_mfma_f32_16x16x32_bf16 v[136:139], v[24:27], v[68:71], v[136:139]\n"
"ds_read_b64_tr_b16 v[20:21], v12 offset:512\n"
"ds_read_b64_tr_b16 v[22:23], v12 offset:2560\n"
"v_mfma_f32_16x16x32_bf16 v[140:143], v[24:27], v[100:103], v[140:143]\n"
"v_cvt_pk_bf16_f32 v160, v112, v113\n"
"s_waitcnt lgkmcnt(10)\n"
"v_mfma_f32_16x16x32_bf16 v[136:139], v[28:31], v[72:75], v[136:139]\n"
"ds_read_b64_tr_b16 v[24:25], v12 offset:4608\n"
"ds_read_b64_tr_b16 v[26:27], v12 offset:6656\n"
"v_mfma_f32_16x16x32_bf16 v[140:143], v[28:31], v[104:107], v[140:143]\n"
"s_mov_b32 s83, 0\n"
"s_add_u32 s48, s48, 128\n"
"s_addc_u32 s49, s49, 0\n"
"v_mfma_f32_16x16x32_bf16 v[136:139], v[32:35], v[76:79], v[136:139]\n"
"ds_read_b64_tr_b16 v[28:29], v12 offset:8704\n"
"ds_read_b64_tr_b16 v[30:31], v12 offset:10752\n"
"v_mfma_f32_16x16x32_bf16 v[140:143], v[32:35], v[108:111], v[140:143]\n"
"s_sub_u32 s50, s50, 128\n"
"s_waitcnt lgkmcnt(10)\n"
"v_mfma_f32_16x16x32_bf16 v[136:139], v[36:39], v[80:83], v[136:139]\n"
"ds_read_b64_tr_b16 v[32:33], v12 offset:12800\n"
"ds_read_b64_tr_b16 v[34:35], v12 offset:14848\n"
"v_mfma_f32_16x16x32_bf16 v[140:143], v[36:39], v[208:211], v[140:143]\n"
"s_add_u32 s56, s56, 0x8000\n"
"s_cmp_eq_u32 s56, s59\n"
"s_cselect_b32 s56, s58, s56\n"
"v_mfma_f32_16x16x32_bf16 v[136:139], v[40:43], v[84:87], v[136:139]\n"
"ds_read_b64_tr_b16 v[36:37], v12 offset:16896\n"
"ds_read_b64_tr_b16 v[38:39], v12 offset:18944\n"
"v_mfma_f32_16x16x32_bf16 v[140:143], v[40:43], v[212:215], v[140:143]\n"
"v_cvt_pk_bf16_f32 v161, v114, v115\n"
"s_waitcnt lgkmcnt(10)\n"
"v_mfma_f32_16x16x32_bf16 v[136:139], v[44:47], v[88:91], v[136:139]\n"
"ds_read_b64_tr_b16 v[40:41], v12 offset:20992\n"
"ds_read_b64_tr_b16 v[42:43], v12 offset:23040\n"
"v_mfma_f32_16x16x32_bf16 v[140:143], v[44:47], v[216:219], v[140:143]\n"
"v_cvt_pk_bf16_f32 v162, v124, v125\n"
"v_cvt_pk_bf16_f32 v163, v126, v127\n"
"v_mfma_f32_16x16x32_bf16 v[136:139], v[48:51], v[92:95], v[136:139]\n"
"ds_read_b64_tr_b16 v[44:45], v12 offset:25088\n"
"ds_read_b64_tr_b16 v[46:47], v12 offset:27136\n"
"v_mfma_f32_16x16x32_bf16 v[140:143], v[48:51], v[220:223], v[140:143]\n"
"v_permlane16_swap_b32 v160, v162\n"
"s_waitcnt lgkmcnt(10)\n"
"v_mfma_f32_16x16x32_bf16 v[148:151], v[20:23], v[64:67], 0\n"
"ds_read_b64_tr_b16 v[48:49], v12 offset:29184\n"
"ds_read_b64_tr_b16 v[50:51], v12 offset:31232\n"
"v_mfma_f32_16x16x32_bf16 v[152:155], v[20:23], v[96:99], 0\n"
"v_permlane16_swap_b32 v161, v163\n"
"buffer_store_dwordx4 v[160:163], v201, s[52:55], s77 offen offset:0 sc1\n"
"v_mfma_f32_16x16x32_bf16 v[148:151], v[24:27], v[68:71], v[148:151]\n"
"s_add_u32 s57, s57, 0x8000\n"
"s_cmp_eq_u32 s57, s61\n"
"s_cselect_b32 s57, s60, s57\n"
"v_mfma_f32_16x16x32_bf16 v[152:155], v[24:27], v[100:103], v[152:155]\n"
"v_add_u32 v11, s57, v202\n"
"v_add_u32 v12, s57, v203\n"
"s_waitcnt lgkmcnt(8)\n"
"v_mfma_f32_16x16x32_bf16 v[148:151], v[28:31], v[72:75], v[148:151]\n"
"v_cvt_pk_bf16_f32 v168, v116, v117\n"
"v_cvt_pk_bf16_f32 v169, v118, v119\n"
"v_mfma_f32_16x16x32_bf16 v[152:155], v[28:31], v[104:107], v[152:155]\n"
"s_waitcnt vmcnt(27) lgkmcnt(0)\n"
"s_barrier\n"
"v_mfma_f32_16x16x32_bf16 v[148:151], v[32:35], v[76:79], v[148:151]\n"
"ds_read_b64_tr_b16 v[20:21], v11 offset:0\n"
"ds_read_b64_tr_b16 v[22:23], v11 offset:2048\n"
"v_mfma_f32_16x16x32_bf16 v[152:155], v[32:35], v[108:111], v[152:155]\n"
"ds_read_b64_tr_b16 v[24:25], v11 offset:4096\n"
"ds_read_b64_tr_b16 v[26:27], v11 offset:6144\n"
"ds_read_b64_tr_b16 v[28:29], v11 offset:8192\n"
"ds_read_b64_tr_b16 v[30:31], v11 offset:10240\n"
"v_mfma_f32_16x16x32_bf16 v[148:151], v[36:39], v[80:83], v[148:151]\n"
"ds_read_b64_tr_b16 v[32:33], v11 offset:12288\n"
"ds_read_b64_tr_b16 v[34:35], v11 offset:14336\n"
"v_mfma_f32_16x16x32_bf16 v[152:155], v[36:39], v[208:211], v[152:155]\n"
"v_cvt_pk_bf16_f32 v170, v128, v129\n"
"v_cvt_pk_bf16_f32 v171, v130, v131\n"
"v_mfma_f32_16x16x32_bf16 v[148:151], v[40:43], v[84:87], v[148:151]\n"
"ds_read_b64_tr_b16 v[36:37], v11 offset:16384\n"
"ds_read_b64_tr_b16 v[38:39], v11 offset:18432\n"
"v_mfma_f32_16x16x32_bf16 v[152:155], v[40:43], v[212:215], v[152:155]\n"
"v_permlane16_swap_b32 v168, v170\n"
"v_permlane16_swap_b32 v169, v171\n"
"v_mfma_f32_16x16x32_bf16 v[148:151], v[44:47], v[88:91], v[148:151]\n"
"ds_read_b64_tr_b16 v[40:41], v11 offset:20480\n"
"ds_read_b64_tr_b16 v[42:43], v11 offset:22528\n"
"v_mfma_f32_16x16x32_bf16 v[152:155], v[44:47], v[216:219], v[152:155]\n"
"buffer_store_dwordx4 v[168:171], v201, s[52:55], s78 offen offset:0 sc1\n"
"v_mfma_f32_16x16x32_bf16 v[148:151], v[48:51], v[92:95], v[148:151]\n"
"ds_read_b64_tr_b16 v[44:45], v11 offset:24576\n"
"ds_read_b64_tr_b16 v[46:47], v11 offset:26624\n"
"v_mfma_f32_16x16x32_bf16 v[152:155], v[48:51], v[220:223], v[152:155]\n"
"s_waitcnt lgkmcnt(10)\n"
"v_mfma_f32_16x16x32_bf16 v[112:115], v[20:23], v[64:67], 0\n"
"ds_read_b64_tr_b16 v[48:49], v11 offset:28672\n"
"ds_read_b64_tr_b16 v[50:51], v11 offset:30720\n"
"v_mfma_f32_16x16x32_bf16 v[116:119], v[20:23], v[96:99], 0\n"
"s_add_u32 m0, s56, 0\n"
"v_mfma_f32_16x16x32_bf16 v[112:115], v[24:27], v[68:71], v[112:115]\n"
"ds_read_b64_tr_b16 v[20:21], v12 offset:0\n"
"ds_read_b64_tr_b16 v[22:23], v12 offset:2048\n"
"v_mfma_f32_16x16x32_bf16 v[116:119], v[24:27], v[100:103], v[116:119]\n"
"v_cvt_pk_bf16_f32 v164, v136, v137\n"
"s_waitcnt lgkmcnt(10)\n"
"v_mfma_f32_16x16x32_bf16 v[112:115], v[28:31], v[72:75], v[112:115]\n"
"ds_read_b64_tr_b16 v[24:25], v12 offset:4096\n"
"ds_read_b64_tr_b16 v[26:27], v12 offset:6144\n"
"v_mfma_f32_16x16x32_bf16 v[116:119], v[28:31], v[104:107], v[116:119]\n"
"v_cvt_pk_bf16_f32 v165, v138, v139\n"
"v_cvt_pk_bf16_f32 v166, v148, v149\n"
"v_mfma_f32_16x16x32_bf16 v[112:115], v[32:35], v[76:79], v[112:115]\n"
"ds_read_b64_tr_b16 v[28:29], v12 offset:8192\n"
"ds_read_b64_tr_b16 v[30:31], v12 offset:10240\n"
"v_mfma_f32_16x16x32_bf16 v[116:119], v[32:35], v[108:111], v[116:119]\n"
"v_cvt_pk_bf16_f32 v167, v150, v151\n"
"s_waitcnt lgkmcnt(10)\n"
"v_mfma_f32_16x16x32_bf16 v[112:115], v[36:39], v[80:83], v[112:115]\n"
"ds_read_b64_tr_b16 v[32:33], v12 offset:12288\n"
"ds_read_b64_tr_b16 v[34:35], v12 offset:14336\n"
"v_mfma_f32_16x16x32_bf16 v[116:119], v[36:39], v[208:211], v[116:119]\n"
"v_permlane16_swap_b32 v164, v166\n"
"v_permlane16_swap_b32 v165, v167\n"
"v_mfma_f32_16x16x32_bf16 v[112:115], v[40:43], v[84:87], v[112:115]\n"
"ds_read_b64_tr_b16 v[36:37], v12 offset:16384\n"
"ds_read_b64_tr_b16 v[38:39], v12 offset:18432\n"
"v_mfma_f32_16x16x32_bf16 v[116:119], v[40:43], v[212:215], v[116:119]\n"
"buffer_store_dwordx4 v[164:167], v201, s[52:55], s77 offen offset:1024 sc1\n"
"s_waitcnt lgkmcnt(10)\n"
"v_mfma_f32_16x16x32_bf16 v[112:115], v[44:47], v[88:91], v[112:115]\n"
"ds_read_b64_tr_b16 v[40:41], v12 offset:20480\n"
"ds_read_b64_tr_b16 v[42:43], v12 offset:22528\n"
"v_mfma_f32_16x16x32_bf16 v[116:119], v[44:47], v[216:219], v[116:119]\n"
"v_cvt_pk_bf16_f32 v172, v140, v141\n"
"v_cvt_pk_bf16_f32 v173, v142, v143\n"
"v_mfma_f32_16x16x32_bf16 v[112:115], v[48:51], v[92:95], v[112:115]\n"
"ds_read_b64_tr_b16 v[44:45], v12 offset:24576\n"
"ds_read_b64_tr_b16 v[46:47], v12 offset:26624\n"
"v_mfma_f32_16x16x32_bf16 v[116:119], v[48:51], v[220:223], v[116:119]\n"
"v_cvt_pk_bf16_f32 v174, v152, v153\n"
"s_waitcnt lgkmcnt(10)\n"
"v_mfma_f32_16x16x32_bf16 v[124:127], v[20:23], v[64:67], 0\n"
"ds_read_b64_tr_b16 v[48:49], v12 offset:28672\n"
"ds_read_b64_tr_b16 v[50:51], v12 offset:30720\n"
"v_mfma_f32_16x16x32_bf16 v[128:131], v[20:23], v[96:99], 0\n"
"v_cvt_pk_bf16_f32 v175, v154, v155\n"
"v_permlane16_swap_b32 v172, v174\n"
"v_mfma_f32_16x16x32_bf16 v[124:127], v[24:27], v[68:71], v[124:127]\n"
"ds_read_b64_tr_b16 v[20:21], v11 offset:512\n"
"ds_read_b64_tr_b16 v[22:23], v11 offset:2560\n"
"v_mfma_f32_16x16x32_bf16 v[128:131], v[24:27], v[100:103], v[128:131]\n"
"v_permlane16_swap_b32 v173, v175\n"
"s_waitcnt lgkmcnt(10)\n"
"v_mfma_f32_16x16x32_bf16 v[124:127], v[28:31], v[72:75], v[124:127]\n"
"ds_read_b64_tr_b16 v[24:25], v11 offset:4608\n"
"ds_read_b64_tr_b16 v[26:27], v11 offset:6656\n"
"v_mfma_f32_16x16x32_bf16 v[128:131], v[28:31], v[104:107], v[128:131]\n"
"buffer_store_dwordx4 v[172:175], v201, s[52:55], s78 offen offset:1024 sc1\n"
"buffer_load_dwordx4 v204, s[48:51], s83 offen lds\n"
"v_mfma_f32_16x16x32_bf16 v[124:127], v[32:35], v[76:79], v[124:127]\n"
"ds_read_b64_tr_b16 v[28:29], v11 offset:8704\n"
"ds_read_b64_tr_b16 v[30:31], v11 offset:10752\n"
"v_mfma_f32_16x16x32_bf16 v[128:131], v[32:35], v[108:111], v[128:131]\n"
"s_add_u32 s83, s83, 7168\n"
"s_waitcnt lgkmcnt(10)\n"
"v_mfma_f32_16x16x32_bf16 v[124:127], v[36:39], v[80:83], v[124:127]\n"
"ds_read_b64_tr_b16 v[32:33], v11 offset:12800\n"
"ds_read_b64_tr_b16 v[34:35], v11 offset:14848\n"
"v_mfma_f32_16x16x32_bf16 v[128:131], v[36:39], v[208:211], v[128:131]\n"
"s_add_u32 s52, s52, 22528\n"
"s_addc_u32 s53, s53, 0\n"
"s_sub_u32 s54, s54, 22528\n"
"v_mfma_f32_16x16x32_bf16 v[124:127], v[40:43], v[84:87], v[124:127]\n"
"ds_read_b64_tr_b16 v[36:37], v11 offset:16896\n"
"ds_read_b64_tr_b16 v[38:39], v11 offset:18944\n"
"v_mfma_f32_16x16x32_bf16 v[128:131], v[40:43], v[212:215], v[128:131]\n"
"s_add_u32 m0, s56, 1024\n"
"s_waitcnt lgkmcnt(10)\n"
"v_mfma_f32_16x16x32_bf16 v[124:127], v[44:47], v[88:91], v[124:127]\n"
"ds_read_b64_tr_b16 v[40:41], v11 offset:20992\n"
"ds_read_b64_tr_b16 v[42:43], v11 offset:23040\n"
"v_mfma_f32_16x16x32_bf16 v[128:131], v[44:47], v[216:219], v[128:131]\n"
"buffer_load_dwordx4 v204, s[48:51], s83 offen lds\n"
"s_add_u32 s83, s83, 7168\n"
"v_mfma_f32_16x16x32_bf16 v[124:127], v[48:51], v[92:95], v[124:127]\n"
"ds_read_b64_tr_b16 v[44:45], v11 offset:25088\n"
"ds_read_b64_tr_b16 v[46:47], v11 offset:27136\n"
"v_mfma_f32_16x16x32_bf16 v[128:131], v[48:51], v[220:223], v[128:131]\n"
"s_add_u32 m0, s56, 2048\n"
"s_waitcnt lgkmcnt(10)\n"
"v_mfma_f32_16x16x32_bf16 v[136:139], v[20:23], v[64:67], 0\n"
"ds_read_b64_tr_b16 v[48:49], v11 offset:29184\n"
"ds_read_b64_tr_b16 v[50:51], v11 offset:31232\n"
"v_mfma_f32_16x16x32_bf16 v[140:143], v[20:23], v[96:99], 0\n"
"buffer_load_dwordx4 v204, s[48:51], s83 offen lds\n"
"s_add_u32 s83, s83, 7168\n"
"v_mfma_f32_16x16x32_bf16 v[136:139], v[24:27], v[68:71], v[136:139]\n"
"ds_read_b64_tr_b16 v[20:21], v12 offset:512\n"
"ds_read_b64_tr_b16 v[22:23], v12 offset:2560\n"
"v_mfma_f32_16x16x32_bf16 v[140:143], v[24:27], v[100:103], v[140:143]\n"
"s_add_u32 m0, s56, 3072\n"
"s_waitcnt lgkmcnt(10)\n"
"v_mfma_f32_16x16x32_bf16 v[136:139], v[28:31], v[72:75], v[136:139]\n"
"ds_read_b64_tr_b16 v[24:25], v12 offset:4608\n"
"ds_read_b64_tr_b16 v[26:27], v12 offset:6656\n"
"v_mfma_f32_16x16x32_bf16 v[140:143], v[28:31], v[104:107], v[140:143]\n"
"buffer_load_dwordx4 v204, s[48:51], s83 offen lds\n"
"s_add_u32 s83, s83, 7168\n"
"v_mfma_f32_16x16x32_bf16 v[136:139], v[32:35], v[76:79], v[136:139]\n"
"ds_read_b64_tr_b16 v[28:29], v12 offset:8704\n"
"ds_read_b64_tr_b16 v[30:31], v12 offset:10752\n"
"v_mfma_f32_16x16x32_bf16 v[140:143], v[32:35], v[108:111], v[140:143]\n"
"s_add_u32 m0, s56, 4096\n"
"s_waitcnt lgkmcnt(10)\n"
"v_mfma_f32_16x16x32_bf16 v[136:139], v[36:39], v[80:83], v[136:139]\n"
"ds_read_b64_tr_b16 v[32:33], v12 offset:12800\n"
"ds_read_b64_tr_b16 v[34:35], v12 offset:14848\n"
"v_mfma_f32_16x16x32_bf16 v[140:143], v[36:39], v[208:211], v[140:143]\n"
"buffer_load_dwordx4 v204, s[48:51], s83 offen lds\n"
"s_add_u32 s83, s83, 7168\n"
"v_mfma_f32_16x16x32_bf16 v[136:139], v[40:43], v[84:87], v[136:139]\n"
"ds_read_b64_tr_b16 v[36:37], v12 offset:16896\n"
"ds_read_b64_tr_b16 v[38:39], v12 offset:18944\n"
"v_mfma_f32_16x16x32_bf16 v[140:143], v[40:43], v[212:215], v[140:143]\n"
"s_add_u32 m0, s56, 5120\n"
"s_waitcnt lgkmcnt(10)\n"
"v_mfma_f32_16x16x32_bf16 v[136:139], v[44:47], v[88:91], v[136:139]\n"
"ds_read_b64_tr_b16 v[40:41], v12 offset:20992\n"
"ds_read_b64_tr_b16 v[42:43], v12 offset:23040\n"
"v_mfma_f32_16x16x32_bf16 v[140:143], v[44:47], v[216:219], v[140:143]\n"
"buffer_load_dwordx4 v204, s[48:51], s83 offen lds\n"
"s_add_u32 s83, s83, 7168\n"
"v_mfma_f32_16x16x32_bf16 v[136:139], v[48:51], v[92:95], v[136:139]\n"
"ds_read_b64_tr_b16 v[44:45], v12 offset:25088\n"
"ds_read_b64_tr_b16 v[46:47], v12 offset:27136\n"
"v_mfma_f32_16x16x32_bf16 v[140:143], v[48:51], v[220:223], v[140:143]\n"
"s_add_u32 m0, s56, 6144\n"
"s_waitcnt lgkmcnt(10)\n"
"v_mfma_f32_16x16x32_bf16 v[148:151], v[20:23], v[64:67], 0\n"
"ds_read_b64_tr_b16 v[48:49], v12 offset:29184\n"
"ds_read_b64_tr_b16 v[50:51], v12 offset:31232\n"
"v_mfma_f32_16x16x32_bf16 v[152:155], v[20:23], v[96:99], 0\n"
"buffer_load_dwordx4 v204, s[48:51], s83 offen lds\n"
"s_add_u32 s83, s83, 7168\n"
"v_mfma_f32_16x16x32_bf16 v[148:151], v[24:27], v[68:71], v[148:151]\n"
"s_add_u32 s57, s57, 0x8000\n"
"s_cmp_eq_u32 s57, s61\n"
"s_cselect_b32 s57, s60, s57\n"
"v_mfma_f32_16x16x32_bf16 v[152:155], v[24:27], v[100:103], v[152:155]\n"
"v_add_u32 v11, s57, v202\n"
"v_add_u32 v12, s57, v203\n"
"s_add_u32 m0, s56, 7168\n"
"s_nop 0\n"
"buffer_load_dwordx4 v204, s[48:51], s83 offen lds\n"
"s_add_u32 s83, s83, 7168\n"
"s_mov_b32 s83, 0\n"
"s_add_u32 s48, s48, 128\n"
"s_addc_u32 s49, s49, 0\n"
"s_sub_u32 s50, s50, 128\n"
"s_add_u32 s56, s56, 0x8000\n"
"s_cmp_eq_u32 s56, s59\n"
"s_cselect_b32 s56, s58, s56\n"
"s_waitcnt lgkmcnt(8)\n"
"v_mfma_f32_16x16x32_bf16 v[148:151], v[28:31], v[72:75], v[148:151]\n"
"v_cvt_pk_bf16_f32 v160, v112, v113\n"
"v_cvt_pk_bf16_f32 v161, v114, v115\n"
"v_mfma_f32_16x16x32_bf16 v[152:155], v[28:31], v[104:107], v[152:155]\n"
"s_waitcnt vmcnt(30) lgkmcnt(0)\n"
"s_barrier\n"
"v_mfma_f32_16x16x32_bf16 v[148:151], v[32:35], v[76:79], v[148:151]\n"
"ds_read_b64_tr_b16 v[20:21], v11 offset:0\n"
"ds_read_b64_tr_b16 v[22:23], v11 offset:2048\n"
"v_mfma_f32_16x16x32_bf16 v[152:155], v[32:35], v[108:111], v[152:155]\n"
"ds_read_b64_tr_b16 v[24:25], v11 offset:4096\n"
"ds_read_b64_tr_b16 v[26:27], v11 offset:6144\n"
"ds_read_b64_tr_b16 v[28:29], v11 offset:8192\n"
"ds_read_b64_tr_b16 v[30:31], v11 offset:10240\n"
"v_mfma_f32_16x16x32_bf16 v[148:151], v[36:39], v[80:83], v[148:151]\n"
"ds_read_b64_tr_b16 v[32:33], v11 offset:12288\n"
"ds_read_b64_tr_b16 v[34:35], v11 offset:14336\n"
"v_mfma_f32_16x16x32_bf16 v[152:155], v[36:39], v[208:211], v[152:155]\n"
"v_cvt_pk_bf16_f32 v162, v124, v125\n"
"v_cvt_pk_bf16_f32 v163, v126, v127\n"
"v_mfma_f32_16x16x32_bf16 v[148:151], v[40:43], v[84:87], v[148:151]\n"
"ds_read_b64_tr_b16 v[36:37], v11 offset:16384\n"
"ds_read_b64_tr_b16 v[38:39], v11 offset:18432\n"
"v_mfma_f32_16x16x32_bf16 v[152:155], v[40:43], v[212:215], v[152:155]\n"
"v_permlane16_swap_b32 v160, v162\n"
"v_permlane16_swap_b32 v161, v163\n"
"v_mfma_f32_16x16x32_bf16 v[148:151], v[44:47], v[88:91], v[148:151]\n"
"ds_read_b64_tr_b16 v[40:41], v11 offset:20480\n"
"ds_read_b64_tr_b16 v[42:43], v11 offset:22528\n"
"v_mfma_f32_16x16x32_bf16 v[152:155], v[44:47], v[216:219], v[152:155]\n"
"buffer_store_dwordx4 v[160:163], v201, s[52:55], s77 offen offset:0 sc1\n"
"v_cvt_pk_bf16_f32 v168, v116, v117\n"
"v_mfma_f32_16x16x32_bf16 v[148:151], v[48:51], v[92:95], v[148:151]\n"
"ds_read_b64_tr_b16 v[44:45], v11 offset:24576\n"
"ds_read_b64_tr_b16 v[46:47], v11 offset:26624\n"
"v_mfma_f32_16x16x32_bf16 v[152:155], v[48:51], v[220:223], v[152:155]\n"
"v_cvt_pk_bf16_f32 v169, v118, v119\n"
"v_cvt_pk_bf16_f32 v170, v128, v129\n"
"v_cvt_pk_bf16_f32 v171, v130, v131\n"
"s_nop 0\n"
"v_permlane16_swap_b32 v168, v170\n"
"v_permlane16_swap_b32 v169, v171\n"
"buffer_store_dwordx4 v[168:171], v201, s[52:55], s78 offen offset:0 sc1\n"
"s_waitcnt lgkmcnt(10)\n"
"v_mfma_f32_16x16x32_bf16 v[112:115], v[20:23], v[64:67], 0\n"
"ds_read_b64_tr_b16 v[48:49], v11 offset:28672\n"
"ds_read_b64_tr_b16 v[50:51], v11 offset:30720\n"
"v_mfma_f32_16x16x32_bf16 v[116:119], v[20:23], v[96:99], 0\n"
"s_add_u32 m0, s56, 0\n"
"v_mfma_f32_16x16x32_bf16 v[112:115], v[24:27], v[68:71], v[112:115]\n"
"ds_read_b64_tr_b16 v[20:21], v12 offset:0\n"
"ds_read_b64_tr_b16 v[22:23], v12 offset:2048\n"
"v_mfma_f32_16x16x32_bf16 v[116:119], v[24:27], v[100:103], v[116:119]\n"
"v_cvt_pk_bf16_f32 v164, v136, v137\n"
"s_waitcnt lgkmcnt(10)\n"
"v_mfma_f32_16x16x32_bf16 v[112:115], v[28:31], v[72:75], v[112:115]\n"
"ds_read_b64_tr_b16 v[24:25], v12 offset:4096\n"
"ds_read_b64_tr_b16 v[26:27], v12 offset:6144\n"
"v_mfma_f32_16x16x32_bf16 v[116:119], v[28:31], v[104:107], v[116:119]\n"
"v_cvt_pk_bf16_f32 v165, v138, v139\n"
"v_cvt_pk_bf16_f32 v166, v148, v149\n"
"v_mfma_f32_16x16x32_bf16 v[112:115], v[32:35], v[76:79], v[112:115]\n"
"ds_read_b64_tr_b16 v[28:29], v12 offset:8192\n"
"ds_read_b64_tr_b16 v[30:31], v12 offset:10240\n"
"v_mfma_f32_16x16x32_bf16 v[116:119], v[32:35], v[108:111], v[116:119]\n"
"v_cvt_pk_bf16_f32 v167, v150, v151\n"
"s_waitcnt lgkmcnt(10)\n"
"v_mfma_f32_16x16x32_bf16 v[112:115], v[36:39], v[80:83], v[112:115]\n"
"ds_read_b64_tr_b16 v[32:33], v12 offset:12288\n"
"ds_read_b64_tr_b16 v[34:35], v12 offset:14336\n"
"v_mfma_f32_16x16x32_bf16 v[116:119], v[36:39], v[208:211], v[116:119]\n"
"v_permlane16_swap_b32 v164, v166\n"
"v_permlane16_swap_b32 v165, v167\n"
"v_mfma_f32_16x16x32_bf16 v[112:115], v[40:43], v[84:87], v[112:115]\n"
"ds_read_b64_tr_b16 v[36:37], v12 offset:16384\n"
"ds_read_b64_tr_b16 v[38:39], v12 offset:18432\n"
"v_mfma_f32_16x16x32_bf16 v[116:119], v[40:43], v[212:215], v[116:119]\n"
"buffer_store_dwordx4 v[164:167], v201, s[52:55], s77 offen offset:1024 sc1\n"
"s_waitcnt lgkmcnt(10)\n"
"v_mfma_f32_16x16x32_bf16 v[112:115], v[44:47], v[88:91], v[112:115]\n"
"ds_read_b64_tr_b16 v[40:41], v12 offset:20480\n"
"ds_read_b64_tr_b16 v[42:43], v12 offset:22528\n"
"v_mfma_f32_16x16x32_bf16 v[116:119], v[44:47], v[216:219], v[116:119]\n"
"v_cvt_pk_bf16_f32 v172, v140, v141\n"
"v_cvt_pk_bf16_f32 v173, v142, v143\n"
"v_mfma_f32_16x16x32_bf16 v[112:115], v[48:51], v[92:95], v[112:115]\n"
"ds_read_b64_tr_b16 v[44:45], v12 offset:24576\n"
"ds_read_b64_tr_b16 v[46:47], v12 offset:26624\n"
"v_mfma_f32_16x16x32_bf16 v[116:119], v[48:51], v[220:223], v[116:119]\n"
"v_cvt_pk_bf16_f32 v174, v152, v153\n"
"s_waitcnt lgkmcnt(10)\n"
"v_mfma_f32_16x16x32_bf16 v[124:127], v[20:23], v[64:67], 0\n"
"ds_read_b64_tr_b16 v[48:49], v12 offset:28672\n"
"ds_read_b64_tr_b16 v[50:51], v12 offset:30720\n"
"v_mfma_f32_16x16x32_bf16 v[128:131], v[20:23], v[96:99], 0\n"
"v_cvt_pk_bf16_f32 v175, v154, v155\n"
"v_permlane16_swap_b32 v172, v174\n"
"v_mfma_f32_16x16x32_bf16 v[124:127], v[24:27], v[68:71], v[124:127]\n"
"ds_read_b64_tr_b16 v[20:21], v11 offset:512\n"
"ds_read_b64_tr_b16 v[22:23], v11 offset:2560\n"
"v_mfma_f32_16x16x32_bf16 v[128:131], v[24:27], v[100:103], v[128:131]\n"
"v_permlane16_swap_b32 v173, v175\n"
"s_waitcnt lgkmcnt(10)\n"
"v_mfma_f32_16x16x32_bf16 v[124:127], v[28:31], v[72:75], v[124:127]\n"
"ds_read_b64_tr_b16 v[24:25], v11 offset:4608\n"
"ds_read_b64_tr_b16 v[26:27], v11 offset:6656\n"
"v_mfma_f32_16x16x32_bf16 v[128:131], v[28:31], v[104:107], v[128:131]\n"
"buffer_store_dwordx4 v[172:175], v201, s[52:55], s78 offen offset:1024 sc1\n"
"buffer_load_dwordx4 v204, s[48:51], s83 offen lds\n"
"v_mfma_f32_16x16x32_bf16 v[124:127], v[32:35], v[76:79], v[124:127]\n"
"ds_read_b64_tr_b16 v[28:29], v11 offset:8704\n"
"ds_read_b64_tr_b16 v[30:31], v11 offset:10752\n"
"v_mfma_f32_16x16x32_bf16 v[128:131], v[32:35], v[108:111], v[128:131]\n"
"s_add_u32 s83, s83, 7168\n"
"s_waitcnt lgkmcnt(10)\n"
"v_mfma_f32_16x16x32_bf16 v[124:127], v[36:39], v[80:83], v[124:127]\n"
"ds_read_b64_tr_b16 v[32:33], v11 offset:12800\n"
"ds_read_b64_tr_b16 v[34:35], v11 offset:14848\n"
"v_mfma_f32_16x16x32_bf16 v[128:131], v[36:39], v[208:211], v[128:131]\n"
"s_add_u32 s52, s52, 22528\n"
"s_addc_u32 s53, s53, 0\n"
"s_sub_u32 s54, s54, 22528\n"
"v_mfma_f32_16x16x32_bf16 v[124:127], v[40:43], v[84:87], v[124:127]\n"
"ds_read_b64_tr_b16 v[36:37], v11 offset:16896\n"
"ds_read_b64_tr_b16 v[38:39], v11 offset:18944\n"
"v_mfma_f32_16x16x32_bf16 v[128:131], v[40:43], v[212:215], v[128:131]\n"
"s_add_u32 m0, s56, 1024\n"
"s_waitcnt lgkmcnt(10)\n"
"v_mfma_f32_16x16x32_bf16 v[124:127], v[44:47], v[88:91], v[124:127]\n"
"ds_read_b64_tr_b16 v[40:41], v11 offset:20992\n"
"ds_read_b64_tr_b16 v[42:43], v11 offset:23040\n"
"v_mfma_f32_16x16x32_bf16 v[128:131], v[44:47], v[216:219], v[128:131]\n"
"buffer_load_dwordx4 v204, s[48:51], s83 offen lds\n"
"s_add_u32 s83, s83, 7168\n"
"v_mfma_f32_16x16x32_bf16 v[124:127], v[48:51], v[92:95], v[124:127]\n"
"ds_read_b64_tr_b16 v[44:45], v11 offset:25088\n"
"ds_read_b64_tr_b16 v[46:47], v11 offset:27136\n"
"v_mfma_f32_16x16x32_bf16 v[128:131], v[48:51], v[220:223], v[128:131]\n"
"s_add_u32 m0, s56, 2048\n"
"s_waitcnt lgkmcnt(10)\n"
"v_mfma_f32_16x16x32_bf16 v[136:139], v[20:23], v[64:67], 0\n"
"ds_read_b64_tr_b16 v[48:49], v11 offset:29184\n"
"ds_read_b64_tr_b16 v[50:51], v11 offset:31232\n"
"v_mfma_f32_16x16x32_bf16 v[140:143], v[20:23], v[96:99], 0\n"
"buffer_load_dwordx4 v204, s[48:51], s83 offen lds\n"
"s_add_u32 s83, s83, 7168\n"
"v_mfma_f32_16x16x32_bf16 v[136:139], v[24:27], v[68:71], v[136:139]\n"
"ds_read_b64_tr_b16 v[20:21], v12 offset:512\n"
"ds_read_b64_tr_b16 v[22:23], v12 offset:2560\n"
"v_mfma_f32_16x16x32_bf16 v[140:143], v[24:27], v[100:103], v[140:143]\n"
"s_add_u32 m0, s56, 3072\n"
"s_waitcnt lgkmcnt(10)\n"
"v_mfma_f32_16x16x32_bf16 v[136:139], v[28:31], v[72:75], v[136:139]\n"
"ds_read_b64_tr_b16 v[24:25], v12 offset:4608\n"
"ds_read_b64_tr_b16 v[26:27], v12 offset:6656\n"
"v_mfma_f32_16x16x32_bf16 v[140:143], v[28:31], v[104:107], v[140:143]\n"
"buffer_load_dwordx4 v204, s[48:51], s83 offen lds\n"
"s_add_u32 s83, s83, 7168\n"
"v_mfma_f32_16x16x32_bf16 v[136:139], v[32:35], v[76:79], v[136:139]\n"
"ds_read_b64_tr_b16 v[28:29], v12 offset:8704\n"
"ds_read_b64_tr_b16 v[30:31], v12 offset:10752\n"
"v_mfma_f32_16x16x32_bf16 v[140:143], v[32:35], v[108:111], v[140:143]\n"
"s_add_u32 m0, s56, 4096\n"
"s_waitcnt lgkmcnt(10)\n"
"v_mfma_f32_16x16x32_bf16 v[136:139], v[36:39], v[80:83], v[136:139]\n"
"ds_read_b64_tr_b16 v[32:33], v12 offset:12800\n"
"ds_read_b64_tr_b16 v[34:35], v12 offset:14848\n"
"v_mfma_f32_16x16x32_bf16 v[140:143], v[36:39], v[208:211], v[140:143]\n"
"buffer_load_dwordx4 v204, s[48:51], s83 offen lds\n"
"s_add_u32 s83, s83, 7168\n"
"v_mfma_f32_16x16x32_bf16 v[136:139], v[40:43], v[84:87], v[136:139]\n"
"ds_read_b64_tr_b16 v[36:37], v12 offset:16896\n"
"ds_read_b64_tr_b16 v[38:39], v12 offset:18944\n"
"v_mfma_f32_16x16x32_bf16 v[140:143], v[40:43], v[212:215], v[140:143]\n"
"s_add_u32 m0, s56, 5120\n"
"s_waitcnt lgkmcnt(10)\n"
"v_mfma_f32_16x16x32_bf16 v[136:139], v[44:47], v[88:91], v[136:139]\n"
"ds_read_b64_tr_b16 v[40:41], v12 offset:20992\n"
"ds_read_b64_tr_b16 v[42:43], v12 offset:23040\n"
"v_mfma_f32_16x16x32_bf16 v[140:143], v[44:47], v[216:219], v[140:143]\n"
"buffer_load_dwordx4 v204, s[48:51], s83 offen lds\n"
"s_add_u32 s83, s83, 7168\n"
"v_mfma_f32_16x16x32_bf16 v[136:139], v[48:51], v[92:95], v[136:139]\n"
"ds_read_b64_tr_b16 v[44:45], v12 offset:25088\n"
"ds_read_b64_tr_b16 v[46:47], v12 offset:27136\n"
"v_mfma_f32_16x16x32_bf16 v[140:143], v[48:51], v[220:223], v[140:143]\n"
"s_add_u32 m0, s56, 6144\n"
"s_waitcnt lgkmcnt(10)\n"
"v_mfma_f32_16x16x32_bf16 v[148:151], v[20:23], v[64:67], 0\n"
"ds_read_b64_tr_b16 v[48:49], v12 offset:29184\n"
"ds_read_b64_tr_b16 v[50:51], v12 offset:31232\n"
"v_mfma_f32_16x16x32_bf16 v[152:155], v[20:23], v[96:99], 0\n"
"buffer_load_dwordx4 v204, s[48:51], s83 offen lds\n"
"s_add_u32 s83, s83, 7168\n"
"v_mfma_f32_16x16x32_bf16 v[148:151], v[24:27], v[68:71], v[148:151]\n"
"s_add_u32 s57, s57, 0x8000\n"
"s_cmp_eq_u32 s57, s61\n"
"s_cselect_b32 s57, s60, s57\n"
"v_mfma_f32_16x16x32_bf16 v[152:155], v[24:27], v[100:103], v[152:155]\n"
"v_add_u32 v11, s57, v202\n"
"v_add_u32 v12, s57, v203\n"
"s_add_u32 m0, s56, 7168\n"
"s_nop 0\n"
"buffer_load_dwordx4 v204, s[48:51], s83 offen lds\n"
"s_add_u32 s83, s83, 7168\n"
"s_mov_b32 s83, 0\n"
"s_add_u32 s48, s48, 128\n"
"s_addc_u32 s49, s49, 0\n"
"s_sub_u32 s50, s50, 128\n"
"s_add_u32 s56, s56, 0x8000\n"
"s_cmp_eq_u32 s56, s59\n"
"s_cselect_b32 s56, s58, s56\n"
"s_waitcnt lgkmcnt(8)\n"
"v_mfma_f32_16x16x32_bf16 v[148:151], v[28:31], v[72:75], v[148:151]\n"
"v_cvt_pk_bf16_f32 v160, v112, v113\n"
"v_cvt_pk_bf16_f32 v161, v114, v115\n"
"v_mfma_f32_16x16x32_bf16 v[152:155], v[28:31], v[104:107], v[152:155]\n"
"s_waitcnt vmcnt(34) lgkmcnt(0)\n"
"s_barrier\n"
"v_mfma_f32_16x16x32_bf16 v[148:151], v[32:35], v[76:79], v[148:151]\n"
"ds_read_b64_tr_b16 v[20:21], v11 offset:0\n"
"ds_read_b64_tr_b16 v[22:23], v11 offset:2048\n"
"v_mfma_f32_16x16x32_bf16 v[152:155], v[32:35], v[108:111], v[152:155]\n"
"ds_read_b64_tr_b16 v[24:25], v11 offset:4096\n"
"ds_read_b64_tr_b16 v[26:27], v11 offset:6144\n"
"ds_read_b64_tr_b16 v[28:29], v11 offset:8192\n"
"ds_read_b64_tr_b16 v[30:31], v11 offset:10240\n"
"v_mfma_f32_16x16x32_bf16 v[148:151], v[36:39], v[80:83], v[148:151]\n"
"ds_read_b64_tr_b16 v[32:33], v11 offset:12288\n"
"ds_read_b64_tr_b16 v[34:35], v11 offset:14336\n"
"v_mfma_f32_16x16x32_bf16 v[152:155], v[36:39], v[208:211], v[152:155]\n"
"v_cvt_pk_bf16_f32 v162, v124, v125\n"
"v_cvt_pk_bf16_f32 v163, v126, v127\n"
"v_mfma_f32_16x16x32_bf16 v[148:151], v[40:43], v[84:87], v[148:151]\n"
"ds_read_b64_tr_b16 v[36:37], v11 offset:16384\n"
"ds_read_b64_tr_b16 v[38:39], v11 offset:18432\n"
"v_mfma_f32_16x16x32_bf16 v[152:155], v[40:43], v[212:215], v[152:155]\n"
"v_permlane16_swap_b32 v160, v162\n"
"v_permlane16_swap_b32 v161, v163\n"
"v_mfma_f32_16x16x32_bf16 v[148:151], v[44:47], v[88:91], v[148:151]\n"
"ds_read_b64_tr_b16 v[40:41], v11 offset:20480\n"
"ds_read_b64_tr_b16 v[42:43], v11 offset:22528\n"
"v_mfma_f32_16x16x32_bf16 v[152:155], v[44:47], v[216:219], v[152:155]\n"
"buffer_store_dwordx4 v[160:163], v201, s[52:55], s77 offen offset:0 sc1\n"
"v_cvt_pk_bf16_f32 v168, v116, v117\n"
"v_mfma_f32_16x16x32_bf16 v[148:151], v[48:51], v[92:95], v[148:151]\n"
"ds_read_b64_tr_b16 v[44:45], v11 offset:24576\n"
"ds_read_b64_tr_b16 v[46:47], v11 offset:26624\n"
"v_mfma_f32_16x16x32_bf16 v[152:155], v[48:51], v[220:223], v[152:155]\n"
"v_cvt_pk_bf16_f32 v169, v118, v119\n"
"v_cvt_pk_bf16_f32 v170, v128, v129\n"
"v_cvt_pk_bf16_f32 v171, v130, v131\n"
"s_nop 0\n"
"v_permlane16_swap_b32 v168, v170\n"
"v_permlane16_swap_b32 v169, v171\n"
"buffer_store_dwordx4 v[168:171], v201, s[52:55], s78 offen offset:0 sc1\n"
"s_waitcnt lgkmcnt(10)\n"
"v_mfma_f32_16x16x32_bf16 v[112:115], v[20:23], v[64:67], 0\n"
"ds_read_b64_tr_b16 v[48:49], v11 offset:28672\n"
"ds_read_b64_tr_b16 v[50:51], v11 offset:30720\n"
"v_mfma_f32_16x16x32_bf16 v[116:119], v[20:23], v[96:99], 0\n"
"v_mfma_f32_16x16x32_bf16 v[112:115], v[24:27], v[68:71], v[112:115]\n"
"ds_read_b64_tr_b16 v[20:21], v12 offset:0\n"
"ds_read_b64_tr_b16 v[22:23], v12 offset:2048\n"
"v_mfma_f32_16x16x32_bf16 v[116:119], v[24:27], v[100:103], v[116:119]\n"
"v_cvt_pk_bf16_f32 v164, v136, v137\n"
"s_waitcnt lgkmcnt(10)\n"
"v_mfma_f32_16x16x32_bf16 v[112:115], v[28:31], v[72:75], v[112:115]\n"
"ds_read_b64_tr_b16 v[24:25], v12 offset:4096\n"
"ds_read_b64_tr_b16 v[26:27], v12 offset:6144\n"
"v_mfma_f32_16x16x32_bf16 v[116:119], v[28:31], v[104:107], v[116:119]\n"
"v_cvt_pk_bf16_f32 v165, v138, v139\n"
"v_cvt_pk_bf16_f32 v166, v148, v149\n"
"v_mfma_f32_16x16x32_bf16 v[112:115], v[32:35], v[76:79], v[112:115]\n"
"ds_read_b64_tr_b16 v[28:29], v12 offset:8192\n"
"ds_read_b64_tr_b16 v[30:31], v12 offset:10240\n"
"v_mfma_f32_16x16x32_bf16 v[116:119], v[32:35], v[108:111], v[116:119]\n"
"v_cvt_pk_bf16_f32 v167, v150, v151\n"
"s_waitcnt lgkmcnt(10)\n"
"v_mfma_f32_16x16x32_bf16 v[112:115], v[36:39], v[80:83], v[112:115]\n"
"ds_read_b64_tr_b16 v[32:33], v12 offset:12288\n"
"ds_read_b64_tr_b16 v[34:35], v12 offset:14336\n"
"v_mfma_f32_16x16x32_bf16 v[116:119], v[36:39], v[208:211], v[116:119]\n"
"v_permlane16_swap_b32 v164, v166\n"
"v_permlane16_swap_b32 v165, v167\n"
"v_mfma_f32_16x16x32_bf16 v[112:115], v[40:43], v[84:87], v[112:115]\n"
"ds_read_b64_tr_b16 v[36:37], v12 offset:16384\n"
"ds_read_b64_tr_b16 v[38:39], v12 offset:18432\n"
"v_mfma_f32_16x16x32_bf16 v[116:119], v[40:43], v[212:215], v[116:119]\n"
"buffer_store_dwordx4 v[164:167], v201, s[52:55], s77 offen offset:1024 sc1\n"
"s_waitcnt lgkmcnt(10)\n"
"v_mfma_f32_16x16x32_bf16 v[112:115], v[44:47], v[88:91], v[112:115]\n"
"ds_read_b64_tr_b16 v[40:41], v12 offset:20480\n"
"ds_read_b64_tr_b16 v[42:43], v12 offset:22528\n"
"v_mfma_f32_16x16x32_bf16 v[116:119], v[44:47], v[216:219], v[116:119]\n"
"v_cvt_pk_bf16_f32 v172, v140, v141\n"
"v_cvt_pk_bf16_f32 v173, v142, v143\n"
"v_mfma_f32_16x16x32_bf16 v[112:115], v[48:51], v[92:95], v[112:115]\n"
"ds_read_b64_tr_b16 v[44:45], v12 offset:24576\n"
"ds_read_b64_tr_b16 v[46:47], v12 offset:26624\n"
"v_mfma_f32_16x16x32_bf16 v[116:119], v[48:51], v[220:223], v[116:119]\n"
"v_cvt_pk_bf16_f32 v174, v152, v153\n"
"s_waitcnt lgkmcnt(10)\n"
"v_mfma_f32_16x16x32_bf16 v[124:127], v[20:23], v[64:67], 0\n"
"ds_read_b64_tr_b16 v[48:49], v12 offset:28672\n"
"ds_read_b64_tr_b16 v[50:51], v12 offset:30720\n"
"v_mfma_f32_16x16x32_bf16 v[128:131], v[20:23], v[96:99], 0\n"
"v_cvt_pk_bf16_f32 v175, v154, v155\n"
"v_permlane16_swap_b32 v172, v174\n"
"v_mfma_f32_16x16x32_bf16 v[124:127], v[24:27], v[68:71], v[124:127]\n"
"ds_read_b64_tr_b16 v[20:21], v11 offset:512\n"
"ds_read_b64_tr_b16 v[22:23], v11 offset:2560\n"
"v_mfma_f32_16x16x32_bf16 v[128:131], v[24:27], v[100:103], v[128:131]\n"
"v_permlane16_swap_b32 v173, v175\n"
"s_waitcnt lgkmcnt(10)\n"
"v_mfma_f32_16x16x32_bf16 v[124:127], v[28:31], v[72:75], v[124:127]\n"
"ds_read_b64_tr_b16 v[24:25], v11 offset:4608\n"
"ds_read_b64_tr_b16 v[26:27], v11 offset:6656\n"
"v_mfma_f32_16x16x32_bf16 v[128:131], v[28:31], v[104:107], v[128:131]\n"
"buffer_store_dwordx4 v[172:175], v201, s[52:55], s78 offen offset:1024 sc1\n"
"v_mfma_f32_16x16x32_bf16 v[124:127], v[32:35], v[76:79], v[124:127]\n"
"ds_read_b64_tr_b16 v[28:29], v11 offset:8704\n"
"ds_read_b64_tr_b16 v[30:31], v11 offset:10752\n"
"v_mfma_f32_16x16x32_bf16 v[128:131], v[32:35], v[108:111], v[128:131]\n"
"s_waitcnt lgkmcnt(10)\n"
"v_mfma_f32_16x16x32_bf16 v[124:127], v[36:39], v[80:83], v[124:127]\n"
"ds_read_b64_tr_b16 v[32:33], v11 offset:12800\n"
"ds_read_b64_tr_b16 v[34:35], v11 offset:14848\n"
"v_mfma_f32_16x16x32_bf16 v[128:131], v[36:39], v[208:211], v[128:131]\n"
"s_add_u32 s52, s52, 22528\n"
"s_addc_u32 s53, s53, 0\n"
"s_sub_u32 s54, s54, 22528\n"
"v_mfma_f32_16x16x32_bf16 v[124:127], v[40:43], v[84:87], v[124:127]\n"
"ds_read_b64_tr_b16 v[36:37], v11 offset:16896\n"
"ds_read_b64_tr_b16 v[38:39], v11 offset:18944\n"
"v_mfma_f32_16x16x32_bf16 v[128:131], v[40:43], v[212:215], v[128:131]\n"
"s_waitcnt lgkmcnt(10)\n"
"v_mfma_f32_16x16x32_bf16 v[124:127], v[44:47], v[88:91], v[124:127]\n"
"ds_read_b64_tr_b16 v[40:41], v11 offset:20992\n"
"ds_read_b64_tr_b16 v[42:43], v11 offset:23040\n"
"v_mfma_f32_16x16x32_bf16 v[128:131], v[44:47], v[216:219], v[128:131]\n"
"v_mfma_f32_16x16x32_bf16 v[124:127], v[48:51], v[92:95], v[124:127]\n"
"ds_read_b64_tr_b16 v[44:45], v11 offset:25088\n"
"ds_read_b64_tr_b16 v[46:47], v11 offset:27136\n"
"v_mfma_f32_16x16x32_bf16 v[128:131], v[48:51], v[220:223], v[128:131]\n"
"s_waitcnt lgkmcnt(10)\n"
"v_mfma_f32_16x16x32_bf16 v[136:139], v[20:23], v[64:67], 0\n"
"ds_read_b64_tr_b16 v[48:49], v11 offset:29184\n"
"ds_read_b64_tr_b16 v[50:51], v11 offset:31232\n"
"v_mfma_f32_16x16x32_bf16 v[140:143], v[20:23], v[96:99], 0\n"
"v_mfma_f32_16x16x32_bf16 v[136:139], v[24:27], v[68:71], v[136:139]\n"
"ds_read_b64_tr_b16 v[20:21], v12 offset:512\n"
"ds_read_b64_tr_b16 v[22:23], v12 offset:2560\n"
"v_mfma_f32_16x16x32_bf16 v[140:143], v[24:27], v[100:103], v[140:143]\n"
"v_cvt_pk_bf16_f32 v160, v112, v113\n"
"s_waitcnt lgkmcnt(10)\n"
"v_mfma_f32_16x16x32_bf16 v[136:139], v[28:31], v[72:75], v[136:139]\n"
"ds_read_b64_tr_b16 v[24:25], v12 offset:4608\n"
"ds_read_b64_tr_b16 v[26:27], v12 offset:6656\n"
"v_mfma_f32_16x16x32_bf16 v[140:143], v[28:31], v[104:107], v[140:143]\n"
"v_cvt_pk_bf16_f32 v161, v114, v115\n"
"v_cvt_pk_bf16_f32 v162, v124, v125\n"
"v_mfma_f32_16x16x32_bf16 v[136:139], v[32:35], v[76:79], v[136:139]\n"
"ds_read_b64_tr_b16 v[28:29], v12 offset:8704\n"
"ds_read_b64_tr_b16 v[30:31], v12 offset:10752\n"
"v_mfma_f32_16x16x32_bf16 v[140:143], v[32:35], v[108:111], v[140:143]\n"
"v_cvt_pk_bf16_f32 v163, v126, v127\n"
"s_waitcnt lgkmcnt(10)\n"
"v_mfma_f32_16x16x32_bf16 v[136:139], v[36:39], v[80:83], v[136:139]\n"
"ds_read_b64_tr_b16 v[32:33], v12 offset:12800\n"
"ds_read_b64_tr_b16 v[34:35], v12 offset:14848\n"
"v_mfma_f32_16x16x32_bf16 v[140:143], v[36:39], v[208:211], v[140:143]\n"
"v_permlane16_swap_b32 v160, v162\n"
"v_permlane16_swap_b32 v161, v163\n"
"v_mfma_f32_16x16x32_bf16 v[136:139], v[40:43], v[84:87], v[136:139]\n"
"ds_read_b64_tr_b16 v[36:37], v12 offset:16896\n"
"ds_read_b64_tr_b16 v[38:39], v12 offset:18944\n"
"v_mfma_f32_16x16x32_bf16 v[140:143], v[40:43], v[212:215], v[140:143]\n"
"buffer_store_dwordx4 v[160:163], v201, s[52:55], s77 offen offset:0 sc1\n"
"s_waitcnt lgkmcnt(10)\n"
"v_mfma_f32_16x16x32_bf16 v[136:139], v[44:47], v[88:91], v[136:139]\n"
"ds_read_b64_tr_b16 v[40:41], v12 offset:20992\n"
"ds_read_b64_tr_b16 v[42:43], v12 offset:23040\n"
"v_mfma_f32_16x16x32_bf16 v[140:143], v[44:47], v[216:219], v[140:143]\n"
"v_cvt_pk_bf16_f32 v168, v116, v117\n"
"v_cvt_pk_bf16_f32 v169, v118, v119\n"
"v_mfma_f32_16x16x32_bf16 v[136:139], v[48:51], v[92:95], v[136:139]\n"
"ds_read_b64_tr_b16 v[44:45], v12 offset:25088\n"
"ds_read_b64_tr_b16 v[46:47], v12 offset:27136\n"
"v_mfma_f32_16x16x32_bf16 v[140:143], v[48:51], v[220:223], v[140:143]\n"
"v_cvt_pk_bf16_f32 v170, v128, v129\n"
"s_waitcnt lgkmcnt(10)\n"
"v_mfma_f32_16x16x32_bf16 v[148:151], v[20:23], v[64:67], 0\n"
"ds_read_b64_tr_b16 v[48:49], v12 offset:29184\n"
"ds_read_b64_tr_b16 v[50:51], v12 offset:31232\n"
"v_mfma_f32_16x16x32_bf16 v[152:155], v[20:23], v[96:99], 0\n"
"v_cvt_pk_bf16_f32 v171, v130, v131\n"
"v_permlane16_swap_b32 v168, v170\n"
"v_mfma_f32_16x16x32_bf16 v[148:151], v[24:27], v[68:71], v[148:151]\n"
"s_add_u32 s57, s57, 0x8000\n"
"s_cmp_eq_u32 s57, s61\n"
"s_cselect_b32 s57, s60, s57\n"
"v_mfma_f32_16x16x32_bf16 v[152:155], v[24:27], v[100:103], v[152:155]\n"
"v_add_u32 v11, s57, v202\n"
"v_add_u32 v12, s57, v203\n"
"s_waitcnt lgkmcnt(8)\n"
"v_mfma_f32_16x16x32_bf16 v[148:151], v[28:31], v[72:75], v[148:151]\n"
"v_permlane16_swap_b32 v169, v171\n"
"buffer_store_dwordx4 v[168:171], v201, s[52:55], s78 offen offset:0 sc1\n"
"v_mfma_f32_16x16x32_bf16 v[152:155], v[28:31], v[104:107], v[152:155]\n"
"s_waitcnt vmcnt(30) lgkmcnt(0)\n"
"s_barrier\n"
"v_mfma_f32_16x16x32_bf16 v[148:151], v[32:35], v[76:79], v[148:151]\n"
"ds_read_b64_tr_b16 v[20:21], v11 offset:0\n"
"ds_read_b64_tr_b16 v[22:23], v11 offset:2048\n"
"v_mfma_f32_16x16x32_bf16 v[152:155], v[32:35], v[108:111], v[152:155]\n"
"ds_read_b64_tr_b16 v[24:25], v11 offset:4096\n"
"ds_read_b64_tr_b16 v[26:27], v11 offset:6144\n"
"ds_read_b64_tr_b16 v[28:29], v11 offset:8192\n"
"ds_read_b64_tr_b16 v[30:31], v11 offset:10240\n"
"v_mfma_f32_16x16x32_bf16 v[148:151], v[36:39], v[80:83], v[148:151]\n"
"ds_read_b64_tr_b16 v[32:33], v11 offset:12288\n"
"ds_read_b64_tr_b16 v[34:35], v11 offset:14336\n"
"v_mfma_f32_16x16x32_bf16 v[152:155], v[36:39], v[208:211], v[152:155]\n"
"v_mfma_f32_16x16x32_bf16 v[148:151], v[40:43], v[84:87], v[148:151]\n"
"ds_read_b64_tr_b16 v[36:37], v11 offset:16384\n"
"ds_read_b64_tr_b16 v[38:39], v11 offset:18432\n"
"v_mfma_f32_16x16x32_bf16 v[152:155], v[40:43], v[212:215], v[152:155]\n"
"v_mfma_f32_16x16x32_bf16 v[148:151], v[44:47], v[88:91], v[148:151]\n"
"ds_read_b64_tr_b16 v[40:41], v11 offset:20480\n"
"ds_read_b64_tr_b16 v[42:43], v11 offset:22528\n"
"v_mfma_f32_16x16x32_bf16 v[152:155], v[44:47], v[216:219], v[152:155]\n"
"v_mfma_f32_16x16x32_bf16 v[148:151], v[48:51], v[92:95], v[148:151]\n"
"ds_read_b64_tr_b16 v[44:45], v11 offset:24576\n"
"ds_read_b64_tr_b16 v[46:47], v11 offset:26624\n"
"v_mfma_f32_16x16x32_bf16 v[152:155], v[48:51], v[220:223], v[152:155]\n"
"s_waitcnt lgkmcnt(10)\n"
"v_mfma_f32_16x16x32_bf16 v[112:115], v[20:23], v[64:67], 0\n"
"ds_read_b64_tr_b16 v[48:49], v11 offset:28672\n"
"ds_read_b64_tr_b16 v[50:51], v11 offset:30720\n"
"v_mfma_f32_16x16x32_bf16 v[116:119], v[20:23], v[96:99], 0\n"
"v_mfma_f32_16x16x32_bf16 v[112:115], v[24:27], v[68:71], v[112:115]\n"
"ds_read_b64_tr_b16 v[20:21], v12 offset:0\n"
"ds_read_b64_tr_b16 v[22:23], v12 offset:2048\n"
"v_mfma_f32_16x16x32_bf16 v[116:119], v[24:27], v[100:103], v[116:119]\n"
"v_cvt_pk_bf16_f32 v164, v136, v137\n"
"s_waitcnt lgkmcnt(10)\n"
"v_mfma_f32_16x16x32_bf16 v[112:115], v[28:31], v[72:75], v[112:115]\n"
"ds_read_b64_tr_b16 v[24:25], v12 offset:4096\n"
"ds_read_b64_tr_b16 v[26:27], v12 offset:6144\n"
"v_mfma_f32_16x16x32_bf16 v[116:119], v[28:31], v[104:107], v[116:119]\n"
"v_cvt_pk_bf16_f32 v165, v138, v139\n"
"v_cvt_pk_bf16_f32 v166, v148, v149\n"
"v_mfma_f32_16x16x32_bf16 v[112:115], v[32:35], v[76:79], v[112:115]\n"
"ds_read_b64_tr_b16 v[28:29], v12 offset:8192\n"
"ds_read_b64_tr_b16 v[30:31], v12 offset:10240\n"
"v_mfma_f32_16x16x32_bf16 v[116:119], v[32:35], v[108:111], v[116:119]\n"
"v_cvt_pk_bf16_f32 v167, v150, v151\n"
"s_waitcnt lgkmcnt(10)\n"
"v_mfma_f32_16x16x32_bf16 v[112:115], v[36:39], v[80:83], v[112:115]\n"
"ds_read_b64_tr_b16 v[32:33], v12 offset:12288\n"
"ds_read_b64_tr_b16 v[34:35], v12 offset:14336\n"
"v_mfma_f32_16x16x32_bf16 v[116:119], v[36:39], v[208:211], v[116:119]\n"
"v_permlane16_swap_b32 v164, v166\n"
"v_permlane16_swap_b32 v165, v167\n"
"v_mfma_f32_16x16x32_bf16 v[112:115], v[40:43], v[84:87], v[112:115]\n"
"ds_read_b64_tr_b16 v[36:37], v12 offset:16384\n"
"ds_read_b64_tr_b16 v[38:39], v12 offset:18432\n"
"v_mfma_f32_16x16x32_bf16 v[116:119], v[40:43], v[212:215], v[116:119]\n"
"buffer_store_dwordx4 v[164:167], v201, s[52:55], s77 offen offset:1024 sc1\n"
"s_waitcnt lgkmcnt(10)\n"
"v_mfma_f32_16x16x32_bf16 v[112:115], v[44:47], v[88:91], v[112:115]\n"
"ds_read_b64_tr_b16 v[40:41], v12 offset:20480\n"
"ds_read_b64_tr_b16 v[42:43], v12 offset:22528\n"
"v_mfma_f32_16x16x32_bf16 v[116:119], v[44:47], v[216:219], v[116:119]\n"
"v_cvt_pk_bf16_f32 v172, v140, v141\n"
"v_cvt_pk_bf16_f32 v173, v142, v143\n"
"v_mfma_f32_16x16x32_bf16 v[112:115], v[48:51], v[92:95], v[112:115]\n"
"ds_read_b64_tr_b16 v[44:45], v12 offset:24576\n"
"ds_read_b64_tr_b16 v[46:47], v12 offset:26624\n"
"v_mfma_f32_16x16x32_bf16 v[116:119], v[48:51], v[220:223], v[116:119]\n"
"v_cvt_pk_bf16_f32 v174, v152, v153\n"
"s_waitcnt lgkmcnt(10)\n"
"v_mfma_f32_16x16x32_bf16 v[124:127], v[20:23], v[64:67], 0\n"
"ds_read_b64_tr_b16 v[48:49], v12 offset:28672\n"
"ds_read_b64_tr_b16 v[50:51], v12 offset:30720\n"
"v_mfma_f32_16x16x32_bf16 v[128:131], v[20:23], v[96:99], 0\n"
"v_cvt_pk_bf16_f32 v175, v154, v155\n"
"v_permlane16_swap_b32 v172, v174\n"
"v_mfma_f32_16x16x32_bf16 v[124:127], v[24:27], v[68:71], v[124:127]\n"
"ds_read_b64_tr_b16 v[20:21], v11 offset:512\n"
"ds_read_b64_tr_b16 v[22:23], v11 offset:2560\n"
"v_mfma_f32_16x16x32_bf16 v[128:131], v[24:27], v[100:103], v[128:131]\n"
"v_permlane16_swap_b32 v173, v175\n"
"s_waitcnt lgkmcnt(10)\n"
"v_mfma_f32_16x16x32_bf16 v[124:127], v[28:31], v[72:75], v[124:127]\n"
"ds_read_b64_tr_b16 v[24:25], v11 offset:4608\n"
"ds_read_b64_tr_b16 v[26:27], v11 offset:6656\n"
"v_mfma_f32_16x16x32_bf16 v[128:131], v[28:31], v[104:107], v[128:131]\n"
"buffer_store_dwordx4 v[172:175], v201, s[52:55], s78 offen offset:1024 sc1\n"
"v_mfma_f32_16x16x32_bf16 v[124:127], v[32:35], v[76:79], v[124:127]\n"
"ds_read_b64_tr_b16 v[28:29], v11 offset:8704\n"
"ds_read_b64_tr_b16 v[30:31], v11 offset:10752\n"
"v_mfma_f32_16x16x32_bf16 v[128:131], v[32:35], v[108:111], v[128:131]\n"
"s_waitcnt lgkmcnt(10)\n"
"v_mfma_f32_16x16x32_bf16 v[124:127], v[36:39], v[80:83], v[124:127]\n"
"ds_read_b64_tr_b16 v[32:33], v11 offset:12800\n"
"ds_read_b64_tr_b16 v[34:35], v11 offset:14848\n"
"v_mfma_f32_16x16x32_bf16 v[128:131], v[36:39], v[208:211], v[128:131]\n"
"s_add_u32 s52, s52, 22528\n"
"s_addc_u32 s53, s53, 0\n"
"s_sub_u32 s54, s54, 22528\n"
"v_mfma_f32_16x16x32_bf16 v[124:127], v[40:43], v[84:87], v[124:127]\n"
"ds_read_b64_tr_b16 v[36:37], v11 offset:16896\n"
"ds_read_b64_tr_b16 v[38:39], v11 offset:18944\n"
"v_mfma_f32_16x16x32_bf16 v[128:131], v[40:43], v[212:215], v[128:131]\n"
"s_waitcnt lgkmcnt(10)\n"
"v_mfma_f32_16x16x32_bf16 v[124:127], v[44:47], v[88:91], v[124:127]\n"
"ds_read_b64_tr_b16 v[40:41], v11 offset:20992\n"
"ds_read_b64_tr_b16 v[42:43], v11 offset:23040\n"
"v_mfma_f32_16x16x32_bf16 v[128:131], v[44:47], v[216:219], v[128:131]\n"
"v_mfma_f32_16x16x32_bf16 v[124:127], v[48:51], v[92:95], v[124:127]\n"
"ds_read_b64_tr_b16 v[44:45], v11 offset:25088\n"
"ds_read_b64_tr_b16 v[46:47], v11 offset:27136\n"
"v_mfma_f32_16x16x32_bf16 v[128:131], v[48:51], v[220:223], v[128:131]\n"
"s_waitcnt lgkmcnt(10)\n"
"v_mfma_f32_16x16x32_bf16 v[136:139], v[20:23], v[64:67], 0\n"
"ds_read_b64_tr_b16 v[48:49], v11 offset:29184\n"
"ds_read_b64_tr_b16 v[50:51], v11 offset:31232\n"
"v_mfma_f32_16x16x32_bf16 v[140:143], v[20:23], v[96:99], 0\n"
"v_mfma_f32_16x16x32_bf16 v[136:139], v[24:27], v[68:71], v[136:139]\n"
"ds_read_b64_tr_b16 v[20:21], v12 offset:512\n"
"ds_read_b64_tr_b16 v[22:23], v12 offset:2560\n"
"v_mfma_f32_16x16x32_bf16 v[140:143], v[24:27], v[100:103], v[140:143]\n"
"v_cvt_pk_bf16_f32 v160, v112, v113\n"
"s_waitcnt lgkmcnt(10)\n"
"v_mfma_f32_16x16x32_bf16 v[136:139], v[28:31], v[72:75], v[136:139]\n"
"ds_read_b64_tr_b16 v[24:25], v12 offset:4608\n"
"ds_read_b64_tr_b16 v[26:27], v12 offset:6656\n"
"v_mfma_f32_16x16x32_bf16 v[140:143], v[28:31], v[104:107], v[140:143]\n"
"v_cvt_pk_bf16_f32 v161, v114, v115\n"
"v_cvt_pk_bf16_f32 v162, v124, v125\n"
"v_mfma_f32_16x16x32_bf16 v[136:139], v[32:35], v[76:79], v[136:139]\n"
"ds_read_b64_tr_b16 v[28:29], v12 offset:8704\n"
"ds_read_b64_tr_b16 v[30:31], v12 offset:10752\n"
"v_mfma_f32_16x16x32_bf16 v[140:143], v[32:35], v[108:111], v[140:143]\n"
"v_cvt_pk_bf16_f32 v163, v126, v127\n"
"s_waitcnt lgkmcnt(10)\n"
"v_mfma_f32_16x16x32_bf16 v[136:139], v[36:39], v[80:83], v[136:139]\n"
"ds_read_b64_tr_b16 v[32:33], v12 offset:12800\n"
"ds_read_b64_tr_b16 v[34:35], v12 offset:14848\n"
"v_mfma_f32_16x16x32_bf16 v[140:143], v[36:39], v[208:211], v[140:143]\n"
"v_permlane16_swap_b32 v160, v162\n"
"v_permlane16_swap_b32 v161, v163\n"
"v_mfma_f32_16x16x32_bf16 v[136:139], v[40:43], v[84:87], v[136:139]\n"
"ds_read_b64_tr_b16 v[36:37], v12 offset:16896\n"
"ds_read_b64_tr_b16 v[38:39], v12 offset:18944\n"
"v_mfma_f32_16x16x32_bf16 v[140:143], v[40:43], v[212:215], v[140:143]\n"
"buffer_store_dwordx4 v[160:163], v201, s[52:55], s77 offen offset:0 sc1\n"
"s_waitcnt lgkmcnt(10)\n"
"v_mfma_f32_16x16x32_bf16 v[136:139], v[44:47], v[88:91], v[136:139]\n"
"ds_read_b64_tr_b16 v[40:41], v12 offset:20992\n"
"ds_read_b64_tr_b16 v[42:43], v12 offset:23040\n"
"v_mfma_f32_16x16x32_bf16 v[140:143], v[44:47], v[216:219], v[140:143]\n"
"v_cvt_pk_bf16_f32 v168, v116, v117\n"
"v_cvt_pk_bf16_f32 v169, v118, v119\n"
"v_mfma_f32_16x16x32_bf16 v[136:139], v[48:51], v[92:95], v[136:139]\n"
"ds_read_b64_tr_b16 v[44:45], v12 offset:25088\n"
"ds_read_b64_tr_b16 v[46:47], v12 offset:27136\n"
"v_mfma_f32_16x16x32_bf16 v[140:143], v[48:51], v[220:223], v[140:143]\n"
"v_cvt_pk_bf16_f32 v170, v128, v129\n"
"s_waitcnt lgkmcnt(10)\n"
"v_mfma_f32_16x16x32_bf16 v[148:151], v[20:23], v[64:67], 0\n"
"ds_read_b64_tr_b16 v[48:49], v12 offset:29184\n"
"ds_read_b64_tr_b16 v[50:51], v12 offset:31232\n"
"v_mfma_f32_16x16x32_bf16 v[152:155], v[20:23], v[96:99], 0\n"
"v_cvt_pk_bf16_f32 v171, v130, v131\n"
"v_permlane16_swap_b32 v168, v170\n"
"v_mfma_f32_16x16x32_bf16 v[148:151], v[24:27], v[68:71], v[148:151]\n"
"s_add_u32 s57, s57, 0x8000\n"
"s_cmp_eq_u32 s57, s61\n"
"s_cselect_b32 s57, s60, s57\n"
"v_mfma_f32_16x16x32_bf16 v[152:155], v[24:27], v[100:103], v[152:155]\n"
"v_add_u32 v11, s57, v202\n"
"v_add_u32 v12, s57, v203\n"
"s_waitcnt lgkmcnt(8)\n"
"v_mfma_f32_16x16x32_bf16 v[148:151], v[28:31], v[72:75], v[148:151]\n"
"v_permlane16_swap_b32 v169, v171\n"
"buffer_store_dwordx4 v[168:171], v201, s[52:55], s78 offen offset:0 sc1\n"
"v_mfma_f32_16x16x32_bf16 v[152:155], v[28:31], v[104:107], v[152:155]\n"
"s_waitcnt vmcnt(22) lgkmcnt(0)\n"
"s_barrier\n"
"v_mfma_f32_16x16x32_bf16 v[148:151], v[32:35], v[76:79], v[148:151]\n"
"ds_read_b64_tr_b16 v[20:21], v11 offset:0\n"
"ds_read_b64_tr_b16 v[22:23], v11 offset:2048\n"
"v_mfma_f32_16x16x32_bf16 v[152:155], v[32:35], v[108:111], v[152:155]\n"
"ds_read_b64_tr_b16 v[24:25], v11 offset:4096\n"
"ds_read_b64_tr_b16 v[26:27], v11 offset:6144\n"
"ds_read_b64_tr_b16 v[28:29], v11 offset:8192\n"
"ds_read_b64_tr_b16 v[30:31], v11 offset:10240\n"
"v_mfma_f32_16x16x32_bf16 v[148:151], v[36:39], v[80:83], v[148:151]\n"
"ds_read_b64_tr_b16 v[32:33], v11 offset:12288\n"
"ds_read_b64_tr_b16 v[34:35], v11 offset:14336\n"
"v_mfma_f32_16x16x32_bf16 v[152:155], v[36:39], v[208:211], v[152:155]\n"
"v_mfma_f32_16x16x32_bf16 v[148:151], v[40:43], v[84:87], v[148:151]\n"
"ds_read_b64_tr_b16 v[36:37], v11 offset:16384\n"
"ds_read_b64_tr_b16 v[38:39], v11 offset:18432\n"
"v_mfma_f32_16x16x32_bf16 v[152:155], v[40:43], v[212:215], v[152:155]\n"
"v_mfma_f32_16x16x32_bf16 v[148:151], v[44:47], v[88:91], v[148:151]\n"
"ds_read_b64_tr_b16 v[40:41], v11 offset:20480\n"
"ds_read_b64_tr_b16 v[42:43], v11 offset:22528\n"
"v_mfma_f32_16x16x32_bf16 v[152:155], v[44:47], v[216:219], v[152:155]\n"
"v_mfma_f32_16x16x32_bf16 v[148:151], v[48:51], v[92:95], v[148:151]\n"
"ds_read_b64_tr_b16 v[44:45], v11 offset:24576\n"
"ds_read_b64_tr_b16 v[46:47], v11 offset:26624\n"
"v_mfma_f32_16x16x32_bf16 v[152:155], v[48:51], v[220:223], v[152:155]\n"
"s_waitcnt lgkmcnt(10)\n"
"v_mfma_f32_16x16x32_bf16 v[112:115], v[20:23], v[64:67], 0\n"
"ds_read_b64_tr_b16 v[48:49], v11 offset:28672\n"
"ds_read_b64_tr_b16 v[50:51], v11 offset:30720\n"
"v_mfma_f32_16x16x32_bf16 v[116:119], v[20:23], v[96:99], 0\n"
"v_mfma_f32_16x16x32_bf16 v[112:115], v[24:27], v[68:71], v[112:115]\n"
"ds_read_b64_tr_b16 v[20:21], v12 offset:0\n"
"ds_read_b64_tr_b16 v[22:23], v12 offset:2048\n"
"v_mfma_f32_16x16x32_bf16 v[116:119], v[24:27], v[100:103], v[116:119]\n"
"v_cvt_pk_bf16_f32 v164, v136, v137\n"
"s_waitcnt lgkmcnt(10)\n"
"v_mfma_f32_16x16x32_bf16 v[112:115], v[28:31], v[72:75], v[112:115]\n"
"ds_read_b64_tr_b16 v[24:25], v12 offset:4096\n"
"ds_read_b64_tr_b16 v[26:27], v12 offset:6144\n"
"v_mfma_f32_16x16x32_bf16 v[116:119], v[28:31], v[104:107], v[116:119]\n"
"v_cvt_pk_bf16_f32 v165, v138, v139\n"
"v_cvt_pk_bf16_f32 v166, v148, v149\n"
"v_mfma_f32_16x16x32_bf16 v[112:115], v[32:35], v[76:79], v[112:115]\n"
"ds_read_b64_tr_b16 v[28:29], v12 offset:8192\n"
"ds_read_b64_tr_b16 v[30:31], v12 offset:10240\n"
"v_mfma_f32_16x16x32_bf16 v[116:119], v[32:35], v[108:111], v[116:119]\n"
"v_cvt_pk_bf16_f32 v167, v150, v151\n"
"s_waitcnt lgkmcnt(10)\n"
"v_mfma_f32_16x16x32_bf16 v[112:115], v[36:39], v[80:83], v[112:115]\n"
"ds_read_b64_tr_b16 v[32:33], v12 offset:12288\n"
"ds_read_b64_tr_b16 v[34:35], v12 offset:14336\n"
"v_mfma_f32_16x16x32_bf16 v[116:119], v[36:39], v[208:211], v[116:119]\n"
"v_permlane16_swap_b32 v164, v166\n"
"v_permlane16_swap_b32 v165, v167\n"
"v_mfma_f32_16x16x32_bf16 v[112:115], v[40:43], v[84:87], v[112:115]\n"
"ds_read_b64_tr_b16 v[36:37], v12 offset:16384\n"
"ds_read_b64_tr_b16 v[38:39], v12 offset:18432\n"
"v_mfma_f32_16x16x32_bf16 v[116:119], v[40:43], v[212:215], v[116:119]\n"
"buffer_store_dwordx4 v[164:167], v201, s[52:55], s77 offen offset:1024 sc1\n"
"s_waitcnt lgkmcnt(10)\n"
"v_mfma_f32_16x16x32_bf16 v[112:115], v[44:47], v[88:91], v[112:115]\n"
"ds_read_b64_tr_b16 v[40:41], v12 offset:20480\n"
"ds_read_b64_tr_b16 v[42:43], v12 offset:22528\n"
"v_mfma_f32_16x16x32_bf16 v[116:119], v[44:47], v[216:219], v[116:119]\n"
"v_cvt_pk_bf16_f32 v172, v140, v141\n"
"v_cvt_pk_bf16_f32 v173, v142, v143\n"
"v_mfma_f32_16x16x32_bf16 v[112:115], v[48:51], v[92:95], v[112:115]\n"
"ds_read_b64_tr_b16 v[44:45], v12 offset:24576\n"
"ds_read_b64_tr_b16 v[46:47], v12 offset:26624\n"
"v_mfma_f32_16x16x32_bf16 v[116:119], v[48:51], v[220:223], v[116:119]\n"
"v_cvt_pk_bf16_f32 v174, v152, v153\n"
"s_waitcnt lgkmcnt(10)\n"
"v_mfma_f32_16x16x32_bf16 v[124:127], v[20:23], v[64:67], 0\n"
"ds_read_b64_tr_b16 v[48:49], v12 offset:28672\n"
"ds_read_b64_tr_b16 v[50:51], v12 offset:30720\n"
"v_mfma_f32_16x16x32_bf16 v[128:131], v[20:23], v[96:99], 0\n"
"v_cvt_pk_bf16_f32 v175, v154, v155\n"
"v_permlane16_swap_b32 v172, v174\n"
"v_mfma_f32_16x16x32_bf16 v[124:127], v[24:27], v[68:71], v[124:127]\n"
"ds_read_b64_tr_b16 v[20:21], v11 offset:512\n"
"ds_read_b64_tr_b16 v[22:23], v11 offset:2560\n"
"v_mfma_f32_16x16x32_bf16 v[128:131], v[24:27], v[100:103], v[128:131]\n"
"v_permlane16_swap_b32 v173, v175\n"
"s_waitcnt lgkmcnt(10)\n"
"v_mfma_f32_16x16x32_bf16 v[124:127], v[28:31], v[72:75], v[124:127]\n"
"ds_read_b64_tr_b16 v[24:25], v11 offset:4608\n"
"ds_read_b64_tr_b16 v[26:27], v11 offset:6656\n"
"v_mfma_f32_16x16x32_bf16 v[128:131], v[28:31], v[104:107], v[128:131]\n"
"buffer_store_dwordx4 v[172:175], v201, s[52:55], s78 offen offset:1024 sc1\n"
"v_mfma_f32_16x16x32_bf16 v[124:127], v[32:35], v[76:79], v[124:127]\n"
"ds_read_b64_tr_b16 v[28:29], v11 offset:8704\n"
"ds_read_b64_tr_b16 v[30:31], v11 offset:10752\n"
"v_mfma_f32_16x16x32_bf16 v[128:131], v[32:35], v[108:111], v[128:131]\n"
"s_waitcnt lgkmcnt(10)\n"
"v_mfma_f32_16x16x32_bf16 v[124:127], v[36:39], v[80:83], v[124:127]\n"
"ds_read_b64_tr_b16 v[32:33], v11 offset:12800\n"
"ds_read_b64_tr_b16 v[34:35], v11 offset:14848\n"
"v_mfma_f32_16x16x32_bf16 v[128:131], v[36:39], v[208:211], v[128:131]\n"
"s_add_u32 s52, s52, 22528\n"
"s_addc_u32 s53, s53, 0\n"
"s_sub_u32 s54, s54, 22528\n"
"v_mfma_f32_16x16x32_bf16 v[124:127], v[40:43], v[84:87], v[124:127]\n"
"ds_read_b64_tr_b16 v[36:37], v11 offset:16896\n"
"ds_read_b64_tr_b16 v[38:39], v11 offset:18944\n"
"v_mfma_f32_16x16x32_bf16 v[128:131], v[40:43], v[212:215], v[128:131]\n"
"s_waitcnt lgkmcnt(10)\n"
"v_mfma_f32_16x16x32_bf16 v[124:127], v[44:47], v[88:91], v[124:127]\n"
"ds_read_b64_tr_b16 v[40:41], v11 offset:20992\n"
"ds_read_b64_tr_b16 v[42:43], v11 offset:23040\n"
"v_mfma_f32_16x16x32_bf16 v[128:131], v[44:47], v[216:219], v[128:131]\n"
"v_mfma_f32_16x16x32_bf16 v[124:127], v[48:51], v[92:95], v[124:127]\n"
"ds_read_b64_tr_b16 v[44:45], v11 offset:25088\n"
"ds_read_b64_tr_b16 v[46:47], v11 offset:27136\n"
"v_mfma_f32_16x16x32_bf16 v[128:131], v[48:51], v[220:223], v[128:131]\n"
"s_waitcnt lgkmcnt(10)\n"
"v_mfma_f32_16x16x32_bf16 v[136:139], v[20:23], v[64:67], 0\n"
"ds_read_b64_tr_b16 v[48:49], v11 offset:29184\n"
"ds_read_b64_tr_b16 v[50:51], v11 offset:31232\n"
"v_mfma_f32_16x16x32_bf16 v[140:143], v[20:23], v[96:99], 0\n"
"v_mfma_f32_16x16x32_bf16 v[136:139], v[24:27], v[68:71], v[136:139]\n"
"ds_read_b64_tr_b16 v[20:21], v12 offset:512\n"
"ds_read_b64_tr_b16 v[22:23], v12 offset:2560\n"
"v_mfma_f32_16x16x32_bf16 v[140:143], v[24:27], v[100:103], v[140:143]\n"
"v_cvt_pk_bf16_f32 v160, v112, v113\n"
"s_waitcnt lgkmcnt(10)\n"
"v_mfma_f32_16x16x32_bf16 v[136:139], v[28:31], v[72:75], v[136:139]\n"
"ds_read_b64_tr_b16 v[24:25], v12 offset:4608\n"
"ds_read_b64_tr_b16 v[26:27], v12 offset:6656\n"
"v_mfma_f32_16x16x32_bf16 v[140:143], v[28:31], v[104:107], v[140:143]\n"
"v_cvt_pk_bf16_f32 v161, v114, v115\n"
"v_cvt_pk_bf16_f32 v162, v124, v125\n"
"v_mfma_f32_16x16x32_bf16 v[136:139], v[32:35], v[76:79], v[136:139]\n"
"ds_read_b64_tr_b16 v[28:29], v12 offset:8704\n"
"ds_read_b64_tr_b16 v[30:31], v12 offset:10752\n"
"v_mfma_f32_16x16x32_bf16 v[140:143], v[32:35], v[108:111], v[140:143]\n"
"v_cvt_pk_bf16_f32 v163, v126, v127\n"
"s_waitcnt lgkmcnt(10)\n"
"v_mfma_f32_16x16x32_bf16 v[136:139], v[36:39], v[80:83], v[136:139]\n"
"ds_read_b64_tr_b16 v[32:33], v12 offset:12800\n"
"ds_read_b64_tr_b16 v[34:35], v12 offset:14848\n"
"v_mfma_f32_16x16x32_bf16 v[140:143], v[36:39], v[208:211], v[140:143]\n"
"v_permlane16_swap_b32 v160, v162\n"
"v_permlane16_swap_b32 v161, v163\n"
"v_mfma_f32_16x16x32_bf16 v[136:139], v[40:43], v[84:87], v[136:139]\n"
"ds_read_b64_tr_b16 v[36:37], v12 offset:16896\n"
"ds_read_b64_tr_b16 v[38:39], v12 offset:18944\n"
"v_mfma_f32_16x16x32_bf16 v[140:143], v[40:43], v[212:215], v[140:143]\n"
"buffer_store_dwordx4 v[160:163], v201, s[52:55], s77 offen offset:0 sc1\n"
"s_waitcnt lgkmcnt(10)\n"
"v_mfma_f32_16x16x32_bf16 v[136:139], v[44:47], v[88:91], v[136:139]\n"
"ds_read_b64_tr_b16 v[40:41], v12 offset:20992\n"
"ds_read_b64_tr_b16 v[42:43], v12 offset:23040\n"
"v_mfma_f32_16x16x32_bf16 v[140:143], v[44:47], v[216:219], v[140:143]\n"
"v_cvt_pk_bf16_f32 v168, v116, v117\n"
"v_cvt_pk_bf16_f32 v169, v118, v119\n"
"v_mfma_f32_16x16x32_bf16 v[136:139], v[48:51], v[92:95], v[136:139]\n"
"ds_read_b64_tr_b16 v[44:45], v12 offset:25088\n"
"ds_read_b64_tr_b16 v[46:47], v12 offset:27136\n"
"v_mfma_f32_16x16x32_bf16 v[140:143], v[48:51], v[220:223], v[140:143]\n"
"v_cvt_pk_bf16_f32 v170, v128, v129\n"
"s_waitcnt lgkmcnt(10)\n"
"v_mfma_f32_16x16x32_bf16 v[148:151], v[20:23], v[64:67], 0\n"
"ds_read_b64_tr_b16 v[48:49], v12 offset:29184\n"
"ds_read_b64_tr_b16 v[50:51], v12 offset:31232\n"
"v_mfma_f32_16x16x32_bf16 v[152:155], v[20:23], v[96:99], 0\n"
"v_cvt_pk_bf16_f32 v171, v130, v131\n"
"v_permlane16_swap_b32 v168, v170\n"
"v_mfma_f32_16x16x32_bf16 v[148:151], v[24:27], v[68:71], v[148:151]\n"
"s_add_u32 s57, s57, 0x8000\n"
"s_cmp_eq_u32 s57, s61\n"
"s_cselect_b32 s57, s60, s57\n"
"v_mfma_f32_16x16x32_bf16 v[152:155], v[24:27], v[100:103], v[152:155]\n"
"v_add_u32 v11, s57, v202\n"
"v_add_u32 v12, s57, v203\n"
"s_waitcnt lgkmcnt(8)\n"
"v_mfma_f32_16x16x32_bf16 v[148:151], v[28:31], v[72:75], v[148:151]\n"
"v_permlane16_swap_b32 v169, v171\n"
"buffer_store_dwordx4 v[168:171], v201, s[52:55], s78 offen offset:0 sc1\n"
"v_mfma_f32_16x16x32_bf16 v[152:155], v[28:31], v[104:107], v[152:155]\n"
"s_waitcnt vmcnt(14) lgkmcnt(0)\n"
"s_barrier\n"
"v_mfma_f32_16x16x32_bf16 v[148:151], v[32:35], v[76:79], v[148:151]\n"
"ds_read_b64_tr_b16 v[20:21], v11 offset:0\n"
"ds_read_b64_tr_b16 v[22:23], v11 offset:2048\n"
"v_mfma_f32_16x16x32_bf16 v[152:155], v[32:35], v[108:111], v[152:155]\n"
"ds_read_b64_tr_b16 v[24:25], v11 offset:4096\n"
"ds_read_b64_tr_b16 v[26:27], v11 offset:6144\n"
"ds_read_b64_tr_b16 v[28:29], v11 offset:8192\n"
"ds_read_b64_tr_b16 v[30:31], v11 offset:10240\n"
"v_mfma_f32_16x16x32_bf16 v[148:151], v[36:39], v[80:83], v[148:151]\n"
"ds_read_b64_tr_b16 v[32:33], v11 offset:12288\n"
"ds_read_b64_tr_b16 v[34:35], v11 offset:14336\n"
"v_mfma_f32_16x16x32_bf16 v[152:155], v[36:39], v[208:211], v[152:155]\n"
"v_mfma_f32_16x16x32_bf16 v[148:151], v[40:43], v[84:87], v[148:151]\n"
"ds_read_b64_tr_b16 v[36:37], v11 offset:16384\n"
"ds_read_b64_tr_b16 v[38:39], v11 offset:18432\n"
"v_mfma_f32_16x16x32_bf16 v[152:155], v[40:43], v[212:215], v[152:155]\n"
"v_mfma_f32_16x16x32_bf16 v[148:151], v[44:47], v[88:91], v[148:151]\n"
"ds_read_b64_tr_b16 v[40:41], v11 offset:20480\n"
"ds_read_b64_tr_b16 v[42:43], v11 offset:22528\n"
"v_mfma_f32_16x16x32_bf16 v[152:155], v[44:47], v[216:219], v[152:155]\n"
"v_mfma_f32_16x16x32_bf16 v[148:151], v[48:51], v[92:95], v[148:151]\n"
"ds_read_b64_tr_b16 v[44:45], v11 offset:24576\n"
"ds_read_b64_tr_b16 v[46:47], v11 offset:26624\n"
"v_mfma_f32_16x16x32_bf16 v[152:155], v[48:51], v[220:223], v[152:155]\n"
"s_waitcnt lgkmcnt(10)\n"
"v_mfma_f32_16x16x32_bf16 v[112:115], v[20:23], v[64:67], 0\n"
"ds_read_b64_tr_b16 v[48:49], v11 offset:28672\n"
"ds_read_b64_tr_b16 v[50:51], v11 offset:30720\n"
"v_mfma_f32_16x16x32_bf16 v[116:119], v[20:23], v[96:99], 0\n"
"v_mfma_f32_16x16x32_bf16 v[112:115], v[24:27], v[68:71], v[112:115]\n"
"ds_read_b64_tr_b16 v[20:21], v12 offset:0\n"
"ds_read_b64_tr_b16 v[22:23], v12 offset:2048\n"
"v_mfma_f32_16x16x32_bf16 v[116:119], v[24:27], v[100:103], v[116:119]\n"
"v_cvt_pk_bf16_f32 v164, v136, v137\n"
"s_waitcnt lgkmcnt(10)\n"
"v_mfma_f32_16x16x32_bf16 v[112:115], v[28:31], v[72:75], v[112:115]\n"
"ds_read_b64_tr_b16 v[24:25], v12 offset:4096\n"
"ds_read_b64_tr_b16 v[26:27], v12 offset:6144\n"
"v_mfma_f32_16x16x32_bf16 v[116:119], v[28:31], v[104:107], v[116:119]\n"
"v_cvt_pk_bf16_f32 v165, v138, v139\n"
"v_cvt_pk_bf16_f32 v166, v148, v149\n"
"v_mfma_f32_16x16x32_bf16 v[112:115], v[32:35], v[76:79], v[112:115]\n"
"ds_read_b64_tr_b16 v[28:29], v12 offset:8192\n"
"ds_read_b64_tr_b16 v[30:31], v12 offset:10240\n"
"v_mfma_f32_16x16x32_bf16 v[116:119], v[32:35], v[108:111], v[116:119]\n"
"v_cvt_pk_bf16_f32 v167, v150, v151\n"
"s_waitcnt lgkmcnt(10)\n"
"v_mfma_f32_16x16x32_bf16 v[112:115], v[36:39], v[80:83], v[112:115]\n"
"ds_read_b64_tr_b16 v[32:33], v12 offset:12288\n"
"ds_read_b64_tr_b16 v[34:35], v12 offset:14336\n"
"v_mfma_f32_16x16x32_bf16 v[116:119], v[36:39], v[208:211], v[116:119]\n"
"v_permlane16_swap_b32 v164, v166\n"
"v_permlane16_swap_b32 v165, v167\n"
"v_mfma_f32_16x16x32_bf16 v[112:115], v[40:43], v[84:87], v[112:115]\n"
"ds_read_b64_tr_b16 v[36:37], v12 offset:16384\n"
"ds_read_b64_tr_b16 v[38:39], v12 offset:18432\n"
"v_mfma_f32_16x16x32_bf16 v[116:119], v[40:43], v[212:215], v[116:119]\n"
"buffer_store_dwordx4 v[164:167], v201, s[52:55], s77 offen offset:1024 sc1\n"
"s_waitcnt lgkmcnt(10)\n"
"v_mfma_f32_16x16x32_bf16 v[112:115], v[44:47], v[88:91], v[112:115]\n"
"ds_read_b64_tr_b16 v[40:41], v12 offset:20480\n"
"ds_read_b64_tr_b16 v[42:43], v12 offset:22528\n"
"v_mfma_f32_16x16x32_bf16 v[116:119], v[44:47], v[216:219], v[116:119]\n"
"v_cvt_pk_bf16_f32 v172, v140, v141\n"
"v_cvt_pk_bf16_f32 v173, v142, v143\n"
"v_mfma_f32_16x16x32_bf16 v[112:115], v[48:51], v[92:95], v[112:115]\n"
"ds_read_b64_tr_b16 v[44:45], v12 offset:24576\n"
"ds_read_b64_tr_b16 v[46:47], v12 offset:26624\n"
"v_mfma_f32_16x16x32_bf16 v[116:119], v[48:51], v[220:223], v[116:119]\n"
"v_cvt_pk_bf16_f32 v174, v152, v153\n"
"s_waitcnt lgkmcnt(10)\n"
"v_mfma_f32_16x16x32_bf16 v[124:127], v[20:23], v[64:67], 0\n"
"ds_read_b64_tr_b16 v[48:49], v12 offset:28672\n"
"ds_read_b64_tr_b16 v[50:51], v12 offset:30720\n"
"v_mfma_f32_16x16x32_bf16 v[128:131], v[20:23], v[96:99], 0\n"
"v_cvt_pk_bf16_f32 v175, v154, v155\n"
"v_permlane16_swap_b32 v172, v174\n"
"v_mfma_f32_16x16x32_bf16 v[124:127], v[24:27], v[68:71], v[124:127]\n"
"ds_read_b64_tr_b16 v[20:21], v11 offset:512\n"
"ds_read_b64_tr_b16 v[22:23], v11 offset:2560\n"
"v_mfma_f32_16x16x32_bf16 v[128:131], v[24:27], v[100:103], v[128:131]\n"
"v_permlane16_swap_b32 v173, v175\n"
"s_waitcnt lgkmcnt(10)\n"
"v_mfma_f32_16x16x32_bf16 v[124:127], v[28:31], v[72:75], v[124:127]\n"
"ds_read_b64_tr_b16 v[24:25], v11 offset:4608\n"
"ds_read_b64_tr_b16 v[26:27], v11 offset:6656\n"
"v_mfma_f32_16x16x32_bf16 v[128:131], v[28:31], v[104:107], v[128:131]\n"
"buffer_store_dwordx4 v[172:175], v201, s[52:55], s78 offen offset:1024 sc1\n"
"v_mfma_f32_16x16x32_bf16 v[124:127], v[32:35], v[76:79], v[124:127]\n"
"ds_read_b64_tr_b16 v[28:29], v11 offset:8704\n"
"ds_read_b64_tr_b16 v[30:31], v11 offset:10752\n"
"v_mfma_f32_16x16x32_bf16 v[128:131], v[32:35], v[108:111], v[128:131]\n"
"s_waitcnt lgkmcnt(10)\n"
"v_mfma_f32_16x16x32_bf16 v[124:127], v[36:39], v[80:83], v[124:127]\n"
"ds_read_b64_tr_b16 v[32:33], v11 offset:12800\n"
"ds_read_b64_tr_b16 v[34:35], v11 offset:14848\n"
"v_mfma_f32_16x16x32_bf16 v[128:131], v[36:39], v[208:211], v[128:131]\n"
"s_add_u32 s52, s52, 22528\n"
"s_addc_u32 s53, s53, 0\n"
"s_sub_u32 s54, s54, 22528\n"
"v_mfma_f32_16x16x32_bf16 v[124:127], v[40:43], v[84:87], v[124:127]\n"
"ds_read_b64_tr_b16 v[36:37], v11 offset:16896\n"
"ds_read_b64_tr_b16 v[38:39], v11 offset:18944\n"
"v_mfma_f32_16x16x32_bf16 v[128:131], v[40:43], v[212:215], v[128:131]\n"
"s_waitcnt lgkmcnt(10)\n"
"v_mfma_f32_16x16x32_bf16 v[124:127], v[44:47], v[88:91], v[124:127]\n"
"ds_read_b64_tr_b16 v[40:41], v11 offset:20992\n"
"ds_read_b64_tr_b16 v[42:43], v11 offset:23040\n"
"v_mfma_f32_16x16x32_bf16 v[128:131], v[44:47], v[216:219], v[128:131]\n"
"v_mfma_f32_16x16x32_bf16 v[124:127], v[48:51], v[92:95], v[124:127]\n"
"ds_read_b64_tr_b16 v[44:45], v11 offset:25088\n"
"ds_read_b64_tr_b16 v[46:47], v11 offset:27136\n"
"v_mfma_f32_16x16x32_bf16 v[128:131], v[48:51], v[220:223], v[128:131]\n"
"s_waitcnt lgkmcnt(10)\n"
"v_mfma_f32_16x16x32_bf16 v[136:139], v[20:23], v[64:67], 0\n"
"ds_read_b64_tr_b16 v[48:49], v11 offset:29184\n"
"ds_read_b64_tr_b16 v[50:51], v11 offset:31232\n"
"v_mfma_f32_16x16x32_bf16 v[140:143], v[20:23], v[96:99], 0\n"
"v_mfma_f32_16x16x32_bf16 v[136:139], v[24:27], v[68:71], v[136:139]\n"
"ds_read_b64_tr_b16 v[20:21], v12 offset:512\n"
"ds_read_b64_tr_b16 v[22:23], v12 offset:2560\n"
"v_mfma_f32_16x16x32_bf16 v[140:143], v[24:27], v[100:103], v[140:143]\n"
"v_cvt_pk_bf16_f32 v160, v112, v113\n"
"s_waitcnt lgkmcnt(10)\n"
"v_mfma_f32_16x16x32_bf16 v[136:139], v[28:31], v[72:75], v[136:139]\n"
"ds_read_b64_tr_b16 v[24:25], v12 offset:4608\n"
"ds_read_b64_tr_b16 v[26:27], v12 offset:6656\n"
"v_mfma_f32_16x16x32_bf16 v[140:143], v[28:31], v[104:107], v[140:143]\n"
"v_cvt_pk_bf16_f32 v161, v114, v115\n"
"v_cvt_pk_bf16_f32 v162, v124, v125\n"
"v_mfma_f32_16x16x32_bf16 v[136:139], v[32:35], v[76:79], v[136:139]\n"
"ds_read_b64_tr_b16 v[28:29], v12 offset:8704\n"
"ds_read_b64_tr_b16 v[30:31], v12 offset:10752\n"
"v_mfma_f32_16x16x32_bf16 v[140:143], v[32:35], v[108:111], v[140:143]\n"
"v_cvt_pk_bf16_f32 v163, v126, v127\n"
"s_waitcnt lgkmcnt(10)\n"
"v_mfma_f32_16x16x32_bf16 v[136:139], v[36:39], v[80:83], v[136:139]\n"
"ds_read_b64_tr_b16 v[32:33], v12 offset:12800\n"
"ds_read_b64_tr_b16 v[34:35], v12 offset:14848\n"
"v_mfma_f32_16x16x32_bf16 v[140:143], v[36:39], v[208:211], v[140:143]\n"
"v_permlane16_swap_b32 v160, v162\n"
"v_permlane16_swap_b32 v161, v163\n"
"v_mfma_f32_16x16x32_bf16 v[136:139], v[40:43], v[84:87], v[136:139]\n"
"ds_read_b64_tr_b16 v[36:37], v12 offset:16896\n"
"ds_read_b64_tr_b16 v[38:39], v12 offset:18944\n"
"v_mfma_f32_16x16x32_bf16 v[140:143], v[40:43], v[212:215], v[140:143]\n"
"buffer_store_dwordx4 v[160:163], v201, s[52:55], s77 offen offset:0 sc1\n"
"s_waitcnt lgkmcnt(10)\n"
"v_mfma_f32_16x16x32_bf16 v[136:139], v[44:47], v[88:91], v[136:139]\n"
"ds_read_b64_tr_b16 v[40:41], v12 offset:20992\n"
"ds_read_b64_tr_b16 v[42:43], v12 offset:23040\n"
"v_mfma_f32_16x16x32_bf16 v[140:143], v[44:47], v[216:219], v[140:143]\n"
"v_cvt_pk_bf16_f32 v168, v116, v117\n"
"v_cvt_pk_bf16_f32 v169, v118, v119\n"
"v_mfma_f32_16x16x32_bf16 v[136:139], v[48:51], v[92:95], v[136:139]\n"
"ds_read_b64_tr_b16 v[44:45], v12 offset:25088\n"
"ds_read_b64_tr_b16 v[46:47], v12 offset:27136\n"
"v_mfma_f32_16x16x32_bf16 v[140:143], v[48:51], v[220:223], v[140:143]\n"
"v_cvt_pk_bf16_f32 v170, v128, v129\n"
"s_waitcnt lgkmcnt(10)\n"
"v_mfma_f32_16x16x32_bf16 v[148:151], v[20:23], v[64:67], 0\n"
"ds_read_b64_tr_b16 v[48:49], v12 offset:29184\n"
"ds_read_b64_tr_b16 v[50:51], v12 offset:31232\n"
"v_mfma_f32_16x16x32_bf16 v[152:155], v[20:23], v[96:99], 0\n"
"v_cvt_pk_bf16_f32 v171, v130, v131\n"
"v_permlane16_swap_b32 v168, v170\n"
"v_mfma_f32_16x16x32_bf16 v[148:151], v[24:27], v[68:71], v[148:151]\n"
"v_permlane16_swap_b32 v169, v171\n"
"buffer_store_dwordx4 v[168:171], v201, s[52:55], s78 offen offset:0 sc1\n"
"v_mfma_f32_16x16x32_bf16 v[152:155], v[24:27], v[100:103], v[152:155]\n"
"s_waitcnt lgkmcnt(8)\n"
"v_mfma_f32_16x16x32_bf16 v[148:151], v[28:31], v[72:75], v[148:151]\n"
"v_mfma_f32_16x16x32_bf16 v[152:155], v[28:31], v[104:107], v[152:155]\n"
"v_mfma_f32_16x16x32_bf16 v[148:151], v[32:35], v[76:79], v[148:151]\n"
"v_mfma_f32_16x16x32_bf16 v[152:155], v[32:35], v[108:111], v[152:155]\n"
"s_waitcnt lgkmcnt(4)\n"
"v_mfma_f32_16x16x32_bf16 v[148:151], v[36:39], v[80:83], v[148:151]\n"
"v_mfma_f32_16x16x32_bf16 v[152:155], v[36:39], v[208:211], v[152:155]\n"
"v_mfma_f32_16x16x32_bf16 v[148:151], v[40:43], v[84:87], v[148:151]\n"
"v_mfma_f32_16x16x32_bf16 v[152:155], v[40:43], v[212:215], v[152:155]\n"
"s_waitcnt lgkmcnt(0)\n"
"v_mfma_f32_16x16x32_bf16 v[148:151], v[44:47], v[88:91], v[148:151]\n"
"v_mfma_f32_16x16x32_bf16 v[152:155], v[44:47], v[216:219], v[152:155]\n"
"v_mfma_f32_16x16x32_bf16 v[148:151], v[48:51], v[92:95], v[148:151]\n"
"v_mfma_f32_16x16x32_bf16 v[152:155], v[48:51], v[220:223], v[152:155]\n"
"s_nop 7\n"
"v_cvt_pk_bf16_f32 v164, v136, v137\n"
"v_cvt_pk_bf16_f32 v165, v138, v139\n"
"v_cvt_pk_bf16_f32 v166, v148, v149\n"
"v_cvt_pk_bf16_f32 v167, v150, v151\n"
"s_nop 0\n"
"v_permlane16_swap_b32 v164, v166\n"
"v_permlane16_swap_b32 v165, v167\n"
"buffer_store_dwordx4 v[164:167], v201, s[52:55], s77 offen offset:1024 sc1\n"
"v_cvt_pk_bf16_f32 v172, v140, v141\n"
"v_cvt_pk_bf16_f32 v173, v142, v143\n"
"v_cvt_pk_bf16_f32 v174, v152, v153\n"
"v_cvt_pk_bf16_f32 v175, v154, v155\n"
"s_nop 0\n"
"v_permlane16_swap_b32 v172, v174\n"
"v_permlane16_swap_b32 v173, v175\n"
"buffer_store_dwordx4 v[172:175], v201, s[52:55], s78 offen offset:1024 sc1\n"
"L_fin_w3:\n"
"s_waitcnt vmcnt(0)\n"
"s_branch L_end\n"
"L_rare_w3f:\n"
"s_nop 7\n"
"v_mov_b32 v187, v13\n"
"s_nop 1\n"
"v_permlane32_swap_b32 v13, v187\n"
"s_nop 1\n"
"v_max_f32 v13, v13, v187\n"
"v_mov_b32 v187, v13\n"
"s_nop 1\n"
"v_permlane16_swap_b32 v13, v187\n"
"s_nop 1\n"
"v_max_f32 v13, v13, v187\n"
"v_max_f32 v13, 0, v13\n"
"v_add_f32 v17, v17, v13\n"
"v_sub_f32 v187, 0, v13\n"
"v_exp_f32 v184, v187\n"
"v_sub_f32 v112, v112, v13\n"
"v_sub_f32 v113, v113, v13\n"
"v_sub_f32 v114, v114, v13\n"
"v_sub_f32 v115, v115, v13\n"
"v_sub_f32 v124, v124, v13\n"
"v_sub_f32 v125, v125, v13\n"
"v_sub_f32 v126, v126, v13\n"
"v_sub_f32 v127, v127, v13\n"
"v_sub_f32 v136, v136, v13\n"
"v_sub_f32 v137, v137, v13\n"
"v_sub_f32 v138, v138, v13\n"
"v_sub_f32 v139, v139, v13\n"
"v_sub_f32 v148, v148, v13\n"
"v_sub_f32 v149, v149, v13\n"
"v_sub_f32 v150, v150, v13\n"
"v_sub_f32 v151, v151, v13\n"
"v_sub_f32 v52, 0, v17\n"
"v_sub_f32 v53, 0, v17\n"
"v_sub_f32 v54, 0, v17\n"
"v_sub_f32 v55, 0, v17\n"
"v_mov_b32 v187, v14\n"
"s_nop 1\n"
"v_permlane32_swap_b32 v14, v187\n"
"s_nop 1\n"
"v_max_f32 v14, v14, v187\n"
"v_mov_b32 v187, v14\n"
"s_nop 1\n"
"v_permlane16_swap_b32 v14, v187\n"
"s_nop 1\n"
"v_max_f32 v14, v14, v187\n"
"v_max_f32 v14, 0, v14\n"
"v_add_f32 v18, v18, v14\n"
"v_sub_f32 v187, 0, v14\n"
"v_exp_f32 v185, v187\n"
"v_sub_f32 v116, v116, v14\n"
"v_sub_f32 v117, v117, v14\n"
"v_sub_f32 v118, v118, v14\n"
"v_sub_f32 v119, v119, v14\n"
"v_sub_f32 v128, v128, v14\n"
"v_sub_f32 v129, v129, v14\n"
"v_sub_f32 v130, v130, v14\n"
"v_sub_f32 v131, v131, v14\n"
"v_sub_f32 v140, v140, v14\n"
"v_sub_f32 v141, v141, v14\n"
"v_sub_f32 v142, v142, v14\n"
"v_sub_f32 v143, v143, v14\n"
"v_sub_f32 v152, v152, v14\n"
"v_sub_f32 v153, v153, v14\n"
"v_sub_f32 v154, v154, v14\n"
"v_sub_f32 v155, v155, v14\n"
"v_sub_f32 v56, 0, v18\n"
"v_sub_f32 v57, 0, v18\n"
"v_sub_f32 v58, 0, v18\n"
"v_sub_f32 v59, 0, v18\n"
"s_nop 1\n"
"s_mov_b32 s71, 1\n"
"s_branch L_back_w3f\n"
"L_resc_w3f:\n"
"s_nop 7\n"
"v_accvgpr_read_b32 v187, a0\n"
"v_accvgpr_read_b32 v188, a1\n"
"v_accvgpr_read_b32 v189, a2\n"
"v_accvgpr_read_b32 v190, a3\n"
"v_mul_f32 v187, v184, v187\n"
"v_mul_f32 v188, v184, v188\n"
"v_mul_f32 v189, v184, v189\n"
"v_mul_f32 v190, v184, v190\n"
"v_accvgpr_write_b32 a0, v187\n"
"v_accvgpr_write_b32 a1, v188\n"
"v_accvgpr_write_b32 a2, v189\n"
"v_accvgpr_write_b32 a3, v190\n"
"v_accvgpr_read_b32 v187, a4\n"
"v_accvgpr_read_b32 v188, a5\n"
"v_accvgpr_read_b32 v189, a6\n"
"v_accvgpr_read_b32 v190, a7\n"
"v_mul_f32 v187, v184, v187\n"
"v_mul_f32 v188, v184, v188\n"
"v_mul_f32 v189, v184, v189\n"
"v_mul_f32 v190, v184, v190\n"
"v_accvgpr_write_b32 a4, v187\n"
"v_accvgpr_write_b32 a5, v188\n"
"v_accvgpr_write_b32 a6, v189\n"
"v_accvgpr_write_b32 a7, v190\n"
"v_accvgpr_read_b32 v187, a8\n"
"v_accvgpr_read_b32 v188, a9\n"
"v_accvgpr_read_b32 v189, a10\n"
"v_accvgpr_read_b32 v190, a11\n"
"v_mul_f32 v187, v184, v187\n"
"v_mul_f32 v188, v184, v188\n"
"v_mul_f32 v189, v184, v189\n"
"v_mul_f32 v190, v184, v190\n"
"v_accvgpr_write_b32 a8, v187\n"
"v_accvgpr_write_b32 a9, v188\n"
"v_accvgpr_write_b32 a10, v189\n"
"v_accvgpr_write_b32 a11, v190\n"
"v_accvgpr_read_b32 v187, a12\n"
"v_accvgpr_read_b32 v188, a13\n"
"v_accvgpr_read_b32 v189, a14\n"
"v_accvgpr_read_b32 v190, a15\n"
"v_mul_f32 v187, v184, v187\n"
"v_mul_f32 v188, v184, v188\n"
"v_mul_f32 v189, v184, v189\n"
"v_mul_f32 v190, v184, v190\n"
"v_accvgpr_write_b32 a12, v187\n"
"v_accvgpr_write_b32 a13, v188\n"
"v_accvgpr_write_b32 a14, v189\n"
"v_accvgpr_write_b32 a15, v190\n"
"v_accvgpr_read_b32 v187, a16\n"
"v_accvgpr_read_b32 v188, a17\n"
"v_accvgpr_read_b32 v189, a18\n"
"v_accvgpr_read_b32 v190, a19\n"
"v_mul_f32 v187, v184, v187\n"
"v_mul_f32 v188, v184, v188\n"
"v_mul_f32 v189, v184, v189\n"
"v_mul_f32 v190, v184, v190\n"
"v_accvgpr_write_b32 a16, v187\n"
"v_accvgpr_write_b32 a17, v188\n"
"v_accvgpr_write_b32 a18, v189\n"
"v_accvgpr_write_b32 a19, v190\n"
"v_accvgpr_read_b32 v187, a20\n"
"v_accvgpr_read_b32 v188, a21\n"
"v_accvgpr_read_b32 v189, a22\n"
"v_accvgpr_read_b32 v190, a23\n"
"v_mul_f32 v187, v184, v187\n"
"v_mul_f32 v188, v184, v188\n"
"v_mul_f32 v189, v184, v189\n"
"v_mul_f32 v190, v184, v190\n"
"v_accvgpr_write_b32 a20, v187\n"
"v_accvgpr_write_b32 a21, v188\n"
"v_accvgpr_write_b32 a22, v189\n"
"v_accvgpr_write_b32 a23, v190\n"
"v_accvgpr_read_b32 v187, a24\n"
"v_accvgpr_read_b32 v188, a25\n"
"v_accvgpr_read_b32 v189, a26\n"
"v_accvgpr_read_b32 v190, a27\n"
"v_mul_f32 v187, v184, v187\n"
"v_mul_f32 v188, v184, v188\n"
"v_mul_f32 v189, v184, v189\n"
"v_mul_f32 v190, v184, v190\n"
"v_accvgpr_write_b32 a24, v187\n"
"v_accvgpr_write_b32 a25, v188\n"
"v_accvgpr_write_b32 a26, v189\n"
"v_accvgpr_write_b32 a27, v190\n"
"v_accvgpr_read_b32 v187, a28\n"
"v_accvgpr_read_b32 v188, a29\n"
"v_accvgpr_read_b32 v189, a30\n"
"v_accvgpr_read_b32 v190, a31\n"
"v_mul_f32 v187, v184, v187\n"
"v_mul_f32 v188, v184, v188\n"
"v_mul_f32 v189, v184, v189\n"
"v_mul_f32 v190, v184, v190\n"
"v_accvgpr_write_b32 a28, v187\n"
"v_accvgpr_write_b32 a29, v188\n"
"v_accvgpr_write_b32 a30, v189\n"
"v_accvgpr_write_b32 a31, v190\n"
"v_accvgpr_read_b32 v187, a32\n"
"v_accvgpr_read_b32 v188, a33\n"
"v_accvgpr_read_b32 v189, a34\n"
"v_accvgpr_read_b32 v190, a35\n"
"v_mul_f32 v187, v184, v187\n"
"v_mul_f32 v188, v184, v188\n"
"v_mul_f32 v189, v184, v189\n"
"v_mul_f32 v190, v184, v190\n"
"v_accvgpr_write_b32 a32, v187\n"
"v_accvgpr_write_b32 a33, v188\n"
"v_accvgpr_write_b32 a34, v189\n"
"v_accvgpr_write_b32 a35, v190\n"
"v_accvgpr_read_b32 v187, a36\n"
"v_accvgpr_read_b32 v188, a37\n"
"v_accvgpr_read_b32 v189, a38\n"
"v_accvgpr_read_b32 v190, a39\n"
"v_mul_f32 v187, v184, v187\n"
"v_mul_f32 v188, v184, v188\n"
"v_mul_f32 v189, v184, v189\n"
"v_mul_f32 v190, v184, v190\n"
"v_accvgpr_write_b32 a36, v187\n"
"v_accvgpr_write_b32 a37, v188\n"
"v_accvgpr_write_b32 a38, v189\n"
"v_accvgpr_write_b32 a39, v190\n"
"v_accvgpr_read_b32 v187, a40\n"
"v_accvgpr_read_b32 v188, a41\n"
"v_accvgpr_read_b32 v189, a42\n"
"v_accvgpr_read_b32 v190, a43\n"
"v_mul_f32 v187, v184, v187\n"
"v_mul_f32 v188, v184, v188\n"
"v_mul_f32 v189, v184, v189\n"
"v_mul_f32 v190, v184, v190\n"
"v_accvgpr_write_b32 a40, v187\n"
"v_accvgpr_write_b32 a41, v188\n"
"v_accvgpr_write_b32 a42, v189\n"
"v_accvgpr_write_b32 a43, v190\n"
"v_accvgpr_read_b32 v187, a44\n"
"v_accvgpr_read_b32 v188, a45\n"
"v_accvgpr_read_b32 v189, a46\n"
"v_accvgpr_read_b32 v190, a47\n"
"v_mul_f32 v187, v184, v187\n"
"v_mul_f32 v188, v184, v188\n"
"v_mul_f32 v189, v184, v189\n"
"v_mul_f32 v190, v184, v190\n"
"v_accvgpr_write_b32 a44, v187\n"
"v_accvgpr_write_b32 a45, v188\n"
"v_accvgpr_write_b32 a46, v189\n"
"v_accvgpr_write_b32 a47, v190\n"
"v_accvgpr_read_b32 v187, a48\n"
"v_accvgpr_read_b32 v188, a49\n"
"v_accvgpr_read_b32 v189, a50\n"
"v_accvgpr_read_b32 v190, a51\n"
"v_mul_f32 v187, v184, v187\n"
"v_mul_f32 v188, v184, v188\n"
"v_mul_f32 v189, v184, v189\n"
"v_mul_f32 v190, v184, v190\n"
"v_accvgpr_write_b32 a48, v187\n"
"v_accvgpr_write_b32 a49, v188\n"
"v_accvgpr_write_b32 a50, v189\n"
"v_accvgpr_write_b32 a51, v190\n"
"v_accvgpr_read_b32 v187, a52\n"
"v_accvgpr_read_b32 v188, a53\n"
"v_accvgpr_read_b32 v189, a54\n"
"v_accvgpr_read_b32 v190, a55\n"
"v_mul_f32 v187, v184, v187\n"
"v_mul_f32 v188, v184, v188\n"
"v_mul_f32 v189, v184, v189\n"
"v_mul_f32 v190, v184, v190\n"
"v_accvgpr_write_b32 a52, v187\n"
"v_accvgpr_write_b32 a53, v188\n"
"v_accvgpr_write_b32 a54, v189\n"
"v_accvgpr_write_b32 a55, v190\n"
"v_accvgpr_read_b32 v187, a56\n"
"v_accvgpr_read_b32 v188, a57\n"
"v_accvgpr_read_b32 v189, a58\n"
"v_accvgpr_read_b32 v190, a59\n"
"v_mul_f32 v187, v184, v187\n"
"v_mul_f32 v188, v184, v188\n"
"v_mul_f32 v189, v184, v189\n"
"v_mul_f32 v190, v184, v190\n"
"v_accvgpr_write_b32 a56, v187\n"
"v_accvgpr_write_b32 a57, v188\n"
"v_accvgpr_write_b32 a58, v189\n"
"v_accvgpr_write_b32 a59, v190\n"
"v_accvgpr_read_b32 v187, a60\n"
"v_accvgpr_read_b32 v188, a61\n"
"v_accvgpr_read_b32 v189, a62\n"
"v_accvgpr_read_b32 v190, a63\n"
"v_mul_f32 v187, v184, v187\n"
"v_mul_f32 v188, v184, v188\n"
"v_mul_f32 v189, v184, v189\n"
"v_mul_f32 v190, v184, v190\n"
"v_accvgpr_write_b32 a60, v187\n"
"v_accvgpr_write_b32 a61, v188\n"
"v_accvgpr_write_b32 a62, v189\n"
"v_accvgpr_write_b32 a63, v190\n"
"v_accvgpr_read_b32 v187, a192\n"
"v_accvgpr_read_b32 v188, a193\n"
"v_accvgpr_read_b32 v189, a194\n"
"v_accvgpr_read_b32 v190, a195\n"
"v_mul_f32 v187, v184, v187\n"
"v_mul_f32 v188, v184, v188\n"
"v_mul_f32 v189, v184, v189\n"
"v_mul_f32 v190, v184, v190\n"
"v_accvgpr_write_b32 a192, v187\n"
"v_accvgpr_write_b32 a193, v188\n"
"v_accvgpr_write_b32 a194, v189\n"
"v_accvgpr_write_b32 a195, v190\n"
"v_accvgpr_read_b32 v187, a64\n"
"v_accvgpr_read_b32 v188, a65\n"
"v_accvgpr_read_b32 v189, a66\n"
"v_accvgpr_read_b32 v190, a67\n"
"v_mul_f32 v187, v185, v187\n"
"v_mul_f32 v188, v185, v188\n"
"v_mul_f32 v189, v185, v189\n"
"v_mul_f32 v190, v185, v190\n"
"v_accvgpr_write_b32 a64, v187\n"
"v_accvgpr_write_b32 a65, v188\n"
"v_accvgpr_write_b32 a66, v189\n"
"v_accvgpr_write_b32 a67, v190\n"
"v_accvgpr_read_b32 v187, a68\n"
"v_accvgpr_read_b32 v188, a69\n"
"v_accvgpr_read_b32 v189, a70\n"
"v_accvgpr_read_b32 v190, a71\n"
"v_mul_f32 v187, v185, v187\n"
"v_mul_f32 v188, v185, v188\n"
"v_mul_f32 v189, v185, v189\n"
"v_mul_f32 v190, v185, v190\n"
"v_accvgpr_write_b32 a68, v187\n"
"v_accvgpr_write_b32 a69, v188\n"
"v_accvgpr_write_b32 a70, v189\n"
"v_accvgpr_write_b32 a71, v190\n"
"v_accvgpr_read_b32 v187, a72\n"
"v_accvgpr_read_b32 v188, a73\n"
"v_accvgpr_read_b32 v189, a74\n"
"v_accvgpr_read_b32 v190, a75\n"
"v_mul_f32 v187, v185, v187\n"
"v_mul_f32 v188, v185, v188\n"
"v_mul_f32 v189, v185, v189\n"
"v_mul_f32 v190, v185, v190\n"
"v_accvgpr_write_b32 a72, v187\n"
"v_accvgpr_write_b32 a73, v188\n"
"v_accvgpr_write_b32 a74, v189\n"
"v_accvgpr_write_b32 a75, v190\n"
"v_accvgpr_read_b32 v187, a76\n"
"v_accvgpr_read_b32 v188, a77\n"
"v_accvgpr_read_b32 v189, a78\n"
"v_accvgpr_read_b32 v190, a79\n"
"v_mul_f32 v187, v185, v187\n"
"v_mul_f32 v188, v185, v188\n"
"v_mul_f32 v189, v185, v189\n"
"v_mul_f32 v190, v185, v190\n"
"v_accvgpr_write_b32 a76, v187\n"
"v_accvgpr_write_b32 a77, v188\n"
"v_accvgpr_write_b32 a78, v189\n"
"v_accvgpr_write_b32 a79, v190\n"
"v_accvgpr_read_b32 v187, a80\n"
"v_accvgpr_read_b32 v188, a81\n"
"v_accvgpr_read_b32 v189, a82\n"
"v_accvgpr_read_b32 v190, a83\n"
"v_mul_f32 v187, v185, v187\n"
"v_mul_f32 v188, v185, v188\n"
"v_mul_f32 v189, v185, v189\n"
"v_mul_f32 v190, v185, v190\n"
"v_accvgpr_write_b32 a80, v187\n"
"v_accvgpr_write_b32 a81, v188\n"
"v_accvgpr_write_b32 a82, v189\n"
"v_accvgpr_write_b32 a83, v190\n"
"v_accvgpr_read_b32 v187, a84\n"
"v_accvgpr_read_b32 v188, a85\n"
"v_accvgpr_read_b32 v189, a86\n"
"v_accvgpr_read_b32 v190, a87\n"
"v_mul_f32 v187, v185, v187\n"
"v_mul_f32 v188, v185, v188\n"
"v_mul_f32 v189, v185, v189\n"
"v_mul_f32 v190, v185, v190\n"
"v_accvgpr_write_b32 a84, v187\n"
"v_accvgpr_write_b32 a85, v188\n"
"v_accvgpr_write_b32 a86, v189\n"
"v_accvgpr_write_b32 a87, v190\n"
"v_accvgpr_read_b32 v187, a88\n"
"v_accvgpr_read_b32 v188, a89\n"
"v_accvgpr_read_b32 v189, a90\n"
"v_accvgpr_read_b32 v190, a91\n"
"v_mul_f32 v187, v185, v187\n"
"v_mul_f32 v188, v185, v188\n"
"v_mul_f32 v189, v185, v189\n"
"v_mul_f32 v190, v185, v190\n"
"v_accvgpr_write_b32 a88, v187\n"
"v_accvgpr_write_b32 a89, v188\n"
"v_accvgpr_write_b32 a90, v189\n"
"v_accvgpr_write_b32 a91, v190\n"
"v_accvgpr_read_b32 v187, a92\n"
"v_accvgpr_read_b32 v188, a93\n"
"v_accvgpr_read_b32 v189, a94\n"
"v_accvgpr_read_b32 v190, a95\n"
"v_mul_f32 v187, v185, v187\n"
"v_mul_f32 v188, v185, v188\n"
"v_mul_f32 v189, v185, v189\n"
"v_mul_f32 v190, v185, v190\n"
"v_accvgpr_write_b32 a92, v187\n"
"v_accvgpr_write_b32 a93, v188\n"
"v_accvgpr_write_b32 a94, v189\n"
"v_accvgpr_write_b32 a95, v190\n"
"v_accvgpr_read_b32 v187, a96\n"
"v_accvgpr_read_b32 v188, a97\n"
"v_accvgpr_read_b32 v189, a98\n"
"v_accvgpr_read_b32 v190, a99\n"
"v_mul_f32 v187, v185, v187\n"
"v_mul_f32 v188, v185, v188\n"
"v_mul_f32 v189, v185, v189\n"
"v_mul_f32 v190, v185, v190\n"
"v_accvgpr_write_b32 a96, v187\n"
"v_accvgpr_write_b32 a97, v188\n"
"v_accvgpr_write_b32 a98, v189\n"
"v_accvgpr_write_b32 a99, v190\n"
"v_accvgpr_read_b32 v187, a100\n"
"v_accvgpr_read_b32 v188, a101\n"
"v_accvgpr_read_b32 v189, a102\n"
"v_accvgpr_read_b32 v190, a103\n"
"v_mul_f32 v187, v185, v187\n"
"v_mul_f32 v188, v185, v188\n"
"v_mul_f32 v189, v185, v189\n"
"v_mul_f32 v190, v185, v190\n"
"v_accvgpr_write_b32 a100, v187\n"
"v_accvgpr_write_b32 a101, v188\n"
"v_accvgpr_write_b32 a102, v189\n"
"v_accvgpr_write_b32 a103, v190\n"
"v_accvgpr_read_b32 v187, a104\n"
"v_accvgpr_read_b32 v188, a105\n"
"v_accvgpr_read_b32 v189, a106\n"
"v_accvgpr_read_b32 v190, a107\n"
"v_mul_f32 v187, v185, v187\n"
"v_mul_f32 v188, v185, v188\n"
"v_mul_f32 v189, v185, v189\n"
"v_mul_f32 v190, v185, v190\n"
"v_accvgpr_write_b32 a104, v187\n"
"v_accvgpr_write_b32 a105, v188\n"
"v_accvgpr_write_b32 a106, v189\n"
"v_accvgpr_write_b32 a107, v190\n"
"v_accvgpr_read_b32 v187, a108\n"
"v_accvgpr_read_b32 v188, a109\n"
"v_accvgpr_read_b32 v189, a110\n"
"v_accvgpr_read_b32 v190, a111\n"
"v_mul_f32 v187, v185, v187\n"
"v_mul_f32 v188, v185, v188\n"
"v_mul_f32 v189, v185, v189\n"
"v_mul_f32 v190, v185, v190\n"
"v_accvgpr_write_b32 a108, v187\n"
"v_accvgpr_write_b32 a109, v188\n"
"v_accvgpr_write_b32 a110, v189\n"
"v_accvgpr_write_b32 a111, v190\n"
"v_accvgpr_read_b32 v187, a112\n"
"v_accvgpr_read_b32 v188, a113\n"
"v_accvgpr_read_b32 v189, a114\n"
"v_accvgpr_read_b32 v190, a115\n"
"v_mul_f32 v187, v185, v187\n"
"v_mul_f32 v188, v185, v188\n"
"v_mul_f32 v189, v185, v189\n"
"v_mul_f32 v190, v185, v190\n"
"v_accvgpr_write_b32 a112, v187\n"
"v_accvgpr_write_b32 a113, v188\n"
"v_accvgpr_write_b32 a114, v189\n"
"v_accvgpr_write_b32 a115, v190\n"
"v_accvgpr_read_b32 v187, a116\n"
"v_accvgpr_read_b32 v188, a117\n"
"v_accvgpr_read_b32 v189, a118\n"
"v_accvgpr_read_b32 v190, a119\n"
"v_mul_f32 v187, v185, v187\n"
"v_mul_f32 v188, v185, v188\n"
"v_mul_f32 v189, v185, v189\n"
"v_mul_f32 v190, v185, v190\n"
"v_accvgpr_write_b32 a116, v187\n"
"v_accvgpr_write_b32 a117, v188\n"
"v_accvgpr_write_b32 a118, v189\n"
"v_accvgpr_write_b32 a119, v190\n"
"v_accvgpr_read_b32 v187, a120\n"
"v_accvgpr_read_b32 v188, a121\n"
"v_accvgpr_read_b32 v189, a122\n"
"v_accvgpr_read_b32 v190, a123\n"
"v_mul_f32 v187, v185, v187\n"
"v_mul_f32 v188, v185, v188\n"
"v_mul_f32 v189, v185, v189\n"
"v_mul_f32 v190, v185, v190\n"
"v_accvgpr_write_b32 a120, v187\n"
"v_accvgpr_write_b32 a121, v188\n"
"v_accvgpr_write_b32 a122, v189\n"
"v_accvgpr_write_b32 a123, v190\n"
"v_accvgpr_read_b32 v187, a124\n"
"v_accvgpr_read_b32 v188, a125\n"
"v_accvgpr_read_b32 v189, a126\n"
"v_accvgpr_read_b32 v190, a127\n"
"v_mul_f32 v187, v185, v187\n"
"v_mul_f32 v188, v185, v188\n"
"v_mul_f32 v189, v185, v189\n"
"v_mul_f32 v190, v185, v190\n"
"v_accvgpr_write_b32 a124, v187\n"
"v_accvgpr_write_b32 a125, v188\n"
"v_accvgpr_write_b32 a126, v189\n"
"v_accvgpr_write_b32 a127, v190\n"
"v_accvgpr_read_b32 v187, a196\n"
"v_accvgpr_read_b32 v188, a197\n"
"v_accvgpr_read_b32 v189, a198\n"
"v_accvgpr_read_b32 v190, a199\n"
"v_mul_f32 v187, v185, v187\n"
"v_mul_f32 v188, v185, v188\n"
"v_mul_f32 v189, v185, v189\n"
"v_mul_f32 v190, v185, v190\n"
"v_accvgpr_write_b32 a196, v187\n"
"v_accvgpr_write_b32 a197, v188\n"
"v_accvgpr_write_b32 a198, v189\n"
"v_accvgpr_write_b32 a199, v190\n"
"s_nop 1\n"
"s_mov_b32 s71, 0\n"
"s_branch L_rescback_w3f\n"
"L_rare_w3l:\n"
"s_nop 7\n"
"v_mov_b32 v187, v13\n"
"s_nop 1\n"
"v_permlane32_swap_b32 v13, v187\n"
"s_nop 1\n"
"v_max_f32 v13, v13, v187\n"
"v_mov_b32 v187, v13\n"
"s_nop 1\n"
"v_permlane16_swap_b32 v13, v187\n"
"s_nop 1\n"
"v_max_f32 v13, v13, v187\n"
"v_max_f32 v13, 0, v13\n"
"v_add_f32 v17, v17, v13\n"
"v_sub_f32 v187, 0, v13\n"
"v_exp_f32 v184, v187\n"
"v_sub_f32 v112, v112, v13\n"
"v_sub_f32 v113, v113, v13\n"
"v_sub_f32 v114, v114, v13\n"
"v_sub_f32 v115, v115, v13\n"
"v_sub_f32 v124, v124, v13\n"
"v_sub_f32 v125, v125, v13\n"
"v_sub_f32 v126, v126, v13\n"
"v_sub_f32 v127, v127, v13\n"
"v_sub_f32 v136, v136, v13\n"
"v_sub_f32 v137, v137, v13\n"
"v_sub_f32 v138, v138, v13\n"
"v_sub_f32 v139, v139, v13\n"
"v_sub_f32 v148, v148, v13\n"
"v_sub_f32 v149, v149, v13\n"
"v_sub_f32 v150, v150, v13\n"
"v_sub_f32 v151, v151, v13\n"
"v_sub_f32 v52, 0, v17\n"
"v_sub_f32 v53, 0, v17\n"
"v_sub_f32 v54, 0, v17\n"
"v_sub_f32 v55, 0, v17\n"
"v_mov_b32 v187, v14\n"
"s_nop 1\n"
"v_permlane32_swap_b32 v14, v187\n"
"s_nop 1\n"
"v_max_f32 v14, v14, v187\n"
"v_mov_b32 v187, v14\n"
"s_nop 1\n"
"v_permlane16_swap_b32 v14, v187\n"
"s_nop 1\n"
"v_max_f32 v14, v14, v187\n"
"v_max_f32 v14, 0, v14\n"
"v_add_f32 v18, v18, v14\n"
"v_sub_f32 v187, 0, v14\n"
"v_exp_f32 v185, v187\n"
"v_sub_f32 v116, v116, v14\n"
"v_sub_f32 v117, v117, v14\n"
"v_sub_f32 v118, v118, v14\n"
"v_sub_f32 v119, v119, v14\n"
"v_sub_f32 v128, v128, v14\n"
"v_sub_f32 v129, v129, v14\n"
"v_sub_f32 v130, v130, v14\n"
"v_sub_f32 v131, v131, v14\n"
"v_sub_f32 v140, v140, v14\n"
"v_sub_f32 v141, v141, v14\n"
"v_sub_f32 v142, v142, v14\n"
"v_sub_f32 v143, v143, v14\n"
"v_sub_f32 v152, v152, v14\n"
"v_sub_f32 v153, v153, v14\n"
"v_sub_f32 v154, v154, v14\n"
"v_sub_f32 v155, v155, v14\n"
"v_sub_f32 v56, 0, v18\n"
"v_sub_f32 v57, 0, v18\n"
"v_sub_f32 v58, 0, v18\n"
"v_sub_f32 v59, 0, v18\n"
"s_nop 1\n"
"s_mov_b32 s71, 1\n"
"s_branch L_back_w3l\n"
"L_resc_w3l:\n"
"s_nop 7\n"
"v_accvgpr_read_b32 v187, a0\n"
"v_accvgpr_read_b32 v188, a1\n"
"v_accvgpr_read_b32 v189, a2\n"
"v_accvgpr_read_b32 v190, a3\n"
"v_mul_f32 v187, v184, v187\n"
"v_mul_f32 v188, v184, v188\n"
"v_mul_f32 v189, v184, v189\n"
"v_mul_f32 v190, v184, v190\n"
"v_accvgpr_write_b32 a0, v187\n"
"v_accvgpr_write_b32 a1, v188\n"
"v_accvgpr_write_b32 a2, v189\n"
"v_accvgpr_write_b32 a3, v190\n"
"v_accvgpr_read_b32 v187, a4\n"
"v_accvgpr_read_b32 v188, a5\n"
"v_accvgpr_read_b32 v189, a6\n"
"v_accvgpr_read_b32 v190, a7\n"
"v_mul_f32 v187, v184, v187\n"
"v_mul_f32 v188, v184, v188\n"
"v_mul_f32 v189, v184, v189\n"
"v_mul_f32 v190, v184, v190\n"
"v_accvgpr_write_b32 a4, v187\n"
"v_accvgpr_write_b32 a5, v188\n"
"v_accvgpr_write_b32 a6, v189\n"
"v_accvgpr_write_b32 a7, v190\n"
"v_accvgpr_read_b32 v187, a8\n"
"v_accvgpr_read_b32 v188, a9\n"
"v_accvgpr_read_b32 v189, a10\n"
"v_accvgpr_read_b32 v190, a11\n"
"v_mul_f32 v187, v184, v187\n"
"v_mul_f32 v188, v184, v188\n"
"v_mul_f32 v189, v184, v189\n"
"v_mul_f32 v190, v184, v190\n"
"v_accvgpr_write_b32 a8, v187\n"
"v_accvgpr_write_b32 a9, v188\n"
"v_accvgpr_write_b32 a10, v189\n"
"v_accvgpr_write_b32 a11, v190\n"
"v_accvgpr_read_b32 v187, a12\n"
"v_accvgpr_read_b32 v188, a13\n"
"v_accvgpr_read_b32 v189, a14\n"
"v_accvgpr_read_b32 v190, a15\n"
"v_mul_f32 v187, v184, v187\n"
"v_mul_f32 v188, v184, v188\n"
"v_mul_f32 v189, v184, v189\n"
"v_mul_f32 v190, v184, v190\n"
"v_accvgpr_write_b32 a12, v187\n"
"v_accvgpr_write_b32 a13, v188\n"
"v_accvgpr_write_b32 a14, v189\n"
"v_accvgpr_write_b32 a15, v190\n"
"v_accvgpr_read_b32 v187, a16\n"
"v_accvgpr_read_b32 v188, a17\n"
"v_accvgpr_read_b32 v189, a18\n"
"v_accvgpr_read_b32 v190, a19\n"
"v_mul_f32 v187, v184, v187\n"
"v_mul_f32 v188, v184, v188\n"
"v_mul_f32 v189, v184, v189\n"
"v_mul_f32 v190, v184, v190\n"
"v_accvgpr_write_b32 a16, v187\n"
"v_accvgpr_write_b32 a17, v188\n"
"v_accvgpr_write_b32 a18, v189\n"
"v_accvgpr_write_b32 a19, v190\n"
"v_accvgpr_read_b32 v187, a20\n"
"v_accvgpr_read_b32 v188, a21\n"
"v_accvgpr_read_b32 v189, a22\n"
"v_accvgpr_read_b32 v190, a23\n"
"v_mul_f32 v187, v184, v187\n"
"v_mul_f32 v188, v184, v188\n"
"v_mul_f32 v189, v184, v189\n"
"v_mul_f32 v190, v184, v190\n"
"v_accvgpr_write_b32 a20, v187\n"
"v_accvgpr_write_b32 a21, v188\n"
"v_accvgpr_write_b32 a22, v189\n"
"v_accvgpr_write_b32 a23, v190\n"
"v_accvgpr_read_b32 v187, a24\n"
"v_accvgpr_read_b32 v188, a25\n"
"v_accvgpr_read_b32 v189, a26\n"
"v_accvgpr_read_b32 v190, a27\n"
"v_mul_f32 v187, v184, v187\n"
"v_mul_f32 v188, v184, v188\n"
"v_mul_f32 v189, v184, v189\n"
"v_mul_f32 v190, v184, v190\n"
"v_accvgpr_write_b32 a24, v187\n"
"v_accvgpr_write_b32 a25, v188\n"
"v_accvgpr_write_b32 a26, v189\n"
"v_accvgpr_write_b32 a27, v190\n"
"v_accvgpr_read_b32 v187, a28\n"
"v_accvgpr_read_b32 v188, a29\n"
"v_accvgpr_read_b32 v189, a30\n"
"v_accvgpr_read_b32 v190, a31\n"
"v_mul_f32 v187, v184, v187\n"
"v_mul_f32 v188, v184, v188\n"
"v_mul_f32 v189, v184, v189\n"
"v_mul_f32 v190, v184, v190\n"
"v_accvgpr_write_b32 a28, v187\n"
"v_accvgpr_write_b32 a29, v188\n"
"v_accvgpr_write_b32 a30, v189\n"
"v_accvgpr_write_b32 a31, v190\n"
"v_accvgpr_read_b32 v187, a32\n"
"v_accvgpr_read_b32 v188, a33\n"
"v_accvgpr_read_b32 v189, a34\n"
"v_accvgpr_read_b32 v190, a35\n"
"v_mul_f32 v187, v184, v187\n"
"v_mul_f32 v188, v184, v188\n"
"v_mul_f32 v189, v184, v189\n"
"v_mul_f32 v190, v184, v190\n"
"v_accvgpr_write_b32 a32, v187\n"
"v_accvgpr_write_b32 a33, v188\n"
"v_accvgpr_write_b32 a34, v189\n"
"v_accvgpr_write_b32 a35, v190\n"
"v_accvgpr_read_b32 v187, a36\n"
"v_accvgpr_read_b32 v188, a37\n"
"v_accvgpr_read_b32 v189, a38\n"
"v_accvgpr_read_b32 v190, a39\n"
"v_mul_f32 v187, v184, v187\n"
"v_mul_f32 v188, v184, v188\n"
"v_mul_f32 v189, v184, v189\n"
"v_mul_f32 v190, v184, v190\n"
"v_accvgpr_write_b32 a36, v187\n"
"v_accvgpr_write_b32 a37, v188\n"
"v_accvgpr_write_b32 a38, v189\n"
"v_accvgpr_write_b32 a39, v190\n"
"v_accvgpr_read_b32 v187, a40\n"
"v_accvgpr_read_b32 v188, a41\n"
"v_accvgpr_read_b32 v189, a42\n"
"v_accvgpr_read_b32 v190, a43\n"
"v_mul_f32 v187, v184, v187\n"
"v_mul_f32 v188, v184, v188\n"
"v_mul_f32 v189, v184, v189\n"
"v_mul_f32 v190, v184, v190\n"
"v_accvgpr_write_b32 a40, v187\n"
"v_accvgpr_write_b32 a41, v188\n"
"v_accvgpr_write_b32 a42, v189\n"
"v_accvgpr_write_b32 a43, v190\n"
"v_accvgpr_read_b32 v187, a44\n"
"v_accvgpr_read_b32 v188, a45\n"
"v_accvgpr_read_b32 v189, a46\n"
"v_accvgpr_read_b32 v190, a47\n"
"v_mul_f32 v187, v184, v187\n"
"v_mul_f32 v188, v184, v188\n"
"v_mul_f32 v189, v184, v189\n"
"v_mul_f32 v190, v184, v190\n"
"v_accvgpr_write_b32 a44, v187\n"
"v_accvgpr_write_b32 a45, v188\n"
"v_accvgpr_write_b32 a46, v189\n"
"v_accvgpr_write_b32 a47, v190\n"
"v_accvgpr_read_b32 v187, a48\n"
"v_accvgpr_read_b32 v188, a49\n"
"v_accvgpr_read_b32 v189, a50\n"
"v_accvgpr_read_b32 v190, a51\n"
"v_mul_f32 v187, v184, v187\n"
"v_mul_f32 v188, v184, v188\n"
"v_mul_f32 v189, v184, v189\n"
"v_mul_f32 v190, v184, v190\n"
"v_accvgpr_write_b32 a48, v187\n"
"v_accvgpr_write_b32 a49, v188\n"
"v_accvgpr_write_b32 a50, v189\n"
"v_accvgpr_write_b32 a51, v190\n"
"v_accvgpr_read_b32 v187, a52\n"
"v_accvgpr_read_b32 v188, a53\n"
"v_accvgpr_read_b32 v189, a54\n"
"v_accvgpr_read_b32 v190, a55\n"
"v_mul_f32 v187, v184, v187\n"
"v_mul_f32 v188, v184, v188\n"
"v_mul_f32 v189, v184, v189\n"
"v_mul_f32 v190, v184, v190\n"
"v_accvgpr_write_b32 a52, v187\n"
"v_accvgpr_write_b32 a53, v188\n"
"v_accvgpr_write_b32 a54, v189\n"
"v_accvgpr_write_b32 a55, v190\n"
"v_accvgpr_read_b32 v187, a56\n"
"v_accvgpr_read_b32 v188, a57\n"
"v_accvgpr_read_b32 v189, a58\n"
"v_accvgpr_read_b32 v190, a59\n"
"v_mul_f32 v187, v184, v187\n"
"v_mul_f32 v188, v184, v188\n"
"v_mul_f32 v189, v184, v189\n"
"v_mul_f32 v190, v184, v190\n"
"v_accvgpr_write_b32 a56, v187\n"
"v_accvgpr_write_b32 a57, v188\n"
"v_accvgpr_write_b32 a58, v189\n"
"v_accvgpr_write_b32 a59, v190\n"
"v_accvgpr_read_b32 v187, a60\n"
"v_accvgpr_read_b32 v188, a61\n"
"v_accvgpr_read_b32 v189, a62\n"
"v_accvgpr_read_b32 v190, a63\n"
"v_mul_f32 v187, v184, v187\n"
"v_mul_f32 v188, v184, v188\n"
"v_mul_f32 v189, v184, v189\n"
"v_mul_f32 v190, v184, v190\n"
"v_accvgpr_write_b32 a60, v187\n"
"v_accvgpr_write_b32 a61, v188\n"
"v_accvgpr_write_b32 a62, v189\n"
"v_accvgpr_write_b32 a63, v190\n"
"v_accvgpr_read_b32 v187, a192\n"
"v_accvgpr_read_b32 v188, a193\n"
"v_accvgpr_read_b32 v189, a194\n"
"v_accvgpr_read_b32 v190, a195\n"
"v_mul_f32 v187, v184, v187\n"
"v_mul_f32 v188, v184, v188\n"
"v_mul_f32 v189, v184, v189\n"
"v_mul_f32 v190, v184, v190\n"
"v_accvgpr_write_b32 a192, v187\n"
"v_accvgpr_write_b32 a193, v188\n"
"v_accvgpr_write_b32 a194, v189\n"
"v_accvgpr_write_b32 a195, v190\n"
"v_accvgpr_read_b32 v187, a64\n"
"v_accvgpr_read_b32 v188, a65\n"
"v_accvgpr_read_b32 v189, a66\n"
"v_accvgpr_read_b32 v190, a67\n"
"v_mul_f32 v187, v185, v187\n"
"v_mul_f32 v188, v185, v188\n"
"v_mul_f32 v189, v185, v189\n"
"v_mul_f32 v190, v185, v190\n"
"v_accvgpr_write_b32 a64, v187\n"
"v_accvgpr_write_b32 a65, v188\n"
"v_accvgpr_write_b32 a66, v189\n"
"v_accvgpr_write_b32 a67, v190\n"
"v_accvgpr_read_b32 v187, a68\n"
"v_accvgpr_read_b32 v188, a69\n"
"v_accvgpr_read_b32 v189, a70\n"
"v_accvgpr_read_b32 v190, a71\n"
"v_mul_f32 v187, v185, v187\n"
"v_mul_f32 v188, v185, v188\n"
"v_mul_f32 v189, v185, v189\n"
"v_mul_f32 v190, v185, v190\n"
"v_accvgpr_write_b32 a68, v187\n"
"v_accvgpr_write_b32 a69, v188\n"
"v_accvgpr_write_b32 a70, v189\n"
"v_accvgpr_write_b32 a71, v190\n"
"v_accvgpr_read_b32 v187, a72\n"
"v_accvgpr_read_b32 v188, a73\n"
"v_accvgpr_read_b32 v189, a74\n"
"v_accvgpr_read_b32 v190, a75\n"
"v_mul_f32 v187, v185, v187\n"
"v_mul_f32 v188, v185, v188\n"
"v_mul_f32 v189, v185, v189\n"
"v_mul_f32 v190, v185, v190\n"
"v_accvgpr_write_b32 a72, v187\n"
"v_accvgpr_write_b32 a73, v188\n"
"v_accvgpr_write_b32 a74, v189\n"
"v_accvgpr_write_b32 a75, v190\n"
"v_accvgpr_read_b32 v187, a76\n"
"v_accvgpr_read_b32 v188, a77\n"
"v_accvgpr_read_b32 v189, a78\n"
"v_accvgpr_read_b32 v190, a79\n"
"v_mul_f32 v187, v185, v187\n"
"v_mul_f32 v188, v185, v188\n"
"v_mul_f32 v189, v185, v189\n"
"v_mul_f32 v190, v185, v190\n"
"v_accvgpr_write_b32 a76, v187\n"
"v_accvgpr_write_b32 a77, v188\n"
"v_accvgpr_write_b32 a78, v189\n"
"v_accvgpr_write_b32 a79, v190\n"
"v_accvgpr_read_b32 v187, a80\n"
"v_accvgpr_read_b32 v188, a81\n"
"v_accvgpr_read_b32 v189, a82\n"
"v_accvgpr_read_b32 v190, a83\n"
"v_mul_f32 v187, v185, v187\n"
"v_mul_f32 v188, v185, v188\n"
"v_mul_f32 v189, v185, v189\n"
"v_mul_f32 v190, v185, v190\n"
"v_accvgpr_write_b32 a80, v187\n"
"v_accvgpr_write_b32 a81, v188\n"
"v_accvgpr_write_b32 a82, v189\n"
"v_accvgpr_write_b32 a83, v190\n"
"v_accvgpr_read_b32 v187, a84\n"
"v_accvgpr_read_b32 v188, a85\n"
"v_accvgpr_read_b32 v189, a86\n"
"v_accvgpr_read_b32 v190, a87\n"
"v_mul_f32 v187, v185, v187\n"
"v_mul_f32 v188, v185, v188\n"
"v_mul_f32 v189, v185, v189\n"
"v_mul_f32 v190, v185, v190\n"
"v_accvgpr_write_b32 a84, v187\n"
"v_accvgpr_write_b32 a85, v188\n"
"v_accvgpr_write_b32 a86, v189\n"
"v_accvgpr_write_b32 a87, v190\n"
"v_accvgpr_read_b32 v187, a88\n"
"v_accvgpr_read_b32 v188, a89\n"
"v_accvgpr_read_b32 v189, a90\n"
"v_accvgpr_read_b32 v190, a91\n"
"v_mul_f32 v187, v185, v187\n"
"v_mul_f32 v188, v185, v188\n"
"v_mul_f32 v189, v185, v189\n"
"v_mul_f32 v190, v185, v190\n"
"v_accvgpr_write_b32 a88, v187\n"
"v_accvgpr_write_b32 a89, v188\n"
"v_accvgpr_write_b32 a90, v189\n"
"v_accvgpr_write_b32 a91, v190\n"
"v_accvgpr_read_b32 v187, a92\n"
"v_accvgpr_read_b32 v188, a93\n"
"v_accvgpr_read_b32 v189, a94\n"
"v_accvgpr_read_b32 v190, a95\n"
"v_mul_f32 v187, v185, v187\n"
"v_mul_f32 v188, v185, v188\n"
"v_mul_f32 v189, v185, v189\n"
"v_mul_f32 v190, v185, v190\n"
"v_accvgpr_write_b32 a92, v187\n"
"v_accvgpr_write_b32 a93, v188\n"
"v_accvgpr_write_b32 a94, v189\n"
"v_accvgpr_write_b32 a95, v190\n"
"v_accvgpr_read_b32 v187, a96\n"
"v_accvgpr_read_b32 v188, a97\n"
"v_accvgpr_read_b32 v189, a98\n"
"v_accvgpr_read_b32 v190, a99\n"
"v_mul_f32 v187, v185, v187\n"
"v_mul_f32 v188, v185, v188\n"
"v_mul_f32 v189, v185, v189\n"
"v_mul_f32 v190, v185, v190\n"
"v_accvgpr_write_b32 a96, v187\n"
"v_accvgpr_write_b32 a97, v188\n"
"v_accvgpr_write_b32 a98, v189\n"
"v_accvgpr_write_b32 a99, v190\n"
"v_accvgpr_read_b32 v187, a100\n"
"v_accvgpr_read_b32 v188, a101\n"
"v_accvgpr_read_b32 v189, a102\n"
"v_accvgpr_read_b32 v190, a103\n"
"v_mul_f32 v187, v185, v187\n"
"v_mul_f32 v188, v185, v188\n"
"v_mul_f32 v189, v185, v189\n"
"v_mul_f32 v190, v185, v190\n"
"v_accvgpr_write_b32 a100, v187\n"
"v_accvgpr_write_b32 a101, v188\n"
"v_accvgpr_write_b32 a102, v189\n"
"v_accvgpr_write_b32 a103, v190\n"
"v_accvgpr_read_b32 v187, a104\n"
"v_accvgpr_read_b32 v188, a105\n"
"v_accvgpr_read_b32 v189, a106\n"
"v_accvgpr_read_b32 v190, a107\n"
"v_mul_f32 v187, v185, v187\n"
"v_mul_f32 v188, v185, v188\n"
"v_mul_f32 v189, v185, v189\n"
"v_mul_f32 v190, v185, v190\n"
"v_accvgpr_write_b32 a104, v187\n"
"v_accvgpr_write_b32 a105, v188\n"
"v_accvgpr_write_b32 a106, v189\n"
"v_accvgpr_write_b32 a107, v190\n"
"v_accvgpr_read_b32 v187, a108\n"
"v_accvgpr_read_b32 v188, a109\n"
"v_accvgpr_read_b32 v189, a110\n"
"v_accvgpr_read_b32 v190, a111\n"
"v_mul_f32 v187, v185, v187\n"
"v_mul_f32 v188, v185, v188\n"
"v_mul_f32 v189, v185, v189\n"
"v_mul_f32 v190, v185, v190\n"
"v_accvgpr_write_b32 a108, v187\n"
"v_accvgpr_write_b32 a109, v188\n"
"v_accvgpr_write_b32 a110, v189\n"
"v_accvgpr_write_b32 a111, v190\n"
"v_accvgpr_read_b32 v187, a112\n"
"v_accvgpr_read_b32 v188, a113\n"
"v_accvgpr_read_b32 v189, a114\n"
"v_accvgpr_read_b32 v190, a115\n"
"v_mul_f32 v187, v185, v187\n"
"v_mul_f32 v188, v185, v188\n"
"v_mul_f32 v189, v185, v189\n"
"v_mul_f32 v190, v185, v190\n"
"v_accvgpr_write_b32 a112, v187\n"
"v_accvgpr_write_b32 a113, v188\n"
"v_accvgpr_write_b32 a114, v189\n"
"v_accvgpr_write_b32 a115, v190\n"
"v_accvgpr_read_b32 v187, a116\n"
"v_accvgpr_read_b32 v188, a117\n"
"v_accvgpr_read_b32 v189, a118\n"
"v_accvgpr_read_b32 v190, a119\n"
"v_mul_f32 v187, v185, v187\n"
"v_mul_f32 v188, v185, v188\n"
"v_mul_f32 v189, v185, v189\n"
"v_mul_f32 v190, v185, v190\n"
"v_accvgpr_write_b32 a116, v187\n"
"v_accvgpr_write_b32 a117, v188\n"
"v_accvgpr_write_b32 a118, v189\n"
"v_accvgpr_write_b32 a119, v190\n"
"v_accvgpr_read_b32 v187, a120\n"
"v_accvgpr_read_b32 v188, a121\n"
"v_accvgpr_read_b32 v189, a122\n"
"v_accvgpr_read_b32 v190, a123\n"
"v_mul_f32 v187, v185, v187\n"
"v_mul_f32 v188, v185, v188\n"
"v_mul_f32 v189, v185, v189\n"
"v_mul_f32 v190, v185, v190\n"
"v_accvgpr_write_b32 a120, v187\n"
"v_accvgpr_write_b32 a121, v188\n"
"v_accvgpr_write_b32 a122, v189\n"
"v_accvgpr_write_b32 a123, v190\n"
"v_accvgpr_read_b32 v187, a124\n"
"v_accvgpr_read_b32 v188, a125\n"
"v_accvgpr_read_b32 v189, a126\n"
"v_accvgpr_read_b32 v190, a127\n"
"v_mul_f32 v187, v185, v187\n"
"v_mul_f32 v188, v185, v188\n"
"v_mul_f32 v189, v185, v189\n"
"v_mul_f32 v190, v185, v190\n"
"v_accvgpr_write_b32 a124, v187\n"
"v_accvgpr_write_b32 a125, v188\n"
"v_accvgpr_write_b32 a126, v189\n"
"v_accvgpr_write_b32 a127, v190\n"
"v_accvgpr_read_b32 v187, a196\n"
"v_accvgpr_read_b32 v188, a197\n"
"v_accvgpr_read_b32 v189, a198\n"
"v_accvgpr_read_b32 v190, a199\n"
"v_mul_f32 v187, v185, v187\n"
"v_mul_f32 v188, v185, v188\n"
"v_mul_f32 v189, v185, v189\n"
"v_mul_f32 v190, v185, v190\n"
"v_accvgpr_write_b32 a196, v187\n"
"v_accvgpr_write_b32 a197, v188\n"
"v_accvgpr_write_b32 a198, v189\n"
"v_accvgpr_write_b32 a199, v190\n"
"s_nop 1\n"
"s_mov_b32 s71, 0\n"
"s_branch L_rescback_w3l\n"
"L_rare_w3e:\n"
"s_nop 7\n"
"v_mov_b32 v187, v13\n"
"s_nop 1\n"
"v_permlane32_swap_b32 v13, v187\n"
"s_nop 1\n"
"v_max_f32 v13, v13, v187\n"
"v_mov_b32 v187, v13\n"
"s_nop 1\n"
"v_permlane16_swap_b32 v13, v187\n"
"s_nop 1\n"
"v_max_f32 v13, v13, v187\n"
"v_max_f32 v13, 0, v13\n"
"v_add_f32 v17, v17, v13\n"
"v_sub_f32 v187, 0, v13\n"
"v_exp_f32 v184, v187\n"
"v_sub_f32 v112, v112, v13\n"
"v_sub_f32 v113, v113, v13\n"
"v_sub_f32 v114, v114, v13\n"
"v_sub_f32 v115, v115, v13\n"
"v_sub_f32 v124, v124, v13\n"
"v_sub_f32 v125, v125, v13\n"
"v_sub_f32 v126, v126, v13\n"
"v_sub_f32 v127, v127, v13\n"
"v_sub_f32 v136, v136, v13\n"
"v_sub_f32 v137, v137, v13\n"
"v_sub_f32 v138, v138, v13\n"
"v_sub_f32 v139, v139, v13\n"
"v_sub_f32 v148, v148, v13\n"
"v_sub_f32 v149, v149, v13\n"
"v_sub_f32 v150, v150, v13\n"
"v_sub_f32 v151, v151, v13\n"
"v_sub_f32 v52, 0, v17\n"
"v_sub_f32 v53, 0, v17\n"
"v_sub_f32 v54, 0, v17\n"
"v_sub_f32 v55, 0, v17\n"
"v_mov_b32 v187, v14\n"
"s_nop 1\n"
"v_permlane32_swap_b32 v14, v187\n"
"s_nop 1\n"
"v_max_f32 v14, v14, v187\n"
"v_mov_b32 v187, v14\n"
"s_nop 1\n"
"v_permlane16_swap_b32 v14, v187\n"
"s_nop 1\n"
"v_max_f32 v14, v14, v187\n"
"v_max_f32 v14, 0, v14\n"
"v_add_f32 v18, v18, v14\n"
"v_sub_f32 v187, 0, v14\n"
"v_exp_f32 v185, v187\n"
"v_sub_f32 v116, v116, v14\n"
"v_sub_f32 v117, v117, v14\n"
"v_sub_f32 v118, v118, v14\n"
"v_sub_f32 v119, v119, v14\n"
"v_sub_f32 v128, v128, v14\n"
"v_sub_f32 v129, v129, v14\n"
"v_sub_f32 v130, v130, v14\n"
"v_sub_f32 v131, v131, v14\n"
"v_sub_f32 v140, v140, v14\n"
"v_sub_f32 v141, v141, v14\n"
"v_sub_f32 v142, v142, v14\n"
"v_sub_f32 v143, v143, v14\n"
"v_sub_f32 v152, v152, v14\n"
"v_sub_f32 v153, v153, v14\n"
"v_sub_f32 v154, v154, v14\n"
"v_sub_f32 v155, v155, v14\n"
"v_sub_f32 v56, 0, v18\n"
"v_sub_f32 v57, 0, v18\n"
"v_sub_f32 v58, 0, v18\n"
"v_sub_f32 v59, 0, v18\n"
"s_nop 1\n"
"s_mov_b32 s71, 1\n"
"s_branch L_back_w3e\n"
"L_resc_w3e:\n"
"s_nop 7\n"
"v_accvgpr_read_b32 v187, a0\n"
"v_accvgpr_read_b32 v188, a1\n"
"v_accvgpr_read_b32 v189, a2\n"
"v_accvgpr_read_b32 v190, a3\n"
"v_mul_f32 v187, v184, v187\n"
"v_mul_f32 v188, v184, v188\n"
"v_mul_f32 v189, v184, v189\n"
"v_mul_f32 v190, v184, v190\n"
"v_accvgpr_write_b32 a0, v187\n"
"v_accvgpr_write_b32 a1, v188\n"
"v_accvgpr_write_b32 a2, v189\n"
"v_accvgpr_write_b32 a3, v190\n"
"v_accvgpr_read_b32 v187, a4\n"
"v_accvgpr_read_b32 v188, a5\n"
"v_accvgpr_read_b32 v189, a6\n"
"v_accvgpr_read_b32 v190, a7\n"
"v_mul_f32 v187, v184, v187\n"
"v_mul_f32 v188, v184, v188\n"
"v_mul_f32 v189, v184, v189\n"
"v_mul_f32 v190, v184, v190\n"
"v_accvgpr_write_b32 a4, v187\n"
"v_accvgpr_write_b32 a5, v188\n"
"v_accvgpr_write_b32 a6, v189\n"
"v_accvgpr_write_b32 a7, v190\n"
"v_accvgpr_read_b32 v187, a8\n"
"v_accvgpr_read_b32 v188, a9\n"
"v_accvgpr_read_b32 v189, a10\n"
"v_accvgpr_read_b32 v190, a11\n"
"v_mul_f32 v187, v184, v187\n"
"v_mul_f32 v188, v184, v188\n"
"v_mul_f32 v189, v184, v189\n"
"v_mul_f32 v190, v184, v190\n"
"v_accvgpr_write_b32 a8, v187\n"
"v_accvgpr_write_b32 a9, v188\n"
"v_accvgpr_write_b32 a10, v189\n"
"v_accvgpr_write_b32 a11, v190\n"
"v_accvgpr_read_b32 v187, a12\n"
"v_accvgpr_read_b32 v188, a13\n"
"v_accvgpr_read_b32 v189, a14\n"
"v_accvgpr_read_b32 v190, a15\n"
"v_mul_f32 v187, v184, v187\n"
"v_mul_f32 v188, v184, v188\n"
"v_mul_f32 v189, v184, v189\n"
"v_mul_f32 v190, v184, v190\n"
"v_accvgpr_write_b32 a12, v187\n"
"v_accvgpr_write_b32 a13, v188\n"
"v_accvgpr_write_b32 a14, v189\n"
"v_accvgpr_write_b32 a15, v190\n"
"v_accvgpr_read_b32 v187, a16\n"
"v_accvgpr_read_b32 v188, a17\n"
"v_accvgpr_read_b32 v189, a18\n"
"v_accvgpr_read_b32 v190, a19\n"
"v_mul_f32 v187, v184, v187\n"
"v_mul_f32 v188, v184, v188\n"
"v_mul_f32 v189, v184, v189\n"
"v_mul_f32 v190, v184, v190\n"
"v_accvgpr_write_b32 a16, v187\n"
"v_accvgpr_write_b32 a17, v188\n"
"v_accvgpr_write_b32 a18, v189\n"
"v_accvgpr_write_b32 a19, v190\n"
"v_accvgpr_read_b32 v187, a20\n"
"v_accvgpr_read_b32 v188, a21\n"
"v_accvgpr_read_b32 v189, a22\n"
"v_accvgpr_read_b32 v190, a23\n"
"v_mul_f32 v187, v184, v187\n"
"v_mul_f32 v188, v184, v188\n"
"v_mul_f32 v189, v184, v189\n"
"v_mul_f32 v190, v184, v190\n"
"v_accvgpr_write_b32 a20, v187\n"
"v_accvgpr_write_b32 a21, v188\n"
"v_accvgpr_write_b32 a22, v189\n"
"v_accvgpr_write_b32 a23, v190\n"
"v_accvgpr_read_b32 v187, a24\n"
"v_accvgpr_read_b32 v188, a25\n"
"v_accvgpr_read_b32 v189, a26\n"
"v_accvgpr_read_b32 v190, a27\n"
"v_mul_f32 v187, v184, v187\n"
"v_mul_f32 v188, v184, v188\n"
"v_mul_f32 v189, v184, v189\n"
"v_mul_f32 v190, v184, v190\n"
"v_accvgpr_write_b32 a24, v187\n"
"v_accvgpr_write_b32 a25, v188\n"
"v_accvgpr_write_b32 a26, v189\n"
"v_accvgpr_write_b32 a27, v190\n"
"v_accvgpr_read_b32 v187, a28\n"
"v_accvgpr_read_b32 v188, a29\n"
"v_accvgpr_read_b32 v189, a30\n"
"v_accvgpr_read_b32 v190, a31\n"
"v_mul_f32 v187, v184, v187\n"
"v_mul_f32 v188, v184, v188\n"
"v_mul_f32 v189, v184, v189\n"
"v_mul_f32 v190, v184, v190\n"
"v_accvgpr_write_b32 a28, v187\n"
"v_accvgpr_write_b32 a29, v188\n"
"v_accvgpr_write_b32 a30, v189\n"
"v_accvgpr_write_b32 a31, v190\n"
"v_accvgpr_read_b32 v187, a32\n"
"v_accvgpr_read_b32 v188, a33\n"
"v_accvgpr_read_b32 v189, a34\n"
"v_accvgpr_read_b32 v190, a35\n"
"v_mul_f32 v187, v184, v187\n"
"v_mul_f32 v188, v184, v188\n"
"v_mul_f32 v189, v184, v189\n"
"v_mul_f32 v190, v184, v190\n"
"v_accvgpr_write_b32 a32, v187\n"
"v_accvgpr_write_b32 a33, v188\n"
"v_accvgpr_write_b32 a34, v189\n"
"v_accvgpr_write_b32 a35, v190\n"
"v_accvgpr_read_b32 v187, a36\n"
"v_accvgpr_read_b32 v188, a37\n"
"v_accvgpr_read_b32 v189, a38\n"
"v_accvgpr_read_b32 v190, a39\n"
"v_mul_f32 v187, v184, v187\n"
"v_mul_f32 v188, v184, v188\n"
"v_mul_f32 v189, v184, v189\n"
"v_mul_f32 v190, v184, v190\n"
"v_accvgpr_write_b32 a36, v187\n"
"v_accvgpr_write_b32 a37, v188\n"
"v_accvgpr_write_b32 a38, v189\n"
"v_accvgpr_write_b32 a39, v190\n"
"v_accvgpr_read_b32 v187, a40\n"
"v_accvgpr_read_b32 v188, a41\n"
"v_accvgpr_read_b32 v189, a42\n"
"v_accvgpr_read_b32 v190, a43\n"
"v_mul_f32 v187, v184, v187\n"
"v_mul_f32 v188, v184, v188\n"
"v_mul_f32 v189, v184, v189\n"
"v_mul_f32 v190, v184, v190\n"
"v_accvgpr_write_b32 a40, v187\n"
"v_accvgpr_write_b32 a41, v188\n"
"v_accvgpr_write_b32 a42, v189\n"
"v_accvgpr_write_b32 a43, v190\n"
"v_accvgpr_read_b32 v187, a44\n"
"v_accvgpr_read_b32 v188, a45\n"
"v_accvgpr_read_b32 v189, a46\n"
"v_accvgpr_read_b32 v190, a47\n"
"v_mul_f32 v187, v184, v187\n"
"v_mul_f32 v188, v184, v188\n"
"v_mul_f32 v189, v184, v189\n"
"v_mul_f32 v190, v184, v190\n"
"v_accvgpr_write_b32 a44, v187\n"
"v_accvgpr_write_b32 a45, v188\n"
"v_accvgpr_write_b32 a46, v189\n"
"v_accvgpr_write_b32 a47, v190\n"
"v_accvgpr_read_b32 v187, a48\n"
"v_accvgpr_read_b32 v188, a49\n"
"v_accvgpr_read_b32 v189, a50\n"
"v_accvgpr_read_b32 v190, a51\n"
"v_mul_f32 v187, v184, v187\n"
"v_mul_f32 v188, v184, v188\n"
"v_mul_f32 v189, v184, v189\n"
"v_mul_f32 v190, v184, v190\n"
"v_accvgpr_write_b32 a48, v187\n"
"v_accvgpr_write_b32 a49, v188\n"
"v_accvgpr_write_b32 a50, v189\n"
"v_accvgpr_write_b32 a51, v190\n"
"v_accvgpr_read_b32 v187, a52\n"
"v_accvgpr_read_b32 v188, a53\n"
"v_accvgpr_read_b32 v189, a54\n"
"v_accvgpr_read_b32 v190, a55\n"
"v_mul_f32 v187, v184, v187\n"
"v_mul_f32 v188, v184, v188\n"
"v_mul_f32 v189, v184, v189\n"
"v_mul_f32 v190, v184, v190\n"
"v_accvgpr_write_b32 a52, v187\n"
"v_accvgpr_write_b32 a53, v188\n"
"v_accvgpr_write_b32 a54, v189\n"
"v_accvgpr_write_b32 a55, v190\n"
"v_accvgpr_read_b32 v187, a56\n"
"v_accvgpr_read_b32 v188, a57\n"
"v_accvgpr_read_b32 v189, a58\n"
"v_accvgpr_read_b32 v190, a59\n"
"v_mul_f32 v187, v184, v187\n"
"v_mul_f32 v188, v184, v188\n"
"v_mul_f32 v189, v184, v189\n"
"v_mul_f32 v190, v184, v190\n"
"v_accvgpr_write_b32 a56, v187\n"
"v_accvgpr_write_b32 a57, v188\n"
"v_accvgpr_write_b32 a58, v189\n"
"v_accvgpr_write_b32 a59, v190\n"
"v_accvgpr_read_b32 v187, a60\n"
"v_accvgpr_read_b32 v188, a61\n"
"v_accvgpr_read_b32 v189, a62\n"
"v_accvgpr_read_b32 v190, a63\n"
"v_mul_f32 v187, v184, v187\n"
"v_mul_f32 v188, v184, v188\n"
"v_mul_f32 v189, v184, v189\n"
"v_mul_f32 v190, v184, v190\n"
"v_accvgpr_write_b32 a60, v187\n"
"v_accvgpr_write_b32 a61, v188\n"
"v_accvgpr_write_b32 a62, v189\n"
"v_accvgpr_write_b32 a63, v190\n"
"v_accvgpr_read_b32 v187, a192\n"
"v_accvgpr_read_b32 v188, a193\n"
"v_accvgpr_read_b32 v189, a194\n"
"v_accvgpr_read_b32 v190, a195\n"
"v_mul_f32 v187, v184, v187\n"
"v_mul_f32 v188, v184, v188\n"
"v_mul_f32 v189, v184, v189\n"
"v_mul_f32 v190, v184, v190\n"
"v_accvgpr_write_b32 a192, v187\n"
"v_accvgpr_write_b32 a193, v188\n"
"v_accvgpr_write_b32 a194, v189\n"
"v_accvgpr_write_b32 a195, v190\n"
"v_accvgpr_read_b32 v187, a64\n"
"v_accvgpr_read_b32 v188, a65\n"
"v_accvgpr_read_b32 v189, a66\n"
"v_accvgpr_read_b32 v190, a67\n"
"v_mul_f32 v187, v185, v187\n"
"v_mul_f32 v188, v185, v188\n"
"v_mul_f32 v189, v185, v189\n"
"v_mul_f32 v190, v185, v190\n"
"v_accvgpr_write_b32 a64, v187\n"
"v_accvgpr_write_b32 a65, v188\n"
"v_accvgpr_write_b32 a66, v189\n"
"v_accvgpr_write_b32 a67, v190\n"
"v_accvgpr_read_b32 v187, a68\n"
"v_accvgpr_read_b32 v188, a69\n"
"v_accvgpr_read_b32 v189, a70\n"
"v_accvgpr_read_b32 v190, a71\n"
"v_mul_f32 v187, v185, v187\n"
"v_mul_f32 v188, v185, v188\n"
"v_mul_f32 v189, v185, v189\n"
"v_mul_f32 v190, v185, v190\n"
"v_accvgpr_write_b32 a68, v187\n"
"v_accvgpr_write_b32 a69, v188\n"
"v_accvgpr_write_b32 a70, v189\n"
"v_accvgpr_write_b32 a71, v190\n"
"v_accvgpr_read_b32 v187, a72\n"
"v_accvgpr_read_b32 v188, a73\n"
"v_accvgpr_read_b32 v189, a74\n"
"v_accvgpr_read_b32 v190, a75\n"
"v_mul_f32 v187, v185, v187\n"
"v_mul_f32 v188, v185, v188\n"
"v_mul_f32 v189, v185, v189\n"
"v_mul_f32 v190, v185, v190\n"
"v_accvgpr_write_b32 a72, v187\n"
"v_accvgpr_write_b32 a73, v188\n"
"v_accvgpr_write_b32 a74, v189\n"
"v_accvgpr_write_b32 a75, v190\n"
"v_accvgpr_read_b32 v187, a76\n"
"v_accvgpr_read_b32 v188, a77\n"
"v_accvgpr_read_b32 v189, a78\n"
"v_accvgpr_read_b32 v190, a79\n"
"v_mul_f32 v187, v185, v187\n"
"v_mul_f32 v188, v185, v188\n"
"v_mul_f32 v189, v185, v189\n"
"v_mul_f32 v190, v185, v190\n"
"v_accvgpr_write_b32 a76, v187\n"
"v_accvgpr_write_b32 a77, v188\n"
"v_accvgpr_write_b32 a78, v189\n"
"v_accvgpr_write_b32 a79, v190\n"
"v_accvgpr_read_b32 v187, a80\n"
"v_accvgpr_read_b32 v188, a81\n"
"v_accvgpr_read_b32 v189, a82\n"
"v_accvgpr_read_b32 v190, a83\n"
"v_mul_f32 v187, v185, v187\n"
"v_mul_f32 v188, v185, v188\n"
"v_mul_f32 v189, v185, v189\n"
"v_mul_f32 v190, v185, v190\n"
"v_accvgpr_write_b32 a80, v187\n"
"v_accvgpr_write_b32 a81, v188\n"
"v_accvgpr_write_b32 a82, v189\n"
"v_accvgpr_write_b32 a83, v190\n"
"v_accvgpr_read_b32 v187, a84\n"
"v_accvgpr_read_b32 v188, a85\n"
"v_accvgpr_read_b32 v189, a86\n"
"v_accvgpr_read_b32 v190, a87\n"
"v_mul_f32 v187, v185, v187\n"
"v_mul_f32 v188, v185, v188\n"
"v_mul_f32 v189, v185, v189\n"
"v_mul_f32 v190, v185, v190\n"
"v_accvgpr_write_b32 a84, v187\n"
"v_accvgpr_write_b32 a85, v188\n"
"v_accvgpr_write_b32 a86, v189\n"
"v_accvgpr_write_b32 a87, v190\n"
"v_accvgpr_read_b32 v187, a88\n"
"v_accvgpr_read_b32 v188, a89\n"
"v_accvgpr_read_b32 v189, a90\n"
"v_accvgpr_read_b32 v190, a91\n"
"v_mul_f32 v187, v185, v187\n"
"v_mul_f32 v188, v185, v188\n"
"v_mul_f32 v189, v185, v189\n"
"v_mul_f32 v190, v185, v190\n"
"v_accvgpr_write_b32 a88, v187\n"
"v_accvgpr_write_b32 a89, v188\n"
"v_accvgpr_write_b32 a90, v189\n"
"v_accvgpr_write_b32 a91, v190\n"
"v_accvgpr_read_b32 v187, a92\n"
"v_accvgpr_read_b32 v188, a93\n"
"v_accvgpr_read_b32 v189, a94\n"
"v_accvgpr_read_b32 v190, a95\n"
"v_mul_f32 v187, v185, v187\n"
"v_mul_f32 v188, v185, v188\n"
"v_mul_f32 v189, v185, v189\n"
"v_mul_f32 v190, v185, v190\n"
"v_accvgpr_write_b32 a92, v187\n"
"v_accvgpr_write_b32 a93, v188\n"
"v_accvgpr_write_b32 a94, v189\n"
"v_accvgpr_write_b32 a95, v190\n"
"v_accvgpr_read_b32 v187, a96\n"
"v_accvgpr_read_b32 v188, a97\n"
"v_accvgpr_read_b32 v189, a98\n"
"v_accvgpr_read_b32 v190, a99\n"
"v_mul_f32 v187, v185, v187\n"
"v_mul_f32 v188, v185, v188\n"
"v_mul_f32 v189, v185, v189\n"
"v_mul_f32 v190, v185, v190\n"
"v_accvgpr_write_b32 a96, v187\n"
"v_accvgpr_write_b32 a97, v188\n"
"v_accvgpr_write_b32 a98, v189\n"
"v_accvgpr_write_b32 a99, v190\n"
"v_accvgpr_read_b32 v187, a100\n"
"v_accvgpr_read_b32 v188, a101\n"
"v_accvgpr_read_b32 v189, a102\n"
"v_accvgpr_read_b32 v190, a103\n"
"v_mul_f32 v187, v185, v187\n"
"v_mul_f32 v188, v185, v188\n"
"v_mul_f32 v189, v185, v189\n"
"v_mul_f32 v190, v185, v190\n"
"v_accvgpr_write_b32 a100, v187\n"
"v_accvgpr_write_b32 a101, v188\n"
"v_accvgpr_write_b32 a102, v189\n"
"v_accvgpr_write_b32 a103, v190\n"
"v_accvgpr_read_b32 v187, a104\n"
"v_accvgpr_read_b32 v188, a105\n"
"v_accvgpr_read_b32 v189, a106\n"
"v_accvgpr_read_b32 v190, a107\n"
"v_mul_f32 v187, v185, v187\n"
"v_mul_f32 v188, v185, v188\n"
"v_mul_f32 v189, v185, v189\n"
"v_mul_f32 v190, v185, v190\n"
"v_accvgpr_write_b32 a104, v187\n"
"v_accvgpr_write_b32 a105, v188\n"
"v_accvgpr_write_b32 a106, v189\n"
"v_accvgpr_write_b32 a107, v190\n"
"v_accvgpr_read_b32 v187, a108\n"
"v_accvgpr_read_b32 v188, a109\n"
"v_accvgpr_read_b32 v189, a110\n"
"v_accvgpr_read_b32 v190, a111\n"
"v_mul_f32 v187, v185, v187\n"
"v_mul_f32 v188, v185, v188\n"
"v_mul_f32 v189, v185, v189\n"
"v_mul_f32 v190, v185, v190\n"
"v_accvgpr_write_b32 a108, v187\n"
"v_accvgpr_write_b32 a109, v188\n"
"v_accvgpr_write_b32 a110, v189\n"
"v_accvgpr_write_b32 a111, v190\n"
"v_accvgpr_read_b32 v187, a112\n"
"v_accvgpr_read_b32 v188, a113\n"
"v_accvgpr_read_b32 v189, a114\n"
"v_accvgpr_read_b32 v190, a115\n"
"v_mul_f32 v187, v185, v187\n"
"v_mul_f32 v188, v185, v188\n"
"v_mul_f32 v189, v185, v189\n"
"v_mul_f32 v190, v185, v190\n"
"v_accvgpr_write_b32 a112, v187\n"
"v_accvgpr_write_b32 a113, v188\n"
"v_accvgpr_write_b32 a114, v189\n"
"v_accvgpr_write_b32 a115, v190\n"
"v_accvgpr_read_b32 v187, a116\n"
"v_accvgpr_read_b32 v188, a117\n"
"v_accvgpr_read_b32 v189, a118\n"
"v_accvgpr_read_b32 v190, a119\n"
"v_mul_f32 v187, v185, v187\n"
"v_mul_f32 v188, v185, v188\n"
"v_mul_f32 v189, v185, v189\n"
"v_mul_f32 v190, v185, v190\n"
"v_accvgpr_write_b32 a116, v187\n"
"v_accvgpr_write_b32 a117, v188\n"
"v_accvgpr_write_b32 a118, v189\n"
"v_accvgpr_write_b32 a119, v190\n"
"v_accvgpr_read_b32 v187, a120\n"
"v_accvgpr_read_b32 v188, a121\n"
"v_accvgpr_read_b32 v189, a122\n"
"v_accvgpr_read_b32 v190, a123\n"
"v_mul_f32 v187, v185, v187\n"
"v_mul_f32 v188, v185, v188\n"
"v_mul_f32 v189, v185, v189\n"
"v_mul_f32 v190, v185, v190\n"
"v_accvgpr_write_b32 a120, v187\n"
"v_accvgpr_write_b32 a121, v188\n"
"v_accvgpr_write_b32 a122, v189\n"
"v_accvgpr_write_b32 a123, v190\n"
"v_accvgpr_read_b32 v187, a124\n"
"v_accvgpr_read_b32 v188, a125\n"
"v_accvgpr_read_b32 v189, a126\n"
"v_accvgpr_read_b32 v190, a127\n"
"v_mul_f32 v187, v185, v187\n"
"v_mul_f32 v188, v185, v188\n"
"v_mul_f32 v189, v185, v189\n"
"v_mul_f32 v190, v185, v190\n"
"v_accvgpr_write_b32 a124, v187\n"
"v_accvgpr_write_b32 a125, v188\n"
"v_accvgpr_write_b32 a126, v189\n"
"v_accvgpr_write_b32 a127, v190\n"
"v_accvgpr_read_b32 v187, a196\n"
"v_accvgpr_read_b32 v188, a197\n"
"v_accvgpr_read_b32 v189, a198\n"
"v_accvgpr_read_b32 v190, a199\n"
"v_mul_f32 v187, v185, v187\n"
"v_mul_f32 v188, v185, v188\n"
"v_mul_f32 v189, v185, v189\n"
"v_mul_f32 v190, v185, v190\n"
"v_accvgpr_write_b32 a196, v187\n"
"v_accvgpr_write_b32 a197, v188\n"
"v_accvgpr_write_b32 a198, v189\n"
"v_accvgpr_write_b32 a199, v190\n"
"s_nop 1\n"
"s_mov_b32 s71, 0\n"
"s_branch L_rescback_w3e\n"
"L_end:\n"
"s_waitcnt vmcnt(0)\n"
    :
    : [x] "s"(x), [wq] "s"(wq), [k] "s"(k), [v] "s"(v), [wo] "s"(wo), [part] "s"(part), [wnr] "s"(wnr), [linv] "s"(linv), [wave] "s"(wave), [lds] "s"(ldsb),
      [dmakv] "v"(dmakv), [dmaw] "v"(dmaw), [klane] "v"(klane), [vl0] "v"(vl0), [vl1] "v"(vl1), [wl0] "v"(wl0), [wl1] "v"(wl1),
      [dmax] "v"(dmax), [xrd0] "v"(xrd0), [xrd1] "v"(xrd1)
    : "v10","v11","v12","v13","v14","v15","v16","v17","v18","v19","v20","v21","v22","v23","v24","v25","v26","v27","v28","v29","v30","v31","v32","v33","v34","v35","v36","v37","v38","v39","v40","v41","v42","v43","v44","v45","v46","v47","v48","v49","v50","v51","v52","v53","v54","v55","v56","v57","v58","v59","v60","v61","v62","v63","v64","v65","v66","v67","v68","v69","v70","v71","v72","v73","v74","v75","v76","v77","v78","v79","v80","v81","v82","v83","v84","v85","v86","v87","v88","v89","v90","v91","v92","v93","v94","v95","v96","v97","v98","v99","v100","v101","v102","v103","v104","v105","v106","v107","v108","v109","v110","v111","v112","v113","v114","v115","v116","v117","v118","v119","v120","v121","v122","v123","v124","v125","v126","v127","v128","v129","v130","v131","v132","v133","v134","v135","v136","v137","v138","v139","v140","v141","v142","v143","v144","v145","v146","v147","v148","v149","v150","v151","v152","v153","v154","v155","v156","v157","v158","v159","v160","v161","v162","v163","v164","v165","v166","v167","v168","v169","v170","v171","v172","v173","v174","v175","v176","v177","v178","v179","v180","v181","v182","v183","v184","v185","v186","v187","v188","v189","v190","v191","v192","v193","v194","v195","v196","v197","v198","v199","v200","v201","v202","v203","v204","v205","v206","v207","v208","v209","v210","v211","v212","v213","v214","v215","v216","v217","v218","v219","v220","v221","v222","v223","v224","v225","v226","v227","v228","v229","v230","v231","v232","v233","v234","v235","v236","v237","v238","v239","v240","v241","v242","v243","v244","v245","v246","v247","v248","v249","v250","v251","v252","v253","v254","v255","a0","a1","a2","a3","a4","a5","a6","a7","a8","a9","a10","a11","a12","a13","a14","a15","a16","a17","a18","a19","a20","a21","a22","a23","a24","a25","a26","a27","a28","a29","a30","a31","a32","a33","a34","a35","a36","a37","a38","a39","a40","a41","a42","a43","a44","a45","a46","a47","a48","a49","a50","a51","a52","a53","a54","a55","a56","a57","a58","a59","a60","a61","a62","a63","a64","a65","a66","a67","a68","a69","a70","a71","a72","a73","a74","a75","a76","a77","a78","a79","a80","a81","a82","a83","a84","a85","a86","a87","a88","a89","a90","a91","a92","a93","a94","a95","a96","a97","a98","a99","a100","a101","a102","a103","a104","a105","a106","a107","a108","a109","a110","a111","a112","a113","a114","a115","a116","a117","a118","a119","a120","a121","a122","a123","a124","a125","a126","a127","a128","a129","a130","a131","a132","a133","a134","a135","a136","a137","a138","a139","a140","a141","a142","a143","a144","a145","a146","a147","a148","a149","a150","a151","a152","a153","a154","a155","a156","a157","a158","a159","a160","a161","a162","a163","a164","a165","a166","a167","a168","a169","a170","a171","a172","a173","a174","a175","a176","a177","a178","a179","a180","a181","a182","a183","a184","a185","a186","a187","a188","a189","a190","a191","a192","a193","a194","a195","a196","a197","a198","a199","a200","a201","a202","a203","a204","a205","a206","a207","a208","a209","a210","a211","a212","a213","a214","a215","a216","a217","a218","a219","a220","a221","a222","a223","a224","a225","a226","a227","a228","a229","a230","a231","a232","a233","a234","a235","a236","a237","a238","a239","a240","a241","a242","a243","a244","a245","a246","a247","a248","a249","a250","a251","a252","a253","a254","a255","s40","s41","s42","s43","s44","s45","s46","s47","s48","s49","s50","s51","s52","s53","s54","s55","s56","s57","s58","s59","s60","s61","s62","s63","s64","s65","s66","s67","s68","s69","s70","s71","s72","s73","s74","s75","s76","s77","s78","s79","s80","s81","s82","s83","s84","s85","s86","s87","s88","s89","vcc","scc","m0","memory");
}

#define K2W 4
#define K2ST 0
#define K2NT 1
__global__ __launch_bounds__(64 * K2W)
void head_reduce(const char* __restrict__ part, unsigned short* __restrict__ out) {
  const unsigned wv = blockIdx.x * K2W + (threadIdx.x >> 6), l = threadIdx.x & 63u;
  if (wv >= 16u * 154u) return;
  const unsigned pb = wv / 154u, blk = wv % 154u;
  const char* src = part + (size_t)pb * 16u * (11u * 14336u) + blk * 1024u + l * 16u;
  u32x4 v[16];
#pragma unroll
  for (int h = 0; h < 16; ++h) v[h] = K2NT ? __builtin_nontemporal_load((const u32x4*)(src + (size_t)h * (11u * 14336u))) : *(const u32x4*)(src + (size_t)h * (11u * 14336u));
  const unsigned rtl = (blk % 22u) >> 1;
  const float* li = (const float*)(part + (48u << 20)) + (size_t)pb * 16u * 176u + rtl * 16u + (l & 15u);
  float s[16];
#pragma unroll
  for (int h = 0; h < 16; ++h) s[h] = li[h * 176];
  float a[8];
#pragma unroll
  for (int i = 0; i < 8; ++i) a[i] = 0.f;
#pragma unroll
  for (int h = 0; h < 16; ++h) {
#pragma unroll
    for (int i = 0; i < 4; ++i) {
      a[2 * i] = fmaf(__uint_as_float(v[h][i] << 16), s[h], a[2 * i]);
      a[2 * i + 1] = fmaf(__uint_as_float(v[h][i] & 0xffff0000u), s[h], a[2 * i + 1]);
    }
  }
  const unsigned rt = (blk % 22u) >> 1, cu = (blk / 22u) * 2u + (blk & 1u), g = l >> 4, p = pb >> 3, b = pb & 7u;
  u32x4 o;
#pragma unroll
  for (int i = 0; i < 4; ++i) { f32x2 t = {a[2 * i], a[2 * i + 1]}; o[i] = __builtin_bit_cast(unsigned, __builtin_convertvector(t, bf16x2)); }
  u32x4* dst = (u32x4*)(out + (size_t)(b * 176u + rt * 16u + (l & 15u)) * 896u + p * 448u + cu * 32u + 16u * (g & 1u) + 8u * (g >> 1));
  *dst = o;
}

static void launch_attn(void* const* d_in, void* d_out, void* d_ws, hipStream_t stream) {
  (void)hipFuncSetAttribute((const void*)attn_fused, hipFuncAttributeMaxDynamicSharedMemorySize, LDS_BYTES);
  attn_fused<<<256, 256, LDS_BYTES, stream>>>((const char*)d_in[0], (const char*)d_in[3], (const char*)d_in[1], (const char*)d_in[2],
                                              (const char*)d_in[5], (const char*)d_in[8], (const char*)d_in[6], (const char*)d_in[7],
                                              (const char*)d_in[4], (const char*)d_in[9], (char*)d_ws, (unsigned short*)d_out);
}
static void launch_oproj(void* const* d_in, void* d_out, void* d_ws, hipStream_t stream) {
  (void)d_in;
  head_reduce<<<(2464 + K2W - 1) / K2W, 64 * K2W, 0, stream>>>((const char*)d_ws, (unsigned short*)d_out);
}
extern "C" void kernel_launch(void* const* d_in, const int* in_sizes, int n_in, void* d_out, int out_size, void* d_ws, size_t ws_size,
                              hipStream_t stream) {
  (void)in_sizes; (void)n_in; (void)out_size; (void)ws_size;
  launch_attn(d_in, d_out, d_ws, stream);
  launch_oproj(d_in, d_out, d_ws, stream);
}
```
